```python
import torch

def _softmax(x, axis=-1):
    return torch.softmax(x, dim=axis)

def _gelu(x):
    return torch.nn.functional.gelu(x, approximate='tanh')

def _silu(x):
    return torch.nn.functional.silu(x)

def _sigmoid(x):
    return torch.sigmoid(x)

def _layernorm(x, axis=-1, eps=1e-05):
    m = x.mean(dim=axis, keepdim=True)
    v = x.var(dim=axis, keepdim=True, unbiased=False)
    return (x - m) / torch.sqrt(v + eps)

def _scatter_add(tgt, idx, src, axis=0):
    return tgt.clone().index_add_(axis, idx, src)

def _segment_sum(src, seg, n):
    out = torch.zeros((n,) + src.shape[1:], dtype=src.dtype)
    return out.index_add_(0, seg, src)

def _pad(x, pw):
    flat = tuple((v for pair in reversed(pw) for v in pair))
    return torch.nn.functional.pad(x, flat)
kernel_name = 'sequential_preLN__memory_aug_recurrent_mem_tr__mixer_gmlp__prompt'

def setup_inputs():

    _w = lambda a: (lambda t: t.to(torch.bfloat16) if t.is_floating_point() else t)(torch.as_tensor(a))

    def _A_setup():
        B, S, Dm = (1, 2048, 640)
        H, Dh = (32, 192)
        M, seg = (8, 32)
        seg = min(seg, S)
        S = S // seg * seg
        return {'x': _w((lambda *s: torch.randn(*s, dtype=torch.bfloat16) * 0.05)(B, S, Dm)), 'mem0': _w((lambda *s: torch.randn(*s, dtype=torch.bfloat16) * 0.2)(B, M, Dm)), 'Wq': _w((lambda *s: torch.randn(*s, dtype=torch.bfloat16) * 1.6)(Dm, H, Dh) * Dm ** (-0.5)), 'Wk': _w((lambda *s: torch.randn(*s, dtype=torch.bfloat16) * 1.6)(Dm, H, Dh) * Dm ** (-0.5)), 'Wv': _w((lambda *s: torch.randn(*s, dtype=torch.bfloat16) * 0.2)(Dm, H, Dh) * Dm ** (-0.5)), 'Wo': _w((lambda *s: torch.randn(*s, dtype=torch.bfloat16) * 0.2)(H, Dh, Dm) * (H * Dh) ** (-0.5)), 'seg_len': seg}
    _A_in = _A_setup()

    def _B_setup():
        B, S, D, F = (1, 2048, 640, 1536)
        Fh = F // 2
        return {'x': _w((lambda *s: torch.randn(*s, dtype=torch.bfloat16) * 0.05)(B, S, D)), 'w_in': _w((lambda *s: torch.randn(*s, dtype=torch.bfloat16) * 0.4)(D, 2 * Fh)), 'w_sp': _w(0.01 * (lambda *s: torch.randn(*s, dtype=torch.bfloat16) * 0.4)(S, S)), 'b_sp': _w(1.0 + 0.01 * (lambda *s: torch.randn(*s, dtype=torch.bfloat16) * 0.05)(S)), 'w_out': _w((lambda *s: torch.randn(*s, dtype=torch.bfloat16) * 0.05)(Fh, D))}
    _B_in = _B_setup()
    return {'A_x': _A_in['x'], 'A_mem0': _A_in['mem0'], 'A_Wq': _A_in['Wq'], 'A_Wk': _A_in['Wk'], 'A_Wv': _A_in['Wv'], 'A_Wo': _A_in['Wo'], 'A_seg_len': _A_in['seg_len'], 'B_x': _B_in['x'], 'B_w_in': _B_in['w_in'], 'B_w_sp': _B_in['w_sp'], 'B_b_sp': _B_in['b_sp'], 'B_w_out': _B_in['w_out']}

def reference(A_x, A_mem0, A_Wq, A_Wk, A_Wv, A_Wo, A_seg_len, B_x, B_w_in, B_w_sp, B_b_sp, B_w_out):

    def _A_ref(x, mem0, Wq, Wk, Wv, Wo, seg_len):
        B, S, Dm = x.shape
        H, Dh = (Wq.shape[1], Wq.shape[2])
        M = mem0.shape[1]
        scale = Dh ** (-0.5)
        n_seg = S // seg_len
        L = M + seg_len
        idx = torch.arange(L)
        mask = (idx[:, None] < M) | (idx[:, None] >= idx[None, :])
        mem = mem0
        outs = []
        for s in range(n_seg):
            xs = x[:, s * seg_len:(s + 1) * seg_len, :].clone()
            h = torch.concatenate([mem, xs], axis=1)
            q = torch.einsum('btd,dhe->bhte', h, Wq)
            k = torch.einsum('btd,dhe->bhte', h, Wk)
            v = torch.einsum('btd,dhe->bhte', h, Wv)
            scores = torch.einsum('bhqe,bhke->bhqk', q, k) * scale
            scores = torch.where(mask[None, None], scores, -1000000000.0)
            p = _softmax(scores, axis=-1)
            o = torch.einsum('bhqk,bhke->bhqe', p, v)
            o = torch.einsum('bhte,hed->btd', o, Wo)
            mem = o[:, :M, :].clone()
            outs.append(o[:, M:, :])
        return torch.concatenate(outs, axis=1)

    def _B_ref(x, w_in, w_sp, b_sp, w_out):
        B, S, D = x.shape
        F = w_in.shape[-1]
        Fh = F // 2
        z = _gelu(torch.einsum('bsd,df->bsf', x, w_in))
        z1 = z[..., :Fh].clone()
        z2 = _layernorm(z[..., Fh:], axis=-1)
        g = torch.einsum('ts,bsf->btf', w_sp, z2) + b_sp[None, :, None]
        return torch.einsum('bsf,fd->bsd', z1 * g, w_out)
    x = A_x
    h = x + _A_ref(_layernorm(x, axis=-1), A_mem0, A_Wq, A_Wk, A_Wv, A_Wo, A_seg_len)
    h2 = h + _B_ref(_layernorm(h, axis=-1), B_w_in, B_w_sp, B_b_sp, B_w_out)
    return h2
```

```cpp
#define INITK
#include <hip/hip_runtime.h>
#include <hip/hip_bf16.h>
#include <stdint.h>

typedef unsigned v4u __attribute__((ext_vector_type(4)));
typedef unsigned v2u __attribute__((ext_vector_type(2)));
typedef float f32x4 __attribute__((ext_vector_type(4)));
typedef float f32x2 __attribute__((ext_vector_type(2)));
typedef __bf16 bf16x8 __attribute__((ext_vector_type(8)));
typedef __bf16 bf16x2 __attribute__((ext_vector_type(2)));
typedef short s16x4 __attribute__((ext_vector_type(4)));
typedef unsigned short u16;

constexpr unsigned WS_XF = 2097152u;
constexpr unsigned WS_UB = 46137344u;
constexpr unsigned WS_PX = 44040192u;
constexpr unsigned WS_NCAL = 6u;
constexpr unsigned WS_MEM0 = 11010048u;
constexpr unsigned WS_POL = 11141120u;
constexpr unsigned WS_RDY = 11173888u;
constexpr unsigned WS_REQ = 11206656u;
constexpr unsigned WS_ECHO = 11239424u;
constexpr unsigned WS_HB = 11534336u;
constexpr unsigned WS_FG = 11272192u;
constexpr unsigned WS_FYB = 11337728u;
constexpr unsigned WS_Z2F = 14680064u;
constexpr unsigned WS_Z1F = 17825792u;
constexpr unsigned WS_YFB = 20971520u;
constexpr unsigned WS_STZ = 24117248u;
constexpr unsigned WS_M_P = 1572864u;
constexpr unsigned WS_FL_P = 1593344u;
constexpr unsigned WS_PFQ_P = 1626112u;
constexpr unsigned WS_PFV_P = 1658880u;
constexpr unsigned WS_PFN_P = 1691648u;
constexpr unsigned WS_PFM_P = 1822720u;
constexpr unsigned WS_FX_P = 1724416u;
constexpr unsigned WS_P4 = 45088768u;
constexpr unsigned WS_FH_P = 1755136u;
constexpr unsigned WS_FZ_P = 1789952u;
constexpr unsigned long long WS_KP0 = 253063870841192ull, WS_KP1 = 688860590057006ull;
#define HEAD_Q(r) (r)
#define HEAD_V(r) (r)
constexpr int NPROBE = 3, LATE_SPINS = 0, LATE_SLEEP = 16, LATE_MIN = 4;

#define KST(ws, slot, b)
__device__ __forceinline__ float bf_lo(unsigned u) { return __uint_as_float(u << 16); }
__device__ __forceinline__ float bf_hi(unsigned u) { return __uint_as_float(u & 0xffff0000u); }
__device__ __forceinline__ unsigned pk_bf(float a, float b) {
    f32x2 v = {a, b};
    return __builtin_bit_cast(unsigned, __builtin_convertvector(v, bf16x2));
}
__device__ __forceinline__ int kk_pos(int kk) { return (int)(((kk < 10 ? WS_KP0 : WS_KP1) >> (5 * (kk < 10 ? kk : kk - 10))) & 31ull); }
template <int M> __device__ __forceinline__ float dpp_add(float v) {
    return v + __builtin_bit_cast(float, __builtin_amdgcn_update_dpp(0, __builtin_bit_cast(int, v), M, 0xf, 0xf, true));
}
__device__ __forceinline__ float half_sum(float v) {
    const unsigned u = __float_as_uint(dpp_add<0x121>(dpp_add<0x122>(dpp_add<0x124>(dpp_add<0x128>(v)))));
    const auto a = __builtin_amdgcn_permlane16_swap(u, u, false, false);
    return __uint_as_float(a[0]) + __uint_as_float(a[1]);
}

struct Row { v4u p[3]; };
__device__ __forceinline__ Row ln_load(const u16* __restrict__ x, int row, int l) {
    const v4u* src = (const v4u*)(x + (size_t)row * 640);
    Row r;
    r.p[0] = src[l]; r.p[1] = src[l + 32]; r.p[2] = l < 16 ? src[l + 64] : (v4u){0u, 0u, 0u, 0u};
    return r;
}
__device__ __forceinline__ void ln_store(const Row& r, __amdgpu_buffer_rsrc_t rs, int row, int l) {
    float s = 0.f;
#pragma unroll
    for (int i = 0; i < 3; ++i)
#pragma unroll
        for (int j = 0; j < 4; ++j) s += bf_lo(r.p[i][j]) + bf_hi(r.p[i][j]);
    const float mean = half_sum(s) * (1.f / 640.f);
    float q = 0.f;
#pragma unroll
    for (int i = 0; i < 3; ++i)
#pragma unroll
        for (int j = 0; j < 4; ++j) {
            const float a = bf_lo(r.p[i][j]) - mean, c = bf_hi(r.p[i][j]) - mean;
            if (i < 2 || l < 16) q += a * a + c * c;
        }
    const float rstd = 1.f / sqrtf(half_sum(q) * (1.f / 640.f) + 1e-5f);
    const unsigned dst = WS_XF + (row >> 5) * 40960 + ((row >> 4) & 1) * 1024 + (row & 15) * 16;
#pragma unroll
    for (int i = 0; i < 3; ++i) {
        const int pc = l + 32 * i;
        v4u o;
#pragma unroll
        for (int j = 0; j < 4; ++j) o[j] = pk_bf((bf_lo(r.p[i][j]) - mean) * rstd, (bf_hi(r.p[i][j]) - mean) * rstd);
        if (i < 2 || l < 16) __builtin_amdgcn_raw_buffer_store_b128(o, rs, dst + kk_pos(pc >> 2) * 2048 + (pc & 3) * 256, 0, 16);
    }
}

typedef __attribute__((address_space(1))) unsigned gu32;
__device__ __forceinline__ unsigned ld1(char* ws, unsigned off) { return __hip_atomic_load((gu32*)(ws + off), __ATOMIC_RELAXED, __HIP_MEMORY_SCOPE_AGENT); }
__device__ __forceinline__ void st1(char* ws, unsigned off, unsigned v) { __hip_atomic_store((gu32*)(ws + off), v, __ATOMIC_RELAXED, __HIP_MEMORY_SCOPE_AGENT); }
__device__ __forceinline__ bool all_done(char* ws, int lane) { return __all(ld1(ws, WS_RDY + (lane & 7) * 128) >= 16u); }
constexpr int SPINS = 1 << 18;

__device__ __forceinline__ void set_up(const u16* __restrict__ x, const u16* __restrict__ mem0, char* ws, int u, int par, int shard, int w) {
    extern __shared__ unsigned lat[];
    int lane;
    asm volatile("v_mbcnt_lo_u32_b32 %0, -1, 0\n\tv_mbcnt_hi_u32_b32 %0, -1, %0" : "=v"(lane));
    lane &= 63;
    const int tid = w * 64 + lane;
    const bool server = u == 127 && w == 0;
    const __amdgpu_buffer_rsrc_t rs = __builtin_amdgcn_make_buffer_rsrc(ws, 0, 64 << 20, 0x00020000);
    const unsigned mine = (128 + u) * 128;
    unsigned want = 0;
    KST(ws, 0, u);
    if (server) {
        if (lane < 16) st1(ws, WS_RDY + lane * 128, 0u);
        asm volatile("s_waitcnt vmcnt(0)" ::: "memory");
    } else if (w == 0) {
        want = ld1(ws, WS_ECHO + mine) + 1u;
        if (lane == 0) st1(ws, WS_REQ + mine, want);
        asm volatile("s_waitcnt vmcnt(0)" ::: "memory");
    }
    {
        unsigned best = ~0u;
        for (int rep = 0; rep < NPROBE; ++rep) {
            const unsigned off = (u >> 1) * 16384u + (u & 1) * 4096u + (w & 1) * 8192 + (w >> 1) * 2048 + rep * 128;
            unsigned long long t0, t1;
            asm volatile("s_memtime %0\n\ts_waitcnt lgkmcnt(0)\n\ts_mov_b64 exec, 1\n\tglobal_store_dword %3, %2, %4 sc1\n\ts_mov_b64 exec, -1\n\ts_waitcnt vmcnt(0)\n\ts_memtime %1\n\ts_waitcnt lgkmcnt(0)"
                         : "=&s"(t0), "=&s"(t1) : "v"(~0u), "v"(off), "s"(ws) : "memory");
            best = min(best, (unsigned)(t1 - t0));
        }
        if (lane == 0) lat[w] = best;
    }
    KST(ws, 1, u);
    Row a, b;
    const int r0 = 64 + u * 16 + w * 2 + (lane >> 5);
    if (u < 124) { a = ln_load(x, r0, lane & 31); b = ln_load(x, r0 + 8, lane & 31); }
#pragma unroll
    for (int i = 0; i < 4; ++i) {
        const unsigned off = u * 16384 + i * 4096 + tid * 16, o = off & ~(8192u + 4095u);
        const unsigned v = (o == WS_FL_P || o == WS_FX_P || o == WS_PFQ_P || o == WS_PFV_P || o == WS_PFN_P || o == WS_PFM_P || o == WS_FZ_P || (off & ~8192u) - WS_FH_P < 5120u) ? 0u : ~0u;
        __builtin_amdgcn_raw_buffer_store_b128((v4u){v, v, v, v}, rs, off, 0, 16);
    }
    if (tid < 128) __builtin_amdgcn_raw_buffer_store_b128((v4u){~0u, ~0u, ~0u, ~0u}, rs, WS_P4 + u * 2048 + tid * 16, 0, 16);
    if (tid < 6) __builtin_amdgcn_raw_buffer_store_b128((v4u){0u, 0u, 0u, 0u}, rs, WS_FYB + (u * 6 + tid) * 128, 0, 16);
    if (u >= 108 && u < 124 && tid < 160) {
        const int mi = (u & 3) * 160 + tid;
        const unsigned* src = (const unsigned*)(mem0 + ((mi & 15) >> 1) * 640 + 16 * (mi >> 4) + 2 * (mi & 1));
        v4u m;
#pragma unroll
        for (int j = 0; j < 4; ++j) m[j] = min(src[2 * j], 0xfffffffeu);
        __builtin_amdgcn_raw_buffer_store_b128(m, rs, WS_MEM0 + ((u - 108) >> 2) * 32768 + mi * 16 + (mi < 512 ? 0 : 8192), 0, 16);
    }
    if (u < 124) { ln_store(a, rs, r0, lane & 31); ln_store(b, rs, r0 + 8, lane & 31); }
    asm volatile("s_waitcnt vmcnt(0)" ::: "memory");
    __syncthreads();
    KST(ws, 2, u);
    if (w == 0) {
        if (lane == 0) st1(ws, WS_POL + u * 4, (min(lat[1], lat[3]) < min(lat[0], lat[2]) ? 1u : 0u) ^ (unsigned)par);
        if (!server)
            for (int i = 0; i < SPINS && ld1(ws, WS_ECHO + mine) != want; ++i) __builtin_amdgcn_s_sleep(4);
        asm volatile("s_waitcnt vmcnt(0)" ::: "memory");
        KST(ws, 3, u);
        if (lane == 0) __hip_atomic_fetch_add((gu32*)(ws + WS_RDY + shard * 128), 1u, __ATOMIC_RELAXED, __HIP_MEMORY_SCOPE_AGENT);
        if (server) {
            unsigned l0 = 0, l1 = 0;
            for (int i = 0; i < SPINS; ++i) {
                const unsigned q0 = ld1(ws, WS_REQ + (128 + lane) * 128), q1 = ld1(ws, WS_REQ + (192 + lane) * 128);
                if (i == 0 || q0 != l0) st1(ws, WS_ECHO + (128 + lane) * 128, q0);
                if (i == 0 || q1 != l1) st1(ws, WS_ECHO + (192 + lane) * 128, q1);
                l0 = q0; l1 = q1;
                if (all_done(ws, lane)) break;
                __builtin_amdgcn_s_sleep(4);
            }
            asm volatile("" ::: "memory");
            const unsigned vote = __popcll(__ballot(ld1(ws, WS_POL + lane * 4) & 1u)) + __popcll(__ballot(ld1(ws, WS_POL + 256 + lane * 4) & 1u)) > 64 ? 1u : 0u;
            KST(ws, 4, 0);
            v4u m0 = {}, m1 = {};
            for (int i = 0; i < SPINS; ++i) {
                const v4u q0 = __builtin_amdgcn_raw_buffer_load_b128(rs, WS_REQ + lane * 128, 0, 16), q1 = __builtin_amdgcn_raw_buffer_load_b128(rs, WS_REQ + (64 + lane) * 128, 0, 16);
                const bool c0 = i == 0 || q0.x != m0.x || q0.y != m0.y || q0.z != m0.z || q0.w != m0.w, c1 = i == 0 || q1.x != m1.x || q1.y != m1.y || q1.z != m1.z || q1.w != m1.w;
                if (c0) __builtin_amdgcn_raw_buffer_store_b128((q0 & ~1u) | vote, rs, WS_ECHO + lane * 128, 0, 16);
                if (c1) __builtin_amdgcn_raw_buffer_store_b128((q1 & ~1u) | vote, rs, WS_ECHO + (64 + lane) * 128, 0, 16);
                m0 = q0; m1 = q1;
                asm volatile("" ::: "memory");
                if (__all(ld1(ws, WS_RDY + 1024 + (lane & 7) * 128) >= 16u)) break;
                __builtin_amdgcn_s_sleep(16);
            }
        } else {
            for (int i = 0; i < SPINS && !all_done(ws, lane); ++i) __builtin_amdgcn_s_sleep(16);
        }
        asm volatile("s_waitcnt vmcnt(0)" ::: "memory");
    }
    __syncthreads();
}

typedef __attribute__((address_space(3))) s16x4* lds_tr_ptr;
__device__ __forceinline__ bf16x8 as_frag(v4u v) { return __builtin_bit_cast(bf16x8, v); }

extern "C" __global__ void __launch_bounds__(256, 1) k_rec(const u16* wq, const u16* wk, const u16* wv, const u16* wo, const u16* x, const u16* w_in, const u16* w_sp,
                                                const u16* b_sp, char* ws, const u16* w_out, u16* out, const u16* mem0) {
    const unsigned long long karg = (unsigned long long)__builtin_amdgcn_kernarg_segment_ptr();
    const int b = blockIdx.x;
    const int par = (b & 7) >> 2, xq = b & 3, r = (b >> 3) * 4 + xq;
    const int wave = __builtin_amdgcn_readfirstlane(threadIdx.x >> 6);
    if (r >= 64) set_up(x, mem0, ws, par * 64 + r - 64, par, b & 7, wave);
    if (r < 32) {
        asm volatile(
        "s_mov_b64 s[48:49], %8\n"
        "s_mov_b64 s[90:91], %0\n"
        "s_mov_b64 s[92:93], %1\n"
        "s_mov_b64 s[94:95], %2\n"
        "s_mov_b64 s[96:97], %3\n"
        "s_mov_b32 s40, %4\n"
        "s_mov_b32 s41, %5\n"
        "s_mov_b32 s42, %6\n"
        "s_mov_b32 s43, %7\n"
        "s_mov_b64 s[0:1], s[48:49]\n"
        "s_mov_b64 exec, -1\n"
        "s_mov_b32 s45, s41\n"
        "s_mov_b32 s46, 0x200000\n"
        "v_mbcnt_lo_u32_b32 v0, -1, 0\n"
        "v_mbcnt_hi_u32_b32 v0, -1, v0\n"
        "v_lshlrev_b32 v1, 4, v0\n"
        "s_lshl_b32 s47, s41, 6\n"
        "s_add_u32 s47, s47, s40\n"
        "s_add_u32 s47, s47, 0\n"
        "s_lshl_b32 s47, s47, 5\n"
        "s_add_u32 s47, s47, s43\n"
        "s_lshl_b32 s47, s47, 2\n"
        "s_add_u32 s47, s47, 0xab8000\n"
        "v_mov_b32 v15, s47\n"
        "global_load_dword v26, v15, s[90:91] sc1\n"
        "v_and_b32 v28, 15, v0\n"
        "s_lshl_b32 s47, s41, 5\n"
        "v_add_u32 v28, s47, v28\n"
        "v_mul_u32_u24 v28, 1280, v28\n"
        "v_lshrrev_b32 v29, 4, v0\n"
        "v_lshl_add_u32 v28, v29, 4, v28\n"
        "s_mul_i32 s47, s43, 0x140\n"
        "v_add_u32 v28, s47, v28\n"
        "v_add_u32 v29, 0x5000, v28\n"
        "global_load_dwordx4 v[48:51], v28, s[96:97] offset:0\n"
        "global_load_dwordx4 v[52:55], v29, s[96:97] offset:0\n"
        "global_load_dwordx4 v[56:59], v28, s[96:97] offset:64\n"
        "global_load_dwordx4 v[60:63], v29, s[96:97] offset:64\n"
        "global_load_dwordx4 v[64:67], v28, s[96:97] offset:128\n"
        "global_load_dwordx4 v[68:71], v29, s[96:97] offset:128\n"
        "global_load_dwordx4 v[72:75], v28, s[96:97] offset:192\n"
        "global_load_dwordx4 v[76:79], v29, s[96:97] offset:192\n"
        "global_load_dwordx4 v[80:83], v28, s[96:97] offset:256\n"
        "global_load_dwordx4 v[84:87], v29, s[96:97] offset:256\n"
        "s_sleep 4\n"
        "s_mul_i32 s47, s43, 0x1800\n"
        "s_add_u32 s47, s47, 0x10000\n"
        "v_add_u32 v2, s47, v1\n"
        "v_add_u32 v23, 0xc000, v2\n"
        "s_lshl_b32 s96, s43, 12\n"
        "s_lshl_b32 s97, s43, 11\n"
        "v_lshrrev_b32 v124, 3, v0\n"
        "s_lshl_b32 s47, s43, 3\n"
        "v_add_u32 v124, s47, v124\n"
        "v_bfe_u32 v125, v0, 4, 1\n"
        "s_and_b32 s47, s43, 1\n"
        "s_lshl_b32 s47, s47, 1\n"
        "v_or_b32 v125, s47, v125\n"
        "v_lshlrev_b32 v125, 1, v125\n"
        "v_and_b32 v126, 7, v0\n"
        "v_xor_b32 v125, v125, v126\n"
        "v_lshlrev_b32 v125, 4, v125\n"
        "s_mul_i32 s47, s40, 0x180\n"
        "v_mul_u32_u24 v126, 0x3000, v124\n"
        "v_add3_u32 v3, v126, v125, s47\n"
        "v_lshrrev_b32 v124, 4, v0\n"
        "v_lshlrev_b32 v124, 10, v124\n"
        "v_bfe_u32 v125, v0, 2, 2\n"
        "v_lshl_add_u32 v124, v125, 7, v124\n"
        "v_bfe_u32 v125, v0, 3, 2\n"
        "v_lshl_add_u32 v124, v125, 5, v124\n"
        "v_and_b32 v125, 3, v0\n"
        "v_lshl_add_u32 v124, v125, 3, v124\n"
        "s_mul_i32 s47, s43, 0x3\n"
        "s_add_u32 s47, s47, 0\n"
        "s_and_b32 s68, s47, 3\n"
        "s_lshl_b32 s68, s68, 5\n"
        "s_lshr_b32 s47, s47, 2\n"
        "s_lshl_b32 s47, s47, 12\n"
        "v_xor_b32 v5, s68, v124\n"
        "v_add_u32 v5, s47, v5\n"
        "s_mul_i32 s47, s43, 0x3\n"
        "s_add_u32 s47, s47, 1\n"
        "s_and_b32 s68, s47, 3\n"
        "s_lshl_b32 s68, s68, 5\n"
        "s_lshr_b32 s47, s47, 2\n"
        "s_lshl_b32 s47, s47, 12\n"
        "v_xor_b32 v6, s68, v124\n"
        "v_add_u32 v6, s47, v6\n"
        "s_mul_i32 s47, s43, 0x3\n"
        "s_add_u32 s47, s47, 2\n"
        "s_and_b32 s68, s47, 3\n"
        "s_lshl_b32 s68, s68, 5\n"
        "s_lshr_b32 s47, s47, 2\n"
        "s_lshl_b32 s47, s47, 12\n"
        "v_xor_b32 v7, s68, v124\n"
        "v_add_u32 v7, s47, v7\n"
        "s_lshl_b32 s59, s43, 10\n"
        "s_add_u32 s54, s92, 0xc0000\n"
        "s_addc_u32 s55, s93, 0\n"
        "s_add_u32 m0, s59, 0x10000\n"
        "s_nop 0\n"
        "global_load_lds_dwordx4 v3, s[54:55] offset:0\n"
        "s_add_u32 m0, s59, 0x10f80\n"
        "s_nop 0\n"
        "global_load_lds_dwordx4 v3, s[54:55] offset:128\n"
        "s_add_u32 m0, s59, 0x11f00\n"
        "s_nop 0\n"
        "global_load_lds_dwordx4 v3, s[54:55] offset:256\n"
        "s_add_u32 s54, s94, 0xc0000\n"
        "s_addc_u32 s55, s95, 0\n"
        "s_add_u32 m0, s59, 0x13000\n"
        "s_nop 0\n"
        "global_load_lds_dwordx4 v3, s[54:55] offset:0\n"
        "s_add_u32 m0, s59, 0x13f80\n"
        "s_nop 0\n"
        "global_load_lds_dwordx4 v3, s[54:55] offset:128\n"
        "s_add_u32 m0, s59, 0x14f00\n"
        "s_nop 0\n"
        "global_load_lds_dwordx4 v3, s[54:55] offset:256\n"
        "s_add_u32 s54, s92, 0x120000\n"
        "s_addc_u32 s55, s93, 0\n"
        "s_add_u32 m0, s59, 0x16000\n"
        "s_nop 0\n"
        "global_load_lds_dwordx4 v3, s[54:55] offset:0\n"
        "s_add_u32 m0, s59, 0x16f80\n"
        "s_nop 0\n"
        "global_load_lds_dwordx4 v3, s[54:55] offset:128\n"
        "s_add_u32 m0, s59, 0x17f00\n"
        "s_nop 0\n"
        "global_load_lds_dwordx4 v3, s[54:55] offset:256\n"
        "s_add_u32 s54, s94, 0x120000\n"
        "s_addc_u32 s55, s95, 0\n"
        "s_add_u32 m0, s59, 0x19000\n"
        "s_nop 0\n"
        "global_load_lds_dwordx4 v3, s[54:55] offset:0\n"
        "s_add_u32 m0, s59, 0x19f80\n"
        "s_nop 0\n"
        "global_load_lds_dwordx4 v3, s[54:55] offset:128\n"
        "s_add_u32 m0, s59, 0x1af00\n"
        "s_nop 0\n"
        "global_load_lds_dwordx4 v3, s[54:55] offset:256\n"
        "s_add_u32 s54, s92, 0x180000\n"
        "s_addc_u32 s55, s93, 0\n"
        "s_add_u32 m0, s59, 0x1c000\n"
        "s_nop 0\n"
        "global_load_lds_dwordx4 v3, s[54:55] offset:0\n"
        "s_add_u32 m0, s59, 0x1cf80\n"
        "s_nop 0\n"
        "global_load_lds_dwordx4 v3, s[54:55] offset:128\n"
        "s_add_u32 m0, s59, 0x1df00\n"
        "s_nop 0\n"
        "global_load_lds_dwordx4 v3, s[54:55] offset:256\n"
        "s_add_u32 s54, s94, 0x180000\n"
        "s_addc_u32 s55, s95, 0\n"
        "s_add_u32 m0, s59, 0x1f000\n"
        "s_nop 0\n"
        "global_load_lds_dwordx4 v3, s[54:55] offset:0\n"
        "s_add_u32 m0, s59, 0x1ff80\n"
        "s_nop 0\n"
        "global_load_lds_dwordx4 v3, s[54:55] offset:128\n"
        "s_add_u32 m0, s59, 0x20f00\n"
        "s_nop 0\n"
        "global_load_lds_dwordx4 v3, s[54:55] offset:256\n"
        "s_add_u32 s54, s92, 0x1e0000\n"
        "s_addc_u32 s55, s93, 0\n"
        "s_add_u32 m0, s59, 0x22000\n"
        "s_nop 0\n"
        "global_load_lds_dwordx4 v3, s[54:55] offset:0\n"
        "s_add_u32 m0, s59, 0x22f80\n"
        "s_nop 0\n"
        "global_load_lds_dwordx4 v3, s[54:55] offset:128\n"
        "s_add_u32 m0, s59, 0x23f00\n"
        "s_nop 0\n"
        "global_load_lds_dwordx4 v3, s[54:55] offset:256\n"
        "s_add_u32 s54, s94, 0x1e0000\n"
        "s_addc_u32 s55, s95, 0\n"
        "s_add_u32 m0, s59, 0x25000\n"
        "s_nop 0\n"
        "global_load_lds_dwordx4 v3, s[54:55] offset:0\n"
        "s_add_u32 m0, s59, 0x25f80\n"
        "s_nop 0\n"
        "global_load_lds_dwordx4 v3, s[54:55] offset:128\n"
        "s_add_u32 m0, s59, 0x26f00\n"
        "s_nop 0\n"
        "global_load_lds_dwordx4 v3, s[54:55] offset:256\n"
        "s_add_u32 s54, s92, 0x240000\n"
        "s_addc_u32 s55, s93, 0\n"
        "s_add_u32 m0, s59, 0xa000\n"
        "s_nop 0\n"
        "global_load_lds_dwordx4 v3, s[54:55] offset:0\n"
        "s_add_u32 m0, s59, 0xaf80\n"
        "s_nop 0\n"
        "global_load_lds_dwordx4 v3, s[54:55] offset:128\n"
        "s_add_u32 m0, s59, 0xbf00\n"
        "s_nop 0\n"
        "global_load_lds_dwordx4 v3, s[54:55] offset:256\n"
        "s_add_u32 s54, s94, 0x240000\n"
        "s_addc_u32 s55, s95, 0\n"
        "s_add_u32 m0, s59, 0xd000\n"
        "s_nop 0\n"
        "global_load_lds_dwordx4 v3, s[54:55] offset:0\n"
        "s_add_u32 m0, s59, 0xdf80\n"
        "s_nop 0\n"
        "global_load_lds_dwordx4 v3, s[54:55] offset:128\n"
        "s_add_u32 m0, s59, 0xef00\n"
        "s_nop 0\n"
        "global_load_lds_dwordx4 v3, s[54:55] offset:256\n"
        "s_waitcnt vmcnt(24)\n"
        "s_waitcnt lgkmcnt(0)\n"
        "s_barrier\n"
        "s_waitcnt vmcnt(40)\n"
        "v_readfirstlane_b32 s88, v26\n"
        "v_subrev_u32 v27, 0x8000, v15\n"
        "s_add_u32 s88, s88, 2\n"
        "s_and_b32 s88, s88, -2\n"
        "v_mov_b32 v26, s88\n"
        "s_mov_b64 exec, 1\n"
        "global_store_dword v27, v26, s[90:91] sc1\n"
        "s_mov_b64 exec, -1\n"
        "v_mov_b32 v112, 0x3f803f80\n"
        "v_mov_b32 v113, 0x3f803f80\n"
        "v_mov_b32 v114, 0x3f803f80\n"
        "v_mov_b32 v115, 0x3f803f80\n"
        "v_and_b32 v28, 15, v0\n"
        "v_lshlrev_b32 v28, 7, v28\n"
        "v_and_b32 v29, 3, v0\n"
        "v_lshl_add_u32 v29, v29, 2, v28\n"
        "v_add_u32 v30, 2048, v29\n"
        "s_lshl_b32 s47, s43, 5\n"
        "v_add_u32 v28, s47, v28\n"
        "s_mov_b32 s56, 0xf0000f\n"
        "s_mov_b32 s57, 0xf0000f00\n"
        "s_waitcnt vmcnt(31)\n"
        "v_mfma_f32_16x16x32_bf16 v[96:99], v[112:115], v[48:51], 0\n"
        "v_mfma_f32_16x16x32_bf16 v[100:103], v[48:51], v[48:51], 0\n"
        "v_mfma_f32_16x16x32_bf16 v[104:107], v[112:115], v[52:55], 0\n"
        "v_mfma_f32_16x16x32_bf16 v[108:111], v[52:55], v[52:55], 0\n"
        "v_mfma_f32_16x16x32_bf16 v[96:99], v[112:115], v[56:59], v[96:99]\n"
        "v_mfma_f32_16x16x32_bf16 v[100:103], v[56:59], v[56:59], v[100:103]\n"
        "v_mfma_f32_16x16x32_bf16 v[104:107], v[112:115], v[60:63], v[104:107]\n"
        "v_mfma_f32_16x16x32_bf16 v[108:111], v[60:63], v[60:63], v[108:111]\n"
        "v_mfma_f32_16x16x32_bf16 v[96:99], v[112:115], v[64:67], v[96:99]\n"
        "v_mfma_f32_16x16x32_bf16 v[100:103], v[64:67], v[64:67], v[100:103]\n"
        "v_mfma_f32_16x16x32_bf16 v[104:107], v[112:115], v[68:71], v[104:107]\n"
        "v_mfma_f32_16x16x32_bf16 v[108:111], v[68:71], v[68:71], v[108:111]\n"
        "v_mfma_f32_16x16x32_bf16 v[96:99], v[112:115], v[72:75], v[96:99]\n"
        "v_mfma_f32_16x16x32_bf16 v[100:103], v[72:75], v[72:75], v[100:103]\n"
        "v_mfma_f32_16x16x32_bf16 v[104:107], v[112:115], v[76:79], v[104:107]\n"
        "v_mfma_f32_16x16x32_bf16 v[108:111], v[76:79], v[76:79], v[108:111]\n"
        "v_mfma_f32_16x16x32_bf16 v[96:99], v[112:115], v[80:83], v[96:99]\n"
        "v_mfma_f32_16x16x32_bf16 v[100:103], v[80:83], v[80:83], v[100:103]\n"
        "v_mfma_f32_16x16x32_bf16 v[104:107], v[112:115], v[84:87], v[104:107]\n"
        "v_mfma_f32_16x16x32_bf16 v[108:111], v[84:87], v[84:87], v[108:111]\n"
        "s_mov_b64 exec, s[56:57]\n"
        "s_nop 3\n"
        "ds_write_b128 v28, v[96:99] offset:0\n"
        "ds_write_b128 v28, v[100:103] offset:16\n"
        "ds_write_b128 v28, v[104:107] offset:2048\n"
        "ds_write_b128 v28, v[108:111] offset:2064\n"
        "s_mov_b64 exec, -1\n"
        "v_add_u32 v20, 0x10000, v5\n"
        "ds_read_b64_tr_b16 a[0:1], v20\n"
        "ds_read_b64_tr_b16 a[2:3], v20 offset:512\n"
        "v_add_u32 v21, 0x10000, v6\n"
        "ds_read_b64_tr_b16 a[128:129], v21\n"
        "ds_read_b64_tr_b16 a[130:131], v21 offset:512\n"
        "v_add_u32 v22, 0x10000, v7\n"
        "ds_read_b64_tr_b16 v[128:129], v22\n"
        "ds_read_b64_tr_b16 v[130:131], v22 offset:512\n"
        "v_add_u32 v20, 0x13000, v5\n"
        "ds_read_b64_tr_b16 a[8:9], v20\n"
        "ds_read_b64_tr_b16 a[10:11], v20 offset:512\n"
        "v_add_u32 v21, 0x13000, v6\n"
        "ds_read_b64_tr_b16 a[136:137], v21\n"
        "ds_read_b64_tr_b16 a[138:139], v21 offset:512\n"
        "v_add_u32 v22, 0x13000, v7\n"
        "ds_read_b64_tr_b16 v[136:137], v22\n"
        "ds_read_b64_tr_b16 v[138:139], v22 offset:512\n"
        "s_waitcnt vmcnt(19)\n"
        "s_waitcnt lgkmcnt(0)\n"
        "s_barrier\n"
        "s_add_u32 s54, s92, 0x2a0000\n"
        "s_addc_u32 s55, s93, 0\n"
        "s_add_u32 m0, s59, 0x10000\n"
        "s_nop 0\n"
        "global_load_lds_dwordx4 v3, s[54:55] offset:0\n"
        "s_add_u32 m0, s59, 0x10f80\n"
        "s_nop 0\n"
        "global_load_lds_dwordx4 v3, s[54:55] offset:128\n"
        "s_add_u32 m0, s59, 0x11f00\n"
        "s_nop 0\n"
        "global_load_lds_dwordx4 v3, s[54:55] offset:256\n"
        "s_add_u32 s54, s94, 0x2a0000\n"
        "s_addc_u32 s55, s95, 0\n"
        "s_add_u32 m0, s59, 0x13000\n"
        "s_nop 0\n"
        "global_load_lds_dwordx4 v3, s[54:55] offset:0\n"
        "s_add_u32 m0, s59, 0x13f80\n"
        "s_nop 0\n"
        "global_load_lds_dwordx4 v3, s[54:55] offset:128\n"
        "s_add_u32 m0, s59, 0x14f00\n"
        "s_nop 0\n"
        "global_load_lds_dwordx4 v3, s[54:55] offset:256\n"
        "ds_read2_b32 v[96:97], v29 offset0:0 offset1:4\n"
        "ds_read2_b32 v[98:99], v29 offset0:8 offset1:12\n"
        "ds_read2_b32 v[100:101], v29 offset0:16 offset1:20\n"
        "ds_read2_b32 v[102:103], v29 offset0:24 offset1:28\n"
        "ds_read2_b32 v[104:105], v30 offset0:0 offset1:4\n"
        "ds_read2_b32 v[106:107], v30 offset0:8 offset1:12\n"
        "ds_read2_b32 v[108:109], v30 offset0:16 offset1:20\n"
        "ds_read2_b32 v[110:111], v30 offset0:24 offset1:28\n"
        "s_mov_b32 s58, 0x208168\n"
        "s_cmp_eq_u32 s43, 1\n"
        "s_cselect_b32 s58, 0x731483, s58\n"
        "s_cmp_eq_u32 s43, 2\n"
        "s_cselect_b32 s58, 0xc5262e, s58\n"
        "s_cmp_eq_u32 s43, 3\n"
        "s_cselect_b32 s58, 0x13941ed, s58\n"
        "s_waitcnt lgkmcnt(0)\n"
        "v_pk_add_f32 v[96:97], v[96:97], v[98:99]\n"
        "v_pk_add_f32 v[100:101], v[100:101], v[102:103]\n"
        "v_pk_add_f32 v[96:97], v[96:97], v[100:101]\n"
        "v_mul_f32 v96, 0x3acccccd, v96\n"
        "v_mul_f32 v97, 0x3acccccd, v97\n"
        "v_fma_f32 v97, -v96, v96, v97\n"
        "v_max_f32 v97, 0, v97\n"
        "v_add_f32 v97, 0x3727c5ac, v97\n"
        "v_rsq_f32 v33, v97\n"
        "s_nop 0\n"
        "v_mul_f32 v32, v96, v33\n"
        "v_pk_add_f32 v[104:105], v[104:105], v[106:107]\n"
        "v_pk_add_f32 v[108:109], v[108:109], v[110:111]\n"
        "v_pk_add_f32 v[104:105], v[104:105], v[108:109]\n"
        "v_mul_f32 v104, 0x3acccccd, v104\n"
        "v_mul_f32 v105, 0x3acccccd, v105\n"
        "v_fma_f32 v105, -v104, v104, v105\n"
        "v_max_f32 v105, 0, v105\n"
        "v_add_f32 v105, 0x3727c5ac, v105\n"
        "v_rsq_f32 v35, v105\n"
        "s_nop 0\n"
        "v_mul_f32 v34, v104, v35\n"
        "v_add_u32 v20, 0x16000, v5\n"
        "ds_read_b64_tr_b16 a[4:5], v20\n"
        "ds_read_b64_tr_b16 a[6:7], v20 offset:512\n"
        "v_add_u32 v21, 0x16000, v6\n"
        "ds_read_b64_tr_b16 a[132:133], v21\n"
        "ds_read_b64_tr_b16 a[134:135], v21 offset:512\n"
        "v_add_u32 v22, 0x16000, v7\n"
        "ds_read_b64_tr_b16 v[132:133], v22\n"
        "ds_read_b64_tr_b16 v[134:135], v22 offset:512\n"
        "v_add_u32 v20, 0x19000, v5\n"
        "ds_read_b64_tr_b16 a[12:13], v20\n"
        "ds_read_b64_tr_b16 a[14:15], v20 offset:512\n"
        "v_add_u32 v21, 0x19000, v6\n"
        "ds_read_b64_tr_b16 a[140:141], v21\n"
        "ds_read_b64_tr_b16 a[142:143], v21 offset:512\n"
        "v_add_u32 v22, 0x19000, v7\n"
        "ds_read_b64_tr_b16 v[140:141], v22\n"
        "ds_read_b64_tr_b16 v[142:143], v22 offset:512\n"
        "s_waitcnt vmcnt(19)\n"
        "s_waitcnt lgkmcnt(0)\n"
        "s_barrier\n"
        "s_add_u32 s54, s92, 0x300000\n"
        "s_addc_u32 s55, s93, 0\n"
        "s_add_u32 m0, s59, 0x16000\n"
        "s_nop 0\n"
        "global_load_lds_dwordx4 v3, s[54:55] offset:0\n"
        "s_add_u32 m0, s59, 0x16f80\n"
        "s_nop 0\n"
        "global_load_lds_dwordx4 v3, s[54:55] offset:128\n"
        "s_add_u32 m0, s59, 0x17f00\n"
        "s_nop 0\n"
        "global_load_lds_dwordx4 v3, s[54:55] offset:256\n"
        "s_add_u32 s54, s94, 0x300000\n"
        "s_addc_u32 s55, s95, 0\n"
        "s_add_u32 m0, s59, 0x19000\n"
        "s_nop 0\n"
        "global_load_lds_dwordx4 v3, s[54:55] offset:0\n"
        "s_add_u32 m0, s59, 0x19f80\n"
        "s_nop 0\n"
        "global_load_lds_dwordx4 v3, s[54:55] offset:128\n"
        "s_add_u32 m0, s59, 0x1af00\n"
        "s_nop 0\n"
        "global_load_lds_dwordx4 v3, s[54:55] offset:256\n"
        "s_bfe_u32 s47, s58, 0x50000\n"
        "s_lshl_b32 s47, s47, 11\n"
        "v_add_u32 v28, s47, v1\n"
        "v_lshlrev_b32 v30, 16, v48\n"
        "v_and_b32 v48, 0xffff0000, v48\n"
        "v_fma_f32 v30, v30, v33, -v32\n"
        "v_fma_f32 v48, v48, v33, -v32\n"
        "v_cvt_pk_bf16_f32 v48, v30, v48\n"
        "v_lshlrev_b32 v31, 16, v49\n"
        "v_and_b32 v49, 0xffff0000, v49\n"
        "v_fma_f32 v31, v31, v33, -v32\n"
        "v_fma_f32 v49, v49, v33, -v32\n"
        "v_cvt_pk_bf16_f32 v49, v31, v49\n"
        "v_lshlrev_b32 v30, 16, v50\n"
        "v_and_b32 v50, 0xffff0000, v50\n"
        "v_fma_f32 v30, v30, v33, -v32\n"
        "v_fma_f32 v50, v50, v33, -v32\n"
        "v_cvt_pk_bf16_f32 v50, v30, v50\n"
        "v_lshlrev_b32 v31, 16, v51\n"
        "v_and_b32 v51, 0xffff0000, v51\n"
        "v_fma_f32 v31, v31, v33, -v32\n"
        "v_fma_f32 v51, v51, v33, -v32\n"
        "v_cvt_pk_bf16_f32 v51, v31, v51\n"
        "ds_write_b128 v28, v[48:51] offset:0\n"
        "v_lshlrev_b32 v30, 16, v52\n"
        "v_and_b32 v52, 0xffff0000, v52\n"
        "v_fma_f32 v30, v30, v35, -v34\n"
        "v_fma_f32 v52, v52, v35, -v34\n"
        "v_cvt_pk_bf16_f32 v52, v30, v52\n"
        "v_lshlrev_b32 v31, 16, v53\n"
        "v_and_b32 v53, 0xffff0000, v53\n"
        "v_fma_f32 v31, v31, v35, -v34\n"
        "v_fma_f32 v53, v53, v35, -v34\n"
        "v_cvt_pk_bf16_f32 v53, v31, v53\n"
        "v_lshlrev_b32 v30, 16, v54\n"
        "v_and_b32 v54, 0xffff0000, v54\n"
        "v_fma_f32 v30, v30, v35, -v34\n"
        "v_fma_f32 v54, v54, v35, -v34\n"
        "v_cvt_pk_bf16_f32 v54, v30, v54\n"
        "v_lshlrev_b32 v31, 16, v55\n"
        "v_and_b32 v55, 0xffff0000, v55\n"
        "v_fma_f32 v31, v31, v35, -v34\n"
        "v_fma_f32 v55, v55, v35, -v34\n"
        "v_cvt_pk_bf16_f32 v55, v31, v55\n"
        "ds_write_b128 v28, v[52:55] offset:1024\n"
        "s_bfe_u32 s47, s58, 0x50005\n"
        "s_lshl_b32 s47, s47, 11\n"
        "v_add_u32 v28, s47, v1\n"
        "v_lshlrev_b32 v30, 16, v56\n"
        "v_and_b32 v56, 0xffff0000, v56\n"
        "v_fma_f32 v30, v30, v33, -v32\n"
        "v_fma_f32 v56, v56, v33, -v32\n"
        "v_cvt_pk_bf16_f32 v56, v30, v56\n"
        "v_lshlrev_b32 v31, 16, v57\n"
        "v_and_b32 v57, 0xffff0000, v57\n"
        "v_fma_f32 v31, v31, v33, -v32\n"
        "v_fma_f32 v57, v57, v33, -v32\n"
        "v_cvt_pk_bf16_f32 v57, v31, v57\n"
        "v_lshlrev_b32 v30, 16, v58\n"
        "v_and_b32 v58, 0xffff0000, v58\n"
        "v_fma_f32 v30, v30, v33, -v32\n"
        "v_fma_f32 v58, v58, v33, -v32\n"
        "v_cvt_pk_bf16_f32 v58, v30, v58\n"
        "v_lshlrev_b32 v31, 16, v59\n"
        "v_and_b32 v59, 0xffff0000, v59\n"
        "v_fma_f32 v31, v31, v33, -v32\n"
        "v_fma_f32 v59, v59, v33, -v32\n"
        "v_cvt_pk_bf16_f32 v59, v31, v59\n"
        "ds_write_b128 v28, v[56:59] offset:0\n"
        "v_lshlrev_b32 v30, 16, v60\n"
        "v_and_b32 v60, 0xffff0000, v60\n"
        "v_fma_f32 v30, v30, v35, -v34\n"
        "v_fma_f32 v60, v60, v35, -v34\n"
        "v_cvt_pk_bf16_f32 v60, v30, v60\n"
        "v_lshlrev_b32 v31, 16, v61\n"
        "v_and_b32 v61, 0xffff0000, v61\n"
        "v_fma_f32 v31, v31, v35, -v34\n"
        "v_fma_f32 v61, v61, v35, -v34\n"
        "v_cvt_pk_bf16_f32 v61, v31, v61\n"
        "v_lshlrev_b32 v30, 16, v62\n"
        "v_and_b32 v62, 0xffff0000, v62\n"
        "v_fma_f32 v30, v30, v35, -v34\n"
        "v_fma_f32 v62, v62, v35, -v34\n"
        "v_cvt_pk_bf16_f32 v62, v30, v62\n"
        "v_lshlrev_b32 v31, 16, v63\n"
        "v_and_b32 v63, 0xffff0000, v63\n"
        "v_fma_f32 v31, v31, v35, -v34\n"
        "v_fma_f32 v63, v63, v35, -v34\n"
        "v_cvt_pk_bf16_f32 v63, v31, v63\n"
        "ds_write_b128 v28, v[60:63] offset:1024\n"
        "v_add_u32 v20, 0x1c000, v5\n"
        "ds_read_b64_tr_b16 a[16:17], v20\n"
        "ds_read_b64_tr_b16 a[18:19], v20 offset:512\n"
        "v_add_u32 v21, 0x1c000, v6\n"
        "ds_read_b64_tr_b16 a[144:145], v21\n"
        "ds_read_b64_tr_b16 a[146:147], v21 offset:512\n"
        "v_add_u32 v22, 0x1c000, v7\n"
        "ds_read_b64_tr_b16 v[144:145], v22\n"
        "ds_read_b64_tr_b16 v[146:147], v22 offset:512\n"
        "v_add_u32 v20, 0x1f000, v5\n"
        "ds_read_b64_tr_b16 a[24:25], v20\n"
        "ds_read_b64_tr_b16 a[26:27], v20 offset:512\n"
        "v_add_u32 v21, 0x1f000, v6\n"
        "ds_read_b64_tr_b16 a[152:153], v21\n"
        "ds_read_b64_tr_b16 a[154:155], v21 offset:512\n"
        "v_add_u32 v22, 0x1f000, v7\n"
        "ds_read_b64_tr_b16 v[152:153], v22\n"
        "ds_read_b64_tr_b16 v[154:155], v22 offset:512\n"
        "s_waitcnt vmcnt(19)\n"
        "s_waitcnt lgkmcnt(0)\n"
        "s_barrier\n"
        "s_add_u32 s54, s92, 0x360000\n"
        "s_addc_u32 s55, s93, 0\n"
        "s_add_u32 m0, s59, 0x1c000\n"
        "s_nop 0\n"
        "global_load_lds_dwordx4 v3, s[54:55] offset:0\n"
        "s_add_u32 m0, s59, 0x1cf80\n"
        "s_nop 0\n"
        "global_load_lds_dwordx4 v3, s[54:55] offset:128\n"
        "s_add_u32 m0, s59, 0x1df00\n"
        "s_nop 0\n"
        "global_load_lds_dwordx4 v3, s[54:55] offset:256\n"
        "s_add_u32 s54, s94, 0x360000\n"
        "s_addc_u32 s55, s95, 0\n"
        "s_add_u32 m0, s59, 0x1f000\n"
        "s_nop 0\n"
        "global_load_lds_dwordx4 v3, s[54:55] offset:0\n"
        "s_add_u32 m0, s59, 0x1ff80\n"
        "s_nop 0\n"
        "global_load_lds_dwordx4 v3, s[54:55] offset:128\n"
        "s_add_u32 m0, s59, 0x20f00\n"
        "s_nop 0\n"
        "global_load_lds_dwordx4 v3, s[54:55] offset:256\n"
        "s_bfe_u32 s47, s58, 0x5000a\n"
        "s_lshl_b32 s47, s47, 11\n"
        "v_add_u32 v28, s47, v1\n"
        "v_lshlrev_b32 v30, 16, v64\n"
        "v_and_b32 v64, 0xffff0000, v64\n"
        "v_fma_f32 v30, v30, v33, -v32\n"
        "v_fma_f32 v64, v64, v33, -v32\n"
        "v_cvt_pk_bf16_f32 v64, v30, v64\n"
        "v_lshlrev_b32 v31, 16, v65\n"
        "v_and_b32 v65, 0xffff0000, v65\n"
        "v_fma_f32 v31, v31, v33, -v32\n"
        "v_fma_f32 v65, v65, v33, -v32\n"
        "v_cvt_pk_bf16_f32 v65, v31, v65\n"
        "v_lshlrev_b32 v30, 16, v66\n"
        "v_and_b32 v66, 0xffff0000, v66\n"
        "v_fma_f32 v30, v30, v33, -v32\n"
        "v_fma_f32 v66, v66, v33, -v32\n"
        "v_cvt_pk_bf16_f32 v66, v30, v66\n"
        "v_lshlrev_b32 v31, 16, v67\n"
        "v_and_b32 v67, 0xffff0000, v67\n"
        "v_fma_f32 v31, v31, v33, -v32\n"
        "v_fma_f32 v67, v67, v33, -v32\n"
        "v_cvt_pk_bf16_f32 v67, v31, v67\n"
        "ds_write_b128 v28, v[64:67] offset:0\n"
        "v_lshlrev_b32 v30, 16, v68\n"
        "v_and_b32 v68, 0xffff0000, v68\n"
        "v_fma_f32 v30, v30, v35, -v34\n"
        "v_fma_f32 v68, v68, v35, -v34\n"
        "v_cvt_pk_bf16_f32 v68, v30, v68\n"
        "v_lshlrev_b32 v31, 16, v69\n"
        "v_and_b32 v69, 0xffff0000, v69\n"
        "v_fma_f32 v31, v31, v35, -v34\n"
        "v_fma_f32 v69, v69, v35, -v34\n"
        "v_cvt_pk_bf16_f32 v69, v31, v69\n"
        "v_lshlrev_b32 v30, 16, v70\n"
        "v_and_b32 v70, 0xffff0000, v70\n"
        "v_fma_f32 v30, v30, v35, -v34\n"
        "v_fma_f32 v70, v70, v35, -v34\n"
        "v_cvt_pk_bf16_f32 v70, v30, v70\n"
        "v_lshlrev_b32 v31, 16, v71\n"
        "v_and_b32 v71, 0xffff0000, v71\n"
        "v_fma_f32 v31, v31, v35, -v34\n"
        "v_fma_f32 v71, v71, v35, -v34\n"
        "v_cvt_pk_bf16_f32 v71, v31, v71\n"
        "ds_write_b128 v28, v[68:71] offset:1024\n"
        "s_bfe_u32 s47, s58, 0x5000f\n"
        "s_lshl_b32 s47, s47, 11\n"
        "v_add_u32 v28, s47, v1\n"
        "v_lshlrev_b32 v30, 16, v72\n"
        "v_and_b32 v72, 0xffff0000, v72\n"
        "v_fma_f32 v30, v30, v33, -v32\n"
        "v_fma_f32 v72, v72, v33, -v32\n"
        "v_cvt_pk_bf16_f32 v72, v30, v72\n"
        "v_lshlrev_b32 v31, 16, v73\n"
        "v_and_b32 v73, 0xffff0000, v73\n"
        "v_fma_f32 v31, v31, v33, -v32\n"
        "v_fma_f32 v73, v73, v33, -v32\n"
        "v_cvt_pk_bf16_f32 v73, v31, v73\n"
        "v_lshlrev_b32 v30, 16, v74\n"
        "v_and_b32 v74, 0xffff0000, v74\n"
        "v_fma_f32 v30, v30, v33, -v32\n"
        "v_fma_f32 v74, v74, v33, -v32\n"
        "v_cvt_pk_bf16_f32 v74, v30, v74\n"
        "v_lshlrev_b32 v31, 16, v75\n"
        "v_and_b32 v75, 0xffff0000, v75\n"
        "v_fma_f32 v31, v31, v33, -v32\n"
        "v_fma_f32 v75, v75, v33, -v32\n"
        "v_cvt_pk_bf16_f32 v75, v31, v75\n"
        "ds_write_b128 v28, v[72:75] offset:0\n"
        "v_lshlrev_b32 v30, 16, v76\n"
        "v_and_b32 v76, 0xffff0000, v76\n"
        "v_fma_f32 v30, v30, v35, -v34\n"
        "v_fma_f32 v76, v76, v35, -v34\n"
        "v_cvt_pk_bf16_f32 v76, v30, v76\n"
        "v_lshlrev_b32 v31, 16, v77\n"
        "v_and_b32 v77, 0xffff0000, v77\n"
        "v_fma_f32 v31, v31, v35, -v34\n"
        "v_fma_f32 v77, v77, v35, -v34\n"
        "v_cvt_pk_bf16_f32 v77, v31, v77\n"
        "v_lshlrev_b32 v30, 16, v78\n"
        "v_and_b32 v78, 0xffff0000, v78\n"
        "v_fma_f32 v30, v30, v35, -v34\n"
        "v_fma_f32 v78, v78, v35, -v34\n"
        "v_cvt_pk_bf16_f32 v78, v30, v78\n"
        "v_lshlrev_b32 v31, 16, v79\n"
        "v_and_b32 v79, 0xffff0000, v79\n"
        "v_fma_f32 v31, v31, v35, -v34\n"
        "v_fma_f32 v79, v79, v35, -v34\n"
        "v_cvt_pk_bf16_f32 v79, v31, v79\n"
        "ds_write_b128 v28, v[76:79] offset:1024\n"
        "s_bfe_u32 s47, s58, 0x50014\n"
        "s_lshl_b32 s47, s47, 11\n"
        "v_add_u32 v28, s47, v1\n"
        "v_lshlrev_b32 v30, 16, v80\n"
        "v_and_b32 v80, 0xffff0000, v80\n"
        "v_fma_f32 v30, v30, v33, -v32\n"
        "v_fma_f32 v80, v80, v33, -v32\n"
        "v_cvt_pk_bf16_f32 v80, v30, v80\n"
        "v_lshlrev_b32 v31, 16, v81\n"
        "v_and_b32 v81, 0xffff0000, v81\n"
        "v_fma_f32 v31, v31, v33, -v32\n"
        "v_fma_f32 v81, v81, v33, -v32\n"
        "v_cvt_pk_bf16_f32 v81, v31, v81\n"
        "v_lshlrev_b32 v30, 16, v82\n"
        "v_and_b32 v82, 0xffff0000, v82\n"
        "v_fma_f32 v30, v30, v33, -v32\n"
        "v_fma_f32 v82, v82, v33, -v32\n"
        "v_cvt_pk_bf16_f32 v82, v30, v82\n"
        "v_lshlrev_b32 v31, 16, v83\n"
        "v_and_b32 v83, 0xffff0000, v83\n"
        "v_fma_f32 v31, v31, v33, -v32\n"
        "v_fma_f32 v83, v83, v33, -v32\n"
        "v_cvt_pk_bf16_f32 v83, v31, v83\n"
        "ds_write_b128 v28, v[80:83] offset:0\n"
        "v_lshlrev_b32 v30, 16, v84\n"
        "v_and_b32 v84, 0xffff0000, v84\n"
        "v_fma_f32 v30, v30, v35, -v34\n"
        "v_fma_f32 v84, v84, v35, -v34\n"
        "v_cvt_pk_bf16_f32 v84, v30, v84\n"
        "v_lshlrev_b32 v31, 16, v85\n"
        "v_and_b32 v85, 0xffff0000, v85\n"
        "v_fma_f32 v31, v31, v35, -v34\n"
        "v_fma_f32 v85, v85, v35, -v34\n"
        "v_cvt_pk_bf16_f32 v85, v31, v85\n"
        "v_lshlrev_b32 v30, 16, v86\n"
        "v_and_b32 v86, 0xffff0000, v86\n"
        "v_fma_f32 v30, v30, v35, -v34\n"
        "v_fma_f32 v86, v86, v35, -v34\n"
        "v_cvt_pk_bf16_f32 v86, v30, v86\n"
        "v_lshlrev_b32 v31, 16, v87\n"
        "v_and_b32 v87, 0xffff0000, v87\n"
        "v_fma_f32 v31, v31, v35, -v34\n"
        "v_fma_f32 v87, v87, v35, -v34\n"
        "v_cvt_pk_bf16_f32 v87, v31, v87\n"
        "ds_write_b128 v28, v[84:87] offset:1024\n"
        "v_add_u32 v20, 0x22000, v5\n"
        "ds_read_b64_tr_b16 a[20:21], v20\n"
        "ds_read_b64_tr_b16 a[22:23], v20 offset:512\n"
        "v_add_u32 v21, 0x22000, v6\n"
        "ds_read_b64_tr_b16 a[148:149], v21\n"
        "ds_read_b64_tr_b16 a[150:151], v21 offset:512\n"
        "v_add_u32 v22, 0x22000, v7\n"
        "ds_read_b64_tr_b16 v[148:149], v22\n"
        "ds_read_b64_tr_b16 v[150:151], v22 offset:512\n"
        "v_add_u32 v20, 0x25000, v5\n"
        "ds_read_b64_tr_b16 a[28:29], v20\n"
        "ds_read_b64_tr_b16 a[30:31], v20 offset:512\n"
        "v_add_u32 v21, 0x25000, v6\n"
        "ds_read_b64_tr_b16 a[156:157], v21\n"
        "ds_read_b64_tr_b16 a[158:159], v21 offset:512\n"
        "v_add_u32 v22, 0x25000, v7\n"
        "ds_read_b64_tr_b16 v[156:157], v22\n"
        "ds_read_b64_tr_b16 v[158:159], v22 offset:512\n"
        "s_waitcnt vmcnt(19)\n"
        "s_waitcnt lgkmcnt(0)\n"
        "s_barrier\n"
        "s_add_u32 s54, s92, 0x480000\n"
        "s_addc_u32 s55, s93, 0\n"
        "s_add_u32 m0, s59, 0x22000\n"
        "s_nop 0\n"
        "global_load_lds_dwordx4 v3, s[54:55] offset:0\n"
        "s_add_u32 m0, s59, 0x22f80\n"
        "s_nop 0\n"
        "global_load_lds_dwordx4 v3, s[54:55] offset:128\n"
        "s_add_u32 m0, s59, 0x23f00\n"
        "s_nop 0\n"
        "global_load_lds_dwordx4 v3, s[54:55] offset:256\n"
        "s_add_u32 s54, s94, 0x480000\n"
        "s_addc_u32 s55, s95, 0\n"
        "s_add_u32 m0, s59, 0x25000\n"
        "s_nop 0\n"
        "global_load_lds_dwordx4 v3, s[54:55] offset:0\n"
        "s_add_u32 m0, s59, 0x25f80\n"
        "s_nop 0\n"
        "global_load_lds_dwordx4 v3, s[54:55] offset:128\n"
        "s_add_u32 m0, s59, 0x26f00\n"
        "s_nop 0\n"
        "global_load_lds_dwordx4 v3, s[54:55] offset:256\n"
        "v_and_b32 v24, 15, v0\n"
        "v_lshrrev_b32 v25, 4, v0\n"
        "v_lshlrev_b32 v26, 1, v25\n"
        "v_sub_u32 v26, v24, v26\n"
        "v_mov_b32 v8, 0\n"
        "v_mov_b32 v27, 0x3f80\n"
        "v_cmp_eq_u32 vcc, 0, v26\n"
        "s_nop 1\n"
        "v_cndmask_b32 v8, v8, v27, vcc\n"
        "v_mov_b32 v27, 0x3f800000\n"
        "v_cmp_eq_u32 vcc, 1, v26\n"
        "s_nop 1\n"
        "v_cndmask_b32 v8, v8, v27, vcc\n"
        "v_mov_b32 v9, 0\n"
        "v_mov_b32 v27, 0x3f80\n"
        "v_cmp_eq_u32 vcc, 8, v26\n"
        "s_nop 1\n"
        "v_cndmask_b32 v9, v9, v27, vcc\n"
        "v_mov_b32 v27, 0x3f800000\n"
        "v_cmp_eq_u32 vcc, 9, v26\n"
        "s_nop 1\n"
        "v_cndmask_b32 v9, v9, v27, vcc\n"
        "v_mul_u32_u24 v26, 176, v24\n"
        "v_lshl_add_u32 v26, v25, 4, v26\n"
        "s_mul_i32 s47, s43, 0x600\n"
        "s_add_u32 s47, s47, 40960\n"
        "v_add_u32 v10, s47, v26\n"
        "s_mul_i32 s47, s43, 0x1600\n"
        "s_add_u32 s47, s47, 40960\n"
        "v_add_u32 v16, s47, v26\n"
        "v_and_b32 v26, 1, v0\n"
        "v_lshlrev_b32 v26, 4, v26\n"
        "v_bfe_u32 v27, v0, 3, 1\n"
        "v_lshl_add_u32 v26, v27, 3, v26\n"
        "v_bfe_u32 v27, v0, 1, 1\n"
        "v_lshl_add_u32 v26, v27, 2, v26\n"
        "v_bfe_u32 v27, v0, 2, 1\n"
        "v_lshl_add_u32 v26, v27, 1, v26\n"
        "v_and_b32 v27, 1, v0\n"
        "v_lshlrev_b32 v27, 2, v27\n"
        "v_bfe_u32 v28, v0, 1, 1\n"
        "v_lshl_add_u32 v27, v28, 1, v27\n"
        "v_add_u32 v27, 32, v27\n"
        "v_cmp_gt_u32 vcc, 32, v0\n"
        "s_nop 1\n"
        "v_cndmask_b32 v26, v27, v26, vcc\n"
        "v_bfe_u32 v27, v0, 4, 1\n"
        "s_lshl_b32 s47, s43, 1\n"
        "v_add_u32 v28, s47, v27\n"
        "v_mul_u32_u24 v28, 176, v28\n"
        "v_lshl_add_u32 v28, v26, 2, v28\n"
        "v_add_u32 v11, 40960, v28\n"
        "s_mov_b32 s64, 0xffffffff\n"
        "s_mov_b32 s65, 0xf000f\n"
        "v_mov_b32 v28, 0xff800000\n"
        "v_cndmask_b32 v12, v28, 0, s[64:65]\n"
        "v_mov_b32 v13, v12\n"
        "s_lshl_b32 s47, s43, 1\n"
        "v_add_u32 v27, s47, v27\n"
        "v_lshlrev_b32 v28, 6, v27\n"
        "v_lshlrev_b32 v29, 4, v27\n"
        "v_add_u32 v29, 0x200, v29\n"
        "v_cndmask_b32 v28, v29, v28, vcc\n"
        "v_lshl_add_u32 v28, v24, 2, v28\n"
        "s_mul_i32 s68, s40, 0x56\n"
        "s_lshr_b32 s68, s68, 8\n"
        "s_mul_i32 s47, s68, 0x2200\n"
        "s_mul_i32 s68, s40, 0xa00\n"
        "s_add_u32 s47, s47, s68\n"
        "v_add_u32 v14, s47, v28\n"
        "s_mov_b32 s62, 0xff00ff\n"
        "s_mov_b32 s63, 0xff00ff\n"
        "s_mov_b32 s66, 0x1f1f1f1f\n"
        "s_mov_b32 s67, 0x1f1f1f1f\n"
        "v_lshrrev_b32 v26, 3, v0\n"
        "s_lshl_b32 s47, s43, 3\n"
        "v_add_u32 v26, s47, v26\n"
        "v_and_b32 v27, 7, v0\n"
        "v_mul_u32_u24 v28, 176, v26\n"
        "v_add_u32 v28, 40960, v28\n"
        "v_min_u32 v29, 3, v27\n"
        "v_lshlrev_b32 v29, 4, v29\n"
        "v_add_u32 v30, 64, v29\n"
        "v_cmp_eq_u32 vcc, 4, v27\n"
        "v_mov_b32 v31, 128\n"
        "s_nop 0\n"
        "v_cndmask_b32 v29, v29, v31, vcc\n"
        "v_mov_b32 v31, 144\n"
        "v_cndmask_b32 v30, v30, v31, vcc\n"
        "v_add_u32 v17, v28, v29\n"
        "v_add_u32 v18, v28, v30\n"
        "v_lshlrev_b32 v28, 2, v27\n"
        "v_sub_u32 v28, v26, v28\n"
        "v_subrev_u32 v29, 16, v28\n"
        "v_mov_b32 v31, 100\n"
        "v_cndmask_b32 v28, v28, v31, vcc\n"
        "v_cndmask_b32 v29, v29, v31, vcc\n"
        "v_cmp_lt_u32 vcc, 4, v27\n"
        "s_nop 1\n"
        "v_cndmask_b32 v28, v28, -1, vcc\n"
        "v_cndmask_b32 v29, v29, -1, vcc\n"
        "v_cmp_le_i32 s[70:71], 0, v28\n"
        "v_cmp_le_i32 s[78:79], 0, v29\n"
        "v_cmp_le_i32 s[72:73], 1, v28\n"
        "v_cmp_le_i32 s[80:81], 1, v29\n"
        "v_cmp_le_i32 s[74:75], 2, v28\n"
        "v_cmp_le_i32 s[82:83], 2, v29\n"
        "v_cmp_le_i32 s[76:77], 3, v28\n"
        "v_cmp_le_i32 s[84:85], 3, v29\n"
        "v_lshrrev_b32 v28, 4, v26\n"
        "v_lshlrev_b32 v28, 10, v28\n"
        "v_and_b32 v29, 15, v26\n"
        "v_lshl_add_u32 v28, v29, 4, v28\n"
        "s_and_b32 s47, s40, 7\n"
        "s_lshl_b32 s47, s47, 11\n"
        "v_lshlrev_b32 v29, 8, v27\n"
        "v_add_u32 v29, s47, v29\n"
        "s_and_b32 s47, s40, 4\n"
        "s_lshl_b32 s47, s47, 9\n"
        "s_and_b32 s68, s40, 3\n"
        "s_lshl_b32 s68, s68, 8\n"
        "s_add_u32 s47, s47, s68\n"
        "s_add_u32 s47, s47, 16384\n"
        "v_mov_b32 v30, s47\n"
        "v_cmp_eq_u32 vcc, 4, v27\n"
        "s_nop 1\n"
        "v_cndmask_b32 v29, v29, v30, vcc\n"
        "v_add_u32 v28, v28, v29\n"
        "v_and_b32 v29, 0xffffe000, v28\n"
        "v_add_u32 v28, v28, v29\n"
        "s_lshr_b32 s47, s40, 3\n"
        "s_lshl_b32 s47, s47, 16\n"
        "v_add_u32 v19, s47, v28\n"
        "s_mov_b32 s61, 0x3dd53b95\n"
        "v_add_u32 v20, 0xa000, v5\n"
        "ds_read_b64_tr_b16 a[32:33], v20\n"
        "ds_read_b64_tr_b16 a[34:35], v20 offset:512\n"
        "v_add_u32 v21, 0xa000, v6\n"
        "ds_read_b64_tr_b16 a[160:161], v21\n"
        "ds_read_b64_tr_b16 a[162:163], v21 offset:512\n"
        "v_add_u32 v22, 0xa000, v7\n"
        "ds_read_b64_tr_b16 v[160:161], v22\n"
        "ds_read_b64_tr_b16 v[162:163], v22 offset:512\n"
        "v_add_u32 v20, 0xd000, v5\n"
        "ds_read_b64_tr_b16 a[40:41], v20\n"
        "ds_read_b64_tr_b16 a[42:43], v20 offset:512\n"
        "v_add_u32 v21, 0xd000, v6\n"
        "ds_read_b64_tr_b16 a[168:169], v21\n"
        "ds_read_b64_tr_b16 a[170:171], v21 offset:512\n"
        "v_add_u32 v22, 0xd000, v7\n"
        "ds_read_b64_tr_b16 v[168:169], v22\n"
        "ds_read_b64_tr_b16 v[170:171], v22 offset:512\n"
        "s_waitcnt vmcnt(18)\n"
        "s_waitcnt lgkmcnt(0)\n"
        "s_barrier\n"
        "s_add_u32 s54, s92, 0x4e0000\n"
        "s_addc_u32 s55, s93, 0\n"
        "s_add_u32 m0, s59, 0xa000\n"
        "s_nop 0\n"
        "global_load_lds_dwordx4 v3, s[54:55] offset:0\n"
        "s_add_u32 m0, s59, 0xaf80\n"
        "s_nop 0\n"
        "global_load_lds_dwordx4 v3, s[54:55] offset:128\n"
        "s_add_u32 m0, s59, 0xbf00\n"
        "s_nop 0\n"
        "global_load_lds_dwordx4 v3, s[54:55] offset:256\n"
        "s_add_u32 s54, s94, 0x4e0000\n"
        "s_addc_u32 s55, s95, 0\n"
        "s_add_u32 m0, s59, 0xd000\n"
        "s_nop 0\n"
        "global_load_lds_dwordx4 v3, s[54:55] offset:0\n"
        "s_add_u32 m0, s59, 0xdf80\n"
        "s_nop 0\n"
        "global_load_lds_dwordx4 v3, s[54:55] offset:128\n"
        "s_add_u32 m0, s59, 0xef00\n"
        "s_nop 0\n"
        "global_load_lds_dwordx4 v3, s[54:55] offset:256\n"
        "v_add_u32 v20, 0x10000, v5\n"
        "ds_read_b64_tr_b16 a[36:37], v20\n"
        "ds_read_b64_tr_b16 a[38:39], v20 offset:512\n"
        "v_add_u32 v21, 0x10000, v6\n"
        "ds_read_b64_tr_b16 a[164:165], v21\n"
        "ds_read_b64_tr_b16 a[166:167], v21 offset:512\n"
        "v_add_u32 v22, 0x10000, v7\n"
        "ds_read_b64_tr_b16 v[164:165], v22\n"
        "ds_read_b64_tr_b16 v[166:167], v22 offset:512\n"
        "v_add_u32 v20, 0x13000, v5\n"
        "ds_read_b64_tr_b16 a[44:45], v20\n"
        "ds_read_b64_tr_b16 a[46:47], v20 offset:512\n"
        "v_add_u32 v21, 0x13000, v6\n"
        "ds_read_b64_tr_b16 a[172:173], v21\n"
        "ds_read_b64_tr_b16 a[174:175], v21 offset:512\n"
        "v_add_u32 v22, 0x13000, v7\n"
        "ds_read_b64_tr_b16 v[172:173], v22\n"
        "ds_read_b64_tr_b16 v[174:175], v22 offset:512\n"
        "ds_read_b128 v[24:27], v1 offset:0\n"
        "ds_read_b128 v[28:31], v1 offset:1024\n"
        "s_waitcnt lgkmcnt(0)\n"
        "v_mfma_f32_16x16x32_bf16 v[48:51], a[0:3], v[24:27], 0\n"
        "v_mfma_f32_16x16x32_bf16 v[52:55], a[0:3], v[28:31], 0\n"
        "v_mfma_f32_16x16x32_bf16 v[72:75], a[8:11], v[24:27], 0\n"
        "v_mfma_f32_16x16x32_bf16 v[76:79], a[8:11], v[28:31], 0\n"
        "v_mfma_f32_16x16x32_bf16 v[56:59], a[128:131], v[24:27], 0\n"
        "v_mfma_f32_16x16x32_bf16 v[60:63], a[128:131], v[28:31], 0\n"
        "v_mfma_f32_16x16x32_bf16 v[80:83], a[136:139], v[24:27], 0\n"
        "v_mfma_f32_16x16x32_bf16 v[84:87], a[136:139], v[28:31], 0\n"
        "v_mfma_f32_16x16x32_bf16 v[64:67], v[128:131], v[24:27], 0\n"
        "v_mfma_f32_16x16x32_bf16 v[68:71], v[128:131], v[28:31], 0\n"
        "v_mfma_f32_16x16x32_bf16 v[88:91], v[136:139], v[24:27], 0\n"
        "v_mfma_f32_16x16x32_bf16 v[92:95], v[136:139], v[28:31], 0\n"
        "ds_read_b128 v[24:27], v1 offset:2048\n"
        "ds_read_b128 v[28:31], v1 offset:3072\n"
        "s_waitcnt lgkmcnt(0)\n"
        "v_mfma_f32_16x16x32_bf16 v[48:51], a[4:7], v[24:27], v[48:51]\n"
        "v_mfma_f32_16x16x32_bf16 v[52:55], a[4:7], v[28:31], v[52:55]\n"
        "v_mfma_f32_16x16x32_bf16 v[72:75], a[12:15], v[24:27], v[72:75]\n"
        "v_mfma_f32_16x16x32_bf16 v[76:79], a[12:15], v[28:31], v[76:79]\n"
        "v_mfma_f32_16x16x32_bf16 v[56:59], a[132:135], v[24:27], v[56:59]\n"
        "v_mfma_f32_16x16x32_bf16 v[60:63], a[132:135], v[28:31], v[60:63]\n"
        "v_mfma_f32_16x16x32_bf16 v[80:83], a[140:143], v[24:27], v[80:83]\n"
        "v_mfma_f32_16x16x32_bf16 v[84:87], a[140:143], v[28:31], v[84:87]\n"
        "v_mfma_f32_16x16x32_bf16 v[64:67], v[132:135], v[24:27], v[64:67]\n"
        "v_mfma_f32_16x16x32_bf16 v[68:71], v[132:135], v[28:31], v[68:71]\n"
        "v_mfma_f32_16x16x32_bf16 v[88:91], v[140:143], v[24:27], v[88:91]\n"
        "v_mfma_f32_16x16x32_bf16 v[92:95], v[140:143], v[28:31], v[92:95]\n"
        "s_waitcnt vmcnt(18)\n"
        "s_waitcnt lgkmcnt(0)\n"
        "s_barrier\n"
        "s_add_u32 s54, s92, 0x540000\n"
        "s_addc_u32 s55, s93, 0\n"
        "s_add_u32 m0, s59, 0x10000\n"
        "s_nop 0\n"
        "global_load_lds_dwordx4 v3, s[54:55] offset:0\n"
        "s_add_u32 m0, s59, 0x10f80\n"
        "s_nop 0\n"
        "global_load_lds_dwordx4 v3, s[54:55] offset:128\n"
        "s_add_u32 m0, s59, 0x11f00\n"
        "s_nop 0\n"
        "global_load_lds_dwordx4 v3, s[54:55] offset:256\n"
        "s_add_u32 s54, s94, 0x540000\n"
        "s_addc_u32 s55, s95, 0\n"
        "s_add_u32 m0, s59, 0x13000\n"
        "s_nop 0\n"
        "global_load_lds_dwordx4 v3, s[54:55] offset:0\n"
        "s_add_u32 m0, s59, 0x13f80\n"
        "s_nop 0\n"
        "global_load_lds_dwordx4 v3, s[54:55] offset:128\n"
        "s_add_u32 m0, s59, 0x14f00\n"
        "s_nop 0\n"
        "global_load_lds_dwordx4 v3, s[54:55] offset:256\n"
        "v_add_u32 v20, 0x16000, v5\n"
        "ds_read_b64_tr_b16 a[48:49], v20\n"
        "ds_read_b64_tr_b16 a[50:51], v20 offset:512\n"
        "v_add_u32 v21, 0x16000, v6\n"
        "ds_read_b64_tr_b16 a[176:177], v21\n"
        "ds_read_b64_tr_b16 a[178:179], v21 offset:512\n"
        "v_add_u32 v22, 0x16000, v7\n"
        "ds_read_b64_tr_b16 v[176:177], v22\n"
        "ds_read_b64_tr_b16 v[178:179], v22 offset:512\n"
        "v_add_u32 v20, 0x19000, v5\n"
        "ds_read_b64_tr_b16 a[56:57], v20\n"
        "ds_read_b64_tr_b16 a[58:59], v20 offset:512\n"
        "v_add_u32 v21, 0x19000, v6\n"
        "ds_read_b64_tr_b16 a[184:185], v21\n"
        "ds_read_b64_tr_b16 a[186:187], v21 offset:512\n"
        "v_add_u32 v22, 0x19000, v7\n"
        "ds_read_b64_tr_b16 v[184:185], v22\n"
        "ds_read_b64_tr_b16 v[186:187], v22 offset:512\n"
        "ds_read_b128 v[24:27], v1 offset:4096\n"
        "ds_read_b128 v[28:31], v1 offset:5120\n"
        "s_waitcnt lgkmcnt(0)\n"
        "v_mfma_f32_16x16x32_bf16 v[48:51], a[16:19], v[24:27], v[48:51]\n"
        "v_mfma_f32_16x16x32_bf16 v[52:55], a[16:19], v[28:31], v[52:55]\n"
        "v_mfma_f32_16x16x32_bf16 v[72:75], a[24:27], v[24:27], v[72:75]\n"
        "v_mfma_f32_16x16x32_bf16 v[76:79], a[24:27], v[28:31], v[76:79]\n"
        "v_mfma_f32_16x16x32_bf16 v[56:59], a[144:147], v[24:27], v[56:59]\n"
        "v_mfma_f32_16x16x32_bf16 v[60:63], a[144:147], v[28:31], v[60:63]\n"
        "v_mfma_f32_16x16x32_bf16 v[80:83], a[152:155], v[24:27], v[80:83]\n"
        "v_mfma_f32_16x16x32_bf16 v[84:87], a[152:155], v[28:31], v[84:87]\n"
        "v_mfma_f32_16x16x32_bf16 v[64:67], v[144:147], v[24:27], v[64:67]\n"
        "v_mfma_f32_16x16x32_bf16 v[68:71], v[144:147], v[28:31], v[68:71]\n"
        "v_mfma_f32_16x16x32_bf16 v[88:91], v[152:155], v[24:27], v[88:91]\n"
        "v_mfma_f32_16x16x32_bf16 v[92:95], v[152:155], v[28:31], v[92:95]\n"
        "ds_read_b128 v[24:27], v1 offset:6144\n"
        "ds_read_b128 v[28:31], v1 offset:7168\n"
        "s_waitcnt lgkmcnt(0)\n"
        "v_mfma_f32_16x16x32_bf16 v[48:51], a[20:23], v[24:27], v[48:51]\n"
        "v_mfma_f32_16x16x32_bf16 v[52:55], a[20:23], v[28:31], v[52:55]\n"
        "v_mfma_f32_16x16x32_bf16 v[72:75], a[28:31], v[24:27], v[72:75]\n"
        "v_mfma_f32_16x16x32_bf16 v[76:79], a[28:31], v[28:31], v[76:79]\n"
        "v_mfma_f32_16x16x32_bf16 v[56:59], a[148:151], v[24:27], v[56:59]\n"
        "v_mfma_f32_16x16x32_bf16 v[60:63], a[148:151], v[28:31], v[60:63]\n"
        "v_mfma_f32_16x16x32_bf16 v[80:83], a[156:159], v[24:27], v[80:83]\n"
        "v_mfma_f32_16x16x32_bf16 v[84:87], a[156:159], v[28:31], v[84:87]\n"
        "v_mfma_f32_16x16x32_bf16 v[64:67], v[148:151], v[24:27], v[64:67]\n"
        "v_mfma_f32_16x16x32_bf16 v[68:71], v[148:151], v[28:31], v[68:71]\n"
        "v_mfma_f32_16x16x32_bf16 v[88:91], v[156:159], v[24:27], v[88:91]\n"
        "v_mfma_f32_16x16x32_bf16 v[92:95], v[156:159], v[28:31], v[92:95]\n"
        "s_waitcnt vmcnt(18)\n"
        "s_waitcnt lgkmcnt(0)\n"
        "s_barrier\n"
        "s_add_u32 s54, s92, 0x5a0000\n"
        "s_addc_u32 s55, s93, 0\n"
        "s_add_u32 m0, s59, 0x16000\n"
        "s_nop 0\n"
        "global_load_lds_dwordx4 v3, s[54:55] offset:0\n"
        "s_add_u32 m0, s59, 0x16f80\n"
        "s_nop 0\n"
        "global_load_lds_dwordx4 v3, s[54:55] offset:128\n"
        "s_add_u32 m0, s59, 0x17f00\n"
        "s_nop 0\n"
        "global_load_lds_dwordx4 v3, s[54:55] offset:256\n"
        "s_add_u32 s54, s94, 0x5a0000\n"
        "s_addc_u32 s55, s95, 0\n"
        "s_add_u32 m0, s59, 0x19000\n"
        "s_nop 0\n"
        "global_load_lds_dwordx4 v3, s[54:55] offset:0\n"
        "s_add_u32 m0, s59, 0x19f80\n"
        "s_nop 0\n"
        "global_load_lds_dwordx4 v3, s[54:55] offset:128\n"
        "s_add_u32 m0, s59, 0x1af00\n"
        "s_nop 0\n"
        "global_load_lds_dwordx4 v3, s[54:55] offset:256\n"
        "v_add_u32 v20, 0x1c000, v5\n"
        "ds_read_b64_tr_b16 a[52:53], v20\n"
        "ds_read_b64_tr_b16 a[54:55], v20 offset:512\n"
        "v_add_u32 v21, 0x1c000, v6\n"
        "ds_read_b64_tr_b16 a[180:181], v21\n"
        "ds_read_b64_tr_b16 a[182:183], v21 offset:512\n"
        "v_add_u32 v22, 0x1c000, v7\n"
        "ds_read_b64_tr_b16 v[180:181], v22\n"
        "ds_read_b64_tr_b16 v[182:183], v22 offset:512\n"
        "v_add_u32 v20, 0x1f000, v5\n"
        "ds_read_b64_tr_b16 a[60:61], v20\n"
        "ds_read_b64_tr_b16 a[62:63], v20 offset:512\n"
        "v_add_u32 v21, 0x1f000, v6\n"
        "ds_read_b64_tr_b16 a[188:189], v21\n"
        "ds_read_b64_tr_b16 a[190:191], v21 offset:512\n"
        "v_add_u32 v22, 0x1f000, v7\n"
        "ds_read_b64_tr_b16 v[188:189], v22\n"
        "ds_read_b64_tr_b16 v[190:191], v22 offset:512\n"
        "ds_read_b128 v[24:27], v1 offset:8192\n"
        "ds_read_b128 v[28:31], v1 offset:9216\n"
        "s_waitcnt lgkmcnt(0)\n"
        "v_mfma_f32_16x16x32_bf16 v[48:51], a[32:35], v[24:27], v[48:51]\n"
        "v_mfma_f32_16x16x32_bf16 v[52:55], a[32:35], v[28:31], v[52:55]\n"
        "v_mfma_f32_16x16x32_bf16 v[72:75], a[40:43], v[24:27], v[72:75]\n"
        "v_mfma_f32_16x16x32_bf16 v[76:79], a[40:43], v[28:31], v[76:79]\n"
        "v_mfma_f32_16x16x32_bf16 v[56:59], a[160:163], v[24:27], v[56:59]\n"
        "v_mfma_f32_16x16x32_bf16 v[60:63], a[160:163], v[28:31], v[60:63]\n"
        "v_mfma_f32_16x16x32_bf16 v[80:83], a[168:171], v[24:27], v[80:83]\n"
        "v_mfma_f32_16x16x32_bf16 v[84:87], a[168:171], v[28:31], v[84:87]\n"
        "v_mfma_f32_16x16x32_bf16 v[64:67], v[160:163], v[24:27], v[64:67]\n"
        "v_mfma_f32_16x16x32_bf16 v[68:71], v[160:163], v[28:31], v[68:71]\n"
        "v_mfma_f32_16x16x32_bf16 v[88:91], v[168:171], v[24:27], v[88:91]\n"
        "v_mfma_f32_16x16x32_bf16 v[92:95], v[168:171], v[28:31], v[92:95]\n"
        "ds_read_b128 v[24:27], v1 offset:10240\n"
        "ds_read_b128 v[28:31], v1 offset:11264\n"
        "s_waitcnt lgkmcnt(0)\n"
        "v_mfma_f32_16x16x32_bf16 v[48:51], a[36:39], v[24:27], v[48:51]\n"
        "v_mfma_f32_16x16x32_bf16 v[52:55], a[36:39], v[28:31], v[52:55]\n"
        "v_mfma_f32_16x16x32_bf16 v[72:75], a[44:47], v[24:27], v[72:75]\n"
        "v_mfma_f32_16x16x32_bf16 v[76:79], a[44:47], v[28:31], v[76:79]\n"
        "v_mfma_f32_16x16x32_bf16 v[56:59], a[164:167], v[24:27], v[56:59]\n"
        "v_mfma_f32_16x16x32_bf16 v[60:63], a[164:167], v[28:31], v[60:63]\n"
        "v_mfma_f32_16x16x32_bf16 v[80:83], a[172:175], v[24:27], v[80:83]\n"
        "v_mfma_f32_16x16x32_bf16 v[84:87], a[172:175], v[28:31], v[84:87]\n"
        "v_mfma_f32_16x16x32_bf16 v[64:67], v[164:167], v[24:27], v[64:67]\n"
        "v_mfma_f32_16x16x32_bf16 v[68:71], v[164:167], v[28:31], v[68:71]\n"
        "v_mfma_f32_16x16x32_bf16 v[88:91], v[172:175], v[24:27], v[88:91]\n"
        "v_mfma_f32_16x16x32_bf16 v[92:95], v[172:175], v[28:31], v[92:95]\n"
        "s_waitcnt vmcnt(18)\n"
        "s_waitcnt lgkmcnt(0)\n"
        "s_barrier\n"
        "s_add_u32 s54, s92, 0x600000\n"
        "s_addc_u32 s55, s93, 0\n"
        "s_add_u32 m0, s59, 0x1c000\n"
        "s_nop 0\n"
        "global_load_lds_dwordx4 v3, s[54:55] offset:0\n"
        "s_add_u32 m0, s59, 0x1cf80\n"
        "s_nop 0\n"
        "global_load_lds_dwordx4 v3, s[54:55] offset:128\n"
        "s_add_u32 m0, s59, 0x1df00\n"
        "s_nop 0\n"
        "global_load_lds_dwordx4 v3, s[54:55] offset:256\n"
        "s_add_u32 s54, s94, 0x600000\n"
        "s_addc_u32 s55, s95, 0\n"
        "s_add_u32 m0, s59, 0x1f000\n"
        "s_nop 0\n"
        "global_load_lds_dwordx4 v3, s[54:55] offset:0\n"
        "s_add_u32 m0, s59, 0x1ff80\n"
        "s_nop 0\n"
        "global_load_lds_dwordx4 v3, s[54:55] offset:128\n"
        "s_add_u32 m0, s59, 0x20f00\n"
        "s_nop 0\n"
        "global_load_lds_dwordx4 v3, s[54:55] offset:256\n"
        "v_add_u32 v20, 0x22000, v5\n"
        "ds_read_b64_tr_b16 a[64:65], v20\n"
        "ds_read_b64_tr_b16 a[66:67], v20 offset:512\n"
        "v_add_u32 v21, 0x22000, v6\n"
        "ds_read_b64_tr_b16 a[192:193], v21\n"
        "ds_read_b64_tr_b16 a[194:195], v21 offset:512\n"
        "v_add_u32 v22, 0x22000, v7\n"
        "ds_read_b64_tr_b16 v[192:193], v22\n"
        "ds_read_b64_tr_b16 v[194:195], v22 offset:512\n"
        "v_add_u32 v20, 0x25000, v5\n"
        "ds_read_b64_tr_b16 a[72:73], v20\n"
        "ds_read_b64_tr_b16 a[74:75], v20 offset:512\n"
        "v_add_u32 v21, 0x25000, v6\n"
        "ds_read_b64_tr_b16 a[200:201], v21\n"
        "ds_read_b64_tr_b16 a[202:203], v21 offset:512\n"
        "v_add_u32 v22, 0x25000, v7\n"
        "ds_read_b64_tr_b16 v[200:201], v22\n"
        "ds_read_b64_tr_b16 v[202:203], v22 offset:512\n"
        "ds_read_b128 v[24:27], v1 offset:12288\n"
        "ds_read_b128 v[28:31], v1 offset:13312\n"
        "s_waitcnt lgkmcnt(0)\n"
        "v_mfma_f32_16x16x32_bf16 v[48:51], a[48:51], v[24:27], v[48:51]\n"
        "v_mfma_f32_16x16x32_bf16 v[52:55], a[48:51], v[28:31], v[52:55]\n"
        "v_mfma_f32_16x16x32_bf16 v[72:75], a[56:59], v[24:27], v[72:75]\n"
        "v_mfma_f32_16x16x32_bf16 v[76:79], a[56:59], v[28:31], v[76:79]\n"
        "v_mfma_f32_16x16x32_bf16 v[56:59], a[176:179], v[24:27], v[56:59]\n"
        "v_mfma_f32_16x16x32_bf16 v[60:63], a[176:179], v[28:31], v[60:63]\n"
        "v_mfma_f32_16x16x32_bf16 v[80:83], a[184:187], v[24:27], v[80:83]\n"
        "v_mfma_f32_16x16x32_bf16 v[84:87], a[184:187], v[28:31], v[84:87]\n"
        "v_mfma_f32_16x16x32_bf16 v[64:67], v[176:179], v[24:27], v[64:67]\n"
        "v_mfma_f32_16x16x32_bf16 v[68:71], v[176:179], v[28:31], v[68:71]\n"
        "v_mfma_f32_16x16x32_bf16 v[88:91], v[184:187], v[24:27], v[88:91]\n"
        "v_mfma_f32_16x16x32_bf16 v[92:95], v[184:187], v[28:31], v[92:95]\n"
        "ds_read_b128 v[24:27], v1 offset:14336\n"
        "ds_read_b128 v[28:31], v1 offset:15360\n"
        "s_waitcnt lgkmcnt(0)\n"
        "v_mfma_f32_16x16x32_bf16 v[48:51], a[52:55], v[24:27], v[48:51]\n"
        "v_mfma_f32_16x16x32_bf16 v[52:55], a[52:55], v[28:31], v[52:55]\n"
        "v_mfma_f32_16x16x32_bf16 v[72:75], a[60:63], v[24:27], v[72:75]\n"
        "v_mfma_f32_16x16x32_bf16 v[76:79], a[60:63], v[28:31], v[76:79]\n"
        "v_mfma_f32_16x16x32_bf16 v[56:59], a[180:183], v[24:27], v[56:59]\n"
        "v_mfma_f32_16x16x32_bf16 v[60:63], a[180:183], v[28:31], v[60:63]\n"
        "v_mfma_f32_16x16x32_bf16 v[80:83], a[188:191], v[24:27], v[80:83]\n"
        "v_mfma_f32_16x16x32_bf16 v[84:87], a[188:191], v[28:31], v[84:87]\n"
        "v_mfma_f32_16x16x32_bf16 v[64:67], v[180:183], v[24:27], v[64:67]\n"
        "v_mfma_f32_16x16x32_bf16 v[68:71], v[180:183], v[28:31], v[68:71]\n"
        "v_mfma_f32_16x16x32_bf16 v[88:91], v[188:191], v[24:27], v[88:91]\n"
        "v_mfma_f32_16x16x32_bf16 v[92:95], v[188:191], v[28:31], v[92:95]\n"
        "s_waitcnt vmcnt(18)\n"
        "s_waitcnt lgkmcnt(0)\n"
        "s_barrier\n"
        "s_add_u32 s54, s92, 0x660000\n"
        "s_addc_u32 s55, s93, 0\n"
        "s_add_u32 m0, s59, 0x22000\n"
        "s_nop 0\n"
        "global_load_lds_dwordx4 v3, s[54:55] offset:0\n"
        "s_add_u32 m0, s59, 0x22f80\n"
        "s_nop 0\n"
        "global_load_lds_dwordx4 v3, s[54:55] offset:128\n"
        "s_add_u32 m0, s59, 0x23f00\n"
        "s_nop 0\n"
        "global_load_lds_dwordx4 v3, s[54:55] offset:256\n"
        "s_add_u32 s54, s94, 0x660000\n"
        "s_addc_u32 s55, s95, 0\n"
        "s_add_u32 m0, s59, 0x25000\n"
        "s_nop 0\n"
        "global_load_lds_dwordx4 v3, s[54:55] offset:0\n"
        "s_add_u32 m0, s59, 0x25f80\n"
        "s_nop 0\n"
        "global_load_lds_dwordx4 v3, s[54:55] offset:128\n"
        "s_add_u32 m0, s59, 0x26f00\n"
        "s_nop 0\n"
        "global_load_lds_dwordx4 v3, s[54:55] offset:256\n"
        "v_add_u32 v20, 0xa000, v5\n"
        "ds_read_b64_tr_b16 a[68:69], v20\n"
        "ds_read_b64_tr_b16 a[70:71], v20 offset:512\n"
        "v_add_u32 v21, 0xa000, v6\n"
        "ds_read_b64_tr_b16 a[196:197], v21\n"
        "ds_read_b64_tr_b16 a[198:199], v21 offset:512\n"
        "v_add_u32 v22, 0xa000, v7\n"
        "ds_read_b64_tr_b16 v[196:197], v22\n"
        "ds_read_b64_tr_b16 v[198:199], v22 offset:512\n"
        "v_add_u32 v20, 0xd000, v5\n"
        "ds_read_b64_tr_b16 a[76:77], v20\n"
        "ds_read_b64_tr_b16 a[78:79], v20 offset:512\n"
        "v_add_u32 v21, 0xd000, v6\n"
        "ds_read_b64_tr_b16 a[204:205], v21\n"
        "ds_read_b64_tr_b16 a[206:207], v21 offset:512\n"
        "v_add_u32 v22, 0xd000, v7\n"
        "ds_read_b64_tr_b16 v[204:205], v22\n"
        "ds_read_b64_tr_b16 v[206:207], v22 offset:512\n"
        "ds_read_b128 v[24:27], v1 offset:18432\n"
        "ds_read_b128 v[28:31], v1 offset:19456\n"
        "s_waitcnt lgkmcnt(0)\n"
        "v_mfma_f32_16x16x32_bf16 v[48:51], a[64:67], v[24:27], v[48:51]\n"
        "v_mfma_f32_16x16x32_bf16 v[52:55], a[64:67], v[28:31], v[52:55]\n"
        "v_mfma_f32_16x16x32_bf16 v[72:75], a[72:75], v[24:27], v[72:75]\n"
        "v_mfma_f32_16x16x32_bf16 v[76:79], a[72:75], v[28:31], v[76:79]\n"
        "v_mfma_f32_16x16x32_bf16 v[56:59], a[192:195], v[24:27], v[56:59]\n"
        "v_mfma_f32_16x16x32_bf16 v[60:63], a[192:195], v[28:31], v[60:63]\n"
        "v_mfma_f32_16x16x32_bf16 v[80:83], a[200:203], v[24:27], v[80:83]\n"
        "v_mfma_f32_16x16x32_bf16 v[84:87], a[200:203], v[28:31], v[84:87]\n"
        "v_mfma_f32_16x16x32_bf16 v[64:67], v[192:195], v[24:27], v[64:67]\n"
        "v_mfma_f32_16x16x32_bf16 v[68:71], v[192:195], v[28:31], v[68:71]\n"
        "v_mfma_f32_16x16x32_bf16 v[88:91], v[200:203], v[24:27], v[88:91]\n"
        "v_mfma_f32_16x16x32_bf16 v[92:95], v[200:203], v[28:31], v[92:95]\n"
        "s_waitcnt vmcnt(18)\n"
        "s_waitcnt lgkmcnt(0)\n"
        "s_barrier\n"
        "s_add_u32 s54, s92, 0x6c0000\n"
        "s_addc_u32 s55, s93, 0\n"
        "s_add_u32 m0, s59, 0xa000\n"
        "s_nop 0\n"
        "global_load_lds_dwordx4 v3, s[54:55] offset:0\n"
        "s_add_u32 m0, s59, 0xaf80\n"
        "s_nop 0\n"
        "global_load_lds_dwordx4 v3, s[54:55] offset:128\n"
        "s_add_u32 m0, s59, 0xbf00\n"
        "s_nop 0\n"
        "global_load_lds_dwordx4 v3, s[54:55] offset:256\n"
        "s_add_u32 s54, s94, 0x6c0000\n"
        "s_addc_u32 s55, s95, 0\n"
        "s_add_u32 m0, s59, 0xd000\n"
        "s_nop 0\n"
        "global_load_lds_dwordx4 v3, s[54:55] offset:0\n"
        "s_add_u32 m0, s59, 0xdf80\n"
        "s_nop 0\n"
        "global_load_lds_dwordx4 v3, s[54:55] offset:128\n"
        "s_add_u32 m0, s59, 0xef00\n"
        "s_nop 0\n"
        "global_load_lds_dwordx4 v3, s[54:55] offset:256\n"
        "v_add_u32 v20, 0x10000, v5\n"
        "ds_read_b64_tr_b16 a[80:81], v20\n"
        "ds_read_b64_tr_b16 a[82:83], v20 offset:512\n"
        "v_add_u32 v21, 0x10000, v6\n"
        "ds_read_b64_tr_b16 a[208:209], v21\n"
        "ds_read_b64_tr_b16 a[210:211], v21 offset:512\n"
        "v_add_u32 v22, 0x10000, v7\n"
        "ds_read_b64_tr_b16 v[208:209], v22\n"
        "ds_read_b64_tr_b16 v[210:211], v22 offset:512\n"
        "v_add_u32 v20, 0x13000, v5\n"
        "ds_read_b64_tr_b16 a[88:89], v20\n"
        "ds_read_b64_tr_b16 a[90:91], v20 offset:512\n"
        "v_add_u32 v21, 0x13000, v6\n"
        "ds_read_b64_tr_b16 a[216:217], v21\n"
        "ds_read_b64_tr_b16 a[218:219], v21 offset:512\n"
        "v_add_u32 v22, 0x13000, v7\n"
        "ds_read_b64_tr_b16 v[216:217], v22\n"
        "ds_read_b64_tr_b16 v[218:219], v22 offset:512\n"
        "ds_read_b128 v[24:27], v1 offset:20480\n"
        "ds_read_b128 v[28:31], v1 offset:21504\n"
        "s_waitcnt lgkmcnt(0)\n"
        "v_mfma_f32_16x16x32_bf16 v[48:51], a[68:71], v[24:27], v[48:51]\n"
        "v_mfma_f32_16x16x32_bf16 v[52:55], a[68:71], v[28:31], v[52:55]\n"
        "v_mfma_f32_16x16x32_bf16 v[72:75], a[76:79], v[24:27], v[72:75]\n"
        "v_mfma_f32_16x16x32_bf16 v[76:79], a[76:79], v[28:31], v[76:79]\n"
        "v_mfma_f32_16x16x32_bf16 v[56:59], a[196:199], v[24:27], v[56:59]\n"
        "v_mfma_f32_16x16x32_bf16 v[60:63], a[196:199], v[28:31], v[60:63]\n"
        "v_mfma_f32_16x16x32_bf16 v[80:83], a[204:207], v[24:27], v[80:83]\n"
        "v_mfma_f32_16x16x32_bf16 v[84:87], a[204:207], v[28:31], v[84:87]\n"
        "v_mfma_f32_16x16x32_bf16 v[64:67], v[196:199], v[24:27], v[64:67]\n"
        "v_mfma_f32_16x16x32_bf16 v[68:71], v[196:199], v[28:31], v[68:71]\n"
        "v_mfma_f32_16x16x32_bf16 v[88:91], v[204:207], v[24:27], v[88:91]\n"
        "v_mfma_f32_16x16x32_bf16 v[92:95], v[204:207], v[28:31], v[92:95]\n"
        "s_waitcnt vmcnt(18)\n"
        "s_waitcnt lgkmcnt(0)\n"
        "s_barrier\n"
        "s_add_u32 s54, s92, 0x720000\n"
        "s_addc_u32 s55, s93, 0\n"
        "s_add_u32 m0, s59, 0x10000\n"
        "s_nop 0\n"
        "global_load_lds_dwordx4 v3, s[54:55] offset:0\n"
        "s_add_u32 m0, s59, 0x10f80\n"
        "s_nop 0\n"
        "global_load_lds_dwordx4 v3, s[54:55] offset:128\n"
        "s_add_u32 m0, s59, 0x11f00\n"
        "s_nop 0\n"
        "global_load_lds_dwordx4 v3, s[54:55] offset:256\n"
        "s_add_u32 s54, s94, 0x720000\n"
        "s_addc_u32 s55, s95, 0\n"
        "s_add_u32 m0, s59, 0x13000\n"
        "s_nop 0\n"
        "global_load_lds_dwordx4 v3, s[54:55] offset:0\n"
        "s_add_u32 m0, s59, 0x13f80\n"
        "s_nop 0\n"
        "global_load_lds_dwordx4 v3, s[54:55] offset:128\n"
        "s_add_u32 m0, s59, 0x14f00\n"
        "s_nop 0\n"
        "global_load_lds_dwordx4 v3, s[54:55] offset:256\n"
        "v_add_u32 v20, 0x16000, v5\n"
        "ds_read_b64_tr_b16 a[84:85], v20\n"
        "ds_read_b64_tr_b16 a[86:87], v20 offset:512\n"
        "v_add_u32 v21, 0x16000, v6\n"
        "ds_read_b64_tr_b16 a[212:213], v21\n"
        "ds_read_b64_tr_b16 a[214:215], v21 offset:512\n"
        "v_add_u32 v22, 0x16000, v7\n"
        "ds_read_b64_tr_b16 v[212:213], v22\n"
        "ds_read_b64_tr_b16 v[214:215], v22 offset:512\n"
        "v_add_u32 v20, 0x19000, v5\n"
        "ds_read_b64_tr_b16 a[92:93], v20\n"
        "ds_read_b64_tr_b16 a[94:95], v20 offset:512\n"
        "v_add_u32 v21, 0x19000, v6\n"
        "ds_read_b64_tr_b16 a[220:221], v21\n"
        "ds_read_b64_tr_b16 a[222:223], v21 offset:512\n"
        "v_add_u32 v22, 0x19000, v7\n"
        "ds_read_b64_tr_b16 v[220:221], v22\n"
        "ds_read_b64_tr_b16 v[222:223], v22 offset:512\n"
        "ds_read_b128 v[24:27], v1 offset:24576\n"
        "ds_read_b128 v[28:31], v1 offset:25600\n"
        "s_waitcnt lgkmcnt(0)\n"
        "v_mfma_f32_16x16x32_bf16 v[48:51], a[80:83], v[24:27], v[48:51]\n"
        "v_mfma_f32_16x16x32_bf16 v[52:55], a[80:83], v[28:31], v[52:55]\n"
        "v_mfma_f32_16x16x32_bf16 v[72:75], a[88:91], v[24:27], v[72:75]\n"
        "v_mfma_f32_16x16x32_bf16 v[76:79], a[88:91], v[28:31], v[76:79]\n"
        "v_mfma_f32_16x16x32_bf16 v[56:59], a[208:211], v[24:27], v[56:59]\n"
        "v_mfma_f32_16x16x32_bf16 v[60:63], a[208:211], v[28:31], v[60:63]\n"
        "v_mfma_f32_16x16x32_bf16 v[80:83], a[216:219], v[24:27], v[80:83]\n"
        "v_mfma_f32_16x16x32_bf16 v[84:87], a[216:219], v[28:31], v[84:87]\n"
        "v_mfma_f32_16x16x32_bf16 v[64:67], v[208:211], v[24:27], v[64:67]\n"
        "v_mfma_f32_16x16x32_bf16 v[68:71], v[208:211], v[28:31], v[68:71]\n"
        "v_mfma_f32_16x16x32_bf16 v[88:91], v[216:219], v[24:27], v[88:91]\n"
        "v_mfma_f32_16x16x32_bf16 v[92:95], v[216:219], v[28:31], v[92:95]\n"
        "s_waitcnt vmcnt(18)\n"
        "s_waitcnt lgkmcnt(0)\n"
        "s_barrier\n"
        "s_add_u32 s54, s92, 0x0\n"
        "s_addc_u32 s55, s93, 0\n"
        "s_add_u32 m0, s59, 0x16000\n"
        "s_nop 0\n"
        "global_load_lds_dwordx4 v3, s[54:55] offset:0\n"
        "s_add_u32 m0, s59, 0x16f80\n"
        "s_nop 0\n"
        "global_load_lds_dwordx4 v3, s[54:55] offset:128\n"
        "s_add_u32 m0, s59, 0x17f00\n"
        "s_nop 0\n"
        "global_load_lds_dwordx4 v3, s[54:55] offset:256\n"
        "s_add_u32 s54, s94, 0x0\n"
        "s_addc_u32 s55, s95, 0\n"
        "s_add_u32 m0, s59, 0x19000\n"
        "s_nop 0\n"
        "global_load_lds_dwordx4 v3, s[54:55] offset:0\n"
        "s_add_u32 m0, s59, 0x19f80\n"
        "s_nop 0\n"
        "global_load_lds_dwordx4 v3, s[54:55] offset:128\n"
        "s_add_u32 m0, s59, 0x1af00\n"
        "s_nop 0\n"
        "global_load_lds_dwordx4 v3, s[54:55] offset:256\n"
        "v_add_u32 v20, 0x1c000, v5\n"
        "ds_read_b64_tr_b16 a[96:97], v20\n"
        "ds_read_b64_tr_b16 a[98:99], v20 offset:512\n"
        "v_add_u32 v21, 0x1c000, v6\n"
        "ds_read_b64_tr_b16 a[224:225], v21\n"
        "ds_read_b64_tr_b16 a[226:227], v21 offset:512\n"
        "v_add_u32 v22, 0x1c000, v7\n"
        "ds_read_b64_tr_b16 v[224:225], v22\n"
        "ds_read_b64_tr_b16 v[226:227], v22 offset:512\n"
        "v_add_u32 v20, 0x1f000, v5\n"
        "ds_read_b64_tr_b16 a[104:105], v20\n"
        "ds_read_b64_tr_b16 a[106:107], v20 offset:512\n"
        "v_add_u32 v21, 0x1f000, v6\n"
        "ds_read_b64_tr_b16 a[232:233], v21\n"
        "ds_read_b64_tr_b16 a[234:235], v21 offset:512\n"
        "v_add_u32 v22, 0x1f000, v7\n"
        "ds_read_b64_tr_b16 v[232:233], v22\n"
        "ds_read_b64_tr_b16 v[234:235], v22 offset:512\n"
        "ds_read_b128 v[24:27], v1 offset:26624\n"
        "ds_read_b128 v[28:31], v1 offset:27648\n"
        "s_waitcnt lgkmcnt(0)\n"
        "v_mfma_f32_16x16x32_bf16 v[48:51], a[84:87], v[24:27], v[48:51]\n"
        "v_mfma_f32_16x16x32_bf16 v[52:55], a[84:87], v[28:31], v[52:55]\n"
        "v_mfma_f32_16x16x32_bf16 v[72:75], a[92:95], v[24:27], v[72:75]\n"
        "v_mfma_f32_16x16x32_bf16 v[76:79], a[92:95], v[28:31], v[76:79]\n"
        "v_mfma_f32_16x16x32_bf16 v[56:59], a[212:215], v[24:27], v[56:59]\n"
        "v_mfma_f32_16x16x32_bf16 v[60:63], a[212:215], v[28:31], v[60:63]\n"
        "v_mfma_f32_16x16x32_bf16 v[80:83], a[220:223], v[24:27], v[80:83]\n"
        "v_mfma_f32_16x16x32_bf16 v[84:87], a[220:223], v[28:31], v[84:87]\n"
        "v_mfma_f32_16x16x32_bf16 v[64:67], v[212:215], v[24:27], v[64:67]\n"
        "v_mfma_f32_16x16x32_bf16 v[68:71], v[212:215], v[28:31], v[68:71]\n"
        "v_mfma_f32_16x16x32_bf16 v[88:91], v[220:223], v[24:27], v[88:91]\n"
        "v_mfma_f32_16x16x32_bf16 v[92:95], v[220:223], v[28:31], v[92:95]\n"
        "s_waitcnt vmcnt(18)\n"
        "s_waitcnt lgkmcnt(0)\n"
        "s_barrier\n"
        "s_add_u32 s54, s92, 0x60000\n"
        "s_addc_u32 s55, s93, 0\n"
        "s_add_u32 m0, s59, 0x1c000\n"
        "s_nop 0\n"
        "global_load_lds_dwordx4 v3, s[54:55] offset:0\n"
        "s_add_u32 m0, s59, 0x1cf80\n"
        "s_nop 0\n"
        "global_load_lds_dwordx4 v3, s[54:55] offset:128\n"
        "s_add_u32 m0, s59, 0x1df00\n"
        "s_nop 0\n"
        "global_load_lds_dwordx4 v3, s[54:55] offset:256\n"
        "s_add_u32 s54, s94, 0x60000\n"
        "s_addc_u32 s55, s95, 0\n"
        "s_add_u32 m0, s59, 0x1f000\n"
        "s_nop 0\n"
        "global_load_lds_dwordx4 v3, s[54:55] offset:0\n"
        "s_add_u32 m0, s59, 0x1ff80\n"
        "s_nop 0\n"
        "global_load_lds_dwordx4 v3, s[54:55] offset:128\n"
        "s_add_u32 m0, s59, 0x20f00\n"
        "s_nop 0\n"
        "global_load_lds_dwordx4 v3, s[54:55] offset:256\n"
        "v_add_u32 v20, 0x22000, v5\n"
        "ds_read_b64_tr_b16 a[100:101], v20\n"
        "ds_read_b64_tr_b16 a[102:103], v20 offset:512\n"
        "v_add_u32 v21, 0x22000, v6\n"
        "ds_read_b64_tr_b16 a[228:229], v21\n"
        "ds_read_b64_tr_b16 a[230:231], v21 offset:512\n"
        "v_add_u32 v22, 0x22000, v7\n"
        "ds_read_b64_tr_b16 v[228:229], v22\n"
        "ds_read_b64_tr_b16 v[230:231], v22 offset:512\n"
        "v_add_u32 v20, 0x25000, v5\n"
        "ds_read_b64_tr_b16 a[108:109], v20\n"
        "ds_read_b64_tr_b16 a[110:111], v20 offset:512\n"
        "v_add_u32 v21, 0x25000, v6\n"
        "ds_read_b64_tr_b16 a[236:237], v21\n"
        "ds_read_b64_tr_b16 a[238:239], v21 offset:512\n"
        "v_add_u32 v22, 0x25000, v7\n"
        "ds_read_b64_tr_b16 v[236:237], v22\n"
        "ds_read_b64_tr_b16 v[238:239], v22 offset:512\n"
        "ds_read_b128 v[24:27], v1 offset:30720\n"
        "ds_read_b128 v[28:31], v1 offset:31744\n"
        "s_waitcnt lgkmcnt(0)\n"
        "v_mfma_f32_16x16x32_bf16 v[48:51], a[96:99], v[24:27], v[48:51]\n"
        "v_mfma_f32_16x16x32_bf16 v[52:55], a[96:99], v[28:31], v[52:55]\n"
        "v_mfma_f32_16x16x32_bf16 v[72:75], a[104:107], v[24:27], v[72:75]\n"
        "v_mfma_f32_16x16x32_bf16 v[76:79], a[104:107], v[28:31], v[76:79]\n"
        "v_mfma_f32_16x16x32_bf16 v[56:59], a[224:227], v[24:27], v[56:59]\n"
        "v_mfma_f32_16x16x32_bf16 v[60:63], a[224:227], v[28:31], v[60:63]\n"
        "v_mfma_f32_16x16x32_bf16 v[80:83], a[232:235], v[24:27], v[80:83]\n"
        "v_mfma_f32_16x16x32_bf16 v[84:87], a[232:235], v[28:31], v[84:87]\n"
        "v_mfma_f32_16x16x32_bf16 v[64:67], v[224:227], v[24:27], v[64:67]\n"
        "v_mfma_f32_16x16x32_bf16 v[68:71], v[224:227], v[28:31], v[68:71]\n"
        "v_mfma_f32_16x16x32_bf16 v[88:91], v[232:235], v[24:27], v[88:91]\n"
        "v_mfma_f32_16x16x32_bf16 v[92:95], v[232:235], v[28:31], v[92:95]\n"
        "s_waitcnt vmcnt(18)\n"
        "s_waitcnt lgkmcnt(0)\n"
        "s_barrier\n"
        "s_add_u32 s54, s92, 0x3c0000\n"
        "s_addc_u32 s55, s93, 0\n"
        "s_add_u32 m0, s59, 0x22000\n"
        "s_nop 0\n"
        "global_load_lds_dwordx4 v3, s[54:55] offset:0\n"
        "s_add_u32 m0, s59, 0x22f80\n"
        "s_nop 0\n"
        "global_load_lds_dwordx4 v3, s[54:55] offset:128\n"
        "s_add_u32 m0, s59, 0x23f00\n"
        "s_nop 0\n"
        "global_load_lds_dwordx4 v3, s[54:55] offset:256\n"
        "s_add_u32 s54, s94, 0x3c0000\n"
        "s_addc_u32 s55, s95, 0\n"
        "s_add_u32 m0, s59, 0x25000\n"
        "s_nop 0\n"
        "global_load_lds_dwordx4 v3, s[54:55] offset:0\n"
        "s_add_u32 m0, s59, 0x25f80\n"
        "s_nop 0\n"
        "global_load_lds_dwordx4 v3, s[54:55] offset:128\n"
        "s_add_u32 m0, s59, 0x26f00\n"
        "s_nop 0\n"
        "global_load_lds_dwordx4 v3, s[54:55] offset:256\n"
        "global_load_dword v4, v15, s[90:91] sc1\n"
        "v_add_u32 v20, 0xa000, v5\n"
        "ds_read_b64_tr_b16 a[112:113], v20\n"
        "ds_read_b64_tr_b16 a[114:115], v20 offset:512\n"
        "v_add_u32 v21, 0xa000, v6\n"
        "ds_read_b64_tr_b16 a[240:241], v21\n"
        "ds_read_b64_tr_b16 a[242:243], v21 offset:512\n"
        "v_add_u32 v22, 0xa000, v7\n"
        "ds_read_b64_tr_b16 v[240:241], v22\n"
        "ds_read_b64_tr_b16 v[242:243], v22 offset:512\n"
        "v_add_u32 v20, 0xd000, v5\n"
        "ds_read_b64_tr_b16 a[120:121], v20\n"
        "ds_read_b64_tr_b16 a[122:123], v20 offset:512\n"
        "v_add_u32 v21, 0xd000, v6\n"
        "ds_read_b64_tr_b16 a[248:249], v21\n"
        "ds_read_b64_tr_b16 a[250:251], v21 offset:512\n"
        "v_add_u32 v22, 0xd000, v7\n"
        "ds_read_b64_tr_b16 v[248:249], v22\n"
        "ds_read_b64_tr_b16 v[250:251], v22 offset:512\n"
        "ds_read_b128 v[24:27], v1 offset:32768\n"
        "ds_read_b128 v[28:31], v1 offset:33792\n"
        "s_waitcnt lgkmcnt(0)\n"
        "v_mfma_f32_16x16x32_bf16 v[48:51], a[100:103], v[24:27], v[48:51]\n"
        "v_mfma_f32_16x16x32_bf16 v[52:55], a[100:103], v[28:31], v[52:55]\n"
        "v_mfma_f32_16x16x32_bf16 v[72:75], a[108:111], v[24:27], v[72:75]\n"
        "v_mfma_f32_16x16x32_bf16 v[76:79], a[108:111], v[28:31], v[76:79]\n"
        "v_mfma_f32_16x16x32_bf16 v[56:59], a[228:231], v[24:27], v[56:59]\n"
        "v_mfma_f32_16x16x32_bf16 v[60:63], a[228:231], v[28:31], v[60:63]\n"
        "v_mfma_f32_16x16x32_bf16 v[80:83], a[236:239], v[24:27], v[80:83]\n"
        "v_mfma_f32_16x16x32_bf16 v[84:87], a[236:239], v[28:31], v[84:87]\n"
        "v_mfma_f32_16x16x32_bf16 v[64:67], v[228:231], v[24:27], v[64:67]\n"
        "v_mfma_f32_16x16x32_bf16 v[68:71], v[228:231], v[28:31], v[68:71]\n"
        "v_mfma_f32_16x16x32_bf16 v[88:91], v[236:239], v[24:27], v[88:91]\n"
        "v_mfma_f32_16x16x32_bf16 v[92:95], v[236:239], v[28:31], v[92:95]\n"
        "s_waitcnt vmcnt(19)\n"
        "s_waitcnt lgkmcnt(0)\n"
        "s_barrier\n"
        "s_add_u32 s54, s92, 0x420000\n"
        "s_addc_u32 s55, s93, 0\n"
        "s_add_u32 m0, s59, 0xa000\n"
        "s_nop 0\n"
        "global_load_lds_dwordx4 v3, s[54:55] offset:0\n"
        "s_add_u32 m0, s59, 0xaf80\n"
        "s_nop 0\n"
        "global_load_lds_dwordx4 v3, s[54:55] offset:128\n"
        "s_add_u32 m0, s59, 0xbf00\n"
        "s_nop 0\n"
        "global_load_lds_dwordx4 v3, s[54:55] offset:256\n"
        "s_add_u32 s54, s94, 0x420000\n"
        "s_addc_u32 s55, s95, 0\n"
        "s_add_u32 m0, s59, 0xd000\n"
        "s_nop 0\n"
        "global_load_lds_dwordx4 v3, s[54:55] offset:0\n"
        "s_add_u32 m0, s59, 0xdf80\n"
        "s_nop 0\n"
        "global_load_lds_dwordx4 v3, s[54:55] offset:128\n"
        "s_add_u32 m0, s59, 0xef00\n"
        "s_nop 0\n"
        "global_load_lds_dwordx4 v3, s[54:55] offset:256\n"
        "v_add_u32 v20, 0x10000, v5\n"
        "ds_read_b64_tr_b16 a[116:117], v20\n"
        "ds_read_b64_tr_b16 a[118:119], v20 offset:512\n"
        "v_add_u32 v21, 0x10000, v6\n"
        "ds_read_b64_tr_b16 a[244:245], v21\n"
        "ds_read_b64_tr_b16 a[246:247], v21 offset:512\n"
        "v_add_u32 v22, 0x10000, v7\n"
        "ds_read_b64_tr_b16 v[244:245], v22\n"
        "ds_read_b64_tr_b16 v[246:247], v22 offset:512\n"
        "v_add_u32 v20, 0x13000, v5\n"
        "ds_read_b64_tr_b16 a[124:125], v20\n"
        "ds_read_b64_tr_b16 a[126:127], v20 offset:512\n"
        "v_add_u32 v21, 0x13000, v6\n"
        "ds_read_b64_tr_b16 a[252:253], v21\n"
        "ds_read_b64_tr_b16 a[254:255], v21 offset:512\n"
        "v_add_u32 v22, 0x13000, v7\n"
        "ds_read_b64_tr_b16 v[252:253], v22\n"
        "ds_read_b64_tr_b16 v[254:255], v22 offset:512\n"
        "ds_read_b128 v[24:27], v1 offset:36864\n"
        "ds_read_b128 v[28:31], v1 offset:37888\n"
        "s_waitcnt lgkmcnt(0)\n"
        "v_mfma_f32_16x16x32_bf16 v[48:51], a[112:115], v[24:27], v[48:51]\n"
        "v_mfma_f32_16x16x32_bf16 v[52:55], a[112:115], v[28:31], v[52:55]\n"
        "v_mfma_f32_16x16x32_bf16 v[72:75], a[120:123], v[24:27], v[72:75]\n"
        "v_mfma_f32_16x16x32_bf16 v[76:79], a[120:123], v[28:31], v[76:79]\n"
        "v_mfma_f32_16x16x32_bf16 v[56:59], a[240:243], v[24:27], v[56:59]\n"
        "v_mfma_f32_16x16x32_bf16 v[60:63], a[240:243], v[28:31], v[60:63]\n"
        "v_mfma_f32_16x16x32_bf16 v[80:83], a[248:251], v[24:27], v[80:83]\n"
        "v_mfma_f32_16x16x32_bf16 v[84:87], a[248:251], v[28:31], v[84:87]\n"
        "v_mfma_f32_16x16x32_bf16 v[64:67], v[240:243], v[24:27], v[64:67]\n"
        "v_mfma_f32_16x16x32_bf16 v[68:71], v[240:243], v[28:31], v[68:71]\n"
        "v_mfma_f32_16x16x32_bf16 v[88:91], v[248:251], v[24:27], v[88:91]\n"
        "v_mfma_f32_16x16x32_bf16 v[92:95], v[248:251], v[28:31], v[92:95]\n"
        "s_waitcnt vmcnt(19)\n"
        "s_waitcnt lgkmcnt(0)\n"
        "s_barrier\n"
        "v_add_u32 v20, 0x16000, v5\n"
        "ds_read_b64_tr_b16 v[96:97], v20\n"
        "ds_read_b64_tr_b16 v[98:99], v20 offset:512\n"
        "v_add_u32 v21, 0x16000, v6\n"
        "ds_read_b64_tr_b16 v[104:105], v21\n"
        "ds_read_b64_tr_b16 v[106:107], v21 offset:512\n"
        "v_add_u32 v22, 0x16000, v7\n"
        "ds_read_b64_tr_b16 v[112:113], v22\n"
        "ds_read_b64_tr_b16 v[114:115], v22 offset:512\n"
        "v_add_u32 v20, 0x19000, v5\n"
        "ds_read_b64_tr_b16 v[100:101], v20\n"
        "ds_read_b64_tr_b16 v[102:103], v20 offset:512\n"
        "v_add_u32 v21, 0x19000, v6\n"
        "ds_read_b64_tr_b16 v[108:109], v21\n"
        "ds_read_b64_tr_b16 v[110:111], v21 offset:512\n"
        "v_add_u32 v22, 0x19000, v7\n"
        "ds_read_b64_tr_b16 v[116:117], v22\n"
        "ds_read_b64_tr_b16 v[118:119], v22 offset:512\n"
        "ds_read_b128 v[24:27], v1 offset:38912\n"
        "ds_read_b128 v[28:31], v1 offset:39936\n"
        "s_waitcnt lgkmcnt(0)\n"
        "v_mfma_f32_16x16x32_bf16 v[48:51], a[116:119], v[24:27], v[48:51]\n"
        "v_mfma_f32_16x16x32_bf16 v[52:55], a[116:119], v[28:31], v[52:55]\n"
        "v_mfma_f32_16x16x32_bf16 v[72:75], a[124:127], v[24:27], v[72:75]\n"
        "v_mfma_f32_16x16x32_bf16 v[76:79], a[124:127], v[28:31], v[76:79]\n"
        "v_mfma_f32_16x16x32_bf16 v[56:59], a[244:247], v[24:27], v[56:59]\n"
        "v_mfma_f32_16x16x32_bf16 v[60:63], a[244:247], v[28:31], v[60:63]\n"
        "v_mfma_f32_16x16x32_bf16 v[80:83], a[252:255], v[24:27], v[80:83]\n"
        "v_mfma_f32_16x16x32_bf16 v[84:87], a[252:255], v[28:31], v[84:87]\n"
        "v_mfma_f32_16x16x32_bf16 v[64:67], v[244:247], v[24:27], v[64:67]\n"
        "v_mfma_f32_16x16x32_bf16 v[68:71], v[244:247], v[28:31], v[68:71]\n"
        "v_mfma_f32_16x16x32_bf16 v[88:91], v[252:255], v[24:27], v[88:91]\n"
        "v_mfma_f32_16x16x32_bf16 v[92:95], v[252:255], v[28:31], v[92:95]\n"
        "s_waitcnt vmcnt(13)\n"
        "s_waitcnt lgkmcnt(0)\n"
        "s_barrier\n"
        "v_add_u32 v20, 0x1c000, v5\n"
        "ds_read_b64_tr_b16 v[32:33], v20\n"
        "ds_read_b64_tr_b16 v[34:35], v20 offset:512\n"
        "v_add_u32 v21, 0x1c000, v6\n"
        "ds_read_b64_tr_b16 v[40:41], v21\n"
        "ds_read_b64_tr_b16 v[42:43], v21 offset:512\n"
        "v_add_u32 v22, 0x1c000, v7\n"
        "ds_read_b64_tr_b16 v[120:121], v22\n"
        "ds_read_b64_tr_b16 v[122:123], v22 offset:512\n"
        "v_add_u32 v20, 0x1f000, v5\n"
        "ds_read_b64_tr_b16 v[36:37], v20\n"
        "ds_read_b64_tr_b16 v[38:39], v20 offset:512\n"
        "v_add_u32 v21, 0x1f000, v6\n"
        "ds_read_b64_tr_b16 v[44:45], v21\n"
        "ds_read_b64_tr_b16 v[46:47], v21 offset:512\n"
        "v_add_u32 v22, 0x1f000, v7\n"
        "ds_read_b64_tr_b16 v[124:125], v22\n"
        "ds_read_b64_tr_b16 v[126:127], v22 offset:512\n"
        "ds_read_b128 v[24:27], v1 offset:16384\n"
        "ds_read_b128 v[28:31], v1 offset:17408\n"
        "s_waitcnt lgkmcnt(0)\n"
        "v_mfma_f32_16x16x32_bf16 v[48:51], v[96:99], v[24:27], v[48:51]\n"
        "v_mfma_f32_16x16x32_bf16 v[52:55], v[96:99], v[28:31], v[52:55]\n"
        "ds_write_b128 v2, v[96:99] offset:0\n"
        "v_mfma_f32_16x16x32_bf16 v[72:75], v[100:103], v[24:27], v[72:75]\n"
        "v_mfma_f32_16x16x32_bf16 v[76:79], v[100:103], v[28:31], v[76:79]\n"
        "ds_write_b128 v2, v[100:103] offset:1024\n"
        "v_mfma_f32_16x16x32_bf16 v[56:59], v[104:107], v[24:27], v[56:59]\n"
        "v_mfma_f32_16x16x32_bf16 v[60:63], v[104:107], v[28:31], v[60:63]\n"
        "ds_write_b128 v2, v[104:107] offset:2048\n"
        "v_mfma_f32_16x16x32_bf16 v[80:83], v[108:111], v[24:27], v[80:83]\n"
        "v_mfma_f32_16x16x32_bf16 v[84:87], v[108:111], v[28:31], v[84:87]\n"
        "ds_write_b128 v2, v[108:111] offset:3072\n"
        "v_mfma_f32_16x16x32_bf16 v[64:67], v[112:115], v[24:27], v[64:67]\n"
        "v_mfma_f32_16x16x32_bf16 v[68:71], v[112:115], v[28:31], v[68:71]\n"
        "ds_write_b128 v2, v[112:115] offset:4096\n"
        "v_mfma_f32_16x16x32_bf16 v[88:91], v[116:119], v[24:27], v[88:91]\n"
        "v_mfma_f32_16x16x32_bf16 v[92:95], v[116:119], v[28:31], v[92:95]\n"
        "ds_write_b128 v2, v[116:119] offset:5120\n"
        "s_waitcnt vmcnt(7)\n"
        "s_waitcnt lgkmcnt(0)\n"
        "s_barrier\n"
        "v_add_u32 v20, 0x22000, v5\n"
        "ds_read_b64_tr_b16 v[96:97], v20\n"
        "ds_read_b64_tr_b16 v[98:99], v20 offset:512\n"
        "v_add_u32 v21, 0x22000, v6\n"
        "ds_read_b64_tr_b16 v[104:105], v21\n"
        "ds_read_b64_tr_b16 v[106:107], v21 offset:512\n"
        "v_add_u32 v22, 0x22000, v7\n"
        "ds_read_b64_tr_b16 v[112:113], v22\n"
        "ds_read_b64_tr_b16 v[114:115], v22 offset:512\n"
        "v_add_u32 v20, 0x25000, v5\n"
        "ds_read_b64_tr_b16 v[100:101], v20\n"
        "ds_read_b64_tr_b16 v[102:103], v20 offset:512\n"
        "v_add_u32 v21, 0x25000, v6\n"
        "ds_read_b64_tr_b16 v[108:109], v21\n"
        "ds_read_b64_tr_b16 v[110:111], v21 offset:512\n"
        "v_add_u32 v22, 0x25000, v7\n"
        "ds_read_b64_tr_b16 v[116:117], v22\n"
        "ds_read_b64_tr_b16 v[118:119], v22 offset:512\n"
        "ds_read_b128 v[24:27], v1 offset:22528\n"
        "ds_read_b128 v[28:31], v1 offset:23552\n"
        "s_waitcnt lgkmcnt(0)\n"
        "v_mfma_f32_16x16x32_bf16 v[48:51], v[32:35], v[24:27], v[48:51]\n"
        "v_mfma_f32_16x16x32_bf16 v[52:55], v[32:35], v[28:31], v[52:55]\n"
        "ds_write_b128 v2, v[32:35] offset:24576\n"
        "v_mfma_f32_16x16x32_bf16 v[72:75], v[36:39], v[24:27], v[72:75]\n"
        "v_mfma_f32_16x16x32_bf16 v[76:79], v[36:39], v[28:31], v[76:79]\n"
        "ds_write_b128 v2, v[36:39] offset:25600\n"
        "v_mfma_f32_16x16x32_bf16 v[56:59], v[40:43], v[24:27], v[56:59]\n"
        "v_mfma_f32_16x16x32_bf16 v[60:63], v[40:43], v[28:31], v[60:63]\n"
        "ds_write_b128 v2, v[40:43] offset:26624\n"
        "v_mfma_f32_16x16x32_bf16 v[80:83], v[44:47], v[24:27], v[80:83]\n"
        "v_mfma_f32_16x16x32_bf16 v[84:87], v[44:47], v[28:31], v[84:87]\n"
        "ds_write_b128 v2, v[44:47] offset:27648\n"
        "v_mfma_f32_16x16x32_bf16 v[64:67], v[120:123], v[24:27], v[64:67]\n"
        "v_mfma_f32_16x16x32_bf16 v[68:71], v[120:123], v[28:31], v[68:71]\n"
        "ds_write_b128 v2, v[120:123] offset:28672\n"
        "v_mfma_f32_16x16x32_bf16 v[88:91], v[124:127], v[24:27], v[88:91]\n"
        "v_mfma_f32_16x16x32_bf16 v[92:95], v[124:127], v[28:31], v[92:95]\n"
        "ds_write_b128 v2, v[124:127] offset:29696\n"
        "s_waitcnt vmcnt(0)\n"
        "s_waitcnt lgkmcnt(0)\n"
        "s_barrier\n"
        "v_add_u32 v20, 0xa000, v5\n"
        "ds_read_b64_tr_b16 v[32:33], v20\n"
        "ds_read_b64_tr_b16 v[34:35], v20 offset:512\n"
        "v_add_u32 v21, 0xa000, v6\n"
        "ds_read_b64_tr_b16 v[40:41], v21\n"
        "ds_read_b64_tr_b16 v[42:43], v21 offset:512\n"
        "v_add_u32 v22, 0xa000, v7\n"
        "ds_read_b64_tr_b16 v[120:121], v22\n"
        "ds_read_b64_tr_b16 v[122:123], v22 offset:512\n"
        "v_add_u32 v20, 0xd000, v5\n"
        "ds_read_b64_tr_b16 v[36:37], v20\n"
        "ds_read_b64_tr_b16 v[38:39], v20 offset:512\n"
        "v_add_u32 v21, 0xd000, v6\n"
        "ds_read_b64_tr_b16 v[44:45], v21\n"
        "ds_read_b64_tr_b16 v[46:47], v21 offset:512\n"
        "v_add_u32 v22, 0xd000, v7\n"
        "ds_read_b64_tr_b16 v[124:125], v22\n"
        "ds_read_b64_tr_b16 v[126:127], v22 offset:512\n"
        "ds_read_b128 v[24:27], v1 offset:28672\n"
        "ds_read_b128 v[28:31], v1 offset:29696\n"
        "s_waitcnt lgkmcnt(0)\n"
        "v_mfma_f32_16x16x32_bf16 v[48:51], v[96:99], v[24:27], v[48:51]\n"
        "v_mfma_f32_16x16x32_bf16 v[52:55], v[96:99], v[28:31], v[52:55]\n"
        "ds_write_b128 v23, v[96:99] offset:0\n"
        "v_mfma_f32_16x16x32_bf16 v[72:75], v[100:103], v[24:27], v[72:75]\n"
        "v_mfma_f32_16x16x32_bf16 v[76:79], v[100:103], v[28:31], v[76:79]\n"
        "ds_write_b128 v23, v[100:103] offset:1024\n"
        "v_mfma_f32_16x16x32_bf16 v[56:59], v[104:107], v[24:27], v[56:59]\n"
        "v_mfma_f32_16x16x32_bf16 v[60:63], v[104:107], v[28:31], v[60:63]\n"
        "ds_write_b128 v23, v[104:107] offset:2048\n"
        "v_mfma_f32_16x16x32_bf16 v[80:83], v[108:111], v[24:27], v[80:83]\n"
        "v_mfma_f32_16x16x32_bf16 v[84:87], v[108:111], v[28:31], v[84:87]\n"
        "ds_write_b128 v23, v[108:111] offset:3072\n"
        "v_mfma_f32_16x16x32_bf16 v[64:67], v[112:115], v[24:27], v[64:67]\n"
        "v_mfma_f32_16x16x32_bf16 v[68:71], v[112:115], v[28:31], v[68:71]\n"
        "ds_write_b128 v23, v[112:115] offset:4096\n"
        "v_mfma_f32_16x16x32_bf16 v[88:91], v[116:119], v[24:27], v[88:91]\n"
        "v_mfma_f32_16x16x32_bf16 v[92:95], v[116:119], v[28:31], v[92:95]\n"
        "ds_write_b128 v23, v[116:119] offset:5120\n"
        "s_waitcnt lgkmcnt(0)\n"
        "ds_read_b128 v[24:27], v1 offset:34816\n"
        "ds_read_b128 v[28:31], v1 offset:35840\n"
        "s_waitcnt lgkmcnt(0)\n"
        "v_mfma_f32_16x16x32_bf16 v[48:51], v[32:35], v[24:27], v[48:51]\n"
        "v_mfma_f32_16x16x32_bf16 v[52:55], v[32:35], v[28:31], v[52:55]\n"
        "ds_write_b128 v23, v[32:35] offset:24576\n"
        "v_mfma_f32_16x16x32_bf16 v[72:75], v[36:39], v[24:27], v[72:75]\n"
        "v_mfma_f32_16x16x32_bf16 v[76:79], v[36:39], v[28:31], v[76:79]\n"
        "ds_write_b128 v23, v[36:39] offset:25600\n"
        "v_mfma_f32_16x16x32_bf16 v[56:59], v[40:43], v[24:27], v[56:59]\n"
        "v_mfma_f32_16x16x32_bf16 v[60:63], v[40:43], v[28:31], v[60:63]\n"
        "ds_write_b128 v23, v[40:43] offset:26624\n"
        "v_mfma_f32_16x16x32_bf16 v[80:83], v[44:47], v[24:27], v[80:83]\n"
        "v_mfma_f32_16x16x32_bf16 v[84:87], v[44:47], v[28:31], v[84:87]\n"
        "ds_write_b128 v23, v[44:47] offset:27648\n"
        "v_mfma_f32_16x16x32_bf16 v[64:67], v[120:123], v[24:27], v[64:67]\n"
        "v_mfma_f32_16x16x32_bf16 v[68:71], v[120:123], v[28:31], v[68:71]\n"
        "ds_write_b128 v23, v[120:123] offset:28672\n"
        "v_mfma_f32_16x16x32_bf16 v[88:91], v[124:127], v[24:27], v[88:91]\n"
        "v_mfma_f32_16x16x32_bf16 v[92:95], v[124:127], v[28:31], v[92:95]\n"
        "ds_write_b128 v23, v[124:127] offset:29696\n"
        "s_waitcnt vmcnt(0) lgkmcnt(0)\n"
        "v_readfirstlane_b32 s47, v4\n"
        "s_nop 3\n"
        "s_and_b32 s68, s47, -2\n"
        "s_cmp_lg_u32 s68, s88\n"
        "s_cbranch_scc1 Lqk_rs_1\n"
        "Lqk_rb_2:\n"
        "s_and_b32 s47, s47, 1\n"
        "s_xor_b32 s47, s47, s41\n"
        "s_mul_i32 s89, s47, 0x3f\n"
        "s_xor_b32 s89, s89, 52\n"
        "s_lshr_b32 s44, s89, 0\n"
        "s_and_b32 s44, s44, 1\n"
        "s_lshl_b32 s44, s44, 13\n"
        "s_lshl_b32 s47, s43, 10\n"
        "v_add_u32 v5, s47, v1\n"
        "s_lshl_b32 s68, s42, 15\n"
        "s_add_u32 s69, s68, 2048\n"
        "v_add_u32 v3, s69, v5\n"
        "s_lshl_b32 s47, s43, 9\n"
        "v_lshlrev_b32 v24, 2, v0\n"
        "v_add_u32 v24, s47, v24\n"
        "v_add_u32 v6, 0x2000, v24\n"
        "s_add_u32 s69, s68, 16384\n"
        "v_add_u32 v4, s69, v24\n"
        "v_and_b32 v24, 31, v0\n"
        "v_lshlrev_b32 v15, 7, v24\n"
        "v_and_b32 v24, 1, v0\n"
        "v_mov_b32 v7, 0x4444\n"
        "v_mov_b32 v25, 0xeeee\n"
        "v_cmp_ne_u32 vcc, 0, v24\n"
        "s_nop 1\n"
        "v_cndmask_b32 v7, v7, v25, vcc\n"
        "s_add_u32 s47, s44, 0x1a5000\n"
        "s_add_u32 s86, s90, s47\n"
        "s_addc_u32 s87, s91, 0\n"
        "s_add_u32 s47, s44, 0x19d000\n"
        "s_add_u32 s94, s90, s47\n"
        "s_addc_u32 s95, s91, 0\n"
        "s_lshr_b32 s69, s89, 5\n"
        "s_and_b32 s69, s69, 1\n"
        "s_lshl_b32 s69, s69, 13\n"
        "v_add_u32 v14, s69, v14\n"
        "s_add_u32 s47, s69, 0x2a40000\n"
        "s_add_u32 s52, s90, s47\n"
        "s_addc_u32 s53, s91, 0\n"
        "s_add_u32 s98, s90, 0x0\n"
        "s_addc_u32 s99, s91, 0\n"
        "s_lshl_b32 s47, s40, 7\n"
        "s_add_u32 s68, s47, 0x18d000\n"
        "s_add_u32 s68, s68, s44\n"
        "v_mov_b32 v23, s68\n"
        "s_xor_b32 s68, s44, 0x2000\n"
        "s_add_u32 s68, s68, s47\n"
        "s_add_u32 s68, s68, 0x19d000\n"
        "v_writelane_b32 v23, s68, 1\n"
        "s_cmp_eq_u32 s43, 0\n"
        "s_cselect_b32 s36, 3, 0\n"
        "s_mov_b32 s37, 0\n"
        "s_mov_b32 s35, 110\n"
        "s_mov_b32 s60, 0\n"
        "v_mov_b32 v20, 0xceabfb8f\n"
        "v_mov_b32 v21, -1\n"
        "v_add_u32 v0, 0xc000, v2\n"
        "s_branch Lqk_first\n"
        ".p2align 6\n"
        "Lqk_loop:\n"
        "s_lshr_b32 s58, s45, 1\n"
        "v_mfma_f32_16x16x32_bf16 v[48:51], a[0:3], v[24:27], 0\n"
        "ds_read_b128 v[32:35], v1 offset:2048\n"
        "ds_read_b128 v[96:99], v17 offset:0\n"
        "ds_read_b128 v[100:103], v18 offset:0\n"
        "v_mfma_f32_16x16x32_bf16 v[52:55], a[0:3], v[28:31], 0\n"
        "ds_read_b128 v[36:39], v1 offset:3072\n"
        "ds_read_b128 v[104:107], v17 offset:5632\n"
        "ds_read_b128 v[108:111], v18 offset:5632\n"
        "v_mfma_f32_16x16x32_bf16 v[72:75], a[8:11], v[24:27], 0\n"
        "ds_read_b128 v[112:115], v17 offset:11264\n"
        "ds_read_b128 v[116:119], v18 offset:11264\n"
        "v_mfma_f32_16x16x32_bf16 v[76:79], a[8:11], v[28:31], 0\n"
        "ds_read_b128 v[40:43], v17 offset:16896\n"
        "ds_read_b128 v[44:47], v18 offset:16896\n"
        "v_mfma_f32_16x16x32_bf16 v[56:59], a[128:131], v[24:27], 0\n"
        "v_mfma_f32_16x16x32_bf16 v[60:63], a[128:131], v[28:31], 0\n"
        "v_mfma_f32_16x16x32_bf16 v[80:83], a[136:139], v[24:27], 0\n"
        "v_mfma_f32_16x16x32_bf16 v[84:87], a[136:139], v[28:31], 0\n"
        "v_mfma_f32_16x16x32_bf16 v[64:67], v[128:131], v[24:27], 0\n"
        "s_waitcnt lgkmcnt(4)\n"
        "v_add_f32 v96, v96, v104\n"
        "v_add_f32 v97, v97, v105\n"
        "v_mfma_f32_16x16x32_bf16 v[68:71], v[128:131], v[28:31], 0\n"
        "v_add_f32 v98, v98, v106\n"
        "v_add_f32 v99, v99, v107\n"
        "v_mfma_f32_16x16x32_bf16 v[88:91], v[136:139], v[24:27], 0\n"
        "v_add_f32 v100, v100, v108\n"
        "v_add_f32 v101, v101, v109\n"
        "v_mfma_f32_16x16x32_bf16 v[92:95], v[136:139], v[28:31], 0\n"
        "v_add_f32 v102, v102, v110\n"
        "v_add_f32 v103, v103, v111\n"
        "s_waitcnt lgkmcnt(2)\n"
        "s_waitcnt lgkmcnt(6)\n"
        "v_mfma_f32_16x16x32_bf16 v[48:51], a[4:7], v[32:35], v[48:51]\n"
        "ds_read_b128 v[24:27], v1 offset:4096\n"
        "v_add_f32 v96, v96, v112\n"
        "v_add_f32 v97, v97, v113\n"
        "v_mfma_f32_16x16x32_bf16 v[52:55], a[4:7], v[36:39], v[52:55]\n"
        "ds_read_b128 v[28:31], v1 offset:5120\n"
        "v_add_f32 v98, v98, v114\n"
        "v_add_f32 v99, v99, v115\n"
        "v_mfma_f32_16x16x32_bf16 v[72:75], a[12:15], v[32:35], v[72:75]\n"
        "v_add_f32 v100, v100, v116\n"
        "v_add_f32 v101, v101, v117\n"
        "v_mfma_f32_16x16x32_bf16 v[76:79], a[12:15], v[36:39], v[76:79]\n"
        "v_add_f32 v102, v102, v118\n"
        "v_add_f32 v103, v103, v119\n"
        "s_waitcnt lgkmcnt(2)\n"
        "v_mfma_f32_16x16x32_bf16 v[56:59], a[132:135], v[32:35], v[56:59]\n"
        "v_add_f32 v96, v96, v40\n"
        "v_add_f32 v97, v97, v41\n"
        "v_mfma_f32_16x16x32_bf16 v[60:63], a[132:135], v[36:39], v[60:63]\n"
        "v_add_f32 v98, v98, v42\n"
        "v_add_f32 v99, v99, v43\n"
        "v_mfma_f32_16x16x32_bf16 v[80:83], a[140:143], v[32:35], v[80:83]\n"
        "v_add_f32 v100, v100, v44\n"
        "v_add_f32 v101, v101, v45\n"
        "v_mfma_f32_16x16x32_bf16 v[84:87], a[140:143], v[36:39], v[84:87]\n"
        "v_add_f32 v102, v102, v46\n"
        "v_add_f32 v103, v103, v47\n"
        "v_mfma_f32_16x16x32_bf16 v[64:67], v[132:135], v[32:35], v[64:67]\n"
        "v_mul_f32 v96, s61, v96\n"
        "v_mul_f32 v97, s61, v97\n"
        "v_mfma_f32_16x16x32_bf16 v[68:71], v[132:135], v[36:39], v[68:71]\n"
        "v_mul_f32 v98, s61, v98\n"
        "v_mul_f32 v99, s61, v99\n"
        "v_mfma_f32_16x16x32_bf16 v[88:91], v[140:143], v[32:35], v[88:91]\n"
        "v_mul_f32 v100, s61, v100\n"
        "v_mul_f32 v101, s61, v101\n"
        "v_mfma_f32_16x16x32_bf16 v[92:95], v[140:143], v[36:39], v[92:95]\n"
        "v_mul_f32 v102, s61, v102\n"
        "v_mul_f32 v103, s61, v103\n"
        "s_waitcnt lgkmcnt(0)\n"
        "v_mfma_f32_16x16x32_bf16 v[48:51], a[16:19], v[24:27], v[48:51]\n"
        "ds_read_b128 v[32:35], v1 offset:6144\n"
        "v_cndmask_b32 v96, v20, v96, s[70:71]\n"
        "v_cndmask_b32 v97, v20, v97, s[72:73]\n"
        "v_mfma_f32_16x16x32_bf16 v[52:55], a[16:19], v[28:31], v[52:55]\n"
        "ds_read_b128 v[36:39], v1 offset:7168\n"
        "v_cndmask_b32 v98, v20, v98, s[74:75]\n"
        "v_cndmask_b32 v99, v20, v99, s[76:77]\n"
        "v_mfma_f32_16x16x32_bf16 v[72:75], a[24:27], v[24:27], v[72:75]\n"
        "v_cndmask_b32 v100, v20, v100, s[78:79]\n"
        "v_cndmask_b32 v101, v20, v101, s[80:81]\n"
        "v_mfma_f32_16x16x32_bf16 v[76:79], a[24:27], v[28:31], v[76:79]\n"
        "v_cndmask_b32 v102, v20, v102, s[82:83]\n"
        "v_cndmask_b32 v103, v20, v103, s[84:85]\n"
        "v_mfma_f32_16x16x32_bf16 v[56:59], a[144:147], v[24:27], v[56:59]\n"
        "v_max3_f32 v104, v96, v97, v98\n"
        "v_max3_f32 v105, v99, v100, v101\n"
        "v_mfma_f32_16x16x32_bf16 v[60:63], a[144:147], v[28:31], v[60:63]\n"
        "v_max3_f32 v104, v104, v102, v103\n"
        "v_max_f32 v104, v104, v105\n"
        "v_mfma_f32_16x16x32_bf16 v[80:83], a[152:155], v[24:27], v[80:83]\n"
        "v_mfma_f32_16x16x32_bf16 v[84:87], a[152:155], v[28:31], v[84:87]\n"
        "v_max_f32_dpp v104, v104, v104 quad_perm:[1,0,3,2] row_mask:0xf bank_mask:0xf\n"
        "v_mfma_f32_16x16x32_bf16 v[64:67], v[144:147], v[24:27], v[64:67]\n"
        "v_mfma_f32_16x16x32_bf16 v[68:71], v[144:147], v[28:31], v[68:71]\n"
        "v_max_f32_dpp v104, v104, v104 quad_perm:[2,3,0,1] row_mask:0xf bank_mask:0xf\n"
        "v_mfma_f32_16x16x32_bf16 v[88:91], v[152:155], v[24:27], v[88:91]\n"
        "v_mfma_f32_16x16x32_bf16 v[92:95], v[152:155], v[28:31], v[92:95]\n"
        "v_max_f32_dpp v104, v104, v104 row_half_mirror row_mask:0xf bank_mask:0xf\n"
        "v_sub_f32 v96, v96, v104\n"
        "s_waitcnt lgkmcnt(0)\n"
        "v_mfma_f32_16x16x32_bf16 v[48:51], a[20:23], v[32:35], v[48:51]\n"
        "ds_read_b128 v[24:27], v1 offset:8192\n"
        "v_sub_f32 v97, v97, v104\n"
        "v_sub_f32 v98, v98, v104\n"
        "v_mfma_f32_16x16x32_bf16 v[52:55], a[20:23], v[36:39], v[52:55]\n"
        "ds_read_b128 v[28:31], v1 offset:9216\n"
        "v_sub_f32 v99, v99, v104\n"
        "v_sub_f32 v100, v100, v104\n"
        "v_mfma_f32_16x16x32_bf16 v[72:75], a[28:31], v[32:35], v[72:75]\n"
        "v_sub_f32 v101, v101, v104\n"
        "v_sub_f32 v102, v102, v104\n"
        "v_mfma_f32_16x16x32_bf16 v[76:79], a[28:31], v[36:39], v[76:79]\n"
        "v_sub_f32 v103, v103, v104\n"
        "v_mfma_f32_16x16x32_bf16 v[56:59], a[148:151], v[32:35], v[56:59]\n"
        "v_exp_f32 v96, v96\n"
        "v_mfma_f32_16x16x32_bf16 v[60:63], a[148:151], v[36:39], v[60:63]\n"
        "v_exp_f32 v97, v97\n"
        "v_mfma_f32_16x16x32_bf16 v[80:83], a[156:159], v[32:35], v[80:83]\n"
        "v_exp_f32 v98, v98\n"
        "v_mfma_f32_16x16x32_bf16 v[84:87], a[156:159], v[36:39], v[84:87]\n"
        "v_exp_f32 v99, v99\n"
        "v_mfma_f32_16x16x32_bf16 v[64:67], v[148:151], v[32:35], v[64:67]\n"
        "v_exp_f32 v100, v100\n"
        "v_mfma_f32_16x16x32_bf16 v[68:71], v[148:151], v[36:39], v[68:71]\n"
        "v_exp_f32 v101, v101\n"
        "v_mfma_f32_16x16x32_bf16 v[88:91], v[156:159], v[32:35], v[88:91]\n"
        "v_exp_f32 v102, v102\n"
        "v_mfma_f32_16x16x32_bf16 v[92:95], v[156:159], v[36:39], v[92:95]\n"
        "v_exp_f32 v103, v103\n"
        "s_waitcnt lgkmcnt(0)\n"
        "v_mfma_f32_16x16x32_bf16 v[48:51], a[32:35], v[24:27], v[48:51]\n"
        "ds_read_b128 v[32:35], v1 offset:10240\n"
        "v_add_f32 v106, v96, v97\n"
        "v_add_f32 v107, v98, v99\n"
        "v_mfma_f32_16x16x32_bf16 v[52:55], a[32:35], v[28:31], v[52:55]\n"
        "ds_read_b128 v[36:39], v1 offset:11264\n"
        "v_add_f32 v108, v100, v101\n"
        "v_add_f32 v109, v102, v103\n"
        "v_mfma_f32_16x16x32_bf16 v[72:75], a[40:43], v[24:27], v[72:75]\n"
        "v_add_f32 v106, v106, v107\n"
        "v_add_f32 v108, v108, v109\n"
        "v_mfma_f32_16x16x32_bf16 v[76:79], a[40:43], v[28:31], v[76:79]\n"
        "v_add_f32 v104, v106, v108\n"
        "v_mfma_f32_16x16x32_bf16 v[56:59], a[160:163], v[24:27], v[56:59]\n"
        "v_mfma_f32_16x16x32_bf16 v[60:63], a[160:163], v[28:31], v[60:63]\n"
        "v_add_f32_dpp v104, v104, v104 quad_perm:[1,0,3,2] row_mask:0xf bank_mask:0xf\n"
        "v_mfma_f32_16x16x32_bf16 v[80:83], a[168:171], v[24:27], v[80:83]\n"
        "v_mfma_f32_16x16x32_bf16 v[84:87], a[168:171], v[28:31], v[84:87]\n"
        "v_add_f32_dpp v104, v104, v104 quad_perm:[2,3,0,1] row_mask:0xf bank_mask:0xf\n"
        "v_mfma_f32_16x16x32_bf16 v[64:67], v[160:163], v[24:27], v[64:67]\n"
        "v_mfma_f32_16x16x32_bf16 v[68:71], v[160:163], v[28:31], v[68:71]\n"
        "v_add_f32_dpp v104, v104, v104 row_half_mirror row_mask:0xf bank_mask:0xf\n"
        "v_mfma_f32_16x16x32_bf16 v[88:91], v[168:171], v[24:27], v[88:91]\n"
        "v_rcp_f32 v104, v104\n"
        "v_mfma_f32_16x16x32_bf16 v[92:95], v[168:171], v[28:31], v[92:95]\n"
        "v_mul_f32 v96, v96, v104\n"
        "v_mul_f32 v97, v97, v104\n"
        "s_waitcnt lgkmcnt(0)\n"
        "v_mfma_f32_16x16x32_bf16 v[48:51], a[36:39], v[32:35], v[48:51]\n"
        "ds_read_b128 v[24:27], v1 offset:12288\n"
        "v_mul_f32 v98, v98, v104\n"
        "v_mul_f32 v99, v99, v104\n"
        "v_mfma_f32_16x16x32_bf16 v[52:55], a[36:39], v[36:39], v[52:55]\n"
        "ds_read_b128 v[28:31], v1 offset:13312\n"
        "v_mul_f32 v100, v100, v104\n"
        "v_mul_f32 v101, v101, v104\n"
        "v_mfma_f32_16x16x32_bf16 v[72:75], a[44:47], v[32:35], v[72:75]\n"
        "v_mul_f32 v102, v102, v104\n"
        "v_mul_f32 v103, v103, v104\n"
        "v_mfma_f32_16x16x32_bf16 v[76:79], a[44:47], v[36:39], v[76:79]\n"
        "v_cvt_pk_bf16_f32 v112, v96, v97\n"
        "v_cvt_pk_bf16_f32 v113, v98, v99\n"
        "v_mfma_f32_16x16x32_bf16 v[56:59], a[164:167], v[32:35], v[56:59]\n"
        "v_cvt_pk_bf16_f32 v114, v100, v101\n"
        "v_cvt_pk_bf16_f32 v115, v102, v103\n"
        "s_mov_b64 exec, s[66:67]\n"
        "global_store_dwordx4 v19, v[112:115], s[52:53] sc1\n"
        "s_mov_b64 exec, -1\n"
        "s_add_u32 s68, s45, 2\n"
        "s_min_u32 s68, s68, 63\n"
        "v_mfma_f32_16x16x32_bf16 v[60:63], a[164:167], v[36:39], v[60:63]\n"
        "s_mul_i32 s68, s68, 0xa000\n"
        "s_add_u32 s47, s68, 0x200000\n"
        "s_add_u32 s48, s90, s47\n"
        "v_mfma_f32_16x16x32_bf16 v[80:83], a[172:175], v[32:35], v[80:83]\n"
        "s_addc_u32 s49, s91, 0\n"
        "v_mfma_f32_16x16x32_bf16 v[84:87], a[172:175], v[36:39], v[84:87]\n"
        "v_mfma_f32_16x16x32_bf16 v[64:67], v[164:167], v[32:35], v[64:67]\n"
        "v_mfma_f32_16x16x32_bf16 v[68:71], v[164:167], v[36:39], v[68:71]\n"
        "v_mfma_f32_16x16x32_bf16 v[88:91], v[172:175], v[32:35], v[88:91]\n"
        "v_mfma_f32_16x16x32_bf16 v[92:95], v[172:175], v[36:39], v[92:95]\n"
        "s_waitcnt lgkmcnt(0)\n"
        "v_mfma_f32_16x16x32_bf16 v[48:51], a[48:51], v[24:27], v[48:51]\n"
        "ds_read_b128 v[32:35], v1 offset:14336\n"
        "v_mfma_f32_16x16x32_bf16 v[52:55], a[48:51], v[28:31], v[52:55]\n"
        "ds_read_b128 v[36:39], v1 offset:15360\n"
        "v_mfma_f32_16x16x32_bf16 v[72:75], a[56:59], v[24:27], v[72:75]\n"
        "ds_read_b128 v[40:43], v2 offset:0\n"
        "v_mfma_f32_16x16x32_bf16 v[76:79], a[56:59], v[28:31], v[76:79]\n"
        "ds_read_b128 v[44:47], v2 offset:1024\n"
        "v_mfma_f32_16x16x32_bf16 v[56:59], a[176:179], v[24:27], v[56:59]\n"
        "ds_read_b128 v[96:99], v2 offset:2048\n"
        "v_mfma_f32_16x16x32_bf16 v[60:63], a[176:179], v[28:31], v[60:63]\n"
        "v_mfma_f32_16x16x32_bf16 v[80:83], a[184:187], v[24:27], v[80:83]\n"
        "v_mfma_f32_16x16x32_bf16 v[84:87], a[184:187], v[28:31], v[84:87]\n"
        "v_mfma_f32_16x16x32_bf16 v[64:67], v[176:179], v[24:27], v[64:67]\n"
        "v_mfma_f32_16x16x32_bf16 v[68:71], v[176:179], v[28:31], v[68:71]\n"
        "v_mfma_f32_16x16x32_bf16 v[88:91], v[184:187], v[24:27], v[88:91]\n"
        "v_mfma_f32_16x16x32_bf16 v[92:95], v[184:187], v[28:31], v[92:95]\n"
        "s_waitcnt lgkmcnt(3)\n"
        "v_mfma_f32_16x16x32_bf16 v[48:51], a[52:55], v[32:35], v[48:51]\n"
        "ds_read_b128 v[24:27], v1 offset:16384\n"
        "v_mfma_f32_16x16x32_bf16 v[52:55], a[52:55], v[36:39], v[52:55]\n"
        "ds_read_b128 v[28:31], v1 offset:17408\n"
        "v_mfma_f32_16x16x32_bf16 v[72:75], a[60:63], v[32:35], v[72:75]\n"
        "ds_read_b128 v[100:103], v2 offset:3072\n"
        "v_mfma_f32_16x16x32_bf16 v[76:79], a[60:63], v[36:39], v[76:79]\n"
        "ds_read_b128 v[104:107], v2 offset:4096\n"
        "v_mfma_f32_16x16x32_bf16 v[56:59], a[180:183], v[32:35], v[56:59]\n"
        "ds_read_b128 v[108:111], v2 offset:5120\n"
        "v_mfma_f32_16x16x32_bf16 v[60:63], a[180:183], v[36:39], v[60:63]\n"
        "v_mfma_f32_16x16x32_bf16 v[80:83], a[188:191], v[32:35], v[80:83]\n"
        "v_mfma_f32_16x16x32_bf16 v[84:87], a[188:191], v[36:39], v[84:87]\n"
        "v_mfma_f32_16x16x32_bf16 v[64:67], v[180:183], v[32:35], v[64:67]\n"
        "v_mfma_f32_16x16x32_bf16 v[68:71], v[180:183], v[36:39], v[68:71]\n"
        "v_mfma_f32_16x16x32_bf16 v[88:91], v[188:191], v[32:35], v[88:91]\n"
        "v_mfma_f32_16x16x32_bf16 v[92:95], v[188:191], v[36:39], v[92:95]\n"
        "s_waitcnt vmcnt(0)\n"
        "s_barrier\n"
        "s_add_u32 s47, s58, 0\n"
        "v_mov_b32 v22, s47\n"
        "s_lshl_b32 s47, s40, 7\n"
        "v_mov_b32 v126, s47\n"
        "s_and_b32 exec_lo, s36, 1\n"
        "s_mov_b32 exec_hi, 0\n"
        "global_store_dword v126, v22, s[86:87] sc1\n"
        "s_mov_b64 exec, -1\n"
        "s_waitcnt lgkmcnt(3)\n"
        "s_waitcnt lgkmcnt(7)\n"
        "v_mfma_f32_16x16x32_bf16 v[48:51], v[40:43], v[24:27], v[48:51]\n"
        "ds_read_b128 v[32:35], v1 offset:18432\n"
        "s_add_u32 s68, s58, 1\n"
        "s_and_b32 s68, s68, 3\n"
        "s_lshl_b32 s68, s68, 18\n"
        "s_add_u32 s47, s68, 0x2b00000\n"
        "s_add_u32 s50, s90, s47\n"
        "s_addc_u32 s51, s91, 0\n"
        "v_mfma_f32_16x16x32_bf16 v[52:55], v[40:43], v[28:31], v[52:55]\n"
        "ds_read_b128 v[36:39], v1 offset:19456\n"
        "s_waitcnt lgkmcnt(8)\n"
        "v_mfma_f32_16x16x32_bf16 v[72:75], v[44:47], v[24:27], v[72:75]\n"
        "ds_read_b128 v[112:115], v2 offset:24576\n"
        "s_mov_b64 exec, s[64:65]\n"
        "global_store_dword v14, v21, s[50:51] offset:0 sc1\n"
        "s_mov_b64 exec, -1\n"
        "v_mfma_f32_16x16x32_bf16 v[76:79], v[44:47], v[28:31], v[76:79]\n"
        "ds_read_b128 v[116:119], v2 offset:25600\n"
        "s_mov_b64 exec, s[64:65]\n"
        "global_store_dword v14, v21, s[50:51] offset:640 sc1\n"
        "s_mov_b64 exec, -1\n"
        "s_waitcnt lgkmcnt(9)\n"
        "v_mfma_f32_16x16x32_bf16 v[56:59], v[96:99], v[24:27], v[56:59]\n"
        "ds_read_b128 v[40:43], v2 offset:26624\n"
        "s_mov_b64 exec, s[64:65]\n"
        "global_store_dword v14, v21, s[50:51] offset:1280 sc1\n"
        "s_mov_b64 exec, -1\n"
        "v_mfma_f32_16x16x32_bf16 v[60:63], v[96:99], v[28:31], v[60:63]\n"
        "s_mov_b64 exec, s[64:65]\n"
        "global_store_dword v14, v21, s[50:51] offset:1920 sc1\n"
        "s_mov_b64 exec, -1\n"
        "s_waitcnt lgkmcnt(7)\n"
        "v_mfma_f32_16x16x32_bf16 v[80:83], v[100:103], v[24:27], v[80:83]\n"
        "s_add_u32 s47, s96, 0\n"
        "s_add_u32 s54, s48, s47\n"
        "s_addc_u32 s55, s49, 0\n"
        "v_mfma_f32_16x16x32_bf16 v[84:87], v[100:103], v[28:31], v[84:87]\n"
        "s_add_u32 m0, s47, 0\n"
        "s_waitcnt lgkmcnt(6)\n"
        "v_mfma_f32_16x16x32_bf16 v[64:67], v[104:107], v[24:27], v[64:67]\n"
        "global_load_lds_dwordx4 v1, s[54:55] offset:0 sc1\n"
        "v_mfma_f32_16x16x32_bf16 v[68:71], v[104:107], v[28:31], v[68:71]\n"
        "s_waitcnt lgkmcnt(5)\n"
        "v_mfma_f32_16x16x32_bf16 v[88:91], v[108:111], v[24:27], v[88:91]\n"
        "v_mfma_f32_16x16x32_bf16 v[92:95], v[108:111], v[28:31], v[92:95]\n"
        "global_load_lds_dwordx4 v1, s[54:55] offset:1024 sc1\n"
        "s_waitcnt lgkmcnt(3)\n"
        "v_mfma_f32_16x16x32_bf16 v[48:51], a[64:67], v[32:35], v[48:51]\n"
        "ds_read_b128 v[24:27], v1 offset:20480\n"
        "v_mfma_f32_16x16x32_bf16 v[52:55], a[64:67], v[36:39], v[52:55]\n"
        "ds_read_b128 v[28:31], v1 offset:21504\n"
        "v_mfma_f32_16x16x32_bf16 v[72:75], a[72:75], v[32:35], v[72:75]\n"
        "ds_read_b128 v[44:47], v2 offset:27648\n"
        "global_load_lds_dwordx4 v1, s[54:55] offset:2048 sc1\n"
        "v_mfma_f32_16x16x32_bf16 v[76:79], a[72:75], v[36:39], v[76:79]\n"
        "ds_read_b128 v[96:99], v2 offset:28672\n"
        "v_mfma_f32_16x16x32_bf16 v[56:59], a[192:195], v[32:35], v[56:59]\n"
        "ds_read_b128 v[100:103], v2 offset:29696\n"
        "v_mfma_f32_16x16x32_bf16 v[60:63], a[192:195], v[36:39], v[60:63]\n"
        "global_load_lds_dwordx4 v1, s[54:55] offset:3072 sc1\n"
        "v_mfma_f32_16x16x32_bf16 v[80:83], a[200:203], v[32:35], v[80:83]\n"
        "v_mfma_f32_16x16x32_bf16 v[84:87], a[200:203], v[36:39], v[84:87]\n"
        "v_mfma_f32_16x16x32_bf16 v[64:67], v[192:195], v[32:35], v[64:67]\n"
        "v_mfma_f32_16x16x32_bf16 v[68:71], v[192:195], v[36:39], v[68:71]\n"
        "v_mfma_f32_16x16x32_bf16 v[88:91], v[200:203], v[32:35], v[88:91]\n"
        "v_mfma_f32_16x16x32_bf16 v[92:95], v[200:203], v[36:39], v[92:95]\n"
        "s_waitcnt lgkmcnt(3)\n"
        "v_mfma_f32_16x16x32_bf16 v[48:51], a[68:71], v[24:27], v[48:51]\n"
        "ds_read_b128 v[32:35], v1 offset:22528\n"
        "v_mfma_f32_16x16x32_bf16 v[52:55], a[68:71], v[28:31], v[52:55]\n"
        "ds_read_b128 v[36:39], v1 offset:23552\n"
        "v_mfma_f32_16x16x32_bf16 v[72:75], a[76:79], v[24:27], v[72:75]\n"
        "ds_read_b128 v[104:107], v0 offset:0\n"
        "v_mfma_f32_16x16x32_bf16 v[76:79], a[76:79], v[28:31], v[76:79]\n"
        "ds_read_b128 v[108:111], v0 offset:1024\n"
        "v_mfma_f32_16x16x32_bf16 v[56:59], a[196:199], v[24:27], v[56:59]\n"
        "v_mfma_f32_16x16x32_bf16 v[60:63], a[196:199], v[28:31], v[60:63]\n"
        "v_mfma_f32_16x16x32_bf16 v[80:83], a[204:207], v[24:27], v[80:83]\n"
        "v_mfma_f32_16x16x32_bf16 v[84:87], a[204:207], v[28:31], v[84:87]\n"
        "v_mfma_f32_16x16x32_bf16 v[64:67], v[196:199], v[24:27], v[64:67]\n"
        "v_mfma_f32_16x16x32_bf16 v[68:71], v[196:199], v[28:31], v[68:71]\n"
        "v_mfma_f32_16x16x32_bf16 v[88:91], v[204:207], v[24:27], v[88:91]\n"
        "v_mfma_f32_16x16x32_bf16 v[92:95], v[204:207], v[28:31], v[92:95]\n"
        "s_waitcnt lgkmcnt(2)\n"
        "s_waitcnt lgkmcnt(11)\n"
        "v_mfma_f32_16x16x32_bf16 v[48:51], v[112:115], v[32:35], v[48:51]\n"
        "ds_read_b128 v[24:27], v1 offset:24576\n"
        "v_mfma_f32_16x16x32_bf16 v[52:55], v[112:115], v[36:39], v[52:55]\n"
        "ds_read_b128 v[28:31], v1 offset:25600\n"
        "s_waitcnt lgkmcnt(12)\n"
        "v_mfma_f32_16x16x32_bf16 v[72:75], v[116:119], v[32:35], v[72:75]\n"
        "ds_read_b128 v[112:115], v0 offset:2048\n"
        "v_mfma_f32_16x16x32_bf16 v[76:79], v[116:119], v[36:39], v[76:79]\n"
        "s_waitcnt lgkmcnt(12)\n"
        "v_mfma_f32_16x16x32_bf16 v[56:59], v[40:43], v[32:35], v[56:59]\n"
        "ds_read_b128 v[116:119], v0 offset:3072\n"
        "v_mfma_f32_16x16x32_bf16 v[60:63], v[40:43], v[36:39], v[60:63]\n"
        "s_waitcnt lgkmcnt(10)\n"
        "v_mfma_f32_16x16x32_bf16 v[80:83], v[44:47], v[32:35], v[80:83]\n"
        "v_mfma_f32_16x16x32_bf16 v[84:87], v[44:47], v[36:39], v[84:87]\n"
        "s_waitcnt lgkmcnt(9)\n"
        "v_mfma_f32_16x16x32_bf16 v[64:67], v[96:99], v[32:35], v[64:67]\n"
        "v_mfma_f32_16x16x32_bf16 v[68:71], v[96:99], v[36:39], v[68:71]\n"
        "s_waitcnt lgkmcnt(8)\n"
        "v_mfma_f32_16x16x32_bf16 v[88:91], v[100:103], v[32:35], v[88:91]\n"
        "v_mfma_f32_16x16x32_bf16 v[92:95], v[100:103], v[36:39], v[92:95]\n"
        "s_waitcnt lgkmcnt(2)\n"
        "v_mfma_f32_16x16x32_bf16 v[48:51], a[80:83], v[24:27], v[48:51]\n"
        "ds_read_b128 v[32:35], v1 offset:26624\n"
        "v_mfma_f32_16x16x32_bf16 v[52:55], a[80:83], v[28:31], v[52:55]\n"
        "ds_read_b128 v[36:39], v1 offset:27648\n"
        "v_mfma_f32_16x16x32_bf16 v[72:75], a[88:91], v[24:27], v[72:75]\n"
        "ds_read_b128 v[40:43], v0 offset:4096\n"
        "v_mfma_f32_16x16x32_bf16 v[76:79], a[88:91], v[28:31], v[76:79]\n"
        "ds_read_b128 v[44:47], v0 offset:5120\n"
        "v_mfma_f32_16x16x32_bf16 v[56:59], a[208:211], v[24:27], v[56:59]\n"
        "ds_read_b128 v[96:99], v0 offset:24576\n"
        "v_mfma_f32_16x16x32_bf16 v[60:63], a[208:211], v[28:31], v[60:63]\n"
        "v_mfma_f32_16x16x32_bf16 v[80:83], a[216:219], v[24:27], v[80:83]\n"
        "v_mfma_f32_16x16x32_bf16 v[84:87], a[216:219], v[28:31], v[84:87]\n"
        "v_mfma_f32_16x16x32_bf16 v[64:67], v[208:211], v[24:27], v[64:67]\n"
        "v_mfma_f32_16x16x32_bf16 v[68:71], v[208:211], v[28:31], v[68:71]\n"
        "v_mfma_f32_16x16x32_bf16 v[88:91], v[216:219], v[24:27], v[88:91]\n"
        "v_mfma_f32_16x16x32_bf16 v[92:95], v[216:219], v[28:31], v[92:95]\n"
        "s_waitcnt lgkmcnt(3)\n"
        "v_mfma_f32_16x16x32_bf16 v[48:51], a[84:87], v[32:35], v[48:51]\n"
        "ds_read_b128 v[24:27], v1 offset:28672\n"
        "v_mfma_f32_16x16x32_bf16 v[52:55], a[84:87], v[36:39], v[52:55]\n"
        "ds_read_b128 v[28:31], v1 offset:29696\n"
        "v_mfma_f32_16x16x32_bf16 v[72:75], a[92:95], v[32:35], v[72:75]\n"
        "ds_read_b128 v[100:103], v0 offset:25600\n"
        "v_mfma_f32_16x16x32_bf16 v[76:79], a[92:95], v[36:39], v[76:79]\n"
        "v_mfma_f32_16x16x32_bf16 v[56:59], a[212:215], v[32:35], v[56:59]\n"
        "v_mfma_f32_16x16x32_bf16 v[60:63], a[212:215], v[36:39], v[60:63]\n"
        "v_mfma_f32_16x16x32_bf16 v[80:83], a[220:223], v[32:35], v[80:83]\n"
        "v_mfma_f32_16x16x32_bf16 v[84:87], a[220:223], v[36:39], v[84:87]\n"
        "v_mfma_f32_16x16x32_bf16 v[64:67], v[212:215], v[32:35], v[64:67]\n"
        "v_mfma_f32_16x16x32_bf16 v[68:71], v[212:215], v[36:39], v[68:71]\n"
        "v_mfma_f32_16x16x32_bf16 v[88:91], v[220:223], v[32:35], v[88:91]\n"
        "v_mfma_f32_16x16x32_bf16 v[92:95], v[220:223], v[36:39], v[92:95]\n"
        "s_waitcnt lgkmcnt(1)\n"
        "s_waitcnt lgkmcnt(13)\n"
        "v_mfma_f32_16x16x32_bf16 v[48:51], v[104:107], v[24:27], v[48:51]\n"
        "ds_read_b128 v[32:35], v1 offset:30720\n"
        "v_mfma_f32_16x16x32_bf16 v[52:55], v[104:107], v[28:31], v[52:55]\n"
        "ds_read_b128 v[36:39], v1 offset:31744\n"
        "s_waitcnt lgkmcnt(14)\n"
        "v_mfma_f32_16x16x32_bf16 v[72:75], v[108:111], v[24:27], v[72:75]\n"
        "ds_read_b128 v[104:107], v0 offset:26624\n"
        "v_mfma_f32_16x16x32_bf16 v[76:79], v[108:111], v[28:31], v[76:79]\n"
        "s_waitcnt lgkmcnt(12)\n"
        "v_mfma_f32_16x16x32_bf16 v[56:59], v[112:115], v[24:27], v[56:59]\n"
        "ds_read_b128 v[108:111], v0 offset:27648\n"
        "v_mfma_f32_16x16x32_bf16 v[60:63], v[112:115], v[28:31], v[60:63]\n"
        "s_waitcnt lgkmcnt(12)\n"
        "v_mfma_f32_16x16x32_bf16 v[80:83], v[116:119], v[24:27], v[80:83]\n"
        "v_mfma_f32_16x16x32_bf16 v[84:87], v[116:119], v[28:31], v[84:87]\n"
        "s_waitcnt lgkmcnt(9)\n"
        "v_mfma_f32_16x16x32_bf16 v[64:67], v[40:43], v[24:27], v[64:67]\n"
        "v_mfma_f32_16x16x32_bf16 v[68:71], v[40:43], v[28:31], v[68:71]\n"
        "s_waitcnt lgkmcnt(8)\n"
        "v_mfma_f32_16x16x32_bf16 v[88:91], v[44:47], v[24:27], v[88:91]\n"
        "v_mfma_f32_16x16x32_bf16 v[92:95], v[44:47], v[28:31], v[92:95]\n"
        "s_waitcnt lgkmcnt(2)\n"
        "v_mfma_f32_16x16x32_bf16 v[48:51], a[96:99], v[32:35], v[48:51]\n"
        "ds_read_b128 v[24:27], v1 offset:32768\n"
        "v_mfma_f32_16x16x32_bf16 v[52:55], a[96:99], v[36:39], v[52:55]\n"
        "ds_read_b128 v[28:31], v1 offset:33792\n"
        "v_mfma_f32_16x16x32_bf16 v[72:75], a[104:107], v[32:35], v[72:75]\n"
        "ds_read_b128 v[112:115], v0 offset:28672\n"
        "v_mfma_f32_16x16x32_bf16 v[76:79], a[104:107], v[36:39], v[76:79]\n"
        "ds_read_b128 v[116:119], v0 offset:29696\n"
        "v_mfma_f32_16x16x32_bf16 v[56:59], a[224:227], v[32:35], v[56:59]\n"
        "v_mfma_f32_16x16x32_bf16 v[60:63], a[224:227], v[36:39], v[60:63]\n"
        "v_mfma_f32_16x16x32_bf16 v[80:83], a[232:235], v[32:35], v[80:83]\n"
        "v_mfma_f32_16x16x32_bf16 v[84:87], a[232:235], v[36:39], v[84:87]\n"
        "v_mfma_f32_16x16x32_bf16 v[64:67], v[224:227], v[32:35], v[64:67]\n"
        "v_mfma_f32_16x16x32_bf16 v[68:71], v[224:227], v[36:39], v[68:71]\n"
        "v_mfma_f32_16x16x32_bf16 v[88:91], v[232:235], v[32:35], v[88:91]\n"
        "v_mfma_f32_16x16x32_bf16 v[92:95], v[232:235], v[36:39], v[92:95]\n"
        "s_barrier\n"
        "s_waitcnt lgkmcnt(2)\n"
        "v_mfma_f32_16x16x32_bf16 v[48:51], a[100:103], v[24:27], v[48:51]\n"
        "ds_read_b128 v[32:35], v1 offset:34816\n"
        "global_load_dword v126, v15, s[94:95] sc1\n"
        "s_add_u32 s47, s96, 16384\n"
        "s_add_u32 s54, s48, s47\n"
        "v_mfma_f32_16x16x32_bf16 v[52:55], a[100:103], v[28:31], v[52:55]\n"
        "ds_read_b128 v[36:39], v1 offset:35840\n"
        "s_addc_u32 s55, s49, 0\n"
        "s_add_u32 m0, s47, 0\n"
        "v_mfma_f32_16x16x32_bf16 v[72:75], a[108:111], v[24:27], v[72:75]\n"
        "global_load_lds_dwordx4 v1, s[54:55] offset:0 sc1\n"
        "v_mfma_f32_16x16x32_bf16 v[76:79], a[108:111], v[28:31], v[76:79]\n"
        "v_mfma_f32_16x16x32_bf16 v[56:59], a[228:231], v[24:27], v[56:59]\n"
        "v_mfma_f32_16x16x32_bf16 v[60:63], a[228:231], v[28:31], v[60:63]\n"
        "global_load_lds_dwordx4 v1, s[54:55] offset:1024 sc1\n"
        "v_mfma_f32_16x16x32_bf16 v[80:83], a[236:239], v[24:27], v[80:83]\n"
        "v_mfma_f32_16x16x32_bf16 v[84:87], a[236:239], v[28:31], v[84:87]\n"
        "v_mfma_f32_16x16x32_bf16 v[64:67], v[228:231], v[24:27], v[64:67]\n"
        "global_load_lds_dwordx4 v1, s[54:55] offset:2048 sc1\n"
        "v_mfma_f32_16x16x32_bf16 v[68:71], v[228:231], v[28:31], v[68:71]\n"
        "v_mfma_f32_16x16x32_bf16 v[88:91], v[236:239], v[24:27], v[88:91]\n"
        "v_mfma_f32_16x16x32_bf16 v[92:95], v[236:239], v[28:31], v[92:95]\n"
        "global_load_lds_dwordx4 v1, s[54:55] offset:3072 sc1\n"
        "s_waitcnt lgkmcnt(0)\n"
        "s_waitcnt lgkmcnt(13)\n"
        "v_mfma_f32_16x16x32_bf16 v[48:51], v[96:99], v[32:35], v[48:51]\n"
        "ds_read_b128 v[24:27], v1 offset:36864\n"
        "v_mfma_f32_16x16x32_bf16 v[52:55], v[96:99], v[36:39], v[52:55]\n"
        "ds_read_b128 v[28:31], v1 offset:37888\n"
        "s_waitcnt lgkmcnt(12)\n"
        "v_mfma_f32_16x16x32_bf16 v[72:75], v[100:103], v[32:35], v[72:75]\n"
        "s_lshr_b32 s58, s45, 1\n"
        "s_and_b32 s68, s58, 3\n"
        "s_lshl_b32 s68, s68, 17\n"
        "v_mfma_f32_16x16x32_bf16 v[76:79], v[100:103], v[36:39], v[76:79]\n"
        "s_add_u32 s68, s68, s44\n"
        "s_add_u32 s68, s68, 0x180000\n"
        "s_cmp_eq_u32 s45, 0\n"
        "s_waitcnt lgkmcnt(9)\n"
        "v_mfma_f32_16x16x32_bf16 v[56:59], v[104:107], v[32:35], v[56:59]\n"
        "s_cselect_b32 s68, 0xa80000, s68\n"
        "s_add_u32 s47, s68, 0x0\n"
        "s_add_u32 s56, s90, s47\n"
        "v_mfma_f32_16x16x32_bf16 v[60:63], v[104:107], v[36:39], v[60:63]\n"
        "s_addc_u32 s57, s91, 0\n"
        "s_and_b32 s68, s58, 3\n"
        "s_lshl_b32 s68, s68, 18\n"
        "s_waitcnt lgkmcnt(8)\n"
        "v_mfma_f32_16x16x32_bf16 v[80:83], v[108:111], v[32:35], v[80:83]\n"
        "s_add_u32 s47, s68, 0x2b00000\n"
        "s_add_u32 s50, s90, s47\n"
        "s_addc_u32 s51, s91, 0\n"
        "v_mfma_f32_16x16x32_bf16 v[84:87], v[108:111], v[36:39], v[84:87]\n"
        "s_waitcnt lgkmcnt(5)\n"
        "v_mfma_f32_16x16x32_bf16 v[64:67], v[112:115], v[32:35], v[64:67]\n"
        "v_mfma_f32_16x16x32_bf16 v[68:71], v[112:115], v[36:39], v[68:71]\n"
        "s_waitcnt lgkmcnt(4)\n"
        "v_mfma_f32_16x16x32_bf16 v[88:91], v[116:119], v[32:35], v[88:91]\n"
        "v_mfma_f32_16x16x32_bf16 v[92:95], v[116:119], v[36:39], v[92:95]\n"
        "s_waitcnt lgkmcnt(0)\n"
        "v_mfma_f32_16x16x32_bf16 v[48:51], a[112:115], v[24:27], v[48:51]\n"
        "ds_read_b128 v[32:35], v1 offset:38912\n"
        "v_mfma_f32_16x16x32_bf16 v[52:55], a[112:115], v[28:31], v[52:55]\n"
        "ds_read_b128 v[36:39], v1 offset:39936\n"
        "v_mfma_f32_16x16x32_bf16 v[72:75], a[120:123], v[24:27], v[72:75]\n"
        "v_mfma_f32_16x16x32_bf16 v[76:79], a[120:123], v[28:31], v[76:79]\n"
        "v_mfma_f32_16x16x32_bf16 v[56:59], a[240:243], v[24:27], v[56:59]\n"
        "s_add_u32 s88, s45, 1\n"
        "s_lshr_b32 s88, s88, 1\n"
        "s_waitcnt vmcnt(4)\n"
        "v_mfma_f32_16x16x32_bf16 v[60:63], a[240:243], v[28:31], v[60:63]\n"
        "v_and_b32 v127, 63, v126\n"
        "v_cmp_ne_u32 vcc, s88, v127\n"
        "v_mfma_f32_16x16x32_bf16 v[80:83], a[248:251], v[24:27], v[80:83]\n"
        "v_mfma_f32_16x16x32_bf16 v[84:87], a[248:251], v[28:31], v[84:87]\n"
        "s_cmp_eq_u32 s45, 0\n"
        "s_cselect_b64 vcc, 0, vcc\n"
        "s_cbranch_vccnz Lqk_hs_3\n"
        "Lqk_hb_4:\n"
        "v_readfirstlane_b32 s34, v126\n"
        "v_mfma_f32_16x16x32_bf16 v[64:67], v[240:243], v[24:27], v[64:67]\n"
        "v_mfma_f32_16x16x32_bf16 v[68:71], v[240:243], v[28:31], v[68:71]\n"
        "v_subrev_u32 v126, s34, v126\n"
        "v_mfma_f32_16x16x32_bf16 v[88:91], v[248:251], v[24:27], v[88:91]\n"
        "v_mfma_f32_16x16x32_bf16 v[92:95], v[248:251], v[28:31], v[92:95]\n"
        "v_max_i32_dpp v126, v126, v126 row_ror:8 row_mask:0xf bank_mask:0xf\n"
        "s_waitcnt lgkmcnt(0)\n"
        "v_mfma_f32_16x16x32_bf16 v[48:51], a[116:119], v[32:35], v[48:51]\n"
        "v_max_i32_dpp v126, v126, v126 row_ror:4 row_mask:0xf bank_mask:0xf\n"
        "v_mfma_f32_16x16x32_bf16 v[52:55], a[116:119], v[36:39], v[52:55]\n"
        "v_mfma_f32_16x16x32_bf16 v[72:75], a[124:127], v[32:35], v[72:75]\n"
        "v_max_i32_dpp v126, v126, v126 row_ror:2 row_mask:0xf bank_mask:0xf\n"
        "v_mfma_f32_16x16x32_bf16 v[76:79], a[124:127], v[36:39], v[76:79]\n"
        "v_mfma_f32_16x16x32_bf16 v[56:59], a[244:247], v[32:35], v[56:59]\n"
        "v_max_i32_dpp v126, v126, v126 row_ror:1 row_mask:0xf bank_mask:0xf\n"
        "v_mfma_f32_16x16x32_bf16 v[60:63], a[244:247], v[36:39], v[60:63]\n"
        "v_readfirstlane_b32 s68, v126\n"
        "v_readlane_b32 s69, v126, 16\n"
        "v_mfma_f32_16x16x32_bf16 v[80:83], a[252:255], v[32:35], v[80:83]\n"
        "v_mfma_f32_16x16x32_bf16 v[84:87], a[252:255], v[36:39], v[84:87]\n"
        "s_max_i32 s68, s68, s69\n"
        "s_add_u32 s34, s34, s68\n"
        "s_add_u32 s34, s34, 4928\n"
        "v_mfma_f32_16x16x32_bf16 v[64:67], v[244:247], v[32:35], v[64:67]\n"
        "s_add_u32 s34, s34, s60\n"
        "v_cvt_pk_bf16_f32 v24, v48, v49\n"
        "v_cvt_pk_bf16_f32 v25, v50, v51\n"
        "v_mfma_f32_16x16x32_bf16 v[68:71], v[244:247], v[36:39], v[68:71]\n"
        "v_cvt_pk_bf16_f32 v30, v52, v53\n"
        "v_cvt_pk_bf16_f32 v31, v54, v55\n"
        "v_mfma_f32_16x16x32_bf16 v[88:91], v[252:255], v[32:35], v[88:91]\n"
        "v_cvt_pk_bf16_f32 v26, v56, v57\n"
        "v_cvt_pk_bf16_f32 v27, v58, v59\n"
        "v_mfma_f32_16x16x32_bf16 v[92:95], v[252:255], v[36:39], v[92:95]\n"
        "Lqk_crit:\n"
        "s_mov_b32 s59, s46\n"
        "s_cmp_eq_u32 s45, 0\n"
        "s_cbranch_scc1 Lqk_nw_6\n"
        "s_mov_b32 s69, 48\n"
        ".p2align 6\n"
        "Lqk_w_7:\n"
        "s_memrealtime s[54:55]\n"
        "s_waitcnt lgkmcnt(0)\n"
        "s_lshl_b32 s47, s54, 6\n"
        "s_sub_u32 s47, s34, s47\n"
        "s_cmp_lt_i32 s47, 768\n"
        "s_cbranch_scc1 Lqk_f_8\n"
        "s_sleep 1\n"
        "s_sub_u32 s69, s69, 1\n"
        "s_cbranch_scc0 Lqk_w_7\n"
        "s_mov_b32 s47, 0\n"
        "Lqk_f_8:\n"
        "s_max_i32 s47, s47, 0\n"
        "Lqk_c_9:\n"
        "s_sub_u32 s47, s47, 80\n"
        "s_cbranch_scc0 Lqk_c_9\n"
        "Lqk_nw_6:\n"
        "global_load_dwordx4 v[48:51], v3, s[56:57] offset:-2048 sc1\n"
        "global_load_dwordx4 v[52:55], v3, s[56:57] offset:2048 sc1\n"
        "global_load_dword v56, v4, s[56:57] sc1\n"
        "global_load_dword v57, v4, s[56:57] offset:256 sc1\n"
        "ds_read_b128 v[104:107], v2 offset:0\n"
        "ds_read_b128 v[108:111], v2 offset:24576\n"
        "ds_read_b128 v[112:115], v2 offset:1024\n"
        "ds_read_b128 v[116:119], v2 offset:25600\n"
        "v_cvt_pk_bf16_f32 v36, v72, v73\n"
        "v_cvt_pk_bf16_f32 v37, v74, v75\n"
        "v_cvt_pk_bf16_f32 v42, v76, v77\n"
        "v_cvt_pk_bf16_f32 v43, v78, v79\n"
        "v_cvt_pk_bf16_f32 v32, v60, v61\n"
        "v_cvt_pk_bf16_f32 v33, v62, v63\n"
        "v_cvt_pk_bf16_f32 v38, v80, v81\n"
        "v_cvt_pk_bf16_f32 v39, v82, v83\n"
        "v_cvt_pk_bf16_f32 v44, v84, v85\n"
        "v_cvt_pk_bf16_f32 v45, v86, v87\n"
        "v_cvt_pk_bf16_f32 v28, v64, v65\n"
        "v_cvt_pk_bf16_f32 v29, v66, v67\n"
        "v_cvt_pk_bf16_f32 v34, v68, v69\n"
        "v_cvt_pk_bf16_f32 v35, v70, v71\n"
        "v_cvt_pk_bf16_f32 v40, v88, v89\n"
        "v_cvt_pk_bf16_f32 v41, v90, v91\n"
        "v_cvt_pk_bf16_f32 v46, v92, v93\n"
        "v_cvt_pk_bf16_f32 v47, v94, v95\n"
        "v_mov_b32 v88, 0\n"
        "v_mov_b32 v89, 0\n"
        "v_mov_b32 v90, 0\n"
        "v_mov_b32 v91, 0\n"
        "v_mov_b32 v92, 0\n"
        "v_mov_b32 v93, 0\n"
        "v_mov_b32 v94, 0\n"
        "v_mov_b32 v95, 0\n"
        "v_mov_b32 v96, 0\n"
        "v_mov_b32 v97, 0\n"
        "v_mov_b32 v98, 0\n"
        "v_mov_b32 v99, 0\n"
        "v_mov_b32 v100, 0\n"
        "v_mov_b32 v101, 0\n"
        "v_mov_b32 v102, 0\n"
        "v_mov_b32 v103, 0\n"
        "Lqk_gb_10:\n"
        "s_sub_u32 s46, s46, 1\n"
        "s_cbranch_scc1 Lqk_exit\n"
        "s_waitcnt vmcnt(3)\n"
        "ds_write_b128 v5, v[48:51] offset:47104\n"
        "v_max3_u32 v126, v48, v49, v50\n"
        "s_waitcnt vmcnt(2)\n"
        "ds_write_b128 v5, v[52:55] offset:51200\n"
        "v_max3_u32 v127, v51, v52, v53\n"
        "v_max3_u32 v126, v126, v54, v55\n"
        "s_waitcnt vmcnt(0)\n"
        "ds_write2st64_b32 v6, v56, v57 offset0:184 offset1:185\n"
        "v_max3_u32 v127, v127, v56, v57\n"
        "v_max_u32 v126, v126, v127\n"
        "v_cmp_eq_u32 vcc, -1, v126\n"
        "s_cbranch_vccnz Lqk_gm_5\n"
        "s_waitcnt lgkmcnt(0)\n"
        "s_barrier\n"
        "s_memrealtime s[38:39]\n"
        "ds_read_b128 v[48:51], v1 offset:47104\n"
        "ds_read_b128 v[52:55], v1 offset:48128\n"
        "ds_read_b128 v[56:59], v1 offset:49152\n"
        "ds_read_b128 v[60:63], v1 offset:50176\n"
        "ds_read_b128 v[64:67], v1 offset:51200\n"
        "ds_read_b128 v[68:71], v1 offset:52224\n"
        "ds_read_b128 v[72:75], v1 offset:53248\n"
        "ds_read_b128 v[76:79], v1 offset:54272\n"
        "ds_read_b128 v[80:83], v1 offset:55296\n"
        "ds_read_b128 v[84:87], v1 offset:56320\n"
        "s_waitcnt lgkmcnt(9)\n"
        "v_smfmac_f32_16x16x64_bf16 v[88:91], v[48:51], v[104:111], v7\n"
        "ds_read_b128 v[104:107], v0 offset:0\n"
        "v_smfmac_f32_16x16x64_bf16 v[92:95], v[48:51], v[112:119], v7\n"
        "ds_read_b128 v[108:111], v0 offset:24576\n"
        "s_waitcnt lgkmcnt(10)\n"
        "v_smfmac_f32_16x16x64_bf16 v[88:91], v[52:55], a[0:7], v7\n"
        "ds_read_b128 v[112:115], v0 offset:1024\n"
        "v_smfmac_f32_16x16x64_bf16 v[92:95], v[52:55], a[8:15], v7\n"
        "ds_read_b128 v[116:119], v0 offset:25600\n"
        "s_waitcnt lgkmcnt(11)\n"
        "v_smfmac_f32_16x16x64_bf16 v[88:91], v[56:59], a[16:23], v7\n"
        "v_smfmac_f32_16x16x64_bf16 v[92:95], v[56:59], a[24:31], v7\n"
        "s_waitcnt lgkmcnt(10)\n"
        "v_smfmac_f32_16x16x64_bf16 v[88:91], v[60:63], a[32:39], v7\n"
        "v_smfmac_f32_16x16x64_bf16 v[92:95], v[60:63], a[40:47], v7\n"
        "s_waitcnt lgkmcnt(9)\n"
        "v_smfmac_f32_16x16x64_bf16 v[88:91], v[64:67], a[48:55], v7\n"
        "v_smfmac_f32_16x16x64_bf16 v[92:95], v[64:67], a[56:63], v7\n"
        "s_waitcnt lgkmcnt(0)\n"
        "s_waitcnt lgkmcnt(8)\n"
        "s_waitcnt lgkmcnt(2)\n"
        "v_smfmac_f32_16x16x64_bf16 v[88:91], v[68:71], v[104:111], v7\n"
        "ds_read_b128 v[104:107], v2 offset:2048\n"
        "s_add_u32 s47, s38, s35\n"
        "s_lshl_b32 s47, s47, 6\n"
        "s_add_u32 s47, s47, s58\n"
        "s_waitcnt lgkmcnt(1)\n"
        "v_smfmac_f32_16x16x64_bf16 v[92:95], v[68:71], v[112:119], v7\n"
        "ds_read_b128 v[108:111], v2 offset:26624\n"
        "s_add_u32 s47, s47, 1\n"
        "v_mov_b32 v22, s47\n"
        "s_mov_b64 exec, s[36:37]\n"
        "global_store_dword v23, v22, s[98:99] sc1\n"
        "s_mov_b64 exec, -1\n"
        "s_add_u32 s47, s97, 32768\n"
        "s_waitcnt lgkmcnt(9)\n"
        "v_smfmac_f32_16x16x64_bf16 v[88:91], v[72:75], a[64:71], v7\n"
        "ds_read_b128 v[112:115], v2 offset:3072\n"
        "s_add_u32 s54, s48, s47\n"
        "s_addc_u32 s55, s49, 0\n"
        "s_add_u32 m0, s47, 0\n"
        "v_smfmac_f32_16x16x64_bf16 v[92:95], v[72:75], a[72:79], v7\n"
        "ds_read_b128 v[116:119], v2 offset:27648\n"
        "global_load_lds_dwordx4 v1, s[54:55] offset:0 sc1\n"
        "global_load_lds_dwordx4 v1, s[54:55] offset:1024 sc1\n"
        "s_waitcnt lgkmcnt(10)\n"
        "v_smfmac_f32_16x16x64_bf16 v[88:91], v[76:79], a[80:87], v7\n"
        "v_smfmac_f32_16x16x64_bf16 v[92:95], v[76:79], a[88:95], v7\n"
        "s_waitcnt lgkmcnt(9)\n"
        "v_smfmac_f32_16x16x64_bf16 v[88:91], v[80:83], a[96:103], v7\n"
        "v_smfmac_f32_16x16x64_bf16 v[92:95], v[80:83], a[104:111], v7\n"
        "s_waitcnt lgkmcnt(8)\n"
        "v_smfmac_f32_16x16x64_bf16 v[88:91], v[84:87], a[112:119], v7\n"
        "v_smfmac_f32_16x16x64_bf16 v[92:95], v[84:87], a[120:127], v7\n"
        "s_waitcnt lgkmcnt(2)\n"
        "v_smfmac_f32_16x16x64_bf16 v[96:99], v[48:51], v[104:111], v7\n"
        "ds_read_b128 v[104:107], v0 offset:2048\n"
        "s_waitcnt lgkmcnt(1)\n"
        "v_smfmac_f32_16x16x64_bf16 v[100:103], v[48:51], v[112:119], v7\n"
        "ds_read_b128 v[108:111], v0 offset:26624\n"
        "v_smfmac_f32_16x16x64_bf16 v[96:99], v[52:55], a[128:135], v7\n"
        "ds_read_b128 v[112:115], v0 offset:3072\n"
        "v_add_f32 v92, v92, v93\n"
        "v_smfmac_f32_16x16x64_bf16 v[100:103], v[52:55], a[136:143], v7\n"
        "ds_read_b128 v[116:119], v0 offset:27648\n"
        "v_add_f32 v94, v94, v95\n"
        "v_smfmac_f32_16x16x64_bf16 v[96:99], v[56:59], a[144:151], v7\n"
        "v_add_f32 v88, v88, v89\n"
        "v_smfmac_f32_16x16x64_bf16 v[100:103], v[56:59], a[152:159], v7\n"
        "v_add_f32 v90, v90, v91\n"
        "v_smfmac_f32_16x16x64_bf16 v[96:99], v[60:63], a[160:167], v7\n"
        "v_cvt_pk_bf16_f32 v93, v92, v94\n"
        "v_smfmac_f32_16x16x64_bf16 v[100:103], v[60:63], a[168:175], v7\n"
        "v_cvt_pk_bf16_f32 v92, v88, v90\n"
        "v_smfmac_f32_16x16x64_bf16 v[96:99], v[64:67], a[176:183], v7\n"
        "v_smfmac_f32_16x16x64_bf16 v[100:103], v[64:67], a[184:191], v7\n"
        "v_mfma_f32_16x16x16_bf16 v[88:91], v[92:93], v[8:9], 0\n"
        "s_waitcnt lgkmcnt(2)\n"
        "v_smfmac_f32_16x16x64_bf16 v[96:99], v[68:71], v[104:111], v7\n"
        "ds_read_b128 v[104:107], v2 offset:4096\n"
        "s_waitcnt lgkmcnt(1)\n"
        "v_smfmac_f32_16x16x64_bf16 v[100:103], v[68:71], v[112:119], v7\n"
        "ds_read_b128 v[108:111], v2 offset:28672\n"
        "v_smfmac_f32_16x16x64_bf16 v[96:99], v[72:75], a[192:199], v7\n"
        "ds_read_b128 v[112:115], v2 offset:5120\n"
        "v_cvt_pk_bf16_f32 v120, v88, v89\n"
        "v_smfmac_f32_16x16x64_bf16 v[100:103], v[72:75], a[200:207], v7\n"
        "ds_read_b128 v[116:119], v2 offset:29696\n"
        "v_cvt_pk_bf16_f32 v121, v90, v91\n"
        "v_smfmac_f32_16x16x64_bf16 v[96:99], v[76:79], a[208:215], v7\n"
        "v_mov_b32 v88, 0\n"
        "v_smfmac_f32_16x16x64_bf16 v[100:103], v[76:79], a[216:223], v7\n"
        "v_mov_b32 v89, 0\n"
        "v_smfmac_f32_16x16x64_bf16 v[96:99], v[80:83], a[224:231], v7\n"
        "v_mov_b32 v90, 0\n"
        "v_smfmac_f32_16x16x64_bf16 v[100:103], v[80:83], a[232:239], v7\n"
        "v_mov_b32 v91, 0\n"
        "v_smfmac_f32_16x16x64_bf16 v[96:99], v[84:87], a[240:247], v7\n"
        "v_mov_b32 v92, 0\n"
        "v_smfmac_f32_16x16x64_bf16 v[100:103], v[84:87], a[248:255], v7\n"
        "v_mov_b32 v93, 0\n"
        "v_mov_b32 v94, 0\n"
        "v_mov_b32 v95, 0\n"
        "s_waitcnt lgkmcnt(2)\n"
        "v_smfmac_f32_16x16x64_bf16 v[88:91], v[48:51], v[104:111], v7\n"
        "ds_read_b128 v[104:107], v0 offset:4096\n"
        "s_waitcnt lgkmcnt(1)\n"
        "v_smfmac_f32_16x16x64_bf16 v[92:95], v[48:51], v[112:119], v7\n"
        "ds_read_b128 v[108:111], v0 offset:28672\n"
        "v_add_f32 v100, v100, v101\n"
        "v_smfmac_f32_16x16x64_bf16 v[88:91], v[52:55], v[128:135], v7\n"
        "ds_read_b128 v[112:115], v0 offset:5120\n"
        "v_add_f32 v102, v102, v103\n"
        "v_smfmac_f32_16x16x64_bf16 v[92:95], v[52:55], v[136:143], v7\n"
        "ds_read_b128 v[116:119], v0 offset:29696\n"
        "v_add_f32 v96, v96, v97\n"
        "v_smfmac_f32_16x16x64_bf16 v[88:91], v[56:59], v[144:151], v7\n"
        "v_add_f32 v98, v98, v99\n"
        "v_smfmac_f32_16x16x64_bf16 v[92:95], v[56:59], v[152:159], v7\n"
        "v_cvt_pk_bf16_f32 v101, v100, v102\n"
        "v_smfmac_f32_16x16x64_bf16 v[88:91], v[60:63], v[160:167], v7\n"
        "v_cvt_pk_bf16_f32 v100, v96, v98\n"
        "v_smfmac_f32_16x16x64_bf16 v[92:95], v[60:63], v[168:175], v7\n"
        "v_smfmac_f32_16x16x64_bf16 v[88:91], v[64:67], v[176:183], v7\n"
        "v_mfma_f32_16x16x16_bf16 v[96:99], v[100:101], v[8:9], 0\n"
        "v_smfmac_f32_16x16x64_bf16 v[92:95], v[64:67], v[184:191], v7\n"
        "s_waitcnt lgkmcnt(2)\n"
        "v_smfmac_f32_16x16x64_bf16 v[88:91], v[68:71], v[104:111], v7\n"
        "s_waitcnt lgkmcnt(0)\n"
        "v_smfmac_f32_16x16x64_bf16 v[92:95], v[68:71], v[112:119], v7\n"
        "v_smfmac_f32_16x16x64_bf16 v[88:91], v[72:75], v[192:199], v7\n"
        "v_smfmac_f32_16x16x64_bf16 v[92:95], v[72:75], v[200:207], v7\n"
        "v_smfmac_f32_16x16x64_bf16 v[88:91], v[76:79], v[208:215], v7\n"
        "v_cvt_pk_bf16_f32 v122, v96, v97\n"
        "v_smfmac_f32_16x16x64_bf16 v[92:95], v[76:79], v[216:223], v7\n"
        "v_cvt_pk_bf16_f32 v123, v98, v99\n"
        "s_nop 1\n"
        "v_mfma_f32_16x16x32_bf16 v[104:107], v[120:123], v[120:123], 0\n"
        "v_mfma_f32_16x16x32_bf16 v[108:111], v[36:39], v[120:123], 0\n"
        "v_mfma_f32_16x16x32_bf16 v[112:115], v[42:45], v[120:123], 0\n"
        "v_smfmac_f32_16x16x64_bf16 v[88:91], v[80:83], v[224:231], v7\n"
        "v_smfmac_f32_16x16x64_bf16 v[92:95], v[80:83], v[232:239], v7\n"
        "v_smfmac_f32_16x16x64_bf16 v[88:91], v[84:87], v[240:247], v7\n"
        "v_smfmac_f32_16x16x64_bf16 v[92:95], v[84:87], v[248:255], v7\n"
        "s_cmp_eq_u32 s45, 63\n"
        "s_nop 6\n"
        "v_add_f32 v92, v92, v93\n"
        "v_add_f32 v94, v94, v95\n"
        "v_add_f32 v88, v88, v89\n"
        "v_add_f32 v90, v90, v91\n"
        "v_cvt_pk_bf16_f32 v93, v92, v94\n"
        "v_cvt_pk_bf16_f32 v92, v88, v90\n"
        "s_nop 1\n"
        "v_mfma_f32_16x16x16_bf16 v[88:91], v[92:93], v[8:9], 0\n"
        "s_nop 7\n"
        "v_cvt_pk_bf16_f32 v124, v88, v89\n"
        "v_cvt_pk_bf16_f32 v125, v90, v91\n"
        "s_cbranch_scc1 Lqk_window\n"
        "s_nop 0\n"
        "v_mfma_f32_16x16x16_bf16 v[104:107], v[124:125], v[124:125], v[104:107]\n"
        "v_mfma_f32_16x16x16_bf16 v[108:111], v[40:41], v[124:125], v[108:111]\n"
        "v_mfma_f32_16x16x16_bf16 v[112:115], v[46:47], v[124:125], v[112:115]\n"
        "s_mov_b64 exec, s[62:63]\n"
        "s_nop 4\n"
        "ds_write_b128 v10, v[104:107] offset:96\n"
        "ds_write_b128 v10, v[108:111]\n"
        "ds_write_b128 v10, v[112:115] offset:64\n"
        "s_mov_b64 exec, -1\n"
        "s_waitcnt lgkmcnt(0)\n"
        "s_barrier\n"
        "ds_read2st64_b64 v[48:51], v11 offset0:0 offset1:3\n"
        "ds_read2st64_b64 v[52:55], v11 offset0:6 offset1:9\n"
        "s_waitcnt lgkmcnt(1)\n"
        "v_pk_add_f32 v[48:49], v[48:49], v[50:51]\n"
        "v_pk_add_f32 v[48:49], v[48:49], v[12:13]\n"
        "s_waitcnt lgkmcnt(0)\n"
        "v_pk_add_f32 v[52:53], v[52:53], v[54:55]\n"
        "v_pk_add_f32 v[48:49], v[48:49], v[52:53]\n"
        "v_max_f32 v50, v48, v49\n"
        "s_nop 1\n"
        "v_max_f32_dpp v50, v50, v50 row_ror:8 row_mask:0xf bank_mask:0xf\n"
        "s_nop 1\n"
        "v_max_f32_dpp v50, v50, v50 row_ror:4 row_mask:0xf bank_mask:0xf\n"
        "s_nop 1\n"
        "v_max_f32_dpp v50, v50, v50 row_ror:2 row_mask:0xf bank_mask:0xf\n"
        "s_nop 1\n"
        "v_max_f32_dpp v50, v50, v50 row_ror:1 row_mask:0xf bank_mask:0xf\n"
        "v_mov_b32 v51, v50\n"
        "s_nop 1\n"
        "v_permlane32_swap_b32 v50, v51\n"
        "v_max_f32 v50, v50, v51\n"
        "v_mul_f32 v50, s61, v50\n"
        "v_fma_f32 v48, v48, s61, -v50\n"
        "v_fma_f32 v49, v49, s61, -v50\n"
        "v_exp_f32 v48, v48\n"
        "v_exp_f32 v49, v49\n"
        "s_nop 0\n"
        "v_add_f32 v50, v48, v49\n"
        "s_nop 1\n"
        "v_add_f32_dpp v50, v50, v50 row_ror:8 row_mask:0xf bank_mask:0xf\n"
        "s_nop 1\n"
        "v_add_f32_dpp v50, v50, v50 row_ror:4 row_mask:0xf bank_mask:0xf\n"
        "s_nop 1\n"
        "v_add_f32_dpp v50, v50, v50 row_ror:2 row_mask:0xf bank_mask:0xf\n"
        "s_nop 1\n"
        "v_add_f32_dpp v50, v50, v50 row_ror:1 row_mask:0xf bank_mask:0xf\n"
        "v_mov_b32 v51, v50\n"
        "s_nop 1\n"
        "v_permlane32_swap_b32 v50, v51\n"
        "v_add_f32 v50, v50, v51\n"
        "v_rcp_f32 v50, v50\n"
        "s_nop 0\n"
        "v_mul_f32 v48, v48, v50\n"
        "v_mul_f32 v49, v49, v50\n"
        "v_cvt_pk_bf16_f32 v48, v48, v49\n"
        "s_mov_b64 exec, s[64:65]\n"
        "global_store_dword v14, v48, s[50:51] offset:0 sc1\n"
        "global_store_dword v14, v48, s[50:51] offset:640 sc1\n"
        "global_store_dword v14, v48, s[50:51] offset:1280 sc1\n"
        "global_store_dword v14, v48, s[50:51] offset:1920 sc1\n"
        "s_mov_b64 exec, -1\n"
        "Lqk_window:\n"
        "s_memrealtime s[92:93]\n"
        "s_add_u32 s47, s60, 1\n"
        "s_and_b32 s60, s60, 0xffffffc0\n"
        "s_sub_u32 s68, s59, s46\n"
        "s_cmp_gt_u32 s68, 1\n"
        "s_cselect_b32 s60, s47, s60\n"
        "s_and_b32 s68, s60, 63\n"
        "s_cmp_eq_u32 s68, 2\n"
        "s_cselect_b32 s68, 126, 0\n"
        "s_add_u32 s60, s60, s68\n"
        "s_min_u32 s60, s60, 1920\n"
        "v_mfma_f32_16x16x32_bf16 v[56:59], v[120:123], v[24:27], 0\n"
        "v_mfma_f32_16x16x32_bf16 v[60:63], v[36:39], v[24:27], 0\n"
        "v_mfma_f32_16x16x32_bf16 v[64:67], v[120:123], v[30:33], 0\n"
        "v_mfma_f32_16x16x32_bf16 v[68:71], v[36:39], v[30:33], 0\n"
        "v_mfma_f32_16x16x32_bf16 v[72:75], v[42:45], v[30:33], 0\n"
        "v_mfma_f32_16x16x16_bf16 v[56:59], v[124:125], v[28:29], v[56:59]\n"
        "v_mfma_f32_16x16x16_bf16 v[60:63], v[40:41], v[28:29], v[60:63]\n"
        "v_mfma_f32_16x16x16_bf16 v[64:67], v[124:125], v[34:35], v[64:67]\n"
        "v_mfma_f32_16x16x16_bf16 v[68:71], v[40:41], v[34:35], v[68:71]\n"
        "v_mfma_f32_16x16x16_bf16 v[72:75], v[46:47], v[34:35], v[72:75]\n"
        "ds_read_b128 v[24:27], v1 offset:0\n"
        "ds_read_b128 v[28:31], v1 offset:1024\n"
        "s_barrier\n"
        "s_nop 0\n"
        "ds_write_b128 v16, v[56:59] offset:96\n"
        "ds_write_b128 v16, v[60:63] offset:0\n"
        "ds_write_b128 v16, v[64:67] offset:2912\n"
        "ds_write_b128 v16, v[68:71] offset:2816\n"
        "ds_write_b128 v16, v[72:75] offset:2880\n"
        "s_and_b32 s68, s58, 1\n"
        "s_lshl_b32 s68, s68, 18\n"
        "s_lshr_b32 s69, s89, 5\n"
        "s_and_b32 s69, s69, 1\n"
        "s_lshl_b32 s69, s69, 13\n"
        "s_add_u32 s47, s68, 0x2a00000\n"
        "s_add_u32 s47, s47, s69\n"
        "s_add_u32 s52, s90, s47\n"
        "s_addc_u32 s53, s91, 0\n"
        "s_waitcnt lgkmcnt(0)\n"
        "s_sub_u32 s35, s92, s38\n"
        "s_min_u32 s35, s35, 200\n"
        "s_cmp_eq_u32 s58, 0\n"
        "s_cselect_b32 s35, 93, s35\n"
        "s_barrier\n"
        "s_add_u32 s45, s45, 2\n"
        "s_cmp_lt_u32 s45, 64\n"
        "s_cbranch_scc1 Lqk_loop\n"
        "ds_read_b128 v[96:99], v17 offset:0\n"
        "ds_read_b128 v[100:103], v18 offset:0\n"
        "ds_read_b128 v[104:107], v17 offset:5632\n"
        "ds_read_b128 v[108:111], v18 offset:5632\n"
        "ds_read_b128 v[112:115], v17 offset:11264\n"
        "ds_read_b128 v[116:119], v18 offset:11264\n"
        "ds_read_b128 v[40:43], v17 offset:16896\n"
        "ds_read_b128 v[44:47], v18 offset:16896\n"
        "s_waitcnt lgkmcnt(4)\n"
        "v_add_f32 v96, v96, v104\n"
        "v_add_f32 v97, v97, v105\n"
        "v_add_f32 v98, v98, v106\n"
        "v_add_f32 v99, v99, v107\n"
        "v_add_f32 v100, v100, v108\n"
        "v_add_f32 v101, v101, v109\n"
        "v_add_f32 v102, v102, v110\n"
        "v_add_f32 v103, v103, v111\n"
        "s_waitcnt lgkmcnt(2)\n"
        "v_add_f32 v96, v96, v112\n"
        "v_add_f32 v97, v97, v113\n"
        "v_add_f32 v98, v98, v114\n"
        "v_add_f32 v99, v99, v115\n"
        "v_add_f32 v100, v100, v116\n"
        "v_add_f32 v101, v101, v117\n"
        "v_add_f32 v102, v102, v118\n"
        "v_add_f32 v103, v103, v119\n"
        "s_waitcnt lgkmcnt(0)\n"
        "v_add_f32 v96, v96, v40\n"
        "v_add_f32 v97, v97, v41\n"
        "v_add_f32 v98, v98, v42\n"
        "v_add_f32 v99, v99, v43\n"
        "v_add_f32 v100, v100, v44\n"
        "v_add_f32 v101, v101, v45\n"
        "v_add_f32 v102, v102, v46\n"
        "v_add_f32 v103, v103, v47\n"
        "v_mul_f32 v96, s61, v96\n"
        "v_mul_f32 v97, s61, v97\n"
        "v_mul_f32 v98, s61, v98\n"
        "v_mul_f32 v99, s61, v99\n"
        "v_mul_f32 v100, s61, v100\n"
        "v_mul_f32 v101, s61, v101\n"
        "v_mul_f32 v102, s61, v102\n"
        "v_mul_f32 v103, s61, v103\n"
        "v_cndmask_b32 v96, v20, v96, s[70:71]\n"
        "v_cndmask_b32 v97, v20, v97, s[72:73]\n"
        "v_cndmask_b32 v98, v20, v98, s[74:75]\n"
        "v_cndmask_b32 v99, v20, v99, s[76:77]\n"
        "v_cndmask_b32 v100, v20, v100, s[78:79]\n"
        "v_cndmask_b32 v101, v20, v101, s[80:81]\n"
        "v_cndmask_b32 v102, v20, v102, s[82:83]\n"
        "v_cndmask_b32 v103, v20, v103, s[84:85]\n"
        "v_max3_f32 v104, v96, v97, v98\n"
        "v_max3_f32 v105, v99, v100, v101\n"
        "v_max3_f32 v104, v104, v102, v103\n"
        "v_max_f32 v104, v104, v105\n"
        "s_nop 1\n"
        "v_max_f32_dpp v104, v104, v104 quad_perm:[1,0,3,2] row_mask:0xf bank_mask:0xf\n"
        "s_nop 1\n"
        "v_max_f32_dpp v104, v104, v104 quad_perm:[2,3,0,1] row_mask:0xf bank_mask:0xf\n"
        "s_nop 1\n"
        "v_max_f32_dpp v104, v104, v104 row_half_mirror row_mask:0xf bank_mask:0xf\n"
        "v_sub_f32 v96, v96, v104\n"
        "v_sub_f32 v97, v97, v104\n"
        "v_sub_f32 v98, v98, v104\n"
        "v_sub_f32 v99, v99, v104\n"
        "v_sub_f32 v100, v100, v104\n"
        "v_sub_f32 v101, v101, v104\n"
        "v_sub_f32 v102, v102, v104\n"
        "v_sub_f32 v103, v103, v104\n"
        "v_exp_f32 v96, v96\n"
        "v_exp_f32 v97, v97\n"
        "v_exp_f32 v98, v98\n"
        "v_exp_f32 v99, v99\n"
        "v_exp_f32 v100, v100\n"
        "v_exp_f32 v101, v101\n"
        "v_exp_f32 v102, v102\n"
        "v_exp_f32 v103, v103\n"
        "v_add_f32 v106, v96, v97\n"
        "v_add_f32 v107, v98, v99\n"
        "v_add_f32 v108, v100, v101\n"
        "v_add_f32 v109, v102, v103\n"
        "v_add_f32 v106, v106, v107\n"
        "v_add_f32 v108, v108, v109\n"
        "v_add_f32 v104, v106, v108\n"
        "s_nop 1\n"
        "v_add_f32_dpp v104, v104, v104 quad_perm:[1,0,3,2] row_mask:0xf bank_mask:0xf\n"
        "s_nop 1\n"
        "v_add_f32_dpp v104, v104, v104 quad_perm:[2,3,0,1] row_mask:0xf bank_mask:0xf\n"
        "s_nop 1\n"
        "v_add_f32_dpp v104, v104, v104 row_half_mirror row_mask:0xf bank_mask:0xf\n"
        "v_rcp_f32 v104, v104\n"
        "s_nop 0\n"
        "v_mul_f32 v96, v96, v104\n"
        "v_mul_f32 v97, v97, v104\n"
        "v_mul_f32 v98, v98, v104\n"
        "v_mul_f32 v99, v99, v104\n"
        "v_mul_f32 v100, v100, v104\n"
        "v_mul_f32 v101, v101, v104\n"
        "v_mul_f32 v102, v102, v104\n"
        "v_mul_f32 v103, v103, v104\n"
        "v_cvt_pk_bf16_f32 v112, v96, v97\n"
        "v_cvt_pk_bf16_f32 v113, v98, v99\n"
        "v_cvt_pk_bf16_f32 v114, v100, v101\n"
        "v_cvt_pk_bf16_f32 v115, v102, v103\n"
        "s_mov_b64 exec, s[66:67]\n"
        "global_store_dwordx4 v19, v[112:115], s[52:53] sc1\n"
        "s_mov_b64 exec, -1\n"
        "s_waitcnt vmcnt(0)\n"
        "s_barrier\n"
        "s_add_u32 s47, s58, 1\n"
        "v_mov_b32 v22, s47\n"
        "s_lshl_b32 s47, s40, 7\n"
        "v_mov_b32 v126, s47\n"
        "s_and_b32 exec_lo, s36, 1\n"
        "s_mov_b32 exec_hi, 0\n"
        "global_store_dword v126, v22, s[86:87] sc1\n"
        "s_mov_b64 exec, -1\n"
        "s_mov_b32 s88, 0\n"
        "s_branch Lg3_entry\n"
        "Lqk_exit:\n"
        "s_waitcnt vmcnt(0) lgkmcnt(0)\n"
        "s_endpgm\n"
        "Lqk_rs_1:\n"
        "s_sleep 4\n"
        "global_load_dword v4, v15, s[90:91] sc1\n"
        "s_sub_u32 s46, s46, 1\n"
        "s_cbranch_scc1 Lqk_exit\n"
        "s_waitcnt vmcnt(0)\n"
        "v_readfirstlane_b32 s47, v4\n"
        "s_nop 3\n"
        "s_and_b32 s68, s47, -2\n"
        "s_cmp_lg_u32 s68, s88\n"
        "s_cbranch_scc1 Lqk_rs_1\n"
        "s_branch Lqk_rb_2\n"
        "Lqk_hs_3:\n"
        "s_sleep 4\n"
        "global_load_dword v126, v15, s[94:95] sc1\n"
        "s_sub_u32 s46, s46, 1\n"
        "s_cbranch_scc1 Lqk_exit\n"
        "s_waitcnt vmcnt(0)\n"
        "v_and_b32 v127, 63, v126\n"
        "v_cmp_ne_u32 vcc, s88, v127\n"
        "s_cbranch_vccnz Lqk_hs_3\n"
        "s_branch Lqk_hb_4\n"
        "Lqk_first:\n"
        "s_barrier\n"
        "s_lshl_b32 s47, s41, 2\n"
        "s_add_u32 s47, s47, s42\n"
        "s_lshl_b32 s47, s47, 7\n"
        "s_add_u32 s47, s47, 0xaa8400\n"
        "v_mov_b32 v32, s47\n"
        "v_mov_b32 v33, 1\n"
        "s_and_b32 exec_lo, s36, 1\n"
        "s_mov_b32 exec_hi, 0\n"
        "global_atomic_add v32, v33, s[90:91] sc1\n"
        "s_mov_b64 exec, -1\n"
        "s_lshr_b32 s58, s45, 1\n"
        "s_add_u32 s68, s45, 2\n"
        "s_min_u32 s68, s68, 63\n"
        "s_mul_i32 s68, s68, 0xa000\n"
        "s_add_u32 s47, s68, 0x200000\n"
        "s_add_u32 s48, s90, s47\n"
        "s_addc_u32 s49, s91, 0\n"
        "global_load_dword v126, v15, s[94:95] sc1\n"
        "s_add_u32 s47, s96, 0\n"
        "s_add_u32 s54, s48, s47\n"
        "s_addc_u32 s55, s49, 0\n"
        "s_add_u32 m0, s47, 0\n"
        "s_nop 0\n"
        "global_load_lds_dwordx4 v1, s[54:55] offset:0 sc1\n"
        "global_load_lds_dwordx4 v1, s[54:55] offset:1024 sc1\n"
        "global_load_lds_dwordx4 v1, s[54:55] offset:2048 sc1\n"
        "global_load_lds_dwordx4 v1, s[54:55] offset:3072 sc1\n"
        "s_add_u32 s47, s96, 16384\n"
        "s_add_u32 s54, s48, s47\n"
        "s_addc_u32 s55, s49, 0\n"
        "s_add_u32 m0, s47, 0\n"
        "s_nop 0\n"
        "global_load_lds_dwordx4 v1, s[54:55] offset:0 sc1\n"
        "global_load_lds_dwordx4 v1, s[54:55] offset:1024 sc1\n"
        "global_load_lds_dwordx4 v1, s[54:55] offset:2048 sc1\n"
        "global_load_lds_dwordx4 v1, s[54:55] offset:3072 sc1\n"
        "s_lshr_b32 s58, s45, 1\n"
        "s_and_b32 s68, s58, 3\n"
        "s_lshl_b32 s68, s68, 17\n"
        "s_add_u32 s68, s68, s44\n"
        "s_add_u32 s68, s68, 0x180000\n"
        "s_cmp_eq_u32 s45, 0\n"
        "s_cselect_b32 s68, 0xa80000, s68\n"
        "s_add_u32 s47, s68, 0x0\n"
        "s_add_u32 s56, s90, s47\n"
        "s_addc_u32 s57, s91, 0\n"
        "s_add_u32 s50, s90, 0x2b40000\n"
        "s_addc_u32 s51, s91, 0\n"
        "s_mov_b64 exec, s[64:65]\n"
        "global_store_dword v14, v21, s[50:51] offset:0 sc1\n"
        "global_store_dword v14, v21, s[50:51] offset:640 sc1\n"
        "global_store_dword v14, v21, s[50:51] offset:1280 sc1\n"
        "global_store_dword v14, v21, s[50:51] offset:1920 sc1\n"
        "s_mov_b64 exec, -1\n"
        "s_add_u32 s50, s90, 0x2b00000\n"
        "s_addc_u32 s51, s91, 0\n"
        "v_cvt_pk_bf16_f32 v24, v48, v49\n"
        "v_cvt_pk_bf16_f32 v25, v50, v51\n"
        "v_cvt_pk_bf16_f32 v30, v52, v53\n"
        "v_cvt_pk_bf16_f32 v31, v54, v55\n"
        "v_cvt_pk_bf16_f32 v26, v56, v57\n"
        "v_cvt_pk_bf16_f32 v27, v58, v59\n"
        "s_add_u32 s88, s45, 1\n"
        "s_lshr_b32 s88, s88, 1\n"
        "s_waitcnt vmcnt(12)\n"
        "v_and_b32 v127, 63, v126\n"
        "v_cmp_ne_u32 vcc, s88, v127\n"
        "s_cmp_eq_u32 s45, 0\n"
        "s_cselect_b64 vcc, 0, vcc\n"
        "s_cbranch_vccnz Lqk_hs_11\n"
        "Lqk_hb_12:\n"
        "v_readfirstlane_b32 s34, v126\n"
        "s_nop 1\n"
        "v_subrev_u32 v126, s34, v126\n"
        "s_nop 1\n"
        "v_max_i32_dpp v126, v126, v126 row_ror:8 row_mask:0xf bank_mask:0xf\n"
        "s_nop 1\n"
        "v_max_i32_dpp v126, v126, v126 row_ror:4 row_mask:0xf bank_mask:0xf\n"
        "s_nop 1\n"
        "v_max_i32_dpp v126, v126, v126 row_ror:2 row_mask:0xf bank_mask:0xf\n"
        "s_nop 1\n"
        "v_max_i32_dpp v126, v126, v126 row_ror:1 row_mask:0xf bank_mask:0xf\n"
        "s_nop 0\n"
        "v_readfirstlane_b32 s68, v126\n"
        "v_readlane_b32 s69, v126, 16\n"
        "s_max_i32 s68, s68, s69\n"
        "s_add_u32 s34, s34, s68\n"
        "s_add_u32 s34, s34, 4928\n"
        "s_add_u32 s34, s34, s60\n"
        "s_branch Lqk_crit\n"
        "Lqk_gm_5:\n"
        "global_load_dwordx4 v[48:51], v3, s[56:57] offset:-2048 sc1\n"
        "global_load_dwordx4 v[52:55], v3, s[56:57] offset:2048 sc1\n"
        "global_load_dword v56, v4, s[56:57] sc1\n"
        "global_load_dword v57, v4, s[56:57] offset:256 sc1\n"
        "s_branch Lqk_gb_10\n"
        "Lqk_hs_11:\n"
        "s_sleep 4\n"
        "global_load_dword v126, v15, s[94:95] sc1\n"
        "s_sub_u32 s46, s46, 1\n"
        "s_cbranch_scc1 Lqk_exit\n"
        "s_waitcnt vmcnt(0)\n"
        "v_and_b32 v127, 63, v126\n"
        "v_cmp_ne_u32 vcc, s88, v127\n"
        "s_cbranch_vccnz Lqk_hs_11\n"
        "s_branch Lqk_hb_12\n"
            :: "s"(ws), "s"(wq), "s"(wk), "s"(x), "s"(HEAD_Q(r)), "s"(par), "s"(xq), "s"(wave), "s"(karg) : "v0", "v1", "v2", "v3", "v4", "v5", "v6", "v7", "v8", "v9", "v10", "v11", "v12", "v13", "v14", "v15", "v16", "v17", "v18", "v19", "v20", "v21", "v22", "v23", "v24", "v25", "v26", "v27", "v28", "v29", "v30", "v31", "v32", "v33", "v34", "v35", "v36", "v37", "v38", "v39", "v40", "v41", "v42", "v43", "v44", "v45", "v46", "v47", "v48", "v49", "v50", "v51", "v52", "v53", "v54", "v55", "v56", "v57", "v58", "v59", "v60", "v61", "v62", "v63", "v64", "v65", "v66", "v67", "v68", "v69", "v70", "v71", "v72", "v73", "v74", "v75", "v76", "v77", "v78", "v79", "v80", "v81", "v82", "v83", "v84", "v85", "v86", "v87", "v88", "v89", "v90", "v91", "v92", "v93", "v94", "v95", "v96", "v97", "v98", "v99", "v100", "v101", "v102", "v103", "v104", "v105", "v106", "v107", "v108", "v109", "v110", "v111", "v112", "v113", "v114", "v115", "v116", "v117", "v118", "v119", "v120", "v121", "v122", "v123", "v124", "v125", "v126", "v127", "v128", "v129", "v130", "v131", "v132", "v133", "v134", "v135", "v136", "v137", "v138", "v139", "v140", "v141", "v142", "v143", "v144", "v145", "v146", "v147", "v148", "v149", "v150", "v151", "v152", "v153", "v154", "v155", "v156", "v157", "v158", "v159", "v160", "v161", "v162", "v163", "v164", "v165", "v166", "v167", "v168", "v169", "v170", "v171", "v172", "v173", "v174", "v175", "v176", "v177", "v178", "v179", "v180", "v181", "v182", "v183", "v184", "v185", "v186", "v187", "v188", "v189", "v190", "v191", "v192", "v193", "v194", "v195", "v196", "v197", "v198", "v199", "v200", "v201", "v202", "v203", "v204", "v205", "v206", "v207", "v208", "v209", "v210", "v211", "v212", "v213", "v214", "v215", "v216", "v217", "v218", "v219", "v220", "v221", "v222", "v223", "v224", "v225", "v226", "v227", "v228", "v229", "v230", "v231", "v232", "v233", "v234", "v235", "v236", "v237", "v238", "v239", "v240", "v241", "v242", "v243", "v244", "v245", "v246", "v247", "v248", "v249", "v250", "v251", "v252", "v253", "v254", "v255", "a0", "a1", "a2", "a3", "a4", "a5", "a6", "a7", "a8", "a9", "a10", "a11", "a12", "a13", "a14", "a15", "a16", "a17", "a18", "a19", "a20", "a21", "a22", "a23", "a24", "a25", "a26", "a27", "a28", "a29", "a30", "a31", "a32", "a33", "a34", "a35", "a36", "a37", "a38", "a39", "a40", "a41", "a42", "a43", "a44", "a45", "a46", "a47", "a48", "a49", "a50", "a51", "a52", "a53", "a54", "a55", "a56", "a57", "a58", "a59", "a60", "a61", "a62", "a63", "a64", "a65", "a66", "a67", "a68", "a69", "a70", "a71", "a72", "a73", "a74", "a75", "a76", "a77", "a78", "a79", "a80", "a81", "a82", "a83", "a84", "a85", "a86", "a87", "a88", "a89", "a90", "a91", "a92", "a93", "a94", "a95", "a96", "a97", "a98", "a99", "a100", "a101", "a102", "a103", "a104", "a105", "a106", "a107", "a108", "a109", "a110", "a111", "a112", "a113", "a114", "a115", "a116", "a117", "a118", "a119", "a120", "a121", "a122", "a123", "a124", "a125", "a126", "a127", "a128", "a129", "a130", "a131", "a132", "a133", "a134", "a135", "a136", "a137", "a138", "a139", "a140", "a141", "a142", "a143", "a144", "a145", "a146", "a147", "a148", "a149", "a150", "a151", "a152", "a153", "a154", "a155", "a156", "a157", "a158", "a159", "a160", "a161", "a162", "a163", "a164", "a165", "a166", "a167", "a168", "a169", "a170", "a171", "a172", "a173", "a174", "a175", "a176", "a177", "a178", "a179", "a180", "a181", "a182", "a183", "a184", "a185", "a186", "a187", "a188", "a189", "a190", "a191", "a192", "a193", "a194", "a195", "a196", "a197", "a198", "a199", "a200", "a201", "a202", "a203", "a204", "a205", "a206", "a207", "a208", "a209", "a210", "a211", "a212", "a213", "a214", "a215", "a216", "a217", "a218", "a219", "a220", "a221", "a222", "a223", "a224", "a225", "a226", "a227", "a228", "a229", "a230", "a231", "a232", "a233", "a234", "a235", "a236", "a237", "a238", "a239", "a240", "a241", "a242", "a243", "a244", "a245", "a246", "a247", "a248", "a249", "a250", "a251", "a252", "a253", "a254", "a255", "s34", "s35", "s36", "s37", "s38", "s39", "s40", "s41", "s42", "s43", "s44", "s45", "s46", "s47", "s48", "s49", "s50", "s51", "s52", "s53", "s54", "s55", "s56", "s57", "s58", "s59", "s60", "s61", "s62", "s63", "s64", "s65", "s66", "s67", "s68", "s69", "s70", "s71", "s72", "s73", "s74", "s75", "s76", "s77", "s78", "s79", "s80", "s81", "s82", "s83", "s84", "s85", "s86", "s87", "s88", "s89", "s90", "s91", "s92", "s93", "s94", "s95", "s96", "s97", "s98", "s99", "vcc", "scc", "memory");
        __builtin_amdgcn_endpgm();
    } else if (r < 64) {
        asm volatile(
        "s_mov_b64 s[48:49], %8\n"
        "s_mov_b64 s[90:91], %0\n"
        "s_mov_b64 s[92:93], %1\n"
        "s_mov_b64 s[94:95], %2\n"
        "s_mov_b64 s[96:97], %3\n"
        "s_mov_b32 s40, %4\n"
        "s_mov_b32 s41, %5\n"
        "s_mov_b32 s42, %6\n"
        "s_mov_b32 s43, %7\n"
        "s_mov_b64 s[0:1], s[48:49]\n"
        "s_mov_b64 exec, -1\n"
        "s_mov_b32 s45, s41\n"
        "s_mov_b32 s46, 0x200000\n"
        "v_mbcnt_lo_u32_b32 v0, -1, 0\n"
        "v_mbcnt_hi_u32_b32 v0, -1, v0\n"
        "v_lshlrev_b32 v1, 4, v0\n"
        "s_lshl_b32 s47, s41, 6\n"
        "s_add_u32 s47, s47, s40\n"
        "s_add_u32 s47, s47, 32\n"
        "s_lshl_b32 s47, s47, 5\n"
        "s_add_u32 s47, s47, s43\n"
        "s_lshl_b32 s47, s47, 2\n"
        "s_add_u32 s47, s47, 0xab8000\n"
        "v_mov_b32 v15, s47\n"
        "global_load_dword v26, v15, s[90:91] sc1\n"
        "v_and_b32 v28, 15, v0\n"
        "s_lshl_b32 s47, s41, 5\n"
        "v_add_u32 v28, s47, v28\n"
        "v_mul_u32_u24 v28, 1280, v28\n"
        "v_lshrrev_b32 v29, 4, v0\n"
        "v_lshl_add_u32 v28, v29, 4, v28\n"
        "s_mul_i32 s47, s43, 0x140\n"
        "v_add_u32 v28, s47, v28\n"
        "v_add_u32 v29, 0x5000, v28\n"
        "global_load_dwordx4 v[64:67], v28, s[96:97] offset:0\n"
        "global_load_dwordx4 v[68:71], v29, s[96:97] offset:0\n"
        "global_load_dwordx4 v[72:75], v28, s[96:97] offset:64\n"
        "global_load_dwordx4 v[76:79], v29, s[96:97] offset:64\n"
        "global_load_dwordx4 v[80:83], v28, s[96:97] offset:128\n"
        "global_load_dwordx4 v[84:87], v29, s[96:97] offset:128\n"
        "global_load_dwordx4 v[88:91], v28, s[96:97] offset:192\n"
        "global_load_dwordx4 v[92:95], v29, s[96:97] offset:192\n"
        "global_load_dwordx4 v[116:119], v28, s[96:97] offset:256\n"
        "global_load_dwordx4 v[120:123], v29, s[96:97] offset:256\n"
        "s_sleep 4\n"
        "s_mul_i32 s47, s43, 0x1800\n"
        "s_add_u32 s47, s47, 0x10000\n"
        "v_add_u32 v2, s47, v1\n"
        "v_add_u32 v23, 0xc000, v2\n"
        "s_lshl_b32 s96, s43, 12\n"
        "s_lshl_b32 s97, s43, 11\n"
        "v_lshrrev_b32 v124, 3, v0\n"
        "s_lshl_b32 s47, s43, 3\n"
        "v_add_u32 v124, s47, v124\n"
        "v_bfe_u32 v125, v0, 4, 1\n"
        "s_and_b32 s47, s43, 1\n"
        "s_lshl_b32 s47, s47, 1\n"
        "v_or_b32 v125, s47, v125\n"
        "v_lshlrev_b32 v125, 1, v125\n"
        "v_and_b32 v126, 7, v0\n"
        "v_xor_b32 v125, v125, v126\n"
        "v_lshlrev_b32 v125, 4, v125\n"
        "s_mul_i32 s47, s40, 0x180\n"
        "v_mul_u32_u24 v126, 0x3000, v124\n"
        "v_add3_u32 v3, v126, v125, s47\n"
        "s_mul_i32 s47, s40, 0x3c000\n"
        "v_mul_u32_u24 v126, 0x500, v124\n"
        "v_add3_u32 v4, v126, v125, s47\n"
        "v_lshrrev_b32 v124, 4, v0\n"
        "v_lshlrev_b32 v124, 10, v124\n"
        "v_bfe_u32 v125, v0, 2, 2\n"
        "v_lshl_add_u32 v124, v125, 7, v124\n"
        "v_bfe_u32 v125, v0, 3, 2\n"
        "v_lshl_add_u32 v124, v125, 5, v124\n"
        "v_and_b32 v125, 3, v0\n"
        "v_lshl_add_u32 v124, v125, 3, v124\n"
        "s_mul_i32 s47, s43, 0x3\n"
        "s_add_u32 s47, s47, 0\n"
        "s_and_b32 s68, s47, 3\n"
        "s_lshl_b32 s68, s68, 5\n"
        "s_lshr_b32 s47, s47, 2\n"
        "s_lshl_b32 s47, s47, 12\n"
        "v_xor_b32 v5, s68, v124\n"
        "v_add_u32 v5, s47, v5\n"
        "s_mul_i32 s47, s43, 0x3\n"
        "s_add_u32 s47, s47, 1\n"
        "s_and_b32 s68, s47, 3\n"
        "s_lshl_b32 s68, s68, 5\n"
        "s_lshr_b32 s47, s47, 2\n"
        "s_lshl_b32 s47, s47, 12\n"
        "v_xor_b32 v6, s68, v124\n"
        "v_add_u32 v6, s47, v6\n"
        "s_mul_i32 s47, s43, 0x3\n"
        "s_add_u32 s47, s47, 2\n"
        "s_and_b32 s68, s47, 3\n"
        "s_lshl_b32 s68, s68, 5\n"
        "s_lshr_b32 s47, s47, 2\n"
        "s_lshl_b32 s47, s47, 12\n"
        "v_xor_b32 v7, s68, v124\n"
        "v_add_u32 v7, s47, v7\n"
        "s_mul_i32 s47, s43, 0xa\n"
        "s_add_u32 s47, s47, 0\n"
        "s_and_b32 s68, s47, 3\n"
        "s_lshl_b32 s68, s68, 5\n"
        "s_lshr_b32 s47, s47, 2\n"
        "s_lshl_b32 s47, s47, 12\n"
        "v_xor_b32 v8, s68, v124\n"
        "v_add_u32 v8, s47, v8\n"
        "s_mul_i32 s47, s43, 0xa\n"
        "s_add_u32 s47, s47, 1\n"
        "s_and_b32 s68, s47, 3\n"
        "s_lshl_b32 s68, s68, 5\n"
        "s_lshr_b32 s47, s47, 2\n"
        "s_lshl_b32 s47, s47, 12\n"
        "v_xor_b32 v9, s68, v124\n"
        "v_add_u32 v9, s47, v9\n"
        "s_mul_i32 s47, s43, 0xa\n"
        "s_add_u32 s47, s47, 2\n"
        "s_and_b32 s68, s47, 3\n"
        "s_lshl_b32 s68, s68, 5\n"
        "s_lshr_b32 s47, s47, 2\n"
        "s_lshl_b32 s47, s47, 12\n"
        "v_xor_b32 v10, s68, v124\n"
        "v_add_u32 v10, s47, v10\n"
        "s_mul_i32 s47, s43, 0xa\n"
        "s_add_u32 s47, s47, 3\n"
        "s_and_b32 s68, s47, 3\n"
        "s_lshl_b32 s68, s68, 5\n"
        "s_lshr_b32 s47, s47, 2\n"
        "s_lshl_b32 s47, s47, 12\n"
        "v_xor_b32 v11, s68, v124\n"
        "v_add_u32 v11, s47, v11\n"
        "s_mul_i32 s47, s43, 0xa\n"
        "s_add_u32 s47, s47, 4\n"
        "s_and_b32 s68, s47, 3\n"
        "s_lshl_b32 s68, s68, 5\n"
        "s_lshr_b32 s47, s47, 2\n"
        "s_lshl_b32 s47, s47, 12\n"
        "v_xor_b32 v12, s68, v124\n"
        "v_add_u32 v12, s47, v12\n"
        "s_mul_i32 s47, s43, 0xa\n"
        "s_add_u32 s47, s47, 5\n"
        "s_and_b32 s68, s47, 3\n"
        "s_lshl_b32 s68, s68, 5\n"
        "s_lshr_b32 s47, s47, 2\n"
        "s_lshl_b32 s47, s47, 12\n"
        "v_xor_b32 v13, s68, v124\n"
        "v_add_u32 v13, s47, v13\n"
        "s_mul_i32 s47, s43, 0xa\n"
        "s_add_u32 s47, s47, 6\n"
        "s_and_b32 s68, s47, 3\n"
        "s_lshl_b32 s68, s68, 5\n"
        "s_lshr_b32 s47, s47, 2\n"
        "s_lshl_b32 s47, s47, 12\n"
        "v_xor_b32 v14, s68, v124\n"
        "v_add_u32 v14, s47, v14\n"
        "s_mul_i32 s47, s43, 0xa\n"
        "s_add_u32 s47, s47, 7\n"
        "s_and_b32 s68, s47, 3\n"
        "s_lshl_b32 s68, s68, 5\n"
        "s_lshr_b32 s47, s47, 2\n"
        "s_lshl_b32 s47, s47, 12\n"
        "v_xor_b32 v16, s68, v124\n"
        "v_add_u32 v16, s47, v16\n"
        "s_mul_i32 s47, s43, 0xa\n"
        "s_add_u32 s47, s47, 8\n"
        "s_and_b32 s68, s47, 3\n"
        "s_lshl_b32 s68, s68, 5\n"
        "s_lshr_b32 s47, s47, 2\n"
        "s_lshl_b32 s47, s47, 12\n"
        "v_xor_b32 v17, s68, v124\n"
        "v_add_u32 v17, s47, v17\n"
        "s_mul_i32 s47, s43, 0xa\n"
        "s_add_u32 s47, s47, 9\n"
        "s_and_b32 s68, s47, 3\n"
        "s_lshl_b32 s68, s68, 5\n"
        "s_lshr_b32 s47, s47, 2\n"
        "s_lshl_b32 s47, s47, 12\n"
        "v_xor_b32 v18, s68, v124\n"
        "v_add_u32 v18, s47, v18\n"
        "s_lshl_b32 s59, s43, 10\n"
        "s_add_u32 s54, s94, 0x14000\n"
        "s_addc_u32 s55, s95, 0\n"
        "s_add_u32 m0, s59, 0xa000\n"
        "s_nop 0\n"
        "global_load_lds_dwordx4 v4, s[54:55] offset:0\n"
        "s_add_u32 m0, s59, 0xaf80\n"
        "s_nop 0\n"
        "global_load_lds_dwordx4 v4, s[54:55] offset:128\n"
        "s_add_u32 m0, s59, 0xbf00\n"
        "s_nop 0\n"
        "global_load_lds_dwordx4 v4, s[54:55] offset:256\n"
        "s_add_u32 m0, s59, 0xce80\n"
        "s_nop 0\n"
        "global_load_lds_dwordx4 v4, s[54:55] offset:384\n"
        "s_add_u32 m0, s59, 0xde00\n"
        "s_nop 0\n"
        "global_load_lds_dwordx4 v4, s[54:55] offset:512\n"
        "s_add_u32 m0, s59, 0xed80\n"
        "s_nop 0\n"
        "global_load_lds_dwordx4 v4, s[54:55] offset:640\n"
        "s_add_u32 m0, s59, 0xfd00\n"
        "s_nop 0\n"
        "global_load_lds_dwordx4 v4, s[54:55] offset:768\n"
        "s_add_u32 m0, s59, 0x10c80\n"
        "s_nop 0\n"
        "global_load_lds_dwordx4 v4, s[54:55] offset:896\n"
        "s_add_u32 m0, s59, 0x11c00\n"
        "s_nop 0\n"
        "global_load_lds_dwordx4 v4, s[54:55] offset:1024\n"
        "s_add_u32 m0, s59, 0x12b80\n"
        "s_nop 0\n"
        "global_load_lds_dwordx4 v4, s[54:55] offset:1152\n"
        "s_add_u32 s54, s94, 0x1e000\n"
        "s_addc_u32 s55, s95, 0\n"
        "s_add_u32 m0, s59, 0x14000\n"
        "s_nop 0\n"
        "global_load_lds_dwordx4 v4, s[54:55] offset:0\n"
        "s_add_u32 m0, s59, 0x14f80\n"
        "s_nop 0\n"
        "global_load_lds_dwordx4 v4, s[54:55] offset:128\n"
        "s_add_u32 m0, s59, 0x15f00\n"
        "s_nop 0\n"
        "global_load_lds_dwordx4 v4, s[54:55] offset:256\n"
        "s_add_u32 m0, s59, 0x16e80\n"
        "s_nop 0\n"
        "global_load_lds_dwordx4 v4, s[54:55] offset:384\n"
        "s_add_u32 m0, s59, 0x17e00\n"
        "s_nop 0\n"
        "global_load_lds_dwordx4 v4, s[54:55] offset:512\n"
        "s_add_u32 m0, s59, 0x18d80\n"
        "s_nop 0\n"
        "global_load_lds_dwordx4 v4, s[54:55] offset:640\n"
        "s_add_u32 m0, s59, 0x19d00\n"
        "s_nop 0\n"
        "global_load_lds_dwordx4 v4, s[54:55] offset:768\n"
        "s_add_u32 m0, s59, 0x1ac80\n"
        "s_nop 0\n"
        "global_load_lds_dwordx4 v4, s[54:55] offset:896\n"
        "s_add_u32 m0, s59, 0x1bc00\n"
        "s_nop 0\n"
        "global_load_lds_dwordx4 v4, s[54:55] offset:1024\n"
        "s_add_u32 m0, s59, 0x1cb80\n"
        "s_nop 0\n"
        "global_load_lds_dwordx4 v4, s[54:55] offset:1152\n"
        "s_add_u32 s54, s94, 0x28000\n"
        "s_addc_u32 s55, s95, 0\n"
        "s_add_u32 m0, s59, 0x1e000\n"
        "s_nop 0\n"
        "global_load_lds_dwordx4 v4, s[54:55] offset:0\n"
        "s_add_u32 m0, s59, 0x1ef80\n"
        "s_nop 0\n"
        "global_load_lds_dwordx4 v4, s[54:55] offset:128\n"
        "s_add_u32 m0, s59, 0x1ff00\n"
        "s_nop 0\n"
        "global_load_lds_dwordx4 v4, s[54:55] offset:256\n"
        "s_add_u32 m0, s59, 0x20e80\n"
        "s_nop 0\n"
        "global_load_lds_dwordx4 v4, s[54:55] offset:384\n"
        "s_add_u32 m0, s59, 0x21e00\n"
        "s_nop 0\n"
        "global_load_lds_dwordx4 v4, s[54:55] offset:512\n"
        "s_add_u32 m0, s59, 0x22d80\n"
        "s_nop 0\n"
        "global_load_lds_dwordx4 v4, s[54:55] offset:640\n"
        "s_add_u32 m0, s59, 0x23d00\n"
        "s_nop 0\n"
        "global_load_lds_dwordx4 v4, s[54:55] offset:768\n"
        "s_add_u32 m0, s59, 0x24c80\n"
        "s_nop 0\n"
        "global_load_lds_dwordx4 v4, s[54:55] offset:896\n"
        "s_add_u32 m0, s59, 0x25c00\n"
        "s_nop 0\n"
        "global_load_lds_dwordx4 v4, s[54:55] offset:1024\n"
        "s_add_u32 m0, s59, 0x26b80\n"
        "s_nop 0\n"
        "global_load_lds_dwordx4 v4, s[54:55] offset:1152\n"
        "s_waitcnt vmcnt(20)\n"
        "s_waitcnt lgkmcnt(0)\n"
        "s_barrier\n"
        "s_waitcnt vmcnt(40)\n"
        "v_readfirstlane_b32 s88, v26\n"
        "v_subrev_u32 v27, 0x8000, v15\n"
        "s_add_u32 s88, s88, 2\n"
        "s_and_b32 s88, s88, -2\n"
        "v_mov_b32 v26, s88\n"
        "s_mov_b64 exec, 1\n"
        "global_store_dword v27, v26, s[90:91] sc1\n"
        "s_mov_b64 exec, -1\n"
        "v_mov_b32 v112, 0x3f803f80\n"
        "v_mov_b32 v113, 0x3f803f80\n"
        "v_mov_b32 v114, 0x3f803f80\n"
        "v_mov_b32 v115, 0x3f803f80\n"
        "v_and_b32 v28, 15, v0\n"
        "v_lshlrev_b32 v28, 7, v28\n"
        "v_and_b32 v29, 3, v0\n"
        "v_lshl_add_u32 v29, v29, 2, v28\n"
        "v_add_u32 v30, 2048, v29\n"
        "s_lshl_b32 s47, s43, 5\n"
        "v_add_u32 v28, s47, v28\n"
        "s_mov_b32 s56, 0xf0000f\n"
        "s_mov_b32 s57, 0xf0000f00\n"
        "s_waitcnt vmcnt(31)\n"
        "v_mfma_f32_16x16x32_bf16 v[96:99], v[112:115], v[64:67], 0\n"
        "v_mfma_f32_16x16x32_bf16 v[100:103], v[64:67], v[64:67], 0\n"
        "v_mfma_f32_16x16x32_bf16 v[104:107], v[112:115], v[68:71], 0\n"
        "v_mfma_f32_16x16x32_bf16 v[108:111], v[68:71], v[68:71], 0\n"
        "v_mfma_f32_16x16x32_bf16 v[96:99], v[112:115], v[72:75], v[96:99]\n"
        "v_mfma_f32_16x16x32_bf16 v[100:103], v[72:75], v[72:75], v[100:103]\n"
        "v_mfma_f32_16x16x32_bf16 v[104:107], v[112:115], v[76:79], v[104:107]\n"
        "v_mfma_f32_16x16x32_bf16 v[108:111], v[76:79], v[76:79], v[108:111]\n"
        "v_mfma_f32_16x16x32_bf16 v[96:99], v[112:115], v[80:83], v[96:99]\n"
        "v_mfma_f32_16x16x32_bf16 v[100:103], v[80:83], v[80:83], v[100:103]\n"
        "v_mfma_f32_16x16x32_bf16 v[104:107], v[112:115], v[84:87], v[104:107]\n"
        "v_mfma_f32_16x16x32_bf16 v[108:111], v[84:87], v[84:87], v[108:111]\n"
        "v_mfma_f32_16x16x32_bf16 v[96:99], v[112:115], v[88:91], v[96:99]\n"
        "v_mfma_f32_16x16x32_bf16 v[100:103], v[88:91], v[88:91], v[100:103]\n"
        "v_mfma_f32_16x16x32_bf16 v[104:107], v[112:115], v[92:95], v[104:107]\n"
        "v_mfma_f32_16x16x32_bf16 v[108:111], v[92:95], v[92:95], v[108:111]\n"
        "v_mfma_f32_16x16x32_bf16 v[96:99], v[112:115], v[116:119], v[96:99]\n"
        "v_mfma_f32_16x16x32_bf16 v[100:103], v[116:119], v[116:119], v[100:103]\n"
        "v_mfma_f32_16x16x32_bf16 v[104:107], v[112:115], v[120:123], v[104:107]\n"
        "v_mfma_f32_16x16x32_bf16 v[108:111], v[120:123], v[120:123], v[108:111]\n"
        "s_mov_b64 exec, s[56:57]\n"
        "s_nop 3\n"
        "ds_write_b128 v28, v[96:99] offset:0\n"
        "ds_write_b128 v28, v[100:103] offset:16\n"
        "ds_write_b128 v28, v[104:107] offset:2048\n"
        "ds_write_b128 v28, v[108:111] offset:2064\n"
        "s_mov_b64 exec, -1\n"
        "v_add_u32 v20, 0xa000, v8\n"
        "ds_read_b64_tr_b16 a[192:193], v20\n"
        "ds_read_b64_tr_b16 a[194:195], v20 offset:512\n"
        "v_add_u32 v21, 0xa000, v9\n"
        "ds_read_b64_tr_b16 a[208:209], v21\n"
        "ds_read_b64_tr_b16 a[210:211], v21 offset:512\n"
        "v_add_u32 v22, 0xa000, v10\n"
        "ds_read_b64_tr_b16 a[224:225], v22\n"
        "ds_read_b64_tr_b16 a[226:227], v22 offset:512\n"
        "v_add_u32 v20, 0xa000, v11\n"
        "ds_read_b64_tr_b16 a[240:241], v20\n"
        "ds_read_b64_tr_b16 a[242:243], v20 offset:512\n"
        "v_add_u32 v21, 0xa000, v12\n"
        "ds_read_b64_tr_b16 v[136:137], v21\n"
        "ds_read_b64_tr_b16 v[138:139], v21 offset:512\n"
        "v_add_u32 v22, 0xa000, v13\n"
        "ds_read_b64_tr_b16 v[160:161], v22\n"
        "ds_read_b64_tr_b16 v[162:163], v22 offset:512\n"
        "v_add_u32 v20, 0xa000, v14\n"
        "ds_read_b64_tr_b16 v[184:185], v20\n"
        "ds_read_b64_tr_b16 v[186:187], v20 offset:512\n"
        "v_add_u32 v21, 0xa000, v16\n"
        "ds_read_b64_tr_b16 v[208:209], v21\n"
        "ds_read_b64_tr_b16 v[210:211], v21 offset:512\n"
        "v_add_u32 v22, 0xa000, v17\n"
        "ds_read_b64_tr_b16 v[224:225], v22\n"
        "ds_read_b64_tr_b16 v[226:227], v22 offset:512\n"
        "v_add_u32 v20, 0xa000, v18\n"
        "ds_read_b64_tr_b16 v[240:241], v20\n"
        "ds_read_b64_tr_b16 v[242:243], v20 offset:512\n"
        "s_waitcnt vmcnt(11)\n"
        "s_waitcnt lgkmcnt(0)\n"
        "s_barrier\n"
        "s_add_u32 s54, s94, 0x32000\n"
        "s_addc_u32 s55, s95, 0\n"
        "s_add_u32 m0, s59, 0xa000\n"
        "s_nop 0\n"
        "global_load_lds_dwordx4 v4, s[54:55] offset:0\n"
        "s_add_u32 m0, s59, 0xaf80\n"
        "s_nop 0\n"
        "global_load_lds_dwordx4 v4, s[54:55] offset:128\n"
        "s_add_u32 m0, s59, 0xbf00\n"
        "s_nop 0\n"
        "global_load_lds_dwordx4 v4, s[54:55] offset:256\n"
        "s_add_u32 m0, s59, 0xce80\n"
        "s_nop 0\n"
        "global_load_lds_dwordx4 v4, s[54:55] offset:384\n"
        "s_add_u32 m0, s59, 0xde00\n"
        "s_nop 0\n"
        "global_load_lds_dwordx4 v4, s[54:55] offset:512\n"
        "s_add_u32 m0, s59, 0xed80\n"
        "s_nop 0\n"
        "global_load_lds_dwordx4 v4, s[54:55] offset:640\n"
        "s_add_u32 m0, s59, 0xfd00\n"
        "s_nop 0\n"
        "global_load_lds_dwordx4 v4, s[54:55] offset:768\n"
        "s_add_u32 m0, s59, 0x10c80\n"
        "s_nop 0\n"
        "global_load_lds_dwordx4 v4, s[54:55] offset:896\n"
        "s_add_u32 m0, s59, 0x11c00\n"
        "s_nop 0\n"
        "global_load_lds_dwordx4 v4, s[54:55] offset:1024\n"
        "s_add_u32 m0, s59, 0x12b80\n"
        "s_nop 0\n"
        "global_load_lds_dwordx4 v4, s[54:55] offset:1152\n"
        "ds_read2_b32 v[96:97], v29 offset0:0 offset1:4\n"
        "ds_read2_b32 v[98:99], v29 offset0:8 offset1:12\n"
        "ds_read2_b32 v[100:101], v29 offset0:16 offset1:20\n"
        "ds_read2_b32 v[102:103], v29 offset0:24 offset1:28\n"
        "ds_read2_b32 v[104:105], v30 offset0:0 offset1:4\n"
        "ds_read2_b32 v[106:107], v30 offset0:8 offset1:12\n"
        "ds_read2_b32 v[108:109], v30 offset0:16 offset1:20\n"
        "ds_read2_b32 v[110:111], v30 offset0:24 offset1:28\n"
        "s_mov_b32 s58, 0x208168\n"
        "s_cmp_eq_u32 s43, 1\n"
        "s_cselect_b32 s58, 0x731483, s58\n"
        "s_cmp_eq_u32 s43, 2\n"
        "s_cselect_b32 s58, 0xc5262e, s58\n"
        "s_cmp_eq_u32 s43, 3\n"
        "s_cselect_b32 s58, 0x13941ed, s58\n"
        "s_waitcnt lgkmcnt(0)\n"
        "v_pk_add_f32 v[96:97], v[96:97], v[98:99]\n"
        "v_pk_add_f32 v[100:101], v[100:101], v[102:103]\n"
        "v_pk_add_f32 v[96:97], v[96:97], v[100:101]\n"
        "v_mul_f32 v96, 0x3acccccd, v96\n"
        "v_mul_f32 v97, 0x3acccccd, v97\n"
        "v_fma_f32 v97, -v96, v96, v97\n"
        "v_max_f32 v97, 0, v97\n"
        "v_add_f32 v97, 0x3727c5ac, v97\n"
        "v_rsq_f32 v33, v97\n"
        "s_nop 0\n"
        "v_mul_f32 v32, v96, v33\n"
        "v_pk_add_f32 v[104:105], v[104:105], v[106:107]\n"
        "v_pk_add_f32 v[108:109], v[108:109], v[110:111]\n"
        "v_pk_add_f32 v[104:105], v[104:105], v[108:109]\n"
        "v_mul_f32 v104, 0x3acccccd, v104\n"
        "v_mul_f32 v105, 0x3acccccd, v105\n"
        "v_fma_f32 v105, -v104, v104, v105\n"
        "v_max_f32 v105, 0, v105\n"
        "v_add_f32 v105, 0x3727c5ac, v105\n"
        "v_rsq_f32 v35, v105\n"
        "s_nop 0\n"
        "v_mul_f32 v34, v104, v35\n"
        "v_add_u32 v21, 0x14000, v8\n"
        "ds_read_b64_tr_b16 a[196:197], v21\n"
        "ds_read_b64_tr_b16 a[198:199], v21 offset:512\n"
        "v_add_u32 v22, 0x14000, v9\n"
        "ds_read_b64_tr_b16 a[212:213], v22\n"
        "ds_read_b64_tr_b16 a[214:215], v22 offset:512\n"
        "v_add_u32 v20, 0x14000, v10\n"
        "ds_read_b64_tr_b16 a[228:229], v20\n"
        "ds_read_b64_tr_b16 a[230:231], v20 offset:512\n"
        "v_add_u32 v21, 0x14000, v11\n"
        "ds_read_b64_tr_b16 a[244:245], v21\n"
        "ds_read_b64_tr_b16 a[246:247], v21 offset:512\n"
        "v_add_u32 v22, 0x14000, v12\n"
        "ds_read_b64_tr_b16 v[140:141], v22\n"
        "ds_read_b64_tr_b16 v[142:143], v22 offset:512\n"
        "v_add_u32 v20, 0x14000, v13\n"
        "ds_read_b64_tr_b16 v[164:165], v20\n"
        "ds_read_b64_tr_b16 v[166:167], v20 offset:512\n"
        "v_add_u32 v21, 0x14000, v14\n"
        "ds_read_b64_tr_b16 v[188:189], v21\n"
        "ds_read_b64_tr_b16 v[190:191], v21 offset:512\n"
        "v_add_u32 v22, 0x14000, v16\n"
        "ds_read_b64_tr_b16 v[212:213], v22\n"
        "ds_read_b64_tr_b16 v[214:215], v22 offset:512\n"
        "v_add_u32 v20, 0x14000, v17\n"
        "ds_read_b64_tr_b16 v[228:229], v20\n"
        "ds_read_b64_tr_b16 v[230:231], v20 offset:512\n"
        "v_add_u32 v21, 0x14000, v18\n"
        "ds_read_b64_tr_b16 v[244:245], v21\n"
        "ds_read_b64_tr_b16 v[246:247], v21 offset:512\n"
        "s_waitcnt vmcnt(11)\n"
        "s_waitcnt lgkmcnt(0)\n"
        "s_barrier\n"
        "s_add_u32 s54, s94, 0x0\n"
        "s_addc_u32 s55, s95, 0\n"
        "s_add_u32 m0, s59, 0x14000\n"
        "s_nop 0\n"
        "global_load_lds_dwordx4 v4, s[54:55] offset:0\n"
        "s_add_u32 m0, s59, 0x14f80\n"
        "s_nop 0\n"
        "global_load_lds_dwordx4 v4, s[54:55] offset:128\n"
        "s_add_u32 m0, s59, 0x15f00\n"
        "s_nop 0\n"
        "global_load_lds_dwordx4 v4, s[54:55] offset:256\n"
        "s_add_u32 m0, s59, 0x16e80\n"
        "s_nop 0\n"
        "global_load_lds_dwordx4 v4, s[54:55] offset:384\n"
        "s_add_u32 m0, s59, 0x17e00\n"
        "s_nop 0\n"
        "global_load_lds_dwordx4 v4, s[54:55] offset:512\n"
        "s_add_u32 m0, s59, 0x18d80\n"
        "s_nop 0\n"
        "global_load_lds_dwordx4 v4, s[54:55] offset:640\n"
        "s_add_u32 m0, s59, 0x19d00\n"
        "s_nop 0\n"
        "global_load_lds_dwordx4 v4, s[54:55] offset:768\n"
        "s_add_u32 m0, s59, 0x1ac80\n"
        "s_nop 0\n"
        "global_load_lds_dwordx4 v4, s[54:55] offset:896\n"
        "s_add_u32 m0, s59, 0x1bc00\n"
        "s_nop 0\n"
        "global_load_lds_dwordx4 v4, s[54:55] offset:1024\n"
        "s_add_u32 m0, s59, 0x1cb80\n"
        "s_nop 0\n"
        "global_load_lds_dwordx4 v4, s[54:55] offset:1152\n"
        "s_bfe_u32 s47, s58, 0x50000\n"
        "s_lshl_b32 s47, s47, 11\n"
        "v_add_u32 v28, s47, v1\n"
        "v_lshlrev_b32 v30, 16, v64\n"
        "v_and_b32 v64, 0xffff0000, v64\n"
        "v_fma_f32 v30, v30, v33, -v32\n"
        "v_fma_f32 v64, v64, v33, -v32\n"
        "v_cvt_pk_bf16_f32 v64, v30, v64\n"
        "v_lshlrev_b32 v31, 16, v65\n"
        "v_and_b32 v65, 0xffff0000, v65\n"
        "v_fma_f32 v31, v31, v33, -v32\n"
        "v_fma_f32 v65, v65, v33, -v32\n"
        "v_cvt_pk_bf16_f32 v65, v31, v65\n"
        "v_lshlrev_b32 v30, 16, v66\n"
        "v_and_b32 v66, 0xffff0000, v66\n"
        "v_fma_f32 v30, v30, v33, -v32\n"
        "v_fma_f32 v66, v66, v33, -v32\n"
        "v_cvt_pk_bf16_f32 v66, v30, v66\n"
        "v_lshlrev_b32 v31, 16, v67\n"
        "v_and_b32 v67, 0xffff0000, v67\n"
        "v_fma_f32 v31, v31, v33, -v32\n"
        "v_fma_f32 v67, v67, v33, -v32\n"
        "v_cvt_pk_bf16_f32 v67, v31, v67\n"
        "ds_write_b128 v28, v[64:67] offset:0\n"
        "v_lshlrev_b32 v30, 16, v68\n"
        "v_and_b32 v68, 0xffff0000, v68\n"
        "v_fma_f32 v30, v30, v35, -v34\n"
        "v_fma_f32 v68, v68, v35, -v34\n"
        "v_cvt_pk_bf16_f32 v68, v30, v68\n"
        "v_lshlrev_b32 v31, 16, v69\n"
        "v_and_b32 v69, 0xffff0000, v69\n"
        "v_fma_f32 v31, v31, v35, -v34\n"
        "v_fma_f32 v69, v69, v35, -v34\n"
        "v_cvt_pk_bf16_f32 v69, v31, v69\n"
        "v_lshlrev_b32 v30, 16, v70\n"
        "v_and_b32 v70, 0xffff0000, v70\n"
        "v_fma_f32 v30, v30, v35, -v34\n"
        "v_fma_f32 v70, v70, v35, -v34\n"
        "v_cvt_pk_bf16_f32 v70, v30, v70\n"
        "v_lshlrev_b32 v31, 16, v71\n"
        "v_and_b32 v71, 0xffff0000, v71\n"
        "v_fma_f32 v31, v31, v35, -v34\n"
        "v_fma_f32 v71, v71, v35, -v34\n"
        "v_cvt_pk_bf16_f32 v71, v31, v71\n"
        "ds_write_b128 v28, v[68:71] offset:1024\n"
        "s_bfe_u32 s47, s58, 0x50005\n"
        "s_lshl_b32 s47, s47, 11\n"
        "v_add_u32 v28, s47, v1\n"
        "v_lshlrev_b32 v30, 16, v72\n"
        "v_and_b32 v72, 0xffff0000, v72\n"
        "v_fma_f32 v30, v30, v33, -v32\n"
        "v_fma_f32 v72, v72, v33, -v32\n"
        "v_cvt_pk_bf16_f32 v72, v30, v72\n"
        "v_lshlrev_b32 v31, 16, v73\n"
        "v_and_b32 v73, 0xffff0000, v73\n"
        "v_fma_f32 v31, v31, v33, -v32\n"
        "v_fma_f32 v73, v73, v33, -v32\n"
        "v_cvt_pk_bf16_f32 v73, v31, v73\n"
        "v_lshlrev_b32 v30, 16, v74\n"
        "v_and_b32 v74, 0xffff0000, v74\n"
        "v_fma_f32 v30, v30, v33, -v32\n"
        "v_fma_f32 v74, v74, v33, -v32\n"
        "v_cvt_pk_bf16_f32 v74, v30, v74\n"
        "v_lshlrev_b32 v31, 16, v75\n"
        "v_and_b32 v75, 0xffff0000, v75\n"
        "v_fma_f32 v31, v31, v33, -v32\n"
        "v_fma_f32 v75, v75, v33, -v32\n"
        "v_cvt_pk_bf16_f32 v75, v31, v75\n"
        "ds_write_b128 v28, v[72:75] offset:0\n"
        "v_lshlrev_b32 v30, 16, v76\n"
        "v_and_b32 v76, 0xffff0000, v76\n"
        "v_fma_f32 v30, v30, v35, -v34\n"
        "v_fma_f32 v76, v76, v35, -v34\n"
        "v_cvt_pk_bf16_f32 v76, v30, v76\n"
        "v_lshlrev_b32 v31, 16, v77\n"
        "v_and_b32 v77, 0xffff0000, v77\n"
        "v_fma_f32 v31, v31, v35, -v34\n"
        "v_fma_f32 v77, v77, v35, -v34\n"
        "v_cvt_pk_bf16_f32 v77, v31, v77\n"
        "v_lshlrev_b32 v30, 16, v78\n"
        "v_and_b32 v78, 0xffff0000, v78\n"
        "v_fma_f32 v30, v30, v35, -v34\n"
        "v_fma_f32 v78, v78, v35, -v34\n"
        "v_cvt_pk_bf16_f32 v78, v30, v78\n"
        "v_lshlrev_b32 v31, 16, v79\n"
        "v_and_b32 v79, 0xffff0000, v79\n"
        "v_fma_f32 v31, v31, v35, -v34\n"
        "v_fma_f32 v79, v79, v35, -v34\n"
        "v_cvt_pk_bf16_f32 v79, v31, v79\n"
        "ds_write_b128 v28, v[76:79] offset:1024\n"
        "v_add_u32 v22, 0x1e000, v8\n"
        "ds_read_b64_tr_b16 a[200:201], v22\n"
        "ds_read_b64_tr_b16 a[202:203], v22 offset:512\n"
        "v_add_u32 v20, 0x1e000, v9\n"
        "ds_read_b64_tr_b16 a[216:217], v20\n"
        "ds_read_b64_tr_b16 a[218:219], v20 offset:512\n"
        "v_add_u32 v21, 0x1e000, v10\n"
        "ds_read_b64_tr_b16 a[232:233], v21\n"
        "ds_read_b64_tr_b16 a[234:235], v21 offset:512\n"
        "v_add_u32 v22, 0x1e000, v11\n"
        "ds_read_b64_tr_b16 a[248:249], v22\n"
        "ds_read_b64_tr_b16 a[250:251], v22 offset:512\n"
        "v_add_u32 v20, 0x1e000, v12\n"
        "ds_read_b64_tr_b16 v[144:145], v20\n"
        "ds_read_b64_tr_b16 v[146:147], v20 offset:512\n"
        "v_add_u32 v21, 0x1e000, v13\n"
        "ds_read_b64_tr_b16 v[168:169], v21\n"
        "ds_read_b64_tr_b16 v[170:171], v21 offset:512\n"
        "v_add_u32 v22, 0x1e000, v14\n"
        "ds_read_b64_tr_b16 v[192:193], v22\n"
        "ds_read_b64_tr_b16 v[194:195], v22 offset:512\n"
        "v_add_u32 v20, 0x1e000, v16\n"
        "ds_read_b64_tr_b16 v[216:217], v20\n"
        "ds_read_b64_tr_b16 v[218:219], v20 offset:512\n"
        "v_add_u32 v21, 0x1e000, v17\n"
        "ds_read_b64_tr_b16 v[232:233], v21\n"
        "ds_read_b64_tr_b16 v[234:235], v21 offset:512\n"
        "v_add_u32 v22, 0x1e000, v18\n"
        "ds_read_b64_tr_b16 v[248:249], v22\n"
        "ds_read_b64_tr_b16 v[250:251], v22 offset:512\n"
        "s_waitcnt vmcnt(10)\n"
        "s_waitcnt lgkmcnt(0)\n"
        "s_barrier\n"
        "s_add_u32 s54, s94, 0xa000\n"
        "s_addc_u32 s55, s95, 0\n"
        "s_add_u32 m0, s59, 0x1e000\n"
        "s_nop 0\n"
        "global_load_lds_dwordx4 v4, s[54:55] offset:0\n"
        "s_add_u32 m0, s59, 0x1ef80\n"
        "s_nop 0\n"
        "global_load_lds_dwordx4 v4, s[54:55] offset:128\n"
        "s_add_u32 m0, s59, 0x1ff00\n"
        "s_nop 0\n"
        "global_load_lds_dwordx4 v4, s[54:55] offset:256\n"
        "s_add_u32 m0, s59, 0x20e80\n"
        "s_nop 0\n"
        "global_load_lds_dwordx4 v4, s[54:55] offset:384\n"
        "s_add_u32 m0, s59, 0x21e00\n"
        "s_nop 0\n"
        "global_load_lds_dwordx4 v4, s[54:55] offset:512\n"
        "s_add_u32 m0, s59, 0x22d80\n"
        "s_nop 0\n"
        "global_load_lds_dwordx4 v4, s[54:55] offset:640\n"
        "s_add_u32 m0, s59, 0x23d00\n"
        "s_nop 0\n"
        "global_load_lds_dwordx4 v4, s[54:55] offset:768\n"
        "s_add_u32 m0, s59, 0x24c80\n"
        "s_nop 0\n"
        "global_load_lds_dwordx4 v4, s[54:55] offset:896\n"
        "s_add_u32 m0, s59, 0x25c00\n"
        "s_nop 0\n"
        "global_load_lds_dwordx4 v4, s[54:55] offset:1024\n"
        "s_add_u32 m0, s59, 0x26b80\n"
        "s_nop 0\n"
        "global_load_lds_dwordx4 v4, s[54:55] offset:1152\n"
        "s_bfe_u32 s47, s58, 0x5000a\n"
        "s_lshl_b32 s47, s47, 11\n"
        "v_add_u32 v28, s47, v1\n"
        "v_lshlrev_b32 v30, 16, v80\n"
        "v_and_b32 v80, 0xffff0000, v80\n"
        "v_fma_f32 v30, v30, v33, -v32\n"
        "v_fma_f32 v80, v80, v33, -v32\n"
        "v_cvt_pk_bf16_f32 v80, v30, v80\n"
        "v_lshlrev_b32 v31, 16, v81\n"
        "v_and_b32 v81, 0xffff0000, v81\n"
        "v_fma_f32 v31, v31, v33, -v32\n"
        "v_fma_f32 v81, v81, v33, -v32\n"
        "v_cvt_pk_bf16_f32 v81, v31, v81\n"
        "v_lshlrev_b32 v30, 16, v82\n"
        "v_and_b32 v82, 0xffff0000, v82\n"
        "v_fma_f32 v30, v30, v33, -v32\n"
        "v_fma_f32 v82, v82, v33, -v32\n"
        "v_cvt_pk_bf16_f32 v82, v30, v82\n"
        "v_lshlrev_b32 v31, 16, v83\n"
        "v_and_b32 v83, 0xffff0000, v83\n"
        "v_fma_f32 v31, v31, v33, -v32\n"
        "v_fma_f32 v83, v83, v33, -v32\n"
        "v_cvt_pk_bf16_f32 v83, v31, v83\n"
        "ds_write_b128 v28, v[80:83] offset:0\n"
        "v_lshlrev_b32 v30, 16, v84\n"
        "v_and_b32 v84, 0xffff0000, v84\n"
        "v_fma_f32 v30, v30, v35, -v34\n"
        "v_fma_f32 v84, v84, v35, -v34\n"
        "v_cvt_pk_bf16_f32 v84, v30, v84\n"
        "v_lshlrev_b32 v31, 16, v85\n"
        "v_and_b32 v85, 0xffff0000, v85\n"
        "v_fma_f32 v31, v31, v35, -v34\n"
        "v_fma_f32 v85, v85, v35, -v34\n"
        "v_cvt_pk_bf16_f32 v85, v31, v85\n"
        "v_lshlrev_b32 v30, 16, v86\n"
        "v_and_b32 v86, 0xffff0000, v86\n"
        "v_fma_f32 v30, v30, v35, -v34\n"
        "v_fma_f32 v86, v86, v35, -v34\n"
        "v_cvt_pk_bf16_f32 v86, v30, v86\n"
        "v_lshlrev_b32 v31, 16, v87\n"
        "v_and_b32 v87, 0xffff0000, v87\n"
        "v_fma_f32 v31, v31, v35, -v34\n"
        "v_fma_f32 v87, v87, v35, -v34\n"
        "v_cvt_pk_bf16_f32 v87, v31, v87\n"
        "ds_write_b128 v28, v[84:87] offset:1024\n"
        "s_bfe_u32 s47, s58, 0x5000f\n"
        "s_lshl_b32 s47, s47, 11\n"
        "v_add_u32 v28, s47, v1\n"
        "v_lshlrev_b32 v30, 16, v88\n"
        "v_and_b32 v88, 0xffff0000, v88\n"
        "v_fma_f32 v30, v30, v33, -v32\n"
        "v_fma_f32 v88, v88, v33, -v32\n"
        "v_cvt_pk_bf16_f32 v88, v30, v88\n"
        "v_lshlrev_b32 v31, 16, v89\n"
        "v_and_b32 v89, 0xffff0000, v89\n"
        "v_fma_f32 v31, v31, v33, -v32\n"
        "v_fma_f32 v89, v89, v33, -v32\n"
        "v_cvt_pk_bf16_f32 v89, v31, v89\n"
        "v_lshlrev_b32 v30, 16, v90\n"
        "v_and_b32 v90, 0xffff0000, v90\n"
        "v_fma_f32 v30, v30, v33, -v32\n"
        "v_fma_f32 v90, v90, v33, -v32\n"
        "v_cvt_pk_bf16_f32 v90, v30, v90\n"
        "v_lshlrev_b32 v31, 16, v91\n"
        "v_and_b32 v91, 0xffff0000, v91\n"
        "v_fma_f32 v31, v31, v33, -v32\n"
        "v_fma_f32 v91, v91, v33, -v32\n"
        "v_cvt_pk_bf16_f32 v91, v31, v91\n"
        "ds_write_b128 v28, v[88:91] offset:0\n"
        "v_lshlrev_b32 v30, 16, v92\n"
        "v_and_b32 v92, 0xffff0000, v92\n"
        "v_fma_f32 v30, v30, v35, -v34\n"
        "v_fma_f32 v92, v92, v35, -v34\n"
        "v_cvt_pk_bf16_f32 v92, v30, v92\n"
        "v_lshlrev_b32 v31, 16, v93\n"
        "v_and_b32 v93, 0xffff0000, v93\n"
        "v_fma_f32 v31, v31, v35, -v34\n"
        "v_fma_f32 v93, v93, v35, -v34\n"
        "v_cvt_pk_bf16_f32 v93, v31, v93\n"
        "v_lshlrev_b32 v30, 16, v94\n"
        "v_and_b32 v94, 0xffff0000, v94\n"
        "v_fma_f32 v30, v30, v35, -v34\n"
        "v_fma_f32 v94, v94, v35, -v34\n"
        "v_cvt_pk_bf16_f32 v94, v30, v94\n"
        "v_lshlrev_b32 v31, 16, v95\n"
        "v_and_b32 v95, 0xffff0000, v95\n"
        "v_fma_f32 v31, v31, v35, -v34\n"
        "v_fma_f32 v95, v95, v35, -v34\n"
        "v_cvt_pk_bf16_f32 v95, v31, v95\n"
        "ds_write_b128 v28, v[92:95] offset:1024\n"
        "s_bfe_u32 s47, s58, 0x50014\n"
        "s_lshl_b32 s47, s47, 11\n"
        "v_add_u32 v28, s47, v1\n"
        "v_lshlrev_b32 v30, 16, v116\n"
        "v_and_b32 v116, 0xffff0000, v116\n"
        "v_fma_f32 v30, v30, v33, -v32\n"
        "v_fma_f32 v116, v116, v33, -v32\n"
        "v_cvt_pk_bf16_f32 v116, v30, v116\n"
        "v_lshlrev_b32 v31, 16, v117\n"
        "v_and_b32 v117, 0xffff0000, v117\n"
        "v_fma_f32 v31, v31, v33, -v32\n"
        "v_fma_f32 v117, v117, v33, -v32\n"
        "v_cvt_pk_bf16_f32 v117, v31, v117\n"
        "v_lshlrev_b32 v30, 16, v118\n"
        "v_and_b32 v118, 0xffff0000, v118\n"
        "v_fma_f32 v30, v30, v33, -v32\n"
        "v_fma_f32 v118, v118, v33, -v32\n"
        "v_cvt_pk_bf16_f32 v118, v30, v118\n"
        "v_lshlrev_b32 v31, 16, v119\n"
        "v_and_b32 v119, 0xffff0000, v119\n"
        "v_fma_f32 v31, v31, v33, -v32\n"
        "v_fma_f32 v119, v119, v33, -v32\n"
        "v_cvt_pk_bf16_f32 v119, v31, v119\n"
        "ds_write_b128 v28, v[116:119] offset:0\n"
        "v_lshlrev_b32 v30, 16, v120\n"
        "v_and_b32 v120, 0xffff0000, v120\n"
        "v_fma_f32 v30, v30, v35, -v34\n"
        "v_fma_f32 v120, v120, v35, -v34\n"
        "v_cvt_pk_bf16_f32 v120, v30, v120\n"
        "v_lshlrev_b32 v31, 16, v121\n"
        "v_and_b32 v121, 0xffff0000, v121\n"
        "v_fma_f32 v31, v31, v35, -v34\n"
        "v_fma_f32 v121, v121, v35, -v34\n"
        "v_cvt_pk_bf16_f32 v121, v31, v121\n"
        "v_lshlrev_b32 v30, 16, v122\n"
        "v_and_b32 v122, 0xffff0000, v122\n"
        "v_fma_f32 v30, v30, v35, -v34\n"
        "v_fma_f32 v122, v122, v35, -v34\n"
        "v_cvt_pk_bf16_f32 v122, v30, v122\n"
        "v_lshlrev_b32 v31, 16, v123\n"
        "v_and_b32 v123, 0xffff0000, v123\n"
        "v_fma_f32 v31, v31, v35, -v34\n"
        "v_fma_f32 v123, v123, v35, -v34\n"
        "v_cvt_pk_bf16_f32 v123, v31, v123\n"
        "ds_write_b128 v28, v[120:123] offset:1024\n"
        "v_add_u32 v20, 0xa000, v8\n"
        "ds_read_b64_tr_b16 a[204:205], v20\n"
        "ds_read_b64_tr_b16 a[206:207], v20 offset:512\n"
        "v_add_u32 v21, 0xa000, v9\n"
        "ds_read_b64_tr_b16 a[220:221], v21\n"
        "ds_read_b64_tr_b16 a[222:223], v21 offset:512\n"
        "v_add_u32 v22, 0xa000, v10\n"
        "ds_read_b64_tr_b16 a[236:237], v22\n"
        "ds_read_b64_tr_b16 a[238:239], v22 offset:512\n"
        "v_add_u32 v20, 0xa000, v11\n"
        "ds_read_b64_tr_b16 a[252:253], v20\n"
        "ds_read_b64_tr_b16 a[254:255], v20 offset:512\n"
        "v_add_u32 v21, 0xa000, v12\n"
        "ds_read_b64_tr_b16 v[148:149], v21\n"
        "ds_read_b64_tr_b16 v[150:151], v21 offset:512\n"
        "v_add_u32 v22, 0xa000, v13\n"
        "ds_read_b64_tr_b16 v[172:173], v22\n"
        "ds_read_b64_tr_b16 v[174:175], v22 offset:512\n"
        "v_add_u32 v20, 0xa000, v14\n"
        "ds_read_b64_tr_b16 v[196:197], v20\n"
        "ds_read_b64_tr_b16 v[198:199], v20 offset:512\n"
        "v_add_u32 v21, 0xa000, v16\n"
        "ds_read_b64_tr_b16 v[220:221], v21\n"
        "ds_read_b64_tr_b16 v[222:223], v21 offset:512\n"
        "v_add_u32 v22, 0xa000, v17\n"
        "ds_read_b64_tr_b16 v[236:237], v22\n"
        "ds_read_b64_tr_b16 v[238:239], v22 offset:512\n"
        "v_add_u32 v20, 0xa000, v18\n"
        "ds_read_b64_tr_b16 v[252:253], v20\n"
        "ds_read_b64_tr_b16 v[254:255], v20 offset:512\n"
        "s_waitcnt vmcnt(10)\n"
        "s_waitcnt lgkmcnt(0)\n"
        "s_barrier\n"
        "v_add_u32 v21, 0x14000, v8\n"
        "ds_read_b64_tr_b16 v[40:41], v21\n"
        "ds_read_b64_tr_b16 v[42:43], v21 offset:512\n"
        "v_add_u32 v22, 0x14000, v9\n"
        "ds_read_b64_tr_b16 v[44:45], v22\n"
        "ds_read_b64_tr_b16 v[46:47], v22 offset:512\n"
        "v_add_u32 v20, 0x14000, v10\n"
        "ds_read_b64_tr_b16 v[48:49], v20\n"
        "ds_read_b64_tr_b16 v[50:51], v20 offset:512\n"
        "v_add_u32 v21, 0x14000, v11\n"
        "ds_read_b64_tr_b16 v[52:53], v21\n"
        "ds_read_b64_tr_b16 v[54:55], v21 offset:512\n"
        "v_add_u32 v22, 0x14000, v12\n"
        "ds_read_b64_tr_b16 v[128:129], v22\n"
        "ds_read_b64_tr_b16 v[130:131], v22 offset:512\n"
        "v_add_u32 v20, 0x14000, v13\n"
        "ds_read_b64_tr_b16 v[152:153], v20\n"
        "ds_read_b64_tr_b16 v[154:155], v20 offset:512\n"
        "v_add_u32 v21, 0x14000, v14\n"
        "ds_read_b64_tr_b16 v[176:177], v21\n"
        "ds_read_b64_tr_b16 v[178:179], v21 offset:512\n"
        "v_add_u32 v22, 0x14000, v16\n"
        "ds_read_b64_tr_b16 v[200:201], v22\n"
        "ds_read_b64_tr_b16 v[202:203], v22 offset:512\n"
        "v_add_u32 v20, 0x14000, v17\n"
        "ds_read_b64_tr_b16 v[56:57], v20\n"
        "ds_read_b64_tr_b16 v[58:59], v20 offset:512\n"
        "v_add_u32 v21, 0x14000, v18\n"
        "ds_read_b64_tr_b16 v[60:61], v21\n"
        "ds_read_b64_tr_b16 v[62:63], v21 offset:512\n"
        "s_waitcnt vmcnt(0)\n"
        "s_waitcnt lgkmcnt(0)\n"
        "s_barrier\n"
        "s_add_u32 s54, s92, 0xc0000\n"
        "s_addc_u32 s55, s93, 0\n"
        "s_add_u32 m0, s59, 0x10000\n"
        "s_nop 0\n"
        "global_load_lds_dwordx4 v3, s[54:55] offset:0\n"
        "s_add_u32 m0, s59, 0x10f80\n"
        "s_nop 0\n"
        "global_load_lds_dwordx4 v3, s[54:55] offset:128\n"
        "s_add_u32 m0, s59, 0x11f00\n"
        "s_nop 0\n"
        "global_load_lds_dwordx4 v3, s[54:55] offset:256\n"
        "s_add_u32 s54, s92, 0x120000\n"
        "s_addc_u32 s55, s93, 0\n"
        "s_add_u32 m0, s59, 0x13000\n"
        "s_nop 0\n"
        "global_load_lds_dwordx4 v3, s[54:55] offset:0\n"
        "s_add_u32 m0, s59, 0x13f80\n"
        "s_nop 0\n"
        "global_load_lds_dwordx4 v3, s[54:55] offset:128\n"
        "s_add_u32 m0, s59, 0x14f00\n"
        "s_nop 0\n"
        "global_load_lds_dwordx4 v3, s[54:55] offset:256\n"
        "s_add_u32 s54, s92, 0x180000\n"
        "s_addc_u32 s55, s93, 0\n"
        "s_add_u32 m0, s59, 0x16000\n"
        "s_nop 0\n"
        "global_load_lds_dwordx4 v3, s[54:55] offset:0\n"
        "s_add_u32 m0, s59, 0x16f80\n"
        "s_nop 0\n"
        "global_load_lds_dwordx4 v3, s[54:55] offset:128\n"
        "s_add_u32 m0, s59, 0x17f00\n"
        "s_nop 0\n"
        "global_load_lds_dwordx4 v3, s[54:55] offset:256\n"
        "s_add_u32 s54, s92, 0x1e0000\n"
        "s_addc_u32 s55, s93, 0\n"
        "s_add_u32 m0, s59, 0x19000\n"
        "s_nop 0\n"
        "global_load_lds_dwordx4 v3, s[54:55] offset:0\n"
        "s_add_u32 m0, s59, 0x19f80\n"
        "s_nop 0\n"
        "global_load_lds_dwordx4 v3, s[54:55] offset:128\n"
        "s_add_u32 m0, s59, 0x1af00\n"
        "s_nop 0\n"
        "global_load_lds_dwordx4 v3, s[54:55] offset:256\n"
        "v_add_u32 v22, 0x1e000, v8\n"
        "ds_read_b64_tr_b16 v[96:97], v22\n"
        "ds_read_b64_tr_b16 v[98:99], v22 offset:512\n"
        "v_add_u32 v20, 0x1e000, v9\n"
        "ds_read_b64_tr_b16 v[100:101], v20\n"
        "ds_read_b64_tr_b16 v[102:103], v20 offset:512\n"
        "v_add_u32 v21, 0x1e000, v10\n"
        "ds_read_b64_tr_b16 v[104:105], v21\n"
        "ds_read_b64_tr_b16 v[106:107], v21 offset:512\n"
        "v_add_u32 v22, 0x1e000, v11\n"
        "ds_read_b64_tr_b16 v[108:109], v22\n"
        "ds_read_b64_tr_b16 v[110:111], v22 offset:512\n"
        "v_add_u32 v20, 0x1e000, v12\n"
        "ds_read_b64_tr_b16 v[132:133], v20\n"
        "ds_read_b64_tr_b16 v[134:135], v20 offset:512\n"
        "v_add_u32 v21, 0x1e000, v13\n"
        "ds_read_b64_tr_b16 v[156:157], v21\n"
        "ds_read_b64_tr_b16 v[158:159], v21 offset:512\n"
        "v_add_u32 v22, 0x1e000, v14\n"
        "ds_read_b64_tr_b16 v[180:181], v22\n"
        "ds_read_b64_tr_b16 v[182:183], v22 offset:512\n"
        "v_add_u32 v20, 0x1e000, v16\n"
        "ds_read_b64_tr_b16 v[204:205], v20\n"
        "ds_read_b64_tr_b16 v[206:207], v20 offset:512\n"
        "v_add_u32 v21, 0x1e000, v17\n"
        "ds_read_b64_tr_b16 v[112:113], v21\n"
        "ds_read_b64_tr_b16 v[114:115], v21 offset:512\n"
        "v_add_u32 v22, 0x1e000, v18\n"
        "ds_read_b64_tr_b16 v[116:117], v22\n"
        "ds_read_b64_tr_b16 v[118:119], v22 offset:512\n"
        "s_waitcnt vmcnt(6)\n"
        "s_waitcnt lgkmcnt(0)\n"
        "s_barrier\n"
        "s_add_u32 s54, s92, 0x240000\n"
        "s_addc_u32 s55, s93, 0\n"
        "s_add_u32 m0, s59, 0x1c000\n"
        "s_nop 0\n"
        "global_load_lds_dwordx4 v3, s[54:55] offset:0\n"
        "s_add_u32 m0, s59, 0x1cf80\n"
        "s_nop 0\n"
        "global_load_lds_dwordx4 v3, s[54:55] offset:128\n"
        "s_add_u32 m0, s59, 0x1df00\n"
        "s_nop 0\n"
        "global_load_lds_dwordx4 v3, s[54:55] offset:256\n"
        "s_add_u32 s54, s92, 0x2a0000\n"
        "s_addc_u32 s55, s93, 0\n"
        "s_add_u32 m0, s59, 0x1f000\n"
        "s_nop 0\n"
        "global_load_lds_dwordx4 v3, s[54:55] offset:0\n"
        "s_add_u32 m0, s59, 0x1ff80\n"
        "s_nop 0\n"
        "global_load_lds_dwordx4 v3, s[54:55] offset:128\n"
        "s_add_u32 m0, s59, 0x20f00\n"
        "s_nop 0\n"
        "global_load_lds_dwordx4 v3, s[54:55] offset:256\n"
        "s_add_u32 s54, s92, 0x300000\n"
        "s_addc_u32 s55, s93, 0\n"
        "s_add_u32 m0, s59, 0x22000\n"
        "s_nop 0\n"
        "global_load_lds_dwordx4 v3, s[54:55] offset:0\n"
        "s_add_u32 m0, s59, 0x22f80\n"
        "s_nop 0\n"
        "global_load_lds_dwordx4 v3, s[54:55] offset:128\n"
        "s_add_u32 m0, s59, 0x23f00\n"
        "s_nop 0\n"
        "global_load_lds_dwordx4 v3, s[54:55] offset:256\n"
        "s_add_u32 s54, s92, 0x360000\n"
        "s_addc_u32 s55, s93, 0\n"
        "s_add_u32 m0, s59, 0x25000\n"
        "s_nop 0\n"
        "global_load_lds_dwordx4 v3, s[54:55] offset:0\n"
        "s_add_u32 m0, s59, 0x25f80\n"
        "s_nop 0\n"
        "global_load_lds_dwordx4 v3, s[54:55] offset:128\n"
        "s_add_u32 m0, s59, 0x26f00\n"
        "s_nop 0\n"
        "global_load_lds_dwordx4 v3, s[54:55] offset:256\n"
        "s_add_u32 s54, s92, 0x480000\n"
        "s_addc_u32 s55, s93, 0\n"
        "s_add_u32 m0, s59, 0xa000\n"
        "s_nop 0\n"
        "global_load_lds_dwordx4 v3, s[54:55] offset:0\n"
        "s_add_u32 m0, s59, 0xaf80\n"
        "s_nop 0\n"
        "global_load_lds_dwordx4 v3, s[54:55] offset:128\n"
        "s_add_u32 m0, s59, 0xbf00\n"
        "s_nop 0\n"
        "global_load_lds_dwordx4 v3, s[54:55] offset:256\n"
        "s_add_u32 s54, s92, 0x4e0000\n"
        "s_addc_u32 s55, s93, 0\n"
        "s_add_u32 m0, s59, 0xd000\n"
        "s_nop 0\n"
        "global_load_lds_dwordx4 v3, s[54:55] offset:0\n"
        "s_add_u32 m0, s59, 0xdf80\n"
        "s_nop 0\n"
        "global_load_lds_dwordx4 v3, s[54:55] offset:128\n"
        "s_add_u32 m0, s59, 0xef00\n"
        "s_nop 0\n"
        "global_load_lds_dwordx4 v3, s[54:55] offset:256\n"
        "v_add_u32 v20, 0x10000, v5\n"
        "ds_read_b64_tr_b16 a[0:1], v20\n"
        "ds_read_b64_tr_b16 a[2:3], v20 offset:512\n"
        "v_add_u32 v21, 0x10000, v6\n"
        "ds_read_b64_tr_b16 a[8:9], v21\n"
        "ds_read_b64_tr_b16 a[10:11], v21 offset:512\n"
        "v_add_u32 v22, 0x10000, v7\n"
        "ds_read_b64_tr_b16 a[16:17], v22\n"
        "ds_read_b64_tr_b16 a[18:19], v22 offset:512\n"
        "v_add_u32 v20, 0x13000, v5\n"
        "ds_read_b64_tr_b16 a[4:5], v20\n"
        "ds_read_b64_tr_b16 a[6:7], v20 offset:512\n"
        "v_add_u32 v21, 0x13000, v6\n"
        "ds_read_b64_tr_b16 a[12:13], v21\n"
        "ds_read_b64_tr_b16 a[14:15], v21 offset:512\n"
        "v_add_u32 v22, 0x13000, v7\n"
        "ds_read_b64_tr_b16 a[20:21], v22\n"
        "ds_read_b64_tr_b16 a[22:23], v22 offset:512\n"
        "s_waitcnt vmcnt(18)\n"
        "s_waitcnt lgkmcnt(0)\n"
        "s_barrier\n"
        "s_add_u32 s54, s92, 0x540000\n"
        "s_addc_u32 s55, s93, 0\n"
        "s_add_u32 m0, s59, 0x10000\n"
        "s_nop 0\n"
        "global_load_lds_dwordx4 v3, s[54:55] offset:0\n"
        "s_add_u32 m0, s59, 0x10f80\n"
        "s_nop 0\n"
        "global_load_lds_dwordx4 v3, s[54:55] offset:128\n"
        "s_add_u32 m0, s59, 0x11f00\n"
        "s_nop 0\n"
        "global_load_lds_dwordx4 v3, s[54:55] offset:256\n"
        "s_add_u32 s54, s92, 0x5a0000\n"
        "s_addc_u32 s55, s93, 0\n"
        "s_add_u32 m0, s59, 0x13000\n"
        "s_nop 0\n"
        "global_load_lds_dwordx4 v3, s[54:55] offset:0\n"
        "s_add_u32 m0, s59, 0x13f80\n"
        "s_nop 0\n"
        "global_load_lds_dwordx4 v3, s[54:55] offset:128\n"
        "s_add_u32 m0, s59, 0x14f00\n"
        "s_nop 0\n"
        "global_load_lds_dwordx4 v3, s[54:55] offset:256\n"
        "ds_read_b128 v[24:27], v1 offset:0\n"
        "ds_read_b128 v[28:31], v1 offset:1024\n"
        "ds_read_b128 v[32:35], v1 offset:2048\n"
        "ds_read_b128 v[36:39], v1 offset:3072\n"
        "v_add_u32 v20, 0x16000, v5\n"
        "ds_read_b64_tr_b16 a[24:25], v20\n"
        "ds_read_b64_tr_b16 a[26:27], v20 offset:512\n"
        "v_add_u32 v21, 0x16000, v6\n"
        "ds_read_b64_tr_b16 a[32:33], v21\n"
        "ds_read_b64_tr_b16 a[34:35], v21 offset:512\n"
        "v_add_u32 v22, 0x16000, v7\n"
        "ds_read_b64_tr_b16 a[40:41], v22\n"
        "ds_read_b64_tr_b16 a[42:43], v22 offset:512\n"
        "v_add_u32 v20, 0x19000, v5\n"
        "ds_read_b64_tr_b16 a[28:29], v20\n"
        "ds_read_b64_tr_b16 a[30:31], v20 offset:512\n"
        "v_add_u32 v21, 0x19000, v6\n"
        "ds_read_b64_tr_b16 a[36:37], v21\n"
        "ds_read_b64_tr_b16 a[38:39], v21 offset:512\n"
        "v_add_u32 v22, 0x19000, v7\n"
        "ds_read_b64_tr_b16 a[44:45], v22\n"
        "ds_read_b64_tr_b16 a[46:47], v22 offset:512\n"
        "s_waitcnt lgkmcnt(12)\n"
        "v_mfma_f32_16x16x32_bf16 v[72:75], a[0:3], v[24:27], 0\n"
        "v_mfma_f32_16x16x32_bf16 v[76:79], a[0:3], v[28:31], 0\n"
        "v_mfma_f32_16x16x32_bf16 v[80:83], a[8:11], v[24:27], 0\n"
        "v_mfma_f32_16x16x32_bf16 v[84:87], a[8:11], v[28:31], 0\n"
        "v_mfma_f32_16x16x32_bf16 v[88:91], a[16:19], v[24:27], 0\n"
        "v_mfma_f32_16x16x32_bf16 v[92:95], a[16:19], v[28:31], 0\n"
        "v_mfma_f32_16x16x32_bf16 v[72:75], a[4:7], v[32:35], v[72:75]\n"
        "v_mfma_f32_16x16x32_bf16 v[76:79], a[4:7], v[36:39], v[76:79]\n"
        "v_mfma_f32_16x16x32_bf16 v[80:83], a[12:15], v[32:35], v[80:83]\n"
        "v_mfma_f32_16x16x32_bf16 v[84:87], a[12:15], v[36:39], v[84:87]\n"
        "v_mfma_f32_16x16x32_bf16 v[88:91], a[20:23], v[32:35], v[88:91]\n"
        "v_mfma_f32_16x16x32_bf16 v[92:95], a[20:23], v[36:39], v[92:95]\n"
        "s_waitcnt vmcnt(18)\n"
        "s_waitcnt lgkmcnt(0)\n"
        "s_barrier\n"
        "s_add_u32 s54, s92, 0x600000\n"
        "s_addc_u32 s55, s93, 0\n"
        "s_add_u32 m0, s59, 0x16000\n"
        "s_nop 0\n"
        "global_load_lds_dwordx4 v3, s[54:55] offset:0\n"
        "s_add_u32 m0, s59, 0x16f80\n"
        "s_nop 0\n"
        "global_load_lds_dwordx4 v3, s[54:55] offset:128\n"
        "s_add_u32 m0, s59, 0x17f00\n"
        "s_nop 0\n"
        "global_load_lds_dwordx4 v3, s[54:55] offset:256\n"
        "s_add_u32 s54, s92, 0x660000\n"
        "s_addc_u32 s55, s93, 0\n"
        "s_add_u32 m0, s59, 0x19000\n"
        "s_nop 0\n"
        "global_load_lds_dwordx4 v3, s[54:55] offset:0\n"
        "s_add_u32 m0, s59, 0x19f80\n"
        "s_nop 0\n"
        "global_load_lds_dwordx4 v3, s[54:55] offset:128\n"
        "s_add_u32 m0, s59, 0x1af00\n"
        "s_nop 0\n"
        "global_load_lds_dwordx4 v3, s[54:55] offset:256\n"
        "v_and_b32 v24, 15, v0\n"
        "v_lshrrev_b32 v25, 4, v0\n"
        "v_lshrrev_b32 v26, 2, v24\n"
        "v_lshlrev_b32 v26, 6, v26\n"
        "v_and_b32 v27, 1, v0\n"
        "v_lshl_add_u32 v26, v27, 5, v26\n"
        "v_lshl_add_u32 v26, v25, 3, v26\n"
        "v_bfe_u32 v27, v0, 1, 1\n"
        "v_lshl_add_u32 v26, v27, 1, v26\n"
        "s_mul_i32 s47, s43, 0x300\n"
        "s_add_u32 s47, s47, 53248\n"
        "v_add_u32 v8, s47, v26\n"
        "v_lshlrev_b32 v26, 8, v25\n"
        "v_lshl_add_u32 v26, v24, 3, v26\n"
        "v_add_u32 v9, 53248, v26\n"
        "v_and_b32 v27, 1, v25\n"
        "v_lshlrev_b32 v26, 14, v27\n"
        "v_lshl_add_u32 v26, v24, 4, v26\n"
        "v_lshrrev_b32 v28, 1, v25\n"
        "v_lshl_add_u32 v26, v28, 3, v26\n"
        "s_mul_i32 s47, s43, 0x28000\n"
        "s_lshl_b32 s68, s40, 8\n"
        "s_add_u32 s47, s47, s68\n"
        "v_add_u32 v10, s47, v26\n"
        "v_add_u32 v11, 0x8000, v10\n"
        "v_add_u32 v12, 0x10000, v10\n"
        "v_add_u32 v13, 0x18000, v10\n"
        "v_add_u32 v14, 0x20000, v10\n"
        "v_lshrrev_b32 v26, 1, v25\n"
        "v_lshl_add_u32 v26, v26, 4, v24\n"
        "v_lshlrev_b32 v26, 4, v26\n"
        "v_lshl_add_u32 v26, v27, 3, v26\n"
        "s_mul_i32 s47, s43, 0x3\n"
        "s_add_u32 s47, s47, 0\n"
        "s_and_b32 s68, s47, 1\n"
        "s_lshl_b32 s68, s68, 9\n"
        "s_lshr_b32 s47, s47, 1\n"
        "s_lshl_b32 s47, s47, 11\n"
        "s_add_u32 s47, s47, s68\n"
        "s_add_u32 s47, s47, 40960\n"
        "v_add_u32 v16, s47, v26\n"
        "s_mul_i32 s47, s43, 0x3\n"
        "s_add_u32 s47, s47, 1\n"
        "s_and_b32 s68, s47, 1\n"
        "s_lshl_b32 s68, s68, 9\n"
        "s_lshr_b32 s47, s47, 1\n"
        "s_lshl_b32 s47, s47, 11\n"
        "s_add_u32 s47, s47, s68\n"
        "s_add_u32 s47, s47, 40960\n"
        "v_add_u32 v17, s47, v26\n"
        "s_mul_i32 s47, s43, 0x3\n"
        "s_add_u32 s47, s47, 2\n"
        "s_and_b32 s68, s47, 1\n"
        "s_lshl_b32 s68, s68, 9\n"
        "s_lshr_b32 s47, s47, 1\n"
        "s_lshl_b32 s47, s47, 11\n"
        "s_add_u32 s47, s47, s68\n"
        "s_add_u32 s47, s47, 40960\n"
        "v_add_u32 v18, s47, v26\n"
        "s_mul_i32 s47, s43, 0xa0000\n"
        "s_lshr_b32 s68, s40, 3\n"
        "s_lshl_b32 s68, s68, 14\n"
        "s_add_u32 s47, s47, s68\n"
        "s_and_b32 s68, s40, 7\n"
        "s_lshl_b32 s68, s68, 10\n"
        "s_add_u32 s47, s47, s68\n"
        "v_add_u32 v19, s47, v1\n"
        "s_mov_b32 s62, 0xffffffff\n"
        "s_mov_b32 s63, 0x0\n"
        "ds_read_b128 v[24:27], v1 offset:4096\n"
        "ds_read_b128 v[28:31], v1 offset:5120\n"
        "ds_read_b128 v[32:35], v1 offset:6144\n"
        "ds_read_b128 v[36:39], v1 offset:7168\n"
        "v_add_u32 v20, 0x1c000, v5\n"
        "ds_read_b64_tr_b16 a[48:49], v20\n"
        "ds_read_b64_tr_b16 a[50:51], v20 offset:512\n"
        "v_add_u32 v21, 0x1c000, v6\n"
        "ds_read_b64_tr_b16 a[56:57], v21\n"
        "ds_read_b64_tr_b16 a[58:59], v21 offset:512\n"
        "v_add_u32 v22, 0x1c000, v7\n"
        "ds_read_b64_tr_b16 a[64:65], v22\n"
        "ds_read_b64_tr_b16 a[66:67], v22 offset:512\n"
        "v_add_u32 v20, 0x1f000, v5\n"
        "ds_read_b64_tr_b16 a[52:53], v20\n"
        "ds_read_b64_tr_b16 a[54:55], v20 offset:512\n"
        "v_add_u32 v21, 0x1f000, v6\n"
        "ds_read_b64_tr_b16 a[60:61], v21\n"
        "ds_read_b64_tr_b16 a[62:63], v21 offset:512\n"
        "v_add_u32 v22, 0x1f000, v7\n"
        "ds_read_b64_tr_b16 a[68:69], v22\n"
        "ds_read_b64_tr_b16 a[70:71], v22 offset:512\n"
        "s_waitcnt lgkmcnt(12)\n"
        "v_mfma_f32_16x16x32_bf16 v[72:75], a[24:27], v[24:27], v[72:75]\n"
        "v_mfma_f32_16x16x32_bf16 v[76:79], a[24:27], v[28:31], v[76:79]\n"
        "v_mfma_f32_16x16x32_bf16 v[80:83], a[32:35], v[24:27], v[80:83]\n"
        "v_mfma_f32_16x16x32_bf16 v[84:87], a[32:35], v[28:31], v[84:87]\n"
        "v_mfma_f32_16x16x32_bf16 v[88:91], a[40:43], v[24:27], v[88:91]\n"
        "v_mfma_f32_16x16x32_bf16 v[92:95], a[40:43], v[28:31], v[92:95]\n"
        "v_mfma_f32_16x16x32_bf16 v[72:75], a[28:31], v[32:35], v[72:75]\n"
        "v_mfma_f32_16x16x32_bf16 v[76:79], a[28:31], v[36:39], v[76:79]\n"
        "v_mfma_f32_16x16x32_bf16 v[80:83], a[36:39], v[32:35], v[80:83]\n"
        "v_mfma_f32_16x16x32_bf16 v[84:87], a[36:39], v[36:39], v[84:87]\n"
        "v_mfma_f32_16x16x32_bf16 v[88:91], a[44:47], v[32:35], v[88:91]\n"
        "v_mfma_f32_16x16x32_bf16 v[92:95], a[44:47], v[36:39], v[92:95]\n"
        "s_waitcnt vmcnt(18)\n"
        "s_waitcnt lgkmcnt(0)\n"
        "s_barrier\n"
        "s_add_u32 s54, s92, 0x6c0000\n"
        "s_addc_u32 s55, s93, 0\n"
        "s_add_u32 m0, s59, 0x1c000\n"
        "s_nop 0\n"
        "global_load_lds_dwordx4 v3, s[54:55] offset:0\n"
        "s_add_u32 m0, s59, 0x1cf80\n"
        "s_nop 0\n"
        "global_load_lds_dwordx4 v3, s[54:55] offset:128\n"
        "s_add_u32 m0, s59, 0x1df00\n"
        "s_nop 0\n"
        "global_load_lds_dwordx4 v3, s[54:55] offset:256\n"
        "s_add_u32 s54, s92, 0x720000\n"
        "s_addc_u32 s55, s93, 0\n"
        "s_add_u32 m0, s59, 0x1f000\n"
        "s_nop 0\n"
        "global_load_lds_dwordx4 v3, s[54:55] offset:0\n"
        "s_add_u32 m0, s59, 0x1ff80\n"
        "s_nop 0\n"
        "global_load_lds_dwordx4 v3, s[54:55] offset:128\n"
        "s_add_u32 m0, s59, 0x20f00\n"
        "s_nop 0\n"
        "global_load_lds_dwordx4 v3, s[54:55] offset:256\n"
        "ds_read_b128 v[24:27], v1 offset:8192\n"
        "ds_read_b128 v[28:31], v1 offset:9216\n"
        "ds_read_b128 v[32:35], v1 offset:10240\n"
        "ds_read_b128 v[36:39], v1 offset:11264\n"
        "v_add_u32 v20, 0x22000, v5\n"
        "ds_read_b64_tr_b16 a[72:73], v20\n"
        "ds_read_b64_tr_b16 a[74:75], v20 offset:512\n"
        "v_add_u32 v21, 0x22000, v6\n"
        "ds_read_b64_tr_b16 a[80:81], v21\n"
        "ds_read_b64_tr_b16 a[82:83], v21 offset:512\n"
        "v_add_u32 v22, 0x22000, v7\n"
        "ds_read_b64_tr_b16 a[88:89], v22\n"
        "ds_read_b64_tr_b16 a[90:91], v22 offset:512\n"
        "v_add_u32 v20, 0x25000, v5\n"
        "ds_read_b64_tr_b16 a[76:77], v20\n"
        "ds_read_b64_tr_b16 a[78:79], v20 offset:512\n"
        "v_add_u32 v21, 0x25000, v6\n"
        "ds_read_b64_tr_b16 a[84:85], v21\n"
        "ds_read_b64_tr_b16 a[86:87], v21 offset:512\n"
        "v_add_u32 v22, 0x25000, v7\n"
        "ds_read_b64_tr_b16 a[92:93], v22\n"
        "ds_read_b64_tr_b16 a[94:95], v22 offset:512\n"
        "s_waitcnt lgkmcnt(12)\n"
        "v_mfma_f32_16x16x32_bf16 v[72:75], a[48:51], v[24:27], v[72:75]\n"
        "v_mfma_f32_16x16x32_bf16 v[76:79], a[48:51], v[28:31], v[76:79]\n"
        "v_mfma_f32_16x16x32_bf16 v[80:83], a[56:59], v[24:27], v[80:83]\n"
        "v_mfma_f32_16x16x32_bf16 v[84:87], a[56:59], v[28:31], v[84:87]\n"
        "v_mfma_f32_16x16x32_bf16 v[88:91], a[64:67], v[24:27], v[88:91]\n"
        "v_mfma_f32_16x16x32_bf16 v[92:95], a[64:67], v[28:31], v[92:95]\n"
        "v_mfma_f32_16x16x32_bf16 v[72:75], a[52:55], v[32:35], v[72:75]\n"
        "v_mfma_f32_16x16x32_bf16 v[76:79], a[52:55], v[36:39], v[76:79]\n"
        "v_mfma_f32_16x16x32_bf16 v[80:83], a[60:63], v[32:35], v[80:83]\n"
        "v_mfma_f32_16x16x32_bf16 v[84:87], a[60:63], v[36:39], v[84:87]\n"
        "v_mfma_f32_16x16x32_bf16 v[88:91], a[68:71], v[32:35], v[88:91]\n"
        "v_mfma_f32_16x16x32_bf16 v[92:95], a[68:71], v[36:39], v[92:95]\n"
        "s_waitcnt vmcnt(18)\n"
        "s_waitcnt lgkmcnt(0)\n"
        "s_barrier\n"
        "s_add_u32 s54, s92, 0x0\n"
        "s_addc_u32 s55, s93, 0\n"
        "s_add_u32 m0, s59, 0x22000\n"
        "s_nop 0\n"
        "global_load_lds_dwordx4 v3, s[54:55] offset:0\n"
        "s_add_u32 m0, s59, 0x22f80\n"
        "s_nop 0\n"
        "global_load_lds_dwordx4 v3, s[54:55] offset:128\n"
        "s_add_u32 m0, s59, 0x23f00\n"
        "s_nop 0\n"
        "global_load_lds_dwordx4 v3, s[54:55] offset:256\n"
        "s_add_u32 s54, s92, 0x60000\n"
        "s_addc_u32 s55, s93, 0\n"
        "s_add_u32 m0, s59, 0x25000\n"
        "s_nop 0\n"
        "global_load_lds_dwordx4 v3, s[54:55] offset:0\n"
        "s_add_u32 m0, s59, 0x25f80\n"
        "s_nop 0\n"
        "global_load_lds_dwordx4 v3, s[54:55] offset:128\n"
        "s_add_u32 m0, s59, 0x26f00\n"
        "s_nop 0\n"
        "global_load_lds_dwordx4 v3, s[54:55] offset:256\n"
        "global_load_dword v4, v15, s[90:91] sc1\n"
        "ds_read_b128 v[24:27], v1 offset:12288\n"
        "ds_read_b128 v[28:31], v1 offset:13312\n"
        "ds_read_b128 v[32:35], v1 offset:14336\n"
        "ds_read_b128 v[36:39], v1 offset:15360\n"
        "v_add_u32 v20, 0xa000, v5\n"
        "ds_read_b64_tr_b16 a[96:97], v20\n"
        "ds_read_b64_tr_b16 a[98:99], v20 offset:512\n"
        "v_add_u32 v21, 0xa000, v6\n"
        "ds_read_b64_tr_b16 a[104:105], v21\n"
        "ds_read_b64_tr_b16 a[106:107], v21 offset:512\n"
        "v_add_u32 v22, 0xa000, v7\n"
        "ds_read_b64_tr_b16 a[112:113], v22\n"
        "ds_read_b64_tr_b16 a[114:115], v22 offset:512\n"
        "v_add_u32 v20, 0xd000, v5\n"
        "ds_read_b64_tr_b16 a[100:101], v20\n"
        "ds_read_b64_tr_b16 a[102:103], v20 offset:512\n"
        "v_add_u32 v21, 0xd000, v6\n"
        "ds_read_b64_tr_b16 a[108:109], v21\n"
        "ds_read_b64_tr_b16 a[110:111], v21 offset:512\n"
        "v_add_u32 v22, 0xd000, v7\n"
        "ds_read_b64_tr_b16 a[116:117], v22\n"
        "ds_read_b64_tr_b16 a[118:119], v22 offset:512\n"
        "s_waitcnt lgkmcnt(12)\n"
        "v_mfma_f32_16x16x32_bf16 v[72:75], a[72:75], v[24:27], v[72:75]\n"
        "v_mfma_f32_16x16x32_bf16 v[76:79], a[72:75], v[28:31], v[76:79]\n"
        "v_mfma_f32_16x16x32_bf16 v[80:83], a[80:83], v[24:27], v[80:83]\n"
        "v_mfma_f32_16x16x32_bf16 v[84:87], a[80:83], v[28:31], v[84:87]\n"
        "v_mfma_f32_16x16x32_bf16 v[88:91], a[88:91], v[24:27], v[88:91]\n"
        "v_mfma_f32_16x16x32_bf16 v[92:95], a[88:91], v[28:31], v[92:95]\n"
        "v_mfma_f32_16x16x32_bf16 v[72:75], a[76:79], v[32:35], v[72:75]\n"
        "v_mfma_f32_16x16x32_bf16 v[76:79], a[76:79], v[36:39], v[76:79]\n"
        "v_mfma_f32_16x16x32_bf16 v[80:83], a[84:87], v[32:35], v[80:83]\n"
        "v_mfma_f32_16x16x32_bf16 v[84:87], a[84:87], v[36:39], v[84:87]\n"
        "v_mfma_f32_16x16x32_bf16 v[88:91], a[92:95], v[32:35], v[88:91]\n"
        "v_mfma_f32_16x16x32_bf16 v[92:95], a[92:95], v[36:39], v[92:95]\n"
        "s_waitcnt vmcnt(19)\n"
        "s_waitcnt lgkmcnt(0)\n"
        "s_barrier\n"
        "s_add_u32 s54, s92, 0x3c0000\n"
        "s_addc_u32 s55, s93, 0\n"
        "s_add_u32 m0, s59, 0xa000\n"
        "s_nop 0\n"
        "global_load_lds_dwordx4 v3, s[54:55] offset:0\n"
        "s_add_u32 m0, s59, 0xaf80\n"
        "s_nop 0\n"
        "global_load_lds_dwordx4 v3, s[54:55] offset:128\n"
        "s_add_u32 m0, s59, 0xbf00\n"
        "s_nop 0\n"
        "global_load_lds_dwordx4 v3, s[54:55] offset:256\n"
        "s_add_u32 s54, s92, 0x420000\n"
        "s_addc_u32 s55, s93, 0\n"
        "s_add_u32 m0, s59, 0xd000\n"
        "s_nop 0\n"
        "global_load_lds_dwordx4 v3, s[54:55] offset:0\n"
        "s_add_u32 m0, s59, 0xdf80\n"
        "s_nop 0\n"
        "global_load_lds_dwordx4 v3, s[54:55] offset:128\n"
        "s_add_u32 m0, s59, 0xef00\n"
        "s_nop 0\n"
        "global_load_lds_dwordx4 v3, s[54:55] offset:256\n"
        "ds_read_b128 v[24:27], v1 offset:18432\n"
        "ds_read_b128 v[28:31], v1 offset:19456\n"
        "ds_read_b128 v[32:35], v1 offset:20480\n"
        "ds_read_b128 v[36:39], v1 offset:21504\n"
        "v_add_u32 v20, 0x10000, v5\n"
        "ds_read_b64_tr_b16 a[120:121], v20\n"
        "ds_read_b64_tr_b16 a[122:123], v20 offset:512\n"
        "v_add_u32 v21, 0x10000, v6\n"
        "ds_read_b64_tr_b16 a[128:129], v21\n"
        "ds_read_b64_tr_b16 a[130:131], v21 offset:512\n"
        "v_add_u32 v22, 0x10000, v7\n"
        "ds_read_b64_tr_b16 a[136:137], v22\n"
        "ds_read_b64_tr_b16 a[138:139], v22 offset:512\n"
        "v_add_u32 v20, 0x13000, v5\n"
        "ds_read_b64_tr_b16 a[124:125], v20\n"
        "ds_read_b64_tr_b16 a[126:127], v20 offset:512\n"
        "v_add_u32 v21, 0x13000, v6\n"
        "ds_read_b64_tr_b16 a[132:133], v21\n"
        "ds_read_b64_tr_b16 a[134:135], v21 offset:512\n"
        "v_add_u32 v22, 0x13000, v7\n"
        "ds_read_b64_tr_b16 a[140:141], v22\n"
        "ds_read_b64_tr_b16 a[142:143], v22 offset:512\n"
        "s_waitcnt lgkmcnt(12)\n"
        "v_mfma_f32_16x16x32_bf16 v[72:75], a[96:99], v[24:27], v[72:75]\n"
        "v_mfma_f32_16x16x32_bf16 v[76:79], a[96:99], v[28:31], v[76:79]\n"
        "v_mfma_f32_16x16x32_bf16 v[80:83], a[104:107], v[24:27], v[80:83]\n"
        "v_mfma_f32_16x16x32_bf16 v[84:87], a[104:107], v[28:31], v[84:87]\n"
        "v_mfma_f32_16x16x32_bf16 v[88:91], a[112:115], v[24:27], v[88:91]\n"
        "v_mfma_f32_16x16x32_bf16 v[92:95], a[112:115], v[28:31], v[92:95]\n"
        "v_mfma_f32_16x16x32_bf16 v[72:75], a[100:103], v[32:35], v[72:75]\n"
        "v_mfma_f32_16x16x32_bf16 v[76:79], a[100:103], v[36:39], v[76:79]\n"
        "v_mfma_f32_16x16x32_bf16 v[80:83], a[108:111], v[32:35], v[80:83]\n"
        "v_mfma_f32_16x16x32_bf16 v[84:87], a[108:111], v[36:39], v[84:87]\n"
        "v_mfma_f32_16x16x32_bf16 v[88:91], a[116:119], v[32:35], v[88:91]\n"
        "v_mfma_f32_16x16x32_bf16 v[92:95], a[116:119], v[36:39], v[92:95]\n"
        "s_waitcnt vmcnt(19)\n"
        "s_waitcnt lgkmcnt(0)\n"
        "s_barrier\n"
        "ds_read_b128 v[24:27], v1 offset:24576\n"
        "ds_read_b128 v[28:31], v1 offset:25600\n"
        "ds_read_b128 v[32:35], v1 offset:26624\n"
        "ds_read_b128 v[36:39], v1 offset:27648\n"
        "v_add_u32 v20, 0x16000, v5\n"
        "ds_read_b64_tr_b16 a[144:145], v20\n"
        "ds_read_b64_tr_b16 a[146:147], v20 offset:512\n"
        "v_add_u32 v21, 0x16000, v6\n"
        "ds_read_b64_tr_b16 a[152:153], v21\n"
        "ds_read_b64_tr_b16 a[154:155], v21 offset:512\n"
        "v_add_u32 v22, 0x16000, v7\n"
        "ds_read_b64_tr_b16 a[160:161], v22\n"
        "ds_read_b64_tr_b16 a[162:163], v22 offset:512\n"
        "v_add_u32 v20, 0x19000, v5\n"
        "ds_read_b64_tr_b16 a[148:149], v20\n"
        "ds_read_b64_tr_b16 a[150:151], v20 offset:512\n"
        "v_add_u32 v21, 0x19000, v6\n"
        "ds_read_b64_tr_b16 a[156:157], v21\n"
        "ds_read_b64_tr_b16 a[158:159], v21 offset:512\n"
        "v_add_u32 v22, 0x19000, v7\n"
        "ds_read_b64_tr_b16 a[164:165], v22\n"
        "ds_read_b64_tr_b16 a[166:167], v22 offset:512\n"
        "s_waitcnt lgkmcnt(12)\n"
        "v_mfma_f32_16x16x32_bf16 v[72:75], a[120:123], v[24:27], v[72:75]\n"
        "v_mfma_f32_16x16x32_bf16 v[76:79], a[120:123], v[28:31], v[76:79]\n"
        "v_mfma_f32_16x16x32_bf16 v[80:83], a[128:131], v[24:27], v[80:83]\n"
        "v_mfma_f32_16x16x32_bf16 v[84:87], a[128:131], v[28:31], v[84:87]\n"
        "v_mfma_f32_16x16x32_bf16 v[88:91], a[136:139], v[24:27], v[88:91]\n"
        "v_mfma_f32_16x16x32_bf16 v[92:95], a[136:139], v[28:31], v[92:95]\n"
        "v_mfma_f32_16x16x32_bf16 v[72:75], a[124:127], v[32:35], v[72:75]\n"
        "v_mfma_f32_16x16x32_bf16 v[76:79], a[124:127], v[36:39], v[76:79]\n"
        "v_mfma_f32_16x16x32_bf16 v[80:83], a[132:135], v[32:35], v[80:83]\n"
        "v_mfma_f32_16x16x32_bf16 v[84:87], a[132:135], v[36:39], v[84:87]\n"
        "v_mfma_f32_16x16x32_bf16 v[88:91], a[140:143], v[32:35], v[88:91]\n"
        "v_mfma_f32_16x16x32_bf16 v[92:95], a[140:143], v[36:39], v[92:95]\n"
        "ds_write_b128 v2, v[40:43] offset:0\n"
        "ds_write_b128 v2, v[44:47] offset:1024\n"
        "ds_write_b128 v2, v[48:51] offset:2048\n"
        "ds_write_b128 v2, v[52:55] offset:3072\n"
        "ds_write_b128 v2, v[56:59] offset:4096\n"
        "ds_write_b128 v2, v[60:63] offset:5120\n"
        "s_waitcnt vmcnt(13)\n"
        "s_waitcnt lgkmcnt(0)\n"
        "s_barrier\n"
        "ds_read_b128 v[24:27], v1 offset:30720\n"
        "ds_read_b128 v[28:31], v1 offset:31744\n"
        "ds_read_b128 v[32:35], v1 offset:32768\n"
        "ds_read_b128 v[36:39], v1 offset:33792\n"
        "v_add_u32 v20, 0x1c000, v5\n"
        "ds_read_b64_tr_b16 a[168:169], v20\n"
        "ds_read_b64_tr_b16 a[170:171], v20 offset:512\n"
        "v_add_u32 v21, 0x1c000, v6\n"
        "ds_read_b64_tr_b16 a[176:177], v21\n"
        "ds_read_b64_tr_b16 a[178:179], v21 offset:512\n"
        "v_add_u32 v22, 0x1c000, v7\n"
        "ds_read_b64_tr_b16 a[184:185], v22\n"
        "ds_read_b64_tr_b16 a[186:187], v22 offset:512\n"
        "v_add_u32 v20, 0x1f000, v5\n"
        "ds_read_b64_tr_b16 a[172:173], v20\n"
        "ds_read_b64_tr_b16 a[174:175], v20 offset:512\n"
        "v_add_u32 v21, 0x1f000, v6\n"
        "ds_read_b64_tr_b16 a[180:181], v21\n"
        "ds_read_b64_tr_b16 a[182:183], v21 offset:512\n"
        "v_add_u32 v22, 0x1f000, v7\n"
        "ds_read_b64_tr_b16 a[188:189], v22\n"
        "ds_read_b64_tr_b16 a[190:191], v22 offset:512\n"
        "s_waitcnt lgkmcnt(12)\n"
        "v_mfma_f32_16x16x32_bf16 v[72:75], a[144:147], v[24:27], v[72:75]\n"
        "v_mfma_f32_16x16x32_bf16 v[76:79], a[144:147], v[28:31], v[76:79]\n"
        "v_mfma_f32_16x16x32_bf16 v[80:83], a[152:155], v[24:27], v[80:83]\n"
        "v_mfma_f32_16x16x32_bf16 v[84:87], a[152:155], v[28:31], v[84:87]\n"
        "v_mfma_f32_16x16x32_bf16 v[88:91], a[160:163], v[24:27], v[88:91]\n"
        "v_mfma_f32_16x16x32_bf16 v[92:95], a[160:163], v[28:31], v[92:95]\n"
        "v_mfma_f32_16x16x32_bf16 v[72:75], a[148:151], v[32:35], v[72:75]\n"
        "v_mfma_f32_16x16x32_bf16 v[76:79], a[148:151], v[36:39], v[76:79]\n"
        "v_mfma_f32_16x16x32_bf16 v[80:83], a[156:159], v[32:35], v[80:83]\n"
        "v_mfma_f32_16x16x32_bf16 v[84:87], a[156:159], v[36:39], v[84:87]\n"
        "v_mfma_f32_16x16x32_bf16 v[88:91], a[164:167], v[32:35], v[88:91]\n"
        "v_mfma_f32_16x16x32_bf16 v[92:95], a[164:167], v[36:39], v[92:95]\n"
        "ds_write_b128 v2, v[96:99] offset:24576\n"
        "ds_write_b128 v2, v[100:103] offset:25600\n"
        "ds_write_b128 v2, v[104:107] offset:26624\n"
        "ds_write_b128 v2, v[108:111] offset:27648\n"
        "ds_write_b128 v2, v[112:115] offset:28672\n"
        "ds_write_b128 v2, v[116:119] offset:29696\n"
        "s_waitcnt vmcnt(7)\n"
        "s_waitcnt lgkmcnt(0)\n"
        "s_barrier\n"
        "ds_read_b128 v[24:27], v1 offset:36864\n"
        "ds_read_b128 v[28:31], v1 offset:37888\n"
        "ds_read_b128 v[32:35], v1 offset:38912\n"
        "ds_read_b128 v[36:39], v1 offset:39936\n"
        "v_add_u32 v20, 0x22000, v5\n"
        "ds_read_b64_tr_b16 v[40:41], v20\n"
        "ds_read_b64_tr_b16 v[42:43], v20 offset:512\n"
        "v_add_u32 v21, 0x22000, v6\n"
        "ds_read_b64_tr_b16 v[44:45], v21\n"
        "ds_read_b64_tr_b16 v[46:47], v21 offset:512\n"
        "v_add_u32 v22, 0x22000, v7\n"
        "ds_read_b64_tr_b16 v[48:49], v22\n"
        "ds_read_b64_tr_b16 v[50:51], v22 offset:512\n"
        "v_add_u32 v20, 0x25000, v5\n"
        "ds_read_b64_tr_b16 v[52:53], v20\n"
        "ds_read_b64_tr_b16 v[54:55], v20 offset:512\n"
        "v_add_u32 v21, 0x25000, v6\n"
        "ds_read_b64_tr_b16 v[56:57], v21\n"
        "ds_read_b64_tr_b16 v[58:59], v21 offset:512\n"
        "v_add_u32 v22, 0x25000, v7\n"
        "ds_read_b64_tr_b16 v[60:61], v22\n"
        "ds_read_b64_tr_b16 v[62:63], v22 offset:512\n"
        "s_waitcnt lgkmcnt(12)\n"
        "v_mfma_f32_16x16x32_bf16 v[72:75], a[168:171], v[24:27], v[72:75]\n"
        "v_mfma_f32_16x16x32_bf16 v[76:79], a[168:171], v[28:31], v[76:79]\n"
        "v_mfma_f32_16x16x32_bf16 v[80:83], a[176:179], v[24:27], v[80:83]\n"
        "v_mfma_f32_16x16x32_bf16 v[84:87], a[176:179], v[28:31], v[84:87]\n"
        "v_mfma_f32_16x16x32_bf16 v[88:91], a[184:187], v[24:27], v[88:91]\n"
        "v_mfma_f32_16x16x32_bf16 v[92:95], a[184:187], v[28:31], v[92:95]\n"
        "v_mfma_f32_16x16x32_bf16 v[72:75], a[172:175], v[32:35], v[72:75]\n"
        "v_mfma_f32_16x16x32_bf16 v[76:79], a[172:175], v[36:39], v[76:79]\n"
        "v_mfma_f32_16x16x32_bf16 v[80:83], a[180:183], v[32:35], v[80:83]\n"
        "v_mfma_f32_16x16x32_bf16 v[84:87], a[180:183], v[36:39], v[84:87]\n"
        "v_mfma_f32_16x16x32_bf16 v[88:91], a[188:191], v[32:35], v[88:91]\n"
        "v_mfma_f32_16x16x32_bf16 v[92:95], a[188:191], v[36:39], v[92:95]\n"
        "s_waitcnt vmcnt(0)\n"
        "s_waitcnt lgkmcnt(0)\n"
        "s_barrier\n"
        "ds_read_b128 v[24:27], v1 offset:16384\n"
        "ds_read_b128 v[28:31], v1 offset:17408\n"
        "ds_read_b128 v[32:35], v1 offset:22528\n"
        "ds_read_b128 v[36:39], v1 offset:23552\n"
        "v_add_u32 v20, 0xa000, v5\n"
        "ds_read_b64_tr_b16 v[96:97], v20\n"
        "ds_read_b64_tr_b16 v[98:99], v20 offset:512\n"
        "v_add_u32 v21, 0xa000, v6\n"
        "ds_read_b64_tr_b16 v[100:101], v21\n"
        "ds_read_b64_tr_b16 v[102:103], v21 offset:512\n"
        "v_add_u32 v22, 0xa000, v7\n"
        "ds_read_b64_tr_b16 v[104:105], v22\n"
        "ds_read_b64_tr_b16 v[106:107], v22 offset:512\n"
        "v_add_u32 v20, 0xd000, v5\n"
        "ds_read_b64_tr_b16 v[108:109], v20\n"
        "ds_read_b64_tr_b16 v[110:111], v20 offset:512\n"
        "v_add_u32 v21, 0xd000, v6\n"
        "ds_read_b64_tr_b16 v[112:113], v21\n"
        "ds_read_b64_tr_b16 v[114:115], v21 offset:512\n"
        "v_add_u32 v22, 0xd000, v7\n"
        "ds_read_b64_tr_b16 v[116:117], v22\n"
        "ds_read_b64_tr_b16 v[118:119], v22 offset:512\n"
        "s_waitcnt lgkmcnt(12)\n"
        "v_mfma_f32_16x16x32_bf16 v[72:75], v[40:43], v[24:27], v[72:75]\n"
        "v_mfma_f32_16x16x32_bf16 v[76:79], v[40:43], v[28:31], v[76:79]\n"
        "v_mfma_f32_16x16x32_bf16 v[80:83], v[44:47], v[24:27], v[80:83]\n"
        "v_mfma_f32_16x16x32_bf16 v[84:87], v[44:47], v[28:31], v[84:87]\n"
        "v_mfma_f32_16x16x32_bf16 v[88:91], v[48:51], v[24:27], v[88:91]\n"
        "v_mfma_f32_16x16x32_bf16 v[92:95], v[48:51], v[28:31], v[92:95]\n"
        "v_mfma_f32_16x16x32_bf16 v[72:75], v[52:55], v[32:35], v[72:75]\n"
        "v_mfma_f32_16x16x32_bf16 v[76:79], v[52:55], v[36:39], v[76:79]\n"
        "v_mfma_f32_16x16x32_bf16 v[80:83], v[56:59], v[32:35], v[80:83]\n"
        "v_mfma_f32_16x16x32_bf16 v[84:87], v[56:59], v[36:39], v[84:87]\n"
        "v_mfma_f32_16x16x32_bf16 v[88:91], v[60:63], v[32:35], v[88:91]\n"
        "v_mfma_f32_16x16x32_bf16 v[92:95], v[60:63], v[36:39], v[92:95]\n"
        "ds_write_b128 v23, v[40:43] offset:0\n"
        "ds_write_b128 v23, v[44:47] offset:1024\n"
        "ds_write_b128 v23, v[48:51] offset:2048\n"
        "ds_write_b128 v23, v[52:55] offset:3072\n"
        "ds_write_b128 v23, v[56:59] offset:4096\n"
        "ds_write_b128 v23, v[60:63] offset:5120\n"
        "s_waitcnt lgkmcnt(0)\n"
        "ds_read_b128 v[24:27], v1 offset:28672\n"
        "ds_read_b128 v[28:31], v1 offset:29696\n"
        "ds_read_b128 v[32:35], v1 offset:34816\n"
        "ds_read_b128 v[36:39], v1 offset:35840\n"
        "s_waitcnt lgkmcnt(0)\n"
        "v_mfma_f32_16x16x32_bf16 v[72:75], v[96:99], v[24:27], v[72:75]\n"
        "v_mfma_f32_16x16x32_bf16 v[76:79], v[96:99], v[28:31], v[76:79]\n"
        "v_mfma_f32_16x16x32_bf16 v[80:83], v[100:103], v[24:27], v[80:83]\n"
        "v_mfma_f32_16x16x32_bf16 v[84:87], v[100:103], v[28:31], v[84:87]\n"
        "v_mfma_f32_16x16x32_bf16 v[88:91], v[104:107], v[24:27], v[88:91]\n"
        "v_mfma_f32_16x16x32_bf16 v[92:95], v[104:107], v[28:31], v[92:95]\n"
        "v_mfma_f32_16x16x32_bf16 v[72:75], v[108:111], v[32:35], v[72:75]\n"
        "v_mfma_f32_16x16x32_bf16 v[76:79], v[108:111], v[36:39], v[76:79]\n"
        "v_mfma_f32_16x16x32_bf16 v[80:83], v[112:115], v[32:35], v[80:83]\n"
        "v_mfma_f32_16x16x32_bf16 v[84:87], v[112:115], v[36:39], v[84:87]\n"
        "v_mfma_f32_16x16x32_bf16 v[88:91], v[116:119], v[32:35], v[88:91]\n"
        "v_mfma_f32_16x16x32_bf16 v[92:95], v[116:119], v[36:39], v[92:95]\n"
        "ds_write_b128 v23, v[96:99] offset:24576\n"
        "ds_write_b128 v23, v[100:103] offset:25600\n"
        "ds_write_b128 v23, v[104:107] offset:26624\n"
        "ds_write_b128 v23, v[108:111] offset:27648\n"
        "ds_write_b128 v23, v[112:115] offset:28672\n"
        "ds_write_b128 v23, v[116:119] offset:29696\n"
        "s_waitcnt vmcnt(0) lgkmcnt(0)\n"
        "v_readfirstlane_b32 s47, v4\n"
        "s_nop 3\n"
        "s_and_b32 s68, s47, -2\n"
        "s_cmp_lg_u32 s68, s88\n"
        "s_cbranch_scc1 Lvo_rs_1\n"
        "Lvo_rb_2:\n"
        "s_and_b32 s47, s47, 1\n"
        "s_xor_b32 s47, s47, s41\n"
        "s_mul_i32 s89, s47, 0x3f\n"
        "s_xor_b32 s89, s89, 52\n"
        "s_lshr_b32 s44, s89, 0\n"
        "s_and_b32 s44, s44, 1\n"
        "s_lshl_b32 s44, s44, 13\n"
        "s_lshl_b32 s47, s43, 10\n"
        "v_add_u32 v5, s47, v1\n"
        "s_lshl_b32 s68, s42, 15\n"
        "s_add_u32 s69, s68, 2048\n"
        "v_add_u32 v3, s69, v5\n"
        "s_lshl_b32 s47, s43, 9\n"
        "v_lshlrev_b32 v24, 2, v0\n"
        "v_add_u32 v24, s47, v24\n"
        "v_add_u32 v6, 0x2000, v24\n"
        "s_add_u32 s69, s68, 16384\n"
        "v_add_u32 v4, s69, v24\n"
        "v_and_b32 v24, 31, v0\n"
        "v_lshlrev_b32 v15, 7, v24\n"
        "v_and_b32 v24, 1, v0\n"
        "v_mov_b32 v7, 0x4444\n"
        "v_mov_b32 v25, 0xeeee\n"
        "v_cmp_ne_u32 vcc, 0, v24\n"
        "s_nop 1\n"
        "v_cndmask_b32 v7, v7, v25, vcc\n"
        "s_add_u32 s47, s44, 0x185000\n"
        "s_add_u32 s86, s90, s47\n"
        "s_addc_u32 s87, s91, 0\n"
        "s_add_u32 s47, s44, 0x19d000\n"
        "s_add_u32 s94, s90, s47\n"
        "s_addc_u32 s95, s91, 0\n"
        "s_add_u32 s98, s90, 0x0\n"
        "s_addc_u32 s99, s91, 0\n"
        "s_lshl_b32 s47, s40, 7\n"
        "s_add_u32 s68, s47, 0x195000\n"
        "s_add_u32 s68, s68, s44\n"
        "v_mov_b32 v23, s68\n"
        "s_cmp_eq_u32 s43, 0\n"
        "s_cselect_b32 s36, 1, 0\n"
        "s_mov_b32 s37, 0\n"
        "s_mov_b32 s35, 100\n"
        "s_mov_b32 s60, 0\n"
        "s_mov_b64 s[92:93], 0\n"
        "s_mov_b32 s38, 0xffffff9c\n"
        "v_add_u32 v0, 0xc000, v2\n"
        "s_branch Lvo_first\n"
        ".p2align 6\n"
        "Lvo_loop:\n"
        "s_lshr_b32 s58, s45, 1\n"
        "v_mfma_f32_16x16x32_bf16 v[72:75], a[0:3], v[24:27], 0\n"
        "ds_read_b128 v[40:43], v1 offset:4096\n"
        "v_mfma_f32_16x16x32_bf16 v[76:79], a[0:3], v[28:31], 0\n"
        "ds_read_b128 v[44:47], v1 offset:5120\n"
        "v_mfma_f32_16x16x32_bf16 v[80:83], a[8:11], v[24:27], 0\n"
        "ds_read_b128 v[96:99], v0 offset:0\n"
        "v_mfma_f32_16x16x32_bf16 v[84:87], a[8:11], v[28:31], 0\n"
        "ds_read_b128 v[100:103], v0 offset:1024\n"
        "v_mfma_f32_16x16x32_bf16 v[88:91], a[16:19], v[24:27], 0\n"
        "s_add_u32 s68, s45, 2\n"
        "s_min_u32 s68, s68, 63\n"
        "s_mul_i32 s68, s68, 0xa000\n"
        "v_mfma_f32_16x16x32_bf16 v[92:95], a[16:19], v[28:31], 0\n"
        "s_add_u32 s47, s68, 0x200000\n"
        "s_add_u32 s48, s90, s47\n"
        "s_addc_u32 s49, s91, 0\n"
        "v_mfma_f32_16x16x32_bf16 v[72:75], a[4:7], v[32:35], v[72:75]\n"
        "ds_read_b128 v[24:27], v1 offset:6144\n"
        "v_mfma_f32_16x16x32_bf16 v[76:79], a[4:7], v[36:39], v[76:79]\n"
        "ds_read_b128 v[28:31], v1 offset:7168\n"
        "v_mfma_f32_16x16x32_bf16 v[80:83], a[12:15], v[32:35], v[80:83]\n"
        "ds_read_b128 v[104:107], v0 offset:2048\n"
        "v_mfma_f32_16x16x32_bf16 v[84:87], a[12:15], v[36:39], v[84:87]\n"
        "ds_read_b128 v[108:111], v0 offset:3072\n"
        "v_mfma_f32_16x16x32_bf16 v[88:91], a[20:23], v[32:35], v[88:91]\n"
        "s_and_b32 s88, s58, 1\n"
        "s_lshl_b32 s68, s88, 1\n"
        "s_add_u32 s68, s68, 1\n"
        "v_mfma_f32_16x16x32_bf16 v[92:95], a[20:23], v[36:39], v[92:95]\n"
        "s_lshr_b32 s69, s89, s68\n"
        "s_and_b32 s69, s69, 1\n"
        "s_lshl_b32 s69, s69, 13\n"
        "s_waitcnt lgkmcnt(6)\n"
        "v_mfma_f32_16x16x32_bf16 v[72:75], a[24:27], v[40:43], v[72:75]\n"
        "ds_read_b128 v[32:35], v1 offset:8192\n"
        "v_mfma_f32_16x16x32_bf16 v[76:79], a[24:27], v[44:47], v[76:79]\n"
        "ds_read_b128 v[36:39], v1 offset:9216\n"
        "v_mfma_f32_16x16x32_bf16 v[80:83], a[32:35], v[40:43], v[80:83]\n"
        "ds_read_b128 v[112:115], v0 offset:4096\n"
        "v_mfma_f32_16x16x32_bf16 v[84:87], a[32:35], v[44:47], v[84:87]\n"
        "ds_read_b128 v[116:119], v0 offset:5120\n"
        "v_mfma_f32_16x16x32_bf16 v[88:91], a[40:43], v[40:43], v[88:91]\n"
        "s_and_b32 s88, s58, 1\n"
        "s_lshl_b32 s88, s88, 22\n"
        "s_add_u32 s47, s88, 0x2c00000\n"
        "v_mfma_f32_16x16x32_bf16 v[92:95], a[40:43], v[44:47], v[92:95]\n"
        "s_add_u32 s47, s47, s69\n"
        "s_add_u32 s52, s90, s47\n"
        "s_addc_u32 s53, s91, 0\n"
        "s_waitcnt lgkmcnt(6)\n"
        "v_mfma_f32_16x16x32_bf16 v[72:75], a[28:31], v[24:27], v[72:75]\n"
        "ds_read_b128 v[40:43], v1 offset:10240\n"
        "v_mfma_f32_16x16x32_bf16 v[76:79], a[28:31], v[28:31], v[76:79]\n"
        "ds_read_b128 v[44:47], v1 offset:11264\n"
        "v_mfma_f32_16x16x32_bf16 v[80:83], a[36:39], v[24:27], v[80:83]\n"
        "ds_read_b128 v[120:123], v0 offset:24576\n"
        "v_mfma_f32_16x16x32_bf16 v[84:87], a[36:39], v[28:31], v[84:87]\n"
        "ds_read_b128 v[124:127], v0 offset:25600\n"
        "v_mfma_f32_16x16x32_bf16 v[88:91], a[44:47], v[24:27], v[88:91]\n"
        "s_add_u32 s68, s68, 1\n"
        "s_lshr_b32 s56, s89, s68\n"
        "s_and_b32 s56, s56, 1\n"
        "v_mfma_f32_16x16x32_bf16 v[92:95], a[44:47], v[28:31], v[92:95]\n"
        "s_lshl_b32 s56, s56, 13\n"
        "s_sub_u32 s56, s56, s69\n"
        "s_cmp_eq_u32 s43, 3\n"
        "s_waitcnt lgkmcnt(6)\n"
        "v_mfma_f32_16x16x32_bf16 v[72:75], a[48:51], v[32:35], v[72:75]\n"
        "ds_read_b128 v[24:27], v1 offset:12288\n"
        "v_mfma_f32_16x16x32_bf16 v[76:79], a[48:51], v[36:39], v[76:79]\n"
        "ds_read_b128 v[28:31], v1 offset:13312\n"
        "v_mfma_f32_16x16x32_bf16 v[80:83], a[56:59], v[32:35], v[80:83]\n"
        "s_cselect_b32 s56, s56, 0\n"
        "s_add_u32 s56, s56, 0x10000\n"
        "v_mfma_f32_16x16x32_bf16 v[84:87], a[56:59], v[36:39], v[84:87]\n"
        "v_mfma_f32_16x16x32_bf16 v[88:91], a[64:67], v[32:35], v[88:91]\n"
        "v_mfma_f32_16x16x32_bf16 v[92:95], a[64:67], v[36:39], v[92:95]\n"
        "s_waitcnt lgkmcnt(4)\n"
        "v_mfma_f32_16x16x32_bf16 v[72:75], a[52:55], v[40:43], v[72:75]\n"
        "ds_read_b128 v[32:35], v1 offset:14336\n"
        "v_mfma_f32_16x16x32_bf16 v[76:79], a[52:55], v[44:47], v[76:79]\n"
        "ds_read_b128 v[36:39], v1 offset:15360\n"
        "v_mfma_f32_16x16x32_bf16 v[80:83], a[60:63], v[40:43], v[80:83]\n"
        "v_mfma_f32_16x16x32_bf16 v[84:87], a[60:63], v[44:47], v[84:87]\n"
        "v_mfma_f32_16x16x32_bf16 v[88:91], a[68:71], v[40:43], v[88:91]\n"
        "v_mfma_f32_16x16x32_bf16 v[92:95], a[68:71], v[44:47], v[92:95]\n"
        "s_waitcnt lgkmcnt(2)\n"
        "v_mfma_f32_16x16x32_bf16 v[72:75], a[72:75], v[24:27], v[72:75]\n"
        "ds_read_b128 v[40:43], v1 offset:16384\n"
        "v_mfma_f32_16x16x32_bf16 v[76:79], a[72:75], v[28:31], v[76:79]\n"
        "ds_read_b128 v[44:47], v1 offset:17408\n"
        "v_mfma_f32_16x16x32_bf16 v[80:83], a[80:83], v[24:27], v[80:83]\n"
        "v_mfma_f32_16x16x32_bf16 v[84:87], a[80:83], v[28:31], v[84:87]\n"
        "v_mfma_f32_16x16x32_bf16 v[88:91], a[88:91], v[24:27], v[88:91]\n"
        "v_mfma_f32_16x16x32_bf16 v[92:95], a[88:91], v[28:31], v[92:95]\n"
        "s_waitcnt lgkmcnt(2)\n"
        "v_mfma_f32_16x16x32_bf16 v[72:75], a[76:79], v[32:35], v[72:75]\n"
        "ds_read_b128 v[24:27], v1 offset:18432\n"
        "v_mfma_f32_16x16x32_bf16 v[76:79], a[76:79], v[36:39], v[76:79]\n"
        "ds_read_b128 v[28:31], v1 offset:19456\n"
        "v_mfma_f32_16x16x32_bf16 v[80:83], a[84:87], v[32:35], v[80:83]\n"
        "v_mfma_f32_16x16x32_bf16 v[84:87], a[84:87], v[36:39], v[84:87]\n"
        "v_mfma_f32_16x16x32_bf16 v[88:91], a[92:95], v[32:35], v[88:91]\n"
        "v_mfma_f32_16x16x32_bf16 v[92:95], a[92:95], v[36:39], v[92:95]\n"
        "s_barrier\n"
        "s_waitcnt lgkmcnt(2)\n"
        "s_waitcnt lgkmcnt(15)\n"
        "v_mfma_f32_16x16x32_bf16 v[72:75], v[96:99], v[40:43], v[72:75]\n"
        "ds_read_b128 v[32:35], v1 offset:20480\n"
        "v_mfma_f32_16x16x32_bf16 v[76:79], v[96:99], v[44:47], v[76:79]\n"
        "ds_read_b128 v[36:39], v1 offset:21504\n"
        "s_waitcnt lgkmcnt(15)\n"
        "v_mfma_f32_16x16x32_bf16 v[80:83], v[100:103], v[40:43], v[80:83]\n"
        "ds_read_b128 v[96:99], v0 offset:26624\n"
        "v_mfma_f32_16x16x32_bf16 v[84:87], v[100:103], v[44:47], v[84:87]\n"
        "s_add_u32 s47, s96, 0\n"
        "s_add_u32 s54, s48, s47\n"
        "s_addc_u32 s55, s49, 0\n"
        "s_waitcnt lgkmcnt(15)\n"
        "v_mfma_f32_16x16x32_bf16 v[88:91], v[104:107], v[40:43], v[88:91]\n"
        "s_add_u32 m0, s47, 0\n"
        "v_mfma_f32_16x16x32_bf16 v[92:95], v[104:107], v[44:47], v[92:95]\n"
        "global_load_lds_dwordx4 v1, s[54:55] offset:0 sc1\n"
        "s_waitcnt lgkmcnt(3)\n"
        "v_mfma_f32_16x16x32_bf16 v[72:75], a[96:99], v[24:27], v[72:75]\n"
        "ds_read_b128 v[40:43], v1 offset:22528\n"
        "v_mfma_f32_16x16x32_bf16 v[76:79], a[96:99], v[28:31], v[76:79]\n"
        "ds_read_b128 v[44:47], v1 offset:23552\n"
        "v_mfma_f32_16x16x32_bf16 v[80:83], a[104:107], v[24:27], v[80:83]\n"
        "ds_read_b128 v[100:103], v0 offset:27648\n"
        "v_mfma_f32_16x16x32_bf16 v[84:87], a[104:107], v[28:31], v[84:87]\n"
        "ds_read_b128 v[104:107], v0 offset:28672\n"
        "v_mfma_f32_16x16x32_bf16 v[88:91], a[112:115], v[24:27], v[88:91]\n"
        "v_mfma_f32_16x16x32_bf16 v[92:95], a[112:115], v[28:31], v[92:95]\n"
        "s_waitcnt lgkmcnt(5)\n"
        "v_mfma_f32_16x16x32_bf16 v[72:75], a[100:103], v[32:35], v[72:75]\n"
        "ds_read_b128 v[24:27], v1 offset:24576\n"
        "v_mfma_f32_16x16x32_bf16 v[76:79], a[100:103], v[36:39], v[76:79]\n"
        "ds_read_b128 v[28:31], v1 offset:25600\n"
        "v_mfma_f32_16x16x32_bf16 v[80:83], a[108:111], v[32:35], v[80:83]\n"
        "global_load_lds_dwordx4 v1, s[54:55] offset:1024 sc1\n"
        "v_mfma_f32_16x16x32_bf16 v[84:87], a[108:111], v[36:39], v[84:87]\n"
        "v_mfma_f32_16x16x32_bf16 v[88:91], a[116:119], v[32:35], v[88:91]\n"
        "v_mfma_f32_16x16x32_bf16 v[92:95], a[116:119], v[36:39], v[92:95]\n"
        "global_load_lds_dwordx4 v1, s[54:55] offset:2048 sc1\n"
        "s_waitcnt lgkmcnt(4)\n"
        "s_waitcnt lgkmcnt(15)\n"
        "v_mfma_f32_16x16x32_bf16 v[72:75], v[108:111], v[40:43], v[72:75]\n"
        "ds_read_b128 v[32:35], v1 offset:26624\n"
        "v_mfma_f32_16x16x32_bf16 v[76:79], v[108:111], v[44:47], v[76:79]\n"
        "ds_read_b128 v[36:39], v1 offset:27648\n"
        "s_waitcnt lgkmcnt(15)\n"
        "v_mfma_f32_16x16x32_bf16 v[80:83], v[112:115], v[40:43], v[80:83]\n"
        "ds_read_b128 v[108:111], v0 offset:29696\n"
        "v_mfma_f32_16x16x32_bf16 v[84:87], v[112:115], v[44:47], v[84:87]\n"
        "s_waitcnt lgkmcnt(15)\n"
        "v_mfma_f32_16x16x32_bf16 v[88:91], v[116:119], v[40:43], v[88:91]\n"
        "v_mfma_f32_16x16x32_bf16 v[92:95], v[116:119], v[44:47], v[92:95]\n"
        "global_load_lds_dwordx4 v1, s[54:55] offset:3072 sc1\n"
        "s_waitcnt lgkmcnt(3)\n"
        "v_mfma_f32_16x16x32_bf16 v[72:75], a[120:123], v[24:27], v[72:75]\n"
        "ds_read_b128 v[40:43], v1 offset:28672\n"
        "v_mfma_f32_16x16x32_bf16 v[76:79], a[120:123], v[28:31], v[76:79]\n"
        "ds_read_b128 v[44:47], v1 offset:29696\n"
        "v_mfma_f32_16x16x32_bf16 v[80:83], a[128:131], v[24:27], v[80:83]\n"
        "ds_read_b128 v[112:115], v2 offset:0\n"
        "v_mfma_f32_16x16x32_bf16 v[84:87], a[128:131], v[28:31], v[84:87]\n"
        "ds_read_b128 v[116:119], v2 offset:24576\n"
        "v_mfma_f32_16x16x32_bf16 v[88:91], a[136:139], v[24:27], v[88:91]\n"
        "v_mfma_f32_16x16x32_bf16 v[92:95], a[136:139], v[28:31], v[92:95]\n"
        "s_waitcnt lgkmcnt(5)\n"
        "v_mfma_f32_16x16x32_bf16 v[72:75], a[124:127], v[32:35], v[72:75]\n"
        "ds_read_b128 v[24:27], v1 offset:30720\n"
        "v_mfma_f32_16x16x32_bf16 v[76:79], a[124:127], v[36:39], v[76:79]\n"
        "ds_read_b128 v[28:31], v1 offset:31744\n"
        "v_mfma_f32_16x16x32_bf16 v[80:83], a[132:135], v[32:35], v[80:83]\n"
        "v_mfma_f32_16x16x32_bf16 v[84:87], a[132:135], v[36:39], v[84:87]\n"
        "v_mfma_f32_16x16x32_bf16 v[88:91], a[140:143], v[32:35], v[88:91]\n"
        "v_mfma_f32_16x16x32_bf16 v[92:95], a[140:143], v[36:39], v[92:95]\n"
        "s_waitcnt lgkmcnt(4)\n"
        "s_waitcnt lgkmcnt(15)\n"
        "v_mfma_f32_16x16x32_bf16 v[72:75], v[120:123], v[40:43], v[72:75]\n"
        "ds_read_b128 v[32:35], v1 offset:32768\n"
        "v_mfma_f32_16x16x32_bf16 v[76:79], v[120:123], v[44:47], v[76:79]\n"
        "ds_read_b128 v[36:39], v1 offset:33792\n"
        "s_waitcnt lgkmcnt(15)\n"
        "v_mfma_f32_16x16x32_bf16 v[80:83], v[124:127], v[40:43], v[80:83]\n"
        "ds_read_b128 v[120:123], v2 offset:1024\n"
        "v_mfma_f32_16x16x32_bf16 v[84:87], v[124:127], v[44:47], v[84:87]\n"
        "s_waitcnt lgkmcnt(15)\n"
        "v_mfma_f32_16x16x32_bf16 v[88:91], v[96:99], v[40:43], v[88:91]\n"
        "v_mfma_f32_16x16x32_bf16 v[92:95], v[96:99], v[44:47], v[92:95]\n"
        "s_waitcnt lgkmcnt(3)\n"
        "v_mfma_f32_16x16x32_bf16 v[72:75], a[144:147], v[24:27], v[72:75]\n"
        "ds_read_b128 v[40:43], v1 offset:34816\n"
        "v_mfma_f32_16x16x32_bf16 v[76:79], a[144:147], v[28:31], v[76:79]\n"
        "ds_read_b128 v[44:47], v1 offset:35840\n"
        "v_mfma_f32_16x16x32_bf16 v[80:83], a[152:155], v[24:27], v[80:83]\n"
        "ds_read_b128 v[124:127], v2 offset:25600\n"
        "v_mfma_f32_16x16x32_bf16 v[84:87], a[152:155], v[28:31], v[84:87]\n"
        "ds_read_b128 v[96:99], v2 offset:2048\n"
        "v_mfma_f32_16x16x32_bf16 v[88:91], a[160:163], v[24:27], v[88:91]\n"
        "v_mfma_f32_16x16x32_bf16 v[92:95], a[160:163], v[28:31], v[92:95]\n"
        "s_barrier\n"
        "s_waitcnt lgkmcnt(5)\n"
        "v_mfma_f32_16x16x32_bf16 v[72:75], a[148:151], v[32:35], v[72:75]\n"
        "ds_read_b128 v[24:27], v1 offset:36864\n"
        "v_mfma_f32_16x16x32_bf16 v[76:79], a[148:151], v[36:39], v[76:79]\n"
        "ds_read_b128 v[28:31], v1 offset:37888\n"
        "v_mfma_f32_16x16x32_bf16 v[80:83], a[156:159], v[32:35], v[80:83]\n"
        "s_add_u32 s47, s96, 16384\n"
        "s_add_u32 s54, s48, s47\n"
        "s_addc_u32 s55, s49, 0\n"
        "v_mfma_f32_16x16x32_bf16 v[84:87], a[156:159], v[36:39], v[84:87]\n"
        "s_add_u32 m0, s47, 0\n"
        "v_mfma_f32_16x16x32_bf16 v[88:91], a[164:167], v[32:35], v[88:91]\n"
        "global_load_lds_dwordx4 v1, s[54:55] offset:0 sc1\n"
        "v_mfma_f32_16x16x32_bf16 v[92:95], a[164:167], v[36:39], v[92:95]\n"
        "s_waitcnt lgkmcnt(4)\n"
        "s_waitcnt lgkmcnt(15)\n"
        "v_mfma_f32_16x16x32_bf16 v[72:75], v[100:103], v[40:43], v[72:75]\n"
        "ds_read_b128 v[32:35], v1 offset:38912\n"
        "v_mfma_f32_16x16x32_bf16 v[76:79], v[100:103], v[44:47], v[76:79]\n"
        "ds_read_b128 v[36:39], v1 offset:39936\n"
        "s_waitcnt lgkmcnt(15)\n"
        "v_mfma_f32_16x16x32_bf16 v[80:83], v[104:107], v[40:43], v[80:83]\n"
        "ds_read_b128 v[100:103], v2 offset:26624\n"
        "v_mfma_f32_16x16x32_bf16 v[84:87], v[104:107], v[44:47], v[84:87]\n"
        "s_waitcnt lgkmcnt(15)\n"
        "v_mfma_f32_16x16x32_bf16 v[88:91], v[108:111], v[40:43], v[88:91]\n"
        "global_load_lds_dwordx4 v1, s[54:55] offset:1024 sc1\n"
        "v_mfma_f32_16x16x32_bf16 v[92:95], v[108:111], v[44:47], v[92:95]\n"
        "s_waitcnt lgkmcnt(3)\n"
        "v_mfma_f32_16x16x32_bf16 v[72:75], a[168:171], v[24:27], v[72:75]\n"
        "ds_read_b128 v[104:107], v2 offset:3072\n"
        "v_mfma_f32_16x16x32_bf16 v[76:79], a[168:171], v[28:31], v[76:79]\n"
        "ds_read_b128 v[108:111], v2 offset:27648\n"
        "v_mfma_f32_16x16x32_bf16 v[80:83], a[176:179], v[24:27], v[80:83]\n"
        "v_mfma_f32_16x16x32_bf16 v[84:87], a[176:179], v[28:31], v[84:87]\n"
        "global_load_lds_dwordx4 v1, s[54:55] offset:2048 sc1\n"
        "v_mfma_f32_16x16x32_bf16 v[88:91], a[184:187], v[24:27], v[88:91]\n"
        "v_mfma_f32_16x16x32_bf16 v[92:95], a[184:187], v[28:31], v[92:95]\n"
        "s_waitcnt lgkmcnt(3)\n"
        "v_mfma_f32_16x16x32_bf16 v[72:75], a[172:175], v[32:35], v[72:75]\n"
        "global_load_lds_dwordx4 v1, s[54:55] offset:3072 sc1\n"
        "v_mfma_f32_16x16x32_bf16 v[76:79], a[172:175], v[36:39], v[76:79]\n"
        "v_mfma_f32_16x16x32_bf16 v[80:83], a[180:183], v[32:35], v[80:83]\n"
        "v_mfma_f32_16x16x32_bf16 v[84:87], a[180:183], v[36:39], v[84:87]\n"
        "v_mfma_f32_16x16x32_bf16 v[88:91], a[188:191], v[32:35], v[88:91]\n"
        "v_mfma_f32_16x16x32_bf16 v[92:95], a[188:191], v[36:39], v[92:95]\n"
        "Lvo_vtail:\n"
        "s_nop 1\n"
        "v_cvt_pk_bf16_f32 v72, v72, v73\n"
        "v_cvt_pk_bf16_f32 v73, v74, v75\n"
        "ds_write_b64 v16, v[72:73] offset:0\n"
        "v_cvt_pk_bf16_f32 v76, v76, v77\n"
        "v_cvt_pk_bf16_f32 v77, v78, v79\n"
        "ds_write_b64 v16, v[76:77] offset:1024\n"
        "v_cvt_pk_bf16_f32 v80, v80, v81\n"
        "v_cvt_pk_bf16_f32 v81, v82, v83\n"
        "ds_write_b64 v17, v[80:81] offset:0\n"
        "v_cvt_pk_bf16_f32 v84, v84, v85\n"
        "v_cvt_pk_bf16_f32 v85, v86, v87\n"
        "ds_write_b64 v17, v[84:85] offset:1024\n"
        "v_cvt_pk_bf16_f32 v88, v88, v89\n"
        "v_cvt_pk_bf16_f32 v89, v90, v91\n"
        "ds_write_b64 v18, v[88:89] offset:0\n"
        "v_cvt_pk_bf16_f32 v92, v92, v93\n"
        "v_cvt_pk_bf16_f32 v93, v94, v95\n"
        "ds_write_b64 v18, v[92:93] offset:1024\n"
        "s_waitcnt lgkmcnt(0)\n"
        "s_sub_u32 s35, s92, s38\n"
        "s_min_u32 s35, s35, 200\n"
        "s_cmp_eq_u32 s58, 1\n"
        "s_cselect_b32 s35, 81, s35\n"
        "s_cmp_eq_u32 s58, 0\n"
        "s_cselect_b32 s35, 100, s35\n"
        "s_barrier\n"
        "ds_read_b128 v[24:27], v1 offset:40960\n"
        "ds_read_b128 v[28:31], v1 offset:41984\n"
        "ds_read_b128 v[32:35], v1 offset:43008\n"
        "ds_read_b128 v[36:39], v1 offset:44032\n"
        "ds_read_b128 v[40:43], v1 offset:45056\n"
        "ds_read_b128 v[44:47], v1 offset:46080\n"
        "ds_read_b128 v[48:51], v1 offset:47104\n"
        "ds_read_b128 v[52:55], v1 offset:48128\n"
        "ds_read_b128 v[56:59], v1 offset:49152\n"
        "ds_read_b128 v[60:63], v1 offset:50176\n"
        "ds_read_b128 v[64:67], v1 offset:51200\n"
        "ds_read_b128 v[68:71], v1 offset:52224\n"
        "s_waitcnt lgkmcnt(10)\n"
        "v_mfma_f32_16x16x32_bf16 v[72:75], v[24:27], v[112:115], 0\n"
        "s_add_u32 s47, s97, 32768\n"
        "s_add_u32 s54, s48, s47\n"
        "s_addc_u32 s55, s49, 0\n"
        "v_mfma_f32_16x16x32_bf16 v[76:79], v[28:31], v[112:115], 0\n"
        "s_add_u32 m0, s47, 0\n"
        "s_waitcnt lgkmcnt(8)\n"
        "v_mfma_f32_16x16x32_bf16 v[72:75], v[32:35], v[116:119], v[72:75]\n"
        "ds_read_b128 v[112:115], v2 offset:4096\n"
        "global_load_lds_dwordx4 v1, s[54:55] offset:0 sc1\n"
        "v_mfma_f32_16x16x32_bf16 v[76:79], v[36:39], v[116:119], v[76:79]\n"
        "s_waitcnt lgkmcnt(7)\n"
        "v_mfma_f32_16x16x32_bf16 v[72:75], v[40:43], a[192:195], v[72:75]\n"
        "ds_read_b128 v[116:119], v2 offset:28672\n"
        "v_mfma_f32_16x16x32_bf16 v[76:79], v[44:47], a[192:195], v[76:79]\n"
        "global_load_lds_dwordx4 v1, s[54:55] offset:1024 sc1\n"
        "s_waitcnt lgkmcnt(6)\n"
        "v_mfma_f32_16x16x32_bf16 v[72:75], v[48:51], a[196:199], v[72:75]\n"
        "v_mfma_f32_16x16x32_bf16 v[76:79], v[52:55], a[196:199], v[76:79]\n"
        "s_waitcnt lgkmcnt(4)\n"
        "v_mfma_f32_16x16x32_bf16 v[72:75], v[56:59], a[200:203], v[72:75]\n"
        "v_mfma_f32_16x16x32_bf16 v[76:79], v[60:63], a[200:203], v[76:79]\n"
        "s_waitcnt lgkmcnt(2)\n"
        "v_mfma_f32_16x16x32_bf16 v[72:75], v[64:67], a[204:207], v[72:75]\n"
        "v_mfma_f32_16x16x32_bf16 v[76:79], v[68:71], a[204:207], v[76:79]\n"
        "v_mfma_f32_16x16x32_bf16 v[80:83], v[24:27], v[120:123], 0\n"
        "v_mfma_f32_16x16x32_bf16 v[84:87], v[28:31], v[120:123], 0\n"
        "v_mfma_f32_16x16x32_bf16 v[80:83], v[32:35], v[124:127], v[80:83]\n"
        "ds_read_b128 v[120:123], v2 offset:5120\n"
        "v_mfma_f32_16x16x32_bf16 v[84:87], v[36:39], v[124:127], v[84:87]\n"
        "v_mfma_f32_16x16x32_bf16 v[80:83], v[40:43], a[208:211], v[80:83]\n"
        "ds_read_b128 v[124:127], v2 offset:29696\n"
        "v_cvt_pk_bf16_f32 v88, v72, v73\n"
        "v_cvt_pk_bf16_f32 v89, v74, v75\n"
        "v_mfma_f32_16x16x32_bf16 v[84:87], v[44:47], a[208:211], v[84:87]\n"
        "v_cvt_pk_bf16_f32 v90, v76, v77\n"
        "v_cvt_pk_bf16_f32 v91, v78, v79\n"
        "global_store_dwordx4 v19, v[88:91], s[52:53] sc1\n"
        "s_add_u32 s52, s52, 0x10000\n"
        "s_addc_u32 s53, s53, 0\n"
        "v_mfma_f32_16x16x32_bf16 v[80:83], v[48:51], a[212:215], v[80:83]\n"
        "v_mfma_f32_16x16x32_bf16 v[84:87], v[52:55], a[212:215], v[84:87]\n"
        "v_mfma_f32_16x16x32_bf16 v[80:83], v[56:59], a[216:219], v[80:83]\n"
        "v_mfma_f32_16x16x32_bf16 v[84:87], v[60:63], a[216:219], v[84:87]\n"
        "v_mfma_f32_16x16x32_bf16 v[80:83], v[64:67], a[220:223], v[80:83]\n"
        "v_mfma_f32_16x16x32_bf16 v[84:87], v[68:71], a[220:223], v[84:87]\n"
        "v_mfma_f32_16x16x32_bf16 v[72:75], v[24:27], v[96:99], 0\n"
        "v_mfma_f32_16x16x32_bf16 v[76:79], v[28:31], v[96:99], 0\n"
        "v_mfma_f32_16x16x32_bf16 v[72:75], v[32:35], v[100:103], v[72:75]\n"
        "v_mfma_f32_16x16x32_bf16 v[76:79], v[36:39], v[100:103], v[76:79]\n"
        "v_mfma_f32_16x16x32_bf16 v[72:75], v[40:43], a[224:227], v[72:75]\n"
        "v_mfma_f32_16x16x32_bf16 v[76:79], v[44:47], a[224:227], v[76:79]\n"
        "v_mfma_f32_16x16x32_bf16 v[72:75], v[48:51], a[228:231], v[72:75]\n"
        "v_cvt_pk_bf16_f32 v92, v80, v81\n"
        "v_cvt_pk_bf16_f32 v93, v82, v83\n"
        "v_mfma_f32_16x16x32_bf16 v[76:79], v[52:55], a[228:231], v[76:79]\n"
        "v_cvt_pk_bf16_f32 v94, v84, v85\n"
        "v_cvt_pk_bf16_f32 v95, v86, v87\n"
        "global_store_dwordx4 v19, v[92:95], s[52:53] sc1\n"
        "s_add_u32 s52, s52, s56\n"
        "s_addc_u32 s53, s53, 0\n"
        "v_mfma_f32_16x16x32_bf16 v[72:75], v[56:59], a[232:235], v[72:75]\n"
        "v_mfma_f32_16x16x32_bf16 v[76:79], v[60:63], a[232:235], v[76:79]\n"
        "v_mfma_f32_16x16x32_bf16 v[72:75], v[64:67], a[236:239], v[72:75]\n"
        "v_mfma_f32_16x16x32_bf16 v[76:79], v[68:71], a[236:239], v[76:79]\n"
        "v_mfma_f32_16x16x32_bf16 v[80:83], v[24:27], v[104:107], 0\n"
        "v_mfma_f32_16x16x32_bf16 v[84:87], v[28:31], v[104:107], 0\n"
        "v_mfma_f32_16x16x32_bf16 v[80:83], v[32:35], v[108:111], v[80:83]\n"
        "v_mfma_f32_16x16x32_bf16 v[84:87], v[36:39], v[108:111], v[84:87]\n"
        "v_mfma_f32_16x16x32_bf16 v[80:83], v[40:43], a[240:243], v[80:83]\n"
        "v_mfma_f32_16x16x32_bf16 v[84:87], v[44:47], a[240:243], v[84:87]\n"
        "v_mfma_f32_16x16x32_bf16 v[80:83], v[48:51], a[244:247], v[80:83]\n"
        "v_cvt_pk_bf16_f32 v88, v72, v73\n"
        "v_cvt_pk_bf16_f32 v89, v74, v75\n"
        "v_mfma_f32_16x16x32_bf16 v[84:87], v[52:55], a[244:247], v[84:87]\n"
        "v_cvt_pk_bf16_f32 v90, v76, v77\n"
        "v_cvt_pk_bf16_f32 v91, v78, v79\n"
        "global_store_dwordx4 v19, v[88:91], s[52:53] sc1\n"
        "s_add_u32 s52, s52, 0x10000\n"
        "s_addc_u32 s53, s53, 0\n"
        "v_mfma_f32_16x16x32_bf16 v[80:83], v[56:59], a[248:251], v[80:83]\n"
        "v_mfma_f32_16x16x32_bf16 v[84:87], v[60:63], a[248:251], v[84:87]\n"
        "v_mfma_f32_16x16x32_bf16 v[80:83], v[64:67], a[252:255], v[80:83]\n"
        "v_mfma_f32_16x16x32_bf16 v[84:87], v[68:71], a[252:255], v[84:87]\n"
        "v_mfma_f32_16x16x32_bf16 v[72:75], v[24:27], v[128:131], 0\n"
        "v_mfma_f32_16x16x32_bf16 v[76:79], v[28:31], v[128:131], 0\n"
        "v_mfma_f32_16x16x32_bf16 v[72:75], v[32:35], v[132:135], v[72:75]\n"
        "v_mfma_f32_16x16x32_bf16 v[76:79], v[36:39], v[132:135], v[76:79]\n"
        "v_mfma_f32_16x16x32_bf16 v[72:75], v[40:43], v[136:139], v[72:75]\n"
        "v_mfma_f32_16x16x32_bf16 v[76:79], v[44:47], v[136:139], v[76:79]\n"
        "v_mfma_f32_16x16x32_bf16 v[72:75], v[48:51], v[140:143], v[72:75]\n"
        "v_cvt_pk_bf16_f32 v92, v80, v81\n"
        "v_cvt_pk_bf16_f32 v93, v82, v83\n"
        "v_mfma_f32_16x16x32_bf16 v[76:79], v[52:55], v[140:143], v[76:79]\n"
        "v_cvt_pk_bf16_f32 v94, v84, v85\n"
        "v_cvt_pk_bf16_f32 v95, v86, v87\n"
        "global_store_dwordx4 v19, v[92:95], s[52:53] sc1\n"
        "s_add_u32 s52, s52, 0x10000\n"
        "s_addc_u32 s53, s53, 0\n"
        "v_mfma_f32_16x16x32_bf16 v[72:75], v[56:59], v[144:147], v[72:75]\n"
        "v_mfma_f32_16x16x32_bf16 v[76:79], v[60:63], v[144:147], v[76:79]\n"
        "v_mfma_f32_16x16x32_bf16 v[72:75], v[64:67], v[148:151], v[72:75]\n"
        "v_mfma_f32_16x16x32_bf16 v[76:79], v[68:71], v[148:151], v[76:79]\n"
        "v_mfma_f32_16x16x32_bf16 v[80:83], v[24:27], v[152:155], 0\n"
        "v_mfma_f32_16x16x32_bf16 v[84:87], v[28:31], v[152:155], 0\n"
        "v_mfma_f32_16x16x32_bf16 v[80:83], v[32:35], v[156:159], v[80:83]\n"
        "v_mfma_f32_16x16x32_bf16 v[84:87], v[36:39], v[156:159], v[84:87]\n"
        "v_mfma_f32_16x16x32_bf16 v[80:83], v[40:43], v[160:163], v[80:83]\n"
        "v_mfma_f32_16x16x32_bf16 v[84:87], v[44:47], v[160:163], v[84:87]\n"
        "v_mfma_f32_16x16x32_bf16 v[80:83], v[48:51], v[164:167], v[80:83]\n"
        "v_cvt_pk_bf16_f32 v88, v72, v73\n"
        "v_cvt_pk_bf16_f32 v89, v74, v75\n"
        "v_mfma_f32_16x16x32_bf16 v[84:87], v[52:55], v[164:167], v[84:87]\n"
        "v_cvt_pk_bf16_f32 v90, v76, v77\n"
        "v_cvt_pk_bf16_f32 v91, v78, v79\n"
        "global_store_dwordx4 v19, v[88:91], s[52:53] sc1\n"
        "s_add_u32 s52, s52, 0x10000\n"
        "s_addc_u32 s53, s53, 0\n"
        "v_mfma_f32_16x16x32_bf16 v[80:83], v[56:59], v[168:171], v[80:83]\n"
        "v_mfma_f32_16x16x32_bf16 v[84:87], v[60:63], v[168:171], v[84:87]\n"
        "v_mfma_f32_16x16x32_bf16 v[80:83], v[64:67], v[172:175], v[80:83]\n"
        "v_mfma_f32_16x16x32_bf16 v[84:87], v[68:71], v[172:175], v[84:87]\n"
        "v_mfma_f32_16x16x32_bf16 v[72:75], v[24:27], v[176:179], 0\n"
        "v_mfma_f32_16x16x32_bf16 v[76:79], v[28:31], v[176:179], 0\n"
        "v_mfma_f32_16x16x32_bf16 v[72:75], v[32:35], v[180:183], v[72:75]\n"
        "v_mfma_f32_16x16x32_bf16 v[76:79], v[36:39], v[180:183], v[76:79]\n"
        "v_mfma_f32_16x16x32_bf16 v[72:75], v[40:43], v[184:187], v[72:75]\n"
        "v_mfma_f32_16x16x32_bf16 v[76:79], v[44:47], v[184:187], v[76:79]\n"
        "v_mfma_f32_16x16x32_bf16 v[72:75], v[48:51], v[188:191], v[72:75]\n"
        "v_cvt_pk_bf16_f32 v92, v80, v81\n"
        "v_cvt_pk_bf16_f32 v93, v82, v83\n"
        "v_mfma_f32_16x16x32_bf16 v[76:79], v[52:55], v[188:191], v[76:79]\n"
        "v_cvt_pk_bf16_f32 v94, v84, v85\n"
        "v_cvt_pk_bf16_f32 v95, v86, v87\n"
        "global_store_dwordx4 v19, v[92:95], s[52:53] sc1\n"
        "s_add_u32 s52, s52, 0x10000\n"
        "s_addc_u32 s53, s53, 0\n"
        "v_mfma_f32_16x16x32_bf16 v[72:75], v[56:59], v[192:195], v[72:75]\n"
        "v_mfma_f32_16x16x32_bf16 v[76:79], v[60:63], v[192:195], v[76:79]\n"
        "v_mfma_f32_16x16x32_bf16 v[72:75], v[64:67], v[196:199], v[72:75]\n"
        "v_mfma_f32_16x16x32_bf16 v[76:79], v[68:71], v[196:199], v[76:79]\n"
        "v_mfma_f32_16x16x32_bf16 v[80:83], v[24:27], v[200:203], 0\n"
        "v_mfma_f32_16x16x32_bf16 v[84:87], v[28:31], v[200:203], 0\n"
        "v_mfma_f32_16x16x32_bf16 v[80:83], v[32:35], v[204:207], v[80:83]\n"
        "v_mfma_f32_16x16x32_bf16 v[84:87], v[36:39], v[204:207], v[84:87]\n"
        "v_mfma_f32_16x16x32_bf16 v[80:83], v[40:43], v[208:211], v[80:83]\n"
        "v_mfma_f32_16x16x32_bf16 v[84:87], v[44:47], v[208:211], v[84:87]\n"
        "v_mfma_f32_16x16x32_bf16 v[80:83], v[48:51], v[212:215], v[80:83]\n"
        "v_cvt_pk_bf16_f32 v88, v72, v73\n"
        "v_cvt_pk_bf16_f32 v89, v74, v75\n"
        "v_mfma_f32_16x16x32_bf16 v[84:87], v[52:55], v[212:215], v[84:87]\n"
        "v_cvt_pk_bf16_f32 v90, v76, v77\n"
        "v_cvt_pk_bf16_f32 v91, v78, v79\n"
        "global_store_dwordx4 v19, v[88:91], s[52:53] sc1\n"
        "s_add_u32 s52, s52, 0x10000\n"
        "s_addc_u32 s53, s53, 0\n"
        "v_mfma_f32_16x16x32_bf16 v[80:83], v[56:59], v[216:219], v[80:83]\n"
        "s_lshr_b32 s58, s45, 1\n"
        "s_and_b32 s68, s58, 3\n"
        "s_lshl_b32 s68, s68, 17\n"
        "v_mfma_f32_16x16x32_bf16 v[84:87], v[60:63], v[216:219], v[84:87]\n"
        "s_add_u32 s68, s68, s44\n"
        "s_add_u32 s68, s68, 0x180000\n"
        "s_cmp_eq_u32 s45, 0\n"
        "v_mfma_f32_16x16x32_bf16 v[80:83], v[64:67], v[220:223], v[80:83]\n"
        "s_cselect_b32 s68, 0xa80000, s68\n"
        "s_add_u32 s47, s68, 0x0\n"
        "s_add_u32 s56, s90, s47\n"
        "v_mfma_f32_16x16x32_bf16 v[84:87], v[68:71], v[220:223], v[84:87]\n"
        "s_addc_u32 s57, s91, 0\n"
        "s_and_b32 s68, s58, 1\n"
        "s_mul_i32 s68, s68, 0xa0000\n"
        "s_add_u32 s47, s68, 0x0\n"
        "s_add_u32 s47, s47, s44\n"
        "s_add_u32 s50, s90, s47\n"
        "s_addc_u32 s51, s91, 0\n"
        "s_waitcnt lgkmcnt(3)\n"
        "v_mfma_f32_16x16x32_bf16 v[72:75], v[24:27], v[112:115], 0\n"
        "v_cvt_pk_bf16_f32 v92, v80, v81\n"
        "v_cvt_pk_bf16_f32 v93, v82, v83\n"
        "v_mfma_f32_16x16x32_bf16 v[76:79], v[28:31], v[112:115], 0\n"
        "v_cvt_pk_bf16_f32 v94, v84, v85\n"
        "v_cvt_pk_bf16_f32 v95, v86, v87\n"
        "global_store_dwordx4 v19, v[92:95], s[52:53] sc1\n"
        "s_add_u32 s52, s52, 0x10000\n"
        "s_addc_u32 s53, s53, 0\n"
        "global_load_dword v21, v15, s[94:95] sc1\n"
        "s_waitcnt lgkmcnt(2)\n"
        "v_mfma_f32_16x16x32_bf16 v[72:75], v[32:35], v[116:119], v[72:75]\n"
        "v_mfma_f32_16x16x32_bf16 v[76:79], v[36:39], v[116:119], v[76:79]\n"
        "v_mfma_f32_16x16x32_bf16 v[72:75], v[40:43], v[224:227], v[72:75]\n"
        "v_mfma_f32_16x16x32_bf16 v[76:79], v[44:47], v[224:227], v[76:79]\n"
        "v_mfma_f32_16x16x32_bf16 v[72:75], v[48:51], v[228:231], v[72:75]\n"
        "v_mfma_f32_16x16x32_bf16 v[76:79], v[52:55], v[228:231], v[76:79]\n"
        "v_mfma_f32_16x16x32_bf16 v[72:75], v[56:59], v[232:235], v[72:75]\n"
        "v_mfma_f32_16x16x32_bf16 v[76:79], v[60:63], v[232:235], v[76:79]\n"
        "v_mfma_f32_16x16x32_bf16 v[72:75], v[64:67], v[236:239], v[72:75]\n"
        "v_mfma_f32_16x16x32_bf16 v[76:79], v[68:71], v[236:239], v[76:79]\n"
        "s_waitcnt lgkmcnt(1)\n"
        "v_mfma_f32_16x16x32_bf16 v[80:83], v[24:27], v[120:123], 0\n"
        "v_mfma_f32_16x16x32_bf16 v[84:87], v[28:31], v[120:123], 0\n"
        "s_waitcnt lgkmcnt(0)\n"
        "v_mfma_f32_16x16x32_bf16 v[80:83], v[32:35], v[124:127], v[80:83]\n"
        "v_mfma_f32_16x16x32_bf16 v[84:87], v[36:39], v[124:127], v[84:87]\n"
        "v_mfma_f32_16x16x32_bf16 v[80:83], v[40:43], v[240:243], v[80:83]\n"
        "v_cvt_pk_bf16_f32 v88, v72, v73\n"
        "v_cvt_pk_bf16_f32 v89, v74, v75\n"
        "v_mfma_f32_16x16x32_bf16 v[84:87], v[44:47], v[240:243], v[84:87]\n"
        "v_cvt_pk_bf16_f32 v90, v76, v77\n"
        "v_cvt_pk_bf16_f32 v91, v78, v79\n"
        "global_store_dwordx4 v19, v[88:91], s[52:53] sc1\n"
        "s_add_u32 s52, s52, 0x10000\n"
        "s_addc_u32 s53, s53, 0\n"
        "v_mfma_f32_16x16x32_bf16 v[80:83], v[48:51], v[244:247], v[80:83]\n"
        "v_mfma_f32_16x16x32_bf16 v[84:87], v[52:55], v[244:247], v[84:87]\n"
        "v_mfma_f32_16x16x32_bf16 v[80:83], v[56:59], v[248:251], v[80:83]\n"
        "v_mfma_f32_16x16x32_bf16 v[84:87], v[60:63], v[248:251], v[84:87]\n"
        "v_mfma_f32_16x16x32_bf16 v[80:83], v[64:67], v[252:255], v[80:83]\n"
        "v_mfma_f32_16x16x32_bf16 v[84:87], v[68:71], v[252:255], v[84:87]\n"
        "s_nop 6\n"
        "v_cvt_pk_bf16_f32 v92, v80, v81\n"
        "v_cvt_pk_bf16_f32 v93, v82, v83\n"
        "v_cvt_pk_bf16_f32 v94, v84, v85\n"
        "v_cvt_pk_bf16_f32 v95, v86, v87\n"
        "global_store_dwordx4 v19, v[92:95], s[52:53] sc1\n"
        "s_add_u32 s52, s52, 0x10000\n"
        "s_addc_u32 s53, s53, 0\n"
        "v_mov_b32 v72, 0\n"
        "v_mov_b32 v73, 0\n"
        "v_mov_b32 v74, 0\n"
        "v_mov_b32 v75, 0\n"
        "v_mov_b32 v76, 0\n"
        "v_mov_b32 v77, 0\n"
        "v_mov_b32 v78, 0\n"
        "v_mov_b32 v79, 0\n"
        "v_mov_b32 v80, 0\n"
        "v_mov_b32 v81, 0\n"
        "v_mov_b32 v82, 0\n"
        "v_mov_b32 v83, 0\n"
        "ds_read_b128 v[96:99], v0 offset:0\n"
        "ds_read_b128 v[100:103], v0 offset:3072\n"
        "ds_read_b128 v[104:107], v0 offset:1024\n"
        "ds_read_b128 v[108:111], v0 offset:4096\n"
        "ds_read_b128 v[112:115], v0 offset:24576\n"
        "ds_read_b128 v[116:119], v0 offset:27648\n"
        "ds_read_b128 v[120:123], v0 offset:25600\n"
        "ds_read_b128 v[124:127], v0 offset:28672\n"
        "s_cmp_eq_u32 s45, 0\n"
        "s_cbranch_scc1 Lvo_zero\n"
        "s_add_u32 s88, s45, 1\n"
        "s_lshr_b32 s88, s88, 1\n"
        "s_waitcnt vmcnt(2)\n"
        "v_and_b32 v20, 63, v21\n"
        "v_cmp_ne_u32 vcc, s88, v20\n"
        "s_cmp_eq_u32 s45, 0\n"
        "s_cselect_b64 vcc, 0, vcc\n"
        "s_cbranch_vccnz Lvo_hs_3\n"
        "Lvo_hb_4:\n"
        "v_readfirstlane_b32 s34, v21\n"
        "s_nop 1\n"
        "v_subrev_u32 v21, s34, v21\n"
        "s_nop 1\n"
        "v_max_i32_dpp v21, v21, v21 row_ror:8 row_mask:0xf bank_mask:0xf\n"
        "s_nop 1\n"
        "v_max_i32_dpp v21, v21, v21 row_ror:4 row_mask:0xf bank_mask:0xf\n"
        "s_nop 1\n"
        "v_max_i32_dpp v21, v21, v21 row_ror:2 row_mask:0xf bank_mask:0xf\n"
        "s_nop 1\n"
        "v_max_i32_dpp v21, v21, v21 row_ror:1 row_mask:0xf bank_mask:0xf\n"
        "s_nop 0\n"
        "v_readfirstlane_b32 s68, v21\n"
        "v_readlane_b32 s69, v21, 16\n"
        "s_max_i32 s68, s68, s69\n"
        "s_add_u32 s34, s34, s68\n"
        "s_add_u32 s34, s34, 4928\n"
        "s_add_u32 s34, s34, s60\n"
        "s_waitcnt vmcnt(0)\n"
        "s_barrier\n"
        "s_add_u32 s47, s58, 1\n"
        "v_mov_b32 v22, s47\n"
        "s_lshl_b32 s47, s40, 7\n"
        "v_mov_b32 v20, s47\n"
        "s_and_b32 exec_lo, s36, 1\n"
        "s_mov_b32 exec_hi, 0\n"
        "global_store_dword v20, v22, s[86:87] sc1\n"
        "s_mov_b64 exec, -1\n"
        "s_mov_b32 s59, s46\n"
        "s_cmp_eq_u32 s45, 0\n"
        "s_cbranch_scc1 Lvo_nw_6\n"
        "s_mov_b32 s69, 48\n"
        ".p2align 6\n"
        "Lvo_w_7:\n"
        "s_memrealtime s[54:55]\n"
        "s_waitcnt lgkmcnt(0)\n"
        "s_lshl_b32 s47, s54, 6\n"
        "s_sub_u32 s47, s34, s47\n"
        "s_cmp_lt_i32 s47, 768\n"
        "s_cbranch_scc1 Lvo_f_8\n"
        "s_sleep 1\n"
        "s_sub_u32 s69, s69, 1\n"
        "s_cbranch_scc0 Lvo_w_7\n"
        "s_mov_b32 s47, 0\n"
        "Lvo_f_8:\n"
        "s_max_i32 s47, s47, 0\n"
        "Lvo_c_9:\n"
        "s_sub_u32 s47, s47, 80\n"
        "s_cbranch_scc0 Lvo_c_9\n"
        "Lvo_nw_6:\n"
        "global_load_dwordx4 v[24:27], v3, s[56:57] offset:-2048 sc1\n"
        "global_load_dwordx4 v[28:31], v3, s[56:57] offset:2048 sc1\n"
        "global_load_dword v32, v4, s[56:57] sc1\n"
        "global_load_dword v33, v4, s[56:57] offset:256 sc1\n"
        "Lvo_gb_10:\n"
        "s_sub_u32 s46, s46, 1\n"
        "s_cbranch_scc1 Lvo_exit\n"
        "s_waitcnt vmcnt(3)\n"
        "ds_write_b128 v5, v[24:27] offset:40960\n"
        "v_max3_u32 v20, v24, v25, v26\n"
        "s_waitcnt vmcnt(2)\n"
        "ds_write_b128 v5, v[28:31] offset:45056\n"
        "v_max3_u32 v21, v27, v28, v29\n"
        "v_max3_u32 v20, v20, v30, v31\n"
        "s_waitcnt vmcnt(0)\n"
        "ds_write2st64_b32 v6, v32, v33 offset0:160 offset1:161\n"
        "v_max3_u32 v21, v21, v32, v33\n"
        "v_max_u32 v20, v20, v21\n"
        "v_cmp_eq_u32 vcc, -1, v20\n"
        "s_cbranch_vccnz Lvo_gm_5\n"
        "s_waitcnt lgkmcnt(0)\n"
        "s_barrier\n"
        "s_memrealtime s[38:39]\n"
        "Lvo_got:\n"
        "ds_read_b128 v[24:27], v1 offset:40960\n"
        "ds_read_b128 v[28:31], v1 offset:41984\n"
        "ds_read_b128 v[32:35], v1 offset:43008\n"
        "ds_read_b128 v[36:39], v1 offset:44032\n"
        "ds_read_b128 v[40:43], v1 offset:45056\n"
        "ds_read_b128 v[44:47], v1 offset:46080\n"
        "ds_read_b128 v[48:51], v1 offset:47104\n"
        "ds_read_b128 v[52:55], v1 offset:48128\n"
        "ds_read_b128 v[56:59], v1 offset:49152\n"
        "ds_read_b128 v[60:63], v1 offset:50176\n"
        "s_waitcnt lgkmcnt(9)\n"
        "v_smfmac_f32_16x16x64_bf16 v[72:75], v[24:27], v[96:103], v7\n"
        "ds_read_b128 v[96:99], v0 offset:2048\n"
        "v_smfmac_f32_16x16x64_bf16 v[76:79], v[24:27], v[104:111], v7\n"
        "ds_read_b128 v[100:103], v0 offset:5120\n"
        "s_waitcnt lgkmcnt(10)\n"
        "v_smfmac_f32_16x16x64_bf16 v[72:75], v[28:31], a[0:7], v7\n"
        "ds_read_b128 v[104:107], v0 offset:26624\n"
        "v_smfmac_f32_16x16x64_bf16 v[76:79], v[28:31], a[8:15], v7\n"
        "ds_read_b128 v[108:111], v0 offset:29696\n"
        "s_waitcnt lgkmcnt(11)\n"
        "v_smfmac_f32_16x16x64_bf16 v[72:75], v[32:35], a[24:31], v7\n"
        "v_smfmac_f32_16x16x64_bf16 v[76:79], v[32:35], a[32:39], v7\n"
        "s_waitcnt lgkmcnt(10)\n"
        "v_smfmac_f32_16x16x64_bf16 v[72:75], v[36:39], a[48:55], v7\n"
        "v_smfmac_f32_16x16x64_bf16 v[76:79], v[36:39], a[56:63], v7\n"
        "s_waitcnt lgkmcnt(9)\n"
        "v_smfmac_f32_16x16x64_bf16 v[72:75], v[40:43], a[72:79], v7\n"
        "v_smfmac_f32_16x16x64_bf16 v[76:79], v[40:43], a[80:87], v7\n"
        "s_waitcnt lgkmcnt(0)\n"
        "s_waitcnt lgkmcnt(8)\n"
        "v_smfmac_f32_16x16x64_bf16 v[72:75], v[44:47], v[112:119], v7\n"
        "ds_read_b128 v[112:115], v2 offset:0\n"
        "s_add_u32 s47, s38, s35\n"
        "s_lshl_b32 s47, s47, 6\n"
        "s_add_u32 s47, s47, s58\n"
        "v_smfmac_f32_16x16x64_bf16 v[76:79], v[44:47], v[120:127], v7\n"
        "ds_read_b128 v[116:119], v2 offset:24576\n"
        "s_add_u32 s47, s47, 1\n"
        "v_mov_b32 v22, s47\n"
        "s_mov_b64 exec, s[36:37]\n"
        "global_store_dword v23, v22, s[98:99] sc1\n"
        "s_mov_b64 exec, -1\n"
        "s_waitcnt lgkmcnt(9)\n"
        "v_smfmac_f32_16x16x64_bf16 v[72:75], v[48:51], a[96:103], v7\n"
        "ds_read_b128 v[120:123], v2 offset:1024\n"
        "v_smfmac_f32_16x16x64_bf16 v[76:79], v[48:51], a[104:111], v7\n"
        "ds_read_b128 v[124:127], v2 offset:25600\n"
        "s_waitcnt lgkmcnt(10)\n"
        "v_smfmac_f32_16x16x64_bf16 v[72:75], v[52:55], a[120:127], v7\n"
        "v_smfmac_f32_16x16x64_bf16 v[76:79], v[52:55], a[128:135], v7\n"
        "s_waitcnt lgkmcnt(9)\n"
        "v_smfmac_f32_16x16x64_bf16 v[72:75], v[56:59], a[144:151], v7\n"
        "v_smfmac_f32_16x16x64_bf16 v[76:79], v[56:59], a[152:159], v7\n"
        "s_waitcnt lgkmcnt(8)\n"
        "v_smfmac_f32_16x16x64_bf16 v[72:75], v[60:63], a[168:175], v7\n"
        "v_smfmac_f32_16x16x64_bf16 v[76:79], v[60:63], a[176:183], v7\n"
        "s_waitcnt lgkmcnt(6)\n"
        "v_smfmac_f32_16x16x64_bf16 v[80:83], v[24:27], v[96:103], v7\n"
        "ds_read_b128 v[96:99], v2 offset:2048\n"
        "v_smfmac_f32_16x16x64_bf16 v[80:83], v[28:31], a[16:23], v7\n"
        "ds_read_b128 v[100:103], v2 offset:26624\n"
        "v_smfmac_f32_16x16x64_bf16 v[80:83], v[32:35], a[40:47], v7\n"
        "v_smfmac_f32_16x16x64_bf16 v[80:83], v[36:39], a[64:71], v7\n"
        "v_add_f32 v72, v72, v73\n"
        "v_smfmac_f32_16x16x64_bf16 v[80:83], v[40:43], a[88:95], v7\n"
        "v_add_f32 v74, v74, v75\n"
        "s_waitcnt lgkmcnt(6)\n"
        "v_smfmac_f32_16x16x64_bf16 v[80:83], v[44:47], v[104:111], v7\n"
        "ds_read_b128 v[104:107], v2 offset:3072\n"
        "v_cvt_pk_bf16_f32 v72, v72, v74\n"
        "ds_write_b16 v8, v72 offset:0\n"
        "ds_write_b16_d16_hi v8, v72 offset:4\n"
        "v_smfmac_f32_16x16x64_bf16 v[80:83], v[48:51], a[112:119], v7\n"
        "ds_read_b128 v[108:111], v2 offset:27648\n"
        "v_add_f32 v76, v76, v77\n"
        "v_smfmac_f32_16x16x64_bf16 v[80:83], v[52:55], a[136:143], v7\n"
        "v_add_f32 v78, v78, v79\n"
        "v_smfmac_f32_16x16x64_bf16 v[80:83], v[56:59], a[160:167], v7\n"
        "v_cvt_pk_bf16_f32 v76, v76, v78\n"
        "ds_write_b16 v8, v76 offset:256\n"
        "ds_write_b16_d16_hi v8, v76 offset:260\n"
        "v_smfmac_f32_16x16x64_bf16 v[80:83], v[60:63], a[184:191], v7\n"
        "v_mov_b32 v24, 0\n"
        "v_mov_b32 v25, 0\n"
        "v_mov_b32 v26, 0\n"
        "v_mov_b32 v27, 0\n"
        "v_mov_b32 v28, 0\n"
        "v_mov_b32 v29, 0\n"
        "v_mov_b32 v30, 0\n"
        "v_mov_b32 v31, 0\n"
        "v_add_f32 v80, v80, v81\n"
        "v_add_f32 v82, v82, v83\n"
        "v_cvt_pk_bf16_f32 v80, v80, v82\n"
        "ds_write_b16 v8, v80 offset:512\n"
        "ds_write_b16_d16_hi v8, v80 offset:516\n"
        "v_mov_b32 v32, 0\n"
        "v_mov_b32 v33, 0\n"
        "v_mov_b32 v34, 0\n"
        "v_mov_b32 v35, 0\n"
        "v_mov_b32 v36, 0\n"
        "v_mov_b32 v37, 0\n"
        "v_mov_b32 v38, 0\n"
        "v_mov_b32 v39, 0\n"
        "s_waitcnt lgkmcnt(0)\n"
        "s_barrier\n"
        "ds_read_b64_tr_b16 v[84:85], v9 offset:0\n"
        "ds_read_b64_tr_b16 v[86:87], v9 offset:128\n"
        "ds_read_b64_tr_b16 v[88:89], v9 offset:1024\n"
        "ds_read_b64_tr_b16 v[90:91], v9 offset:1152\n"
        "ds_read_b64_tr_b16 v[92:93], v9 offset:2048\n"
        "ds_read_b64_tr_b16 v[94:95], v9 offset:2176\n"
        "v_mov_b32 v40, 0\n"
        "v_mov_b32 v41, 0\n"
        "v_mov_b32 v42, 0\n"
        "v_mov_b32 v43, 0\n"
        "v_mov_b32 v44, 0\n"
        "v_mov_b32 v45, 0\n"
        "v_mov_b32 v46, 0\n"
        "v_mov_b32 v47, 0\n"
        "v_mov_b32 v48, 0\n"
        "v_mov_b32 v49, 0\n"
        "v_mov_b32 v50, 0\n"
        "v_mov_b32 v51, 0\n"
        "v_mov_b32 v52, 0\n"
        "v_mov_b32 v53, 0\n"
        "v_mov_b32 v54, 0\n"
        "v_mov_b32 v55, 0\n"
        "v_mov_b32 v56, 0\n"
        "v_mov_b32 v57, 0\n"
        "v_mov_b32 v58, 0\n"
        "v_mov_b32 v59, 0\n"
        "v_mov_b32 v60, 0\n"
        "v_mov_b32 v61, 0\n"
        "v_mov_b32 v62, 0\n"
        "v_mov_b32 v63, 0\n"
        "s_waitcnt lgkmcnt(4)\n"
        "v_smfmac_f32_16x16x64_bf16 v[24:27], v[84:87], v[112:119], v7\n"
        "ds_read_b128 v[112:115], v2 offset:4096\n"
        "s_waitcnt lgkmcnt(3)\n"
        "v_smfmac_f32_16x16x64_bf16 v[24:27], v[88:91], a[192:199], v7\n"
        "ds_read_b128 v[116:119], v2 offset:28672\n"
        "s_waitcnt lgkmcnt(2)\n"
        "v_smfmac_f32_16x16x64_bf16 v[24:27], v[92:95], a[200:207], v7\n"
        "v_smfmac_f32_16x16x64_bf16 v[28:31], v[84:87], v[120:127], v7\n"
        "ds_read_b128 v[120:123], v2 offset:5120\n"
        "v_smfmac_f32_16x16x64_bf16 v[28:31], v[88:91], a[208:215], v7\n"
        "ds_read_b128 v[124:127], v2 offset:29696\n"
        "v_smfmac_f32_16x16x64_bf16 v[28:31], v[92:95], a[216:223], v7\n"
        "v_smfmac_f32_16x16x64_bf16 v[32:35], v[84:87], v[96:103], v7\n"
        "v_smfmac_f32_16x16x64_bf16 v[32:35], v[88:91], a[224:231], v7\n"
        "v_smfmac_f32_16x16x64_bf16 v[32:35], v[92:95], a[232:239], v7\n"
        "v_add_f32 v24, v24, v25\n"
        "v_smfmac_f32_16x16x64_bf16 v[36:39], v[84:87], v[104:111], v7\n"
        "v_add_f32 v26, v26, v27\n"
        "v_smfmac_f32_16x16x64_bf16 v[36:39], v[88:91], a[240:247], v7\n"
        "v_smfmac_f32_16x16x64_bf16 v[36:39], v[92:95], a[248:255], v7\n"
        "v_add_f32 v28, v28, v29\n"
        "v_smfmac_f32_16x16x64_bf16 v[40:43], v[84:87], v[128:135], v7\n"
        "v_add_f32 v30, v30, v31\n"
        "v_smfmac_f32_16x16x64_bf16 v[40:43], v[88:91], v[136:143], v7\n"
        "v_cvt_pk_bf16_f32 v24, v24, v26\n"
        "v_smfmac_f32_16x16x64_bf16 v[40:43], v[92:95], v[144:151], v7\n"
        "v_cvt_pk_bf16_f32 v25, v28, v30\n"
        "v_smfmac_f32_16x16x64_bf16 v[44:47], v[84:87], v[152:159], v7\n"
        "v_smfmac_f32_16x16x64_bf16 v[44:47], v[88:91], v[160:167], v7\n"
        "v_permlane16_swap_b32 v24, v25\n"
        "v_smfmac_f32_16x16x64_bf16 v[44:47], v[92:95], v[168:175], v7\n"
        "global_store_dwordx2 v10, v[24:25], s[50:51] sc1\n"
        "v_add_f32 v32, v32, v33\n"
        "v_smfmac_f32_16x16x64_bf16 v[48:51], v[84:87], v[176:183], v7\n"
        "v_add_f32 v34, v34, v35\n"
        "v_smfmac_f32_16x16x64_bf16 v[48:51], v[88:91], v[184:191], v7\n"
        "v_add_f32 v36, v36, v37\n"
        "v_smfmac_f32_16x16x64_bf16 v[48:51], v[92:95], v[192:199], v7\n"
        "v_add_f32 v38, v38, v39\n"
        "v_smfmac_f32_16x16x64_bf16 v[52:55], v[84:87], v[200:207], v7\n"
        "v_cvt_pk_bf16_f32 v32, v32, v34\n"
        "v_smfmac_f32_16x16x64_bf16 v[52:55], v[88:91], v[208:215], v7\n"
        "v_cvt_pk_bf16_f32 v33, v36, v38\n"
        "v_smfmac_f32_16x16x64_bf16 v[52:55], v[92:95], v[216:223], v7\n"
        "s_waitcnt lgkmcnt(2)\n"
        "v_smfmac_f32_16x16x64_bf16 v[56:59], v[84:87], v[112:119], v7\n"
        "v_permlane16_swap_b32 v32, v33\n"
        "v_smfmac_f32_16x16x64_bf16 v[56:59], v[88:91], v[224:231], v7\n"
        "global_store_dwordx2 v11, v[32:33], s[50:51] sc1\n"
        "v_add_f32 v40, v40, v41\n"
        "v_smfmac_f32_16x16x64_bf16 v[56:59], v[92:95], v[232:239], v7\n"
        "v_add_f32 v42, v42, v43\n"
        "s_waitcnt lgkmcnt(0)\n"
        "v_smfmac_f32_16x16x64_bf16 v[60:63], v[84:87], v[120:127], v7\n"
        "v_add_f32 v44, v44, v45\n"
        "v_smfmac_f32_16x16x64_bf16 v[60:63], v[88:91], v[240:247], v7\n"
        "v_add_f32 v46, v46, v47\n"
        "v_smfmac_f32_16x16x64_bf16 v[60:63], v[92:95], v[248:255], v7\n"
        "v_cvt_pk_bf16_f32 v40, v40, v42\n"
        "v_cvt_pk_bf16_f32 v41, v44, v46\n"
        "s_nop 1\n"
        "v_permlane16_swap_b32 v40, v41\n"
        "global_store_dwordx2 v12, v[40:41], s[50:51] sc1\n"
        "ds_read_b128 v[24:27], v1 offset:0\n"
        "ds_read_b128 v[28:31], v1 offset:1024\n"
        "ds_read_b128 v[32:35], v1 offset:2048\n"
        "ds_read_b128 v[36:39], v1 offset:3072\n"
        "v_add_f32 v48, v48, v49\n"
        "v_add_f32 v50, v50, v51\n"
        "v_add_f32 v52, v52, v53\n"
        "v_add_f32 v54, v54, v55\n"
        "v_cvt_pk_bf16_f32 v48, v48, v50\n"
        "v_cvt_pk_bf16_f32 v49, v52, v54\n"
        "s_nop 1\n"
        "v_permlane16_swap_b32 v48, v49\n"
        "global_store_dwordx2 v13, v[48:49], s[50:51] sc1\n"
        "v_add_f32 v56, v56, v57\n"
        "v_add_f32 v58, v58, v59\n"
        "v_add_f32 v60, v60, v61\n"
        "v_add_f32 v62, v62, v63\n"
        "v_cvt_pk_bf16_f32 v56, v56, v58\n"
        "v_cvt_pk_bf16_f32 v57, v60, v62\n"
        "s_nop 1\n"
        "v_permlane16_swap_b32 v56, v57\n"
        "global_store_dwordx2 v14, v[56:57], s[50:51] sc1\n"
        "s_waitcnt lgkmcnt(0)\n"
        "s_memrealtime s[92:93]\n"
        "s_add_u32 s47, s60, 1\n"
        "s_and_b32 s60, s60, 0xffffffc0\n"
        "s_sub_u32 s68, s59, s46\n"
        "s_cmp_gt_u32 s68, 1\n"
        "s_cselect_b32 s60, s47, s60\n"
        "s_and_b32 s68, s60, 63\n"
        "s_cmp_eq_u32 s68, 2\n"
        "s_cselect_b32 s68, 126, 0\n"
        "s_add_u32 s60, s60, s68\n"
        "s_min_u32 s60, s60, 1920\n"
        "s_add_u32 s45, s45, 2\n"
        "s_cmp_lt_u32 s45, 64\n"
        "s_cbranch_scc1 Lvo_loop\n"
        "s_mov_b32 s88, 1\n"
        "Lg3_entry:\n"
        "s_waitcnt vmcnt(0) lgkmcnt(0)\n"
        "s_barrier\n"
        "v_mbcnt_lo_u32_b32 v0, -1, 0\n"
        "v_mbcnt_hi_u32_b32 v0, -1, v0\n"
        "s_load_dwordx4 s[92:95], s[0:1], 72\n"
        "s_lshl_b32 s47, s40, 1\n"
        "s_add_u32 s58, s47, s41\n"
        "s_mov_b32 s40, s88\n"
        "v_lshrrev_b32 v124, 3, v0\n"
        "s_lshl_b32 s47, s43, 3\n"
        "v_add_u32 v124, s47, v124\n"
        "v_bfe_u32 v125, v0, 4, 1\n"
        "s_and_b32 s47, s43, 1\n"
        "s_lshl_b32 s47, s47, 1\n"
        "v_or_b32 v125, s47, v125\n"
        "v_lshlrev_b32 v125, 1, v125\n"
        "v_and_b32 v126, 7, v0\n"
        "v_xor_b32 v125, v125, v126\n"
        "v_lshlrev_b32 v125, 4, v125\n"
        "s_mul_i32 s47, s40, 0x180\n"
        "v_mul_u32_u24 v126, 0x500, v124\n"
        "v_add3_u32 v3, v126, v125, s47\n"
        "v_lshrrev_b32 v124, 4, v0\n"
        "v_lshlrev_b32 v124, 10, v124\n"
        "v_bfe_u32 v125, v0, 2, 2\n"
        "v_lshl_add_u32 v124, v125, 7, v124\n"
        "v_bfe_u32 v125, v0, 3, 2\n"
        "v_lshl_add_u32 v124, v125, 5, v124\n"
        "v_and_b32 v125, 3, v0\n"
        "v_lshl_add_u32 v124, v125, 3, v124\n"
        "v_lshrrev_b32 v124, 4, v0\n"
        "v_lshlrev_b32 v124, 10, v124\n"
        "v_bfe_u32 v125, v0, 2, 2\n"
        "v_lshl_add_u32 v124, v125, 7, v124\n"
        "v_bfe_u32 v125, v0, 3, 2\n"
        "v_bfe_u32 v126, v0, 1, 1\n"
        "v_xor_b32 v125, v125, v126\n"
        "v_lshl_add_u32 v124, v125, 5, v124\n"
        "v_and_b32 v125, 1, v0\n"
        "v_lshl_add_u32 v124, v125, 4, v124\n"
        "s_lshl_b32 s47, s43, 12\n"
        "v_add_u32 v5, s47, v124\n"
        "v_add_u32 v6, 8, v5\n"
        "v_xor_b32 v7, 64, v5\n"
        "v_add_u32 v8, 8, v7\n"
        "s_waitcnt lgkmcnt(0)\n"
        "s_lshl_b32 s59, s43, 10\n"
        "s_add_u32 s54, s92, 0x0\n"
        "s_addc_u32 s55, s93, 0\n"
        "s_add_u32 m0, s59, 0xa000\n"
        "s_nop 0\n"
        "global_load_lds_dwordx4 v3, s[54:55] offset:0\n"
        "s_add_u32 m0, s59, 0xaf80\n"
        "s_nop 0\n"
        "global_load_lds_dwordx4 v3, s[54:55] offset:128\n"
        "s_add_u32 m0, s59, 0xbf00\n"
        "s_nop 0\n"
        "global_load_lds_dwordx4 v3, s[54:55] offset:256\n"
        "s_add_u32 m0, s59, 0xce80\n"
        "s_nop 0\n"
        "global_load_lds_dwordx4 v3, s[54:55] offset:384\n"
        "s_add_u32 s54, s92, 0xa000\n"
        "s_addc_u32 s55, s93, 0\n"
        "s_add_u32 m0, s59, 0xe000\n"
        "s_nop 0\n"
        "global_load_lds_dwordx4 v3, s[54:55] offset:0\n"
        "s_add_u32 m0, s59, 0xef80\n"
        "s_nop 0\n"
        "global_load_lds_dwordx4 v3, s[54:55] offset:128\n"
        "s_add_u32 m0, s59, 0xff00\n"
        "s_nop 0\n"
        "global_load_lds_dwordx4 v3, s[54:55] offset:256\n"
        "s_add_u32 m0, s59, 0x10e80\n"
        "s_nop 0\n"
        "global_load_lds_dwordx4 v3, s[54:55] offset:384\n"
        "s_add_u32 s54, s92, 0x14000\n"
        "s_addc_u32 s55, s93, 0\n"
        "s_add_u32 m0, s59, 0x12000\n"
        "s_nop 0\n"
        "global_load_lds_dwordx4 v3, s[54:55] offset:0\n"
        "s_add_u32 m0, s59, 0x12f80\n"
        "s_nop 0\n"
        "global_load_lds_dwordx4 v3, s[54:55] offset:128\n"
        "s_add_u32 m0, s59, 0x13f00\n"
        "s_nop 0\n"
        "global_load_lds_dwordx4 v3, s[54:55] offset:256\n"
        "s_add_u32 m0, s59, 0x14e80\n"
        "s_nop 0\n"
        "global_load_lds_dwordx4 v3, s[54:55] offset:384\n"
        "s_add_u32 s54, s92, 0x1e000\n"
        "s_addc_u32 s55, s93, 0\n"
        "s_add_u32 m0, s59, 0x16000\n"
        "s_nop 0\n"
        "global_load_lds_dwordx4 v3, s[54:55] offset:0\n"
        "s_add_u32 m0, s59, 0x16f80\n"
        "s_nop 0\n"
        "global_load_lds_dwordx4 v3, s[54:55] offset:128\n"
        "s_add_u32 m0, s59, 0x17f00\n"
        "s_nop 0\n"
        "global_load_lds_dwordx4 v3, s[54:55] offset:256\n"
        "s_add_u32 m0, s59, 0x18e80\n"
        "s_nop 0\n"
        "global_load_lds_dwordx4 v3, s[54:55] offset:384\n"
        "s_add_u32 s54, s92, 0x28000\n"
        "s_addc_u32 s55, s93, 0\n"
        "s_add_u32 m0, s59, 0x1a000\n"
        "s_nop 0\n"
        "global_load_lds_dwordx4 v3, s[54:55] offset:0\n"
        "s_add_u32 m0, s59, 0x1af80\n"
        "s_nop 0\n"
        "global_load_lds_dwordx4 v3, s[54:55] offset:128\n"
        "s_add_u32 m0, s59, 0x1bf00\n"
        "s_nop 0\n"
        "global_load_lds_dwordx4 v3, s[54:55] offset:256\n"
        "s_add_u32 m0, s59, 0x1ce80\n"
        "s_nop 0\n"
        "global_load_lds_dwordx4 v3, s[54:55] offset:384\n"
        "s_waitcnt vmcnt(16)\n"
        "s_waitcnt lgkmcnt(0)\n"
        "s_barrier\n"
        "v_add_u32 v20, 0xa000, v5\n"
        "ds_read_b64_tr_b16 a[0:1], v20\n"
        "ds_read_b64_tr_b16 a[2:3], v20 offset:512\n"
        "v_add_u32 v21, 0xa000, v6\n"
        "ds_read_b64_tr_b16 a[4:5], v21\n"
        "ds_read_b64_tr_b16 a[6:7], v21 offset:512\n"
        "v_add_u32 v22, 0xa000, v7\n"
        "ds_read_b64_tr_b16 a[8:9], v22\n"
        "ds_read_b64_tr_b16 a[10:11], v22 offset:512\n"
        "v_add_u32 v20, 0xa000, v8\n"
        "ds_read_b64_tr_b16 a[12:13], v20\n"
        "ds_read_b64_tr_b16 a[14:15], v20 offset:512\n"
        "s_waitcnt vmcnt(12)\n"
        "s_waitcnt lgkmcnt(0)\n"
        "s_barrier\n"
        "s_add_u32 s54, s92, 0x32000\n"
        "s_addc_u32 s55, s93, 0\n"
        "s_add_u32 m0, s59, 0x1e000\n"
        "s_nop 0\n"
        "global_load_lds_dwordx4 v3, s[54:55] offset:0\n"
        "s_add_u32 m0, s59, 0x1ef80\n"
        "s_nop 0\n"
        "global_load_lds_dwordx4 v3, s[54:55] offset:128\n"
        "s_add_u32 m0, s59, 0x1ff00\n"
        "s_nop 0\n"
        "global_load_lds_dwordx4 v3, s[54:55] offset:256\n"
        "s_add_u32 m0, s59, 0x20e80\n"
        "s_nop 0\n"
        "global_load_lds_dwordx4 v3, s[54:55] offset:384\n"
        "v_add_u32 v21, 0xe000, v5\n"
        "ds_read_b64_tr_b16 a[16:17], v21\n"
        "ds_read_b64_tr_b16 a[18:19], v21 offset:512\n"
        "v_add_u32 v22, 0xe000, v6\n"
        "ds_read_b64_tr_b16 a[20:21], v22\n"
        "ds_read_b64_tr_b16 a[22:23], v22 offset:512\n"
        "v_add_u32 v20, 0xe000, v7\n"
        "ds_read_b64_tr_b16 a[24:25], v20\n"
        "ds_read_b64_tr_b16 a[26:27], v20 offset:512\n"
        "v_add_u32 v21, 0xe000, v8\n"
        "ds_read_b64_tr_b16 a[28:29], v21\n"
        "ds_read_b64_tr_b16 a[30:31], v21 offset:512\n"
        "s_waitcnt vmcnt(12)\n"
        "s_waitcnt lgkmcnt(0)\n"
        "s_barrier\n"
        "s_add_u32 s54, s92, 0x3c000\n"
        "s_addc_u32 s55, s93, 0\n"
        "s_add_u32 m0, s59, 0x22000\n"
        "s_nop 0\n"
        "global_load_lds_dwordx4 v3, s[54:55] offset:0\n"
        "s_add_u32 m0, s59, 0x22f80\n"
        "s_nop 0\n"
        "global_load_lds_dwordx4 v3, s[54:55] offset:128\n"
        "s_add_u32 m0, s59, 0x23f00\n"
        "s_nop 0\n"
        "global_load_lds_dwordx4 v3, s[54:55] offset:256\n"
        "s_add_u32 m0, s59, 0x24e80\n"
        "s_nop 0\n"
        "global_load_lds_dwordx4 v3, s[54:55] offset:384\n"
        "v_add_u32 v22, 0x12000, v5\n"
        "ds_read_b64_tr_b16 a[32:33], v22\n"
        "ds_read_b64_tr_b16 a[34:35], v22 offset:512\n"
        "v_add_u32 v20, 0x12000, v6\n"
        "ds_read_b64_tr_b16 a[36:37], v20\n"
        "ds_read_b64_tr_b16 a[38:39], v20 offset:512\n"
        "v_add_u32 v21, 0x12000, v7\n"
        "ds_read_b64_tr_b16 a[40:41], v21\n"
        "ds_read_b64_tr_b16 a[42:43], v21 offset:512\n"
        "v_add_u32 v22, 0x12000, v8\n"
        "ds_read_b64_tr_b16 a[44:45], v22\n"
        "ds_read_b64_tr_b16 a[46:47], v22 offset:512\n"
        "s_waitcnt vmcnt(12)\n"
        "s_waitcnt lgkmcnt(0)\n"
        "s_barrier\n"
        "s_add_u32 s54, s92, 0x46000\n"
        "s_addc_u32 s55, s93, 0\n"
        "s_add_u32 m0, s59, 0xa000\n"
        "s_nop 0\n"
        "global_load_lds_dwordx4 v3, s[54:55] offset:0\n"
        "s_add_u32 m0, s59, 0xaf80\n"
        "s_nop 0\n"
        "global_load_lds_dwordx4 v3, s[54:55] offset:128\n"
        "s_add_u32 m0, s59, 0xbf00\n"
        "s_nop 0\n"
        "global_load_lds_dwordx4 v3, s[54:55] offset:256\n"
        "s_add_u32 m0, s59, 0xce80\n"
        "s_nop 0\n"
        "global_load_lds_dwordx4 v3, s[54:55] offset:384\n"
        "v_add_u32 v20, 0x16000, v5\n"
        "ds_read_b64_tr_b16 a[48:49], v20\n"
        "ds_read_b64_tr_b16 a[50:51], v20 offset:512\n"
        "v_add_u32 v21, 0x16000, v6\n"
        "ds_read_b64_tr_b16 a[52:53], v21\n"
        "ds_read_b64_tr_b16 a[54:55], v21 offset:512\n"
        "v_add_u32 v22, 0x16000, v7\n"
        "ds_read_b64_tr_b16 a[56:57], v22\n"
        "ds_read_b64_tr_b16 a[58:59], v22 offset:512\n"
        "v_add_u32 v20, 0x16000, v8\n"
        "ds_read_b64_tr_b16 a[60:61], v20\n"
        "ds_read_b64_tr_b16 a[62:63], v20 offset:512\n"
        "s_waitcnt vmcnt(12)\n"
        "s_waitcnt lgkmcnt(0)\n"
        "s_barrier\n"
        "s_add_u32 s54, s92, 0x50000\n"
        "s_addc_u32 s55, s93, 0\n"
        "s_add_u32 m0, s59, 0xe000\n"
        "s_nop 0\n"
        "global_load_lds_dwordx4 v3, s[54:55] offset:0\n"
        "s_add_u32 m0, s59, 0xef80\n"
        "s_nop 0\n"
        "global_load_lds_dwordx4 v3, s[54:55] offset:128\n"
        "s_add_u32 m0, s59, 0xff00\n"
        "s_nop 0\n"
        "global_load_lds_dwordx4 v3, s[54:55] offset:256\n"
        "s_add_u32 m0, s59, 0x10e80\n"
        "s_nop 0\n"
        "global_load_lds_dwordx4 v3, s[54:55] offset:384\n"
        "v_add_u32 v21, 0x1a000, v5\n"
        "ds_read_b64_tr_b16 a[64:65], v21\n"
        "ds_read_b64_tr_b16 a[66:67], v21 offset:512\n"
        "v_add_u32 v22, 0x1a000, v6\n"
        "ds_read_b64_tr_b16 a[68:69], v22\n"
        "ds_read_b64_tr_b16 a[70:71], v22 offset:512\n"
        "v_add_u32 v20, 0x1a000, v7\n"
        "ds_read_b64_tr_b16 a[72:73], v20\n"
        "ds_read_b64_tr_b16 a[74:75], v20 offset:512\n"
        "v_add_u32 v21, 0x1a000, v8\n"
        "ds_read_b64_tr_b16 a[76:77], v21\n"
        "ds_read_b64_tr_b16 a[78:79], v21 offset:512\n"
        "s_waitcnt vmcnt(12)\n"
        "s_waitcnt lgkmcnt(0)\n"
        "s_barrier\n"
        "s_add_u32 s54, s92, 0x5a000\n"
        "s_addc_u32 s55, s93, 0\n"
        "s_add_u32 m0, s59, 0x12000\n"
        "s_nop 0\n"
        "global_load_lds_dwordx4 v3, s[54:55] offset:0\n"
        "s_add_u32 m0, s59, 0x12f80\n"
        "s_nop 0\n"
        "global_load_lds_dwordx4 v3, s[54:55] offset:128\n"
        "s_add_u32 m0, s59, 0x13f00\n"
        "s_nop 0\n"
        "global_load_lds_dwordx4 v3, s[54:55] offset:256\n"
        "s_add_u32 m0, s59, 0x14e80\n"
        "s_nop 0\n"
        "global_load_lds_dwordx4 v3, s[54:55] offset:384\n"
        "v_add_u32 v22, 0x1e000, v5\n"
        "ds_read_b64_tr_b16 a[80:81], v22\n"
        "ds_read_b64_tr_b16 a[82:83], v22 offset:512\n"
        "v_add_u32 v20, 0x1e000, v6\n"
        "ds_read_b64_tr_b16 a[84:85], v20\n"
        "ds_read_b64_tr_b16 a[86:87], v20 offset:512\n"
        "v_add_u32 v21, 0x1e000, v7\n"
        "ds_read_b64_tr_b16 a[88:89], v21\n"
        "ds_read_b64_tr_b16 a[90:91], v21 offset:512\n"
        "v_add_u32 v22, 0x1e000, v8\n"
        "ds_read_b64_tr_b16 a[92:93], v22\n"
        "ds_read_b64_tr_b16 a[94:95], v22 offset:512\n"
        "s_waitcnt vmcnt(12)\n"
        "s_waitcnt lgkmcnt(0)\n"
        "s_barrier\n"
        "s_add_u32 s54, s92, 0x64000\n"
        "s_addc_u32 s55, s93, 0\n"
        "s_add_u32 m0, s59, 0x16000\n"
        "s_nop 0\n"
        "global_load_lds_dwordx4 v3, s[54:55] offset:0\n"
        "s_add_u32 m0, s59, 0x16f80\n"
        "s_nop 0\n"
        "global_load_lds_dwordx4 v3, s[54:55] offset:128\n"
        "s_add_u32 m0, s59, 0x17f00\n"
        "s_nop 0\n"
        "global_load_lds_dwordx4 v3, s[54:55] offset:256\n"
        "s_add_u32 m0, s59, 0x18e80\n"
        "s_nop 0\n"
        "global_load_lds_dwordx4 v3, s[54:55] offset:384\n"
        "v_add_u32 v20, 0x22000, v5\n"
        "ds_read_b64_tr_b16 a[96:97], v20\n"
        "ds_read_b64_tr_b16 a[98:99], v20 offset:512\n"
        "v_add_u32 v21, 0x22000, v6\n"
        "ds_read_b64_tr_b16 a[100:101], v21\n"
        "ds_read_b64_tr_b16 a[102:103], v21 offset:512\n"
        "v_add_u32 v22, 0x22000, v7\n"
        "ds_read_b64_tr_b16 a[104:105], v22\n"
        "ds_read_b64_tr_b16 a[106:107], v22 offset:512\n"
        "v_add_u32 v20, 0x22000, v8\n"
        "ds_read_b64_tr_b16 a[108:109], v20\n"
        "ds_read_b64_tr_b16 a[110:111], v20 offset:512\n"
        "s_waitcnt vmcnt(12)\n"
        "s_waitcnt lgkmcnt(0)\n"
        "s_barrier\n"
        "s_add_u32 s54, s92, 0x6e000\n"
        "s_addc_u32 s55, s93, 0\n"
        "s_add_u32 m0, s59, 0x1a000\n"
        "s_nop 0\n"
        "global_load_lds_dwordx4 v3, s[54:55] offset:0\n"
        "s_add_u32 m0, s59, 0x1af80\n"
        "s_nop 0\n"
        "global_load_lds_dwordx4 v3, s[54:55] offset:128\n"
        "s_add_u32 m0, s59, 0x1bf00\n"
        "s_nop 0\n"
        "global_load_lds_dwordx4 v3, s[54:55] offset:256\n"
        "s_add_u32 m0, s59, 0x1ce80\n"
        "s_nop 0\n"
        "global_load_lds_dwordx4 v3, s[54:55] offset:384\n"
        "v_add_u32 v21, 0xa000, v5\n"
        "ds_read_b64_tr_b16 a[112:113], v21\n"
        "ds_read_b64_tr_b16 a[114:115], v21 offset:512\n"
        "v_add_u32 v22, 0xa000, v6\n"
        "ds_read_b64_tr_b16 a[116:117], v22\n"
        "ds_read_b64_tr_b16 a[118:119], v22 offset:512\n"
        "v_add_u32 v20, 0xa000, v7\n"
        "ds_read_b64_tr_b16 a[120:121], v20\n"
        "ds_read_b64_tr_b16 a[122:123], v20 offset:512\n"
        "v_add_u32 v21, 0xa000, v8\n"
        "ds_read_b64_tr_b16 a[124:125], v21\n"
        "ds_read_b64_tr_b16 a[126:127], v21 offset:512\n"
        "s_waitcnt vmcnt(12)\n"
        "s_waitcnt lgkmcnt(0)\n"
        "s_barrier\n"
        "s_add_u32 s54, s92, 0x78000\n"
        "s_addc_u32 s55, s93, 0\n"
        "s_add_u32 m0, s59, 0x1e000\n"
        "s_nop 0\n"
        "global_load_lds_dwordx4 v3, s[54:55] offset:0\n"
        "s_add_u32 m0, s59, 0x1ef80\n"
        "s_nop 0\n"
        "global_load_lds_dwordx4 v3, s[54:55] offset:128\n"
        "s_add_u32 m0, s59, 0x1ff00\n"
        "s_nop 0\n"
        "global_load_lds_dwordx4 v3, s[54:55] offset:256\n"
        "s_add_u32 m0, s59, 0x20e80\n"
        "s_nop 0\n"
        "global_load_lds_dwordx4 v3, s[54:55] offset:384\n"
        "v_add_u32 v22, 0xe000, v5\n"
        "ds_read_b64_tr_b16 a[128:129], v22\n"
        "ds_read_b64_tr_b16 a[130:131], v22 offset:512\n"
        "v_add_u32 v20, 0xe000, v6\n"
        "ds_read_b64_tr_b16 a[132:133], v20\n"
        "ds_read_b64_tr_b16 a[134:135], v20 offset:512\n"
        "v_add_u32 v21, 0xe000, v7\n"
        "ds_read_b64_tr_b16 a[136:137], v21\n"
        "ds_read_b64_tr_b16 a[138:139], v21 offset:512\n"
        "v_add_u32 v22, 0xe000, v8\n"
        "ds_read_b64_tr_b16 a[140:141], v22\n"
        "ds_read_b64_tr_b16 a[142:143], v22 offset:512\n"
        "s_waitcnt vmcnt(12)\n"
        "s_waitcnt lgkmcnt(0)\n"
        "s_barrier\n"
        "s_add_u32 s54, s92, 0x82000\n"
        "s_addc_u32 s55, s93, 0\n"
        "s_add_u32 m0, s59, 0x22000\n"
        "s_nop 0\n"
        "global_load_lds_dwordx4 v3, s[54:55] offset:0\n"
        "s_add_u32 m0, s59, 0x22f80\n"
        "s_nop 0\n"
        "global_load_lds_dwordx4 v3, s[54:55] offset:128\n"
        "s_add_u32 m0, s59, 0x23f00\n"
        "s_nop 0\n"
        "global_load_lds_dwordx4 v3, s[54:55] offset:256\n"
        "s_add_u32 m0, s59, 0x24e80\n"
        "s_nop 0\n"
        "global_load_lds_dwordx4 v3, s[54:55] offset:384\n"
        "v_add_u32 v20, 0x12000, v5\n"
        "ds_read_b64_tr_b16 a[144:145], v20\n"
        "ds_read_b64_tr_b16 a[146:147], v20 offset:512\n"
        "v_add_u32 v21, 0x12000, v6\n"
        "ds_read_b64_tr_b16 a[148:149], v21\n"
        "ds_read_b64_tr_b16 a[150:151], v21 offset:512\n"
        "v_add_u32 v22, 0x12000, v7\n"
        "ds_read_b64_tr_b16 a[152:153], v22\n"
        "ds_read_b64_tr_b16 a[154:155], v22 offset:512\n"
        "v_add_u32 v20, 0x12000, v8\n"
        "ds_read_b64_tr_b16 a[156:157], v20\n"
        "ds_read_b64_tr_b16 a[158:159], v20 offset:512\n"
        "s_waitcnt vmcnt(12)\n"
        "s_waitcnt lgkmcnt(0)\n"
        "s_barrier\n"
        "s_add_u32 s54, s92, 0x8c000\n"
        "s_addc_u32 s55, s93, 0\n"
        "s_add_u32 m0, s59, 0xa000\n"
        "s_nop 0\n"
        "global_load_lds_dwordx4 v3, s[54:55] offset:0\n"
        "s_add_u32 m0, s59, 0xaf80\n"
        "s_nop 0\n"
        "global_load_lds_dwordx4 v3, s[54:55] offset:128\n"
        "s_add_u32 m0, s59, 0xbf00\n"
        "s_nop 0\n"
        "global_load_lds_dwordx4 v3, s[54:55] offset:256\n"
        "s_add_u32 m0, s59, 0xce80\n"
        "s_nop 0\n"
        "global_load_lds_dwordx4 v3, s[54:55] offset:384\n"
        "v_add_u32 v21, 0x16000, v5\n"
        "ds_read_b64_tr_b16 a[160:161], v21\n"
        "ds_read_b64_tr_b16 a[162:163], v21 offset:512\n"
        "v_add_u32 v22, 0x16000, v6\n"
        "ds_read_b64_tr_b16 a[164:165], v22\n"
        "ds_read_b64_tr_b16 a[166:167], v22 offset:512\n"
        "v_add_u32 v20, 0x16000, v7\n"
        "ds_read_b64_tr_b16 a[168:169], v20\n"
        "ds_read_b64_tr_b16 a[170:171], v20 offset:512\n"
        "v_add_u32 v21, 0x16000, v8\n"
        "ds_read_b64_tr_b16 a[172:173], v21\n"
        "ds_read_b64_tr_b16 a[174:175], v21 offset:512\n"
        "s_waitcnt vmcnt(12)\n"
        "s_waitcnt lgkmcnt(0)\n"
        "s_barrier\n"
        "s_add_u32 s54, s92, 0x96000\n"
        "s_addc_u32 s55, s93, 0\n"
        "s_add_u32 m0, s59, 0xe000\n"
        "s_nop 0\n"
        "global_load_lds_dwordx4 v3, s[54:55] offset:0\n"
        "s_add_u32 m0, s59, 0xef80\n"
        "s_nop 0\n"
        "global_load_lds_dwordx4 v3, s[54:55] offset:128\n"
        "s_add_u32 m0, s59, 0xff00\n"
        "s_nop 0\n"
        "global_load_lds_dwordx4 v3, s[54:55] offset:256\n"
        "s_add_u32 m0, s59, 0x10e80\n"
        "s_nop 0\n"
        "global_load_lds_dwordx4 v3, s[54:55] offset:384\n"
        "v_add_u32 v22, 0x1a000, v5\n"
        "ds_read_b64_tr_b16 a[176:177], v22\n"
        "ds_read_b64_tr_b16 a[178:179], v22 offset:512\n"
        "v_add_u32 v20, 0x1a000, v6\n"
        "ds_read_b64_tr_b16 a[180:181], v20\n"
        "ds_read_b64_tr_b16 a[182:183], v20 offset:512\n"
        "v_add_u32 v21, 0x1a000, v7\n"
        "ds_read_b64_tr_b16 a[184:185], v21\n"
        "ds_read_b64_tr_b16 a[186:187], v21 offset:512\n"
        "v_add_u32 v22, 0x1a000, v8\n"
        "ds_read_b64_tr_b16 a[188:189], v22\n"
        "ds_read_b64_tr_b16 a[190:191], v22 offset:512\n"
        "s_waitcnt vmcnt(12)\n"
        "s_waitcnt lgkmcnt(0)\n"
        "s_barrier\n"
        "s_add_u32 s54, s92, 0xa0000\n"
        "s_addc_u32 s55, s93, 0\n"
        "s_add_u32 m0, s59, 0x12000\n"
        "s_nop 0\n"
        "global_load_lds_dwordx4 v3, s[54:55] offset:0\n"
        "s_add_u32 m0, s59, 0x12f80\n"
        "s_nop 0\n"
        "global_load_lds_dwordx4 v3, s[54:55] offset:128\n"
        "s_add_u32 m0, s59, 0x13f00\n"
        "s_nop 0\n"
        "global_load_lds_dwordx4 v3, s[54:55] offset:256\n"
        "s_add_u32 m0, s59, 0x14e80\n"
        "s_nop 0\n"
        "global_load_lds_dwordx4 v3, s[54:55] offset:384\n"
        "v_add_u32 v20, 0x1e000, v5\n"
        "ds_read_b64_tr_b16 a[192:193], v20\n"
        "ds_read_b64_tr_b16 a[194:195], v20 offset:512\n"
        "v_add_u32 v21, 0x1e000, v6\n"
        "ds_read_b64_tr_b16 a[196:197], v21\n"
        "ds_read_b64_tr_b16 a[198:199], v21 offset:512\n"
        "v_add_u32 v22, 0x1e000, v7\n"
        "ds_read_b64_tr_b16 a[200:201], v22\n"
        "ds_read_b64_tr_b16 a[202:203], v22 offset:512\n"
        "v_add_u32 v20, 0x1e000, v8\n"
        "ds_read_b64_tr_b16 a[204:205], v20\n"
        "ds_read_b64_tr_b16 a[206:207], v20 offset:512\n"
        "s_waitcnt vmcnt(12)\n"
        "s_waitcnt lgkmcnt(0)\n"
        "s_barrier\n"
        "s_add_u32 s54, s92, 0xaa000\n"
        "s_addc_u32 s55, s93, 0\n"
        "s_add_u32 m0, s59, 0x16000\n"
        "s_nop 0\n"
        "global_load_lds_dwordx4 v3, s[54:55] offset:0\n"
        "s_add_u32 m0, s59, 0x16f80\n"
        "s_nop 0\n"
        "global_load_lds_dwordx4 v3, s[54:55] offset:128\n"
        "s_add_u32 m0, s59, 0x17f00\n"
        "s_nop 0\n"
        "global_load_lds_dwordx4 v3, s[54:55] offset:256\n"
        "s_add_u32 m0, s59, 0x18e80\n"
        "s_nop 0\n"
        "global_load_lds_dwordx4 v3, s[54:55] offset:384\n"
        "v_add_u32 v21, 0x22000, v5\n"
        "ds_read_b64_tr_b16 a[208:209], v21\n"
        "ds_read_b64_tr_b16 a[210:211], v21 offset:512\n"
        "v_add_u32 v22, 0x22000, v6\n"
        "ds_read_b64_tr_b16 a[212:213], v22\n"
        "ds_read_b64_tr_b16 a[214:215], v22 offset:512\n"
        "v_add_u32 v20, 0x22000, v7\n"
        "ds_read_b64_tr_b16 a[216:217], v20\n"
        "ds_read_b64_tr_b16 a[218:219], v20 offset:512\n"
        "v_add_u32 v21, 0x22000, v8\n"
        "ds_read_b64_tr_b16 a[220:221], v21\n"
        "ds_read_b64_tr_b16 a[222:223], v21 offset:512\n"
        "s_waitcnt vmcnt(12)\n"
        "s_waitcnt lgkmcnt(0)\n"
        "s_barrier\n"
        "s_add_u32 s54, s92, 0xb4000\n"
        "s_addc_u32 s55, s93, 0\n"
        "s_add_u32 m0, s59, 0x1a000\n"
        "s_nop 0\n"
        "global_load_lds_dwordx4 v3, s[54:55] offset:0\n"
        "s_add_u32 m0, s59, 0x1af80\n"
        "s_nop 0\n"
        "global_load_lds_dwordx4 v3, s[54:55] offset:128\n"
        "s_add_u32 m0, s59, 0x1bf00\n"
        "s_nop 0\n"
        "global_load_lds_dwordx4 v3, s[54:55] offset:256\n"
        "s_add_u32 m0, s59, 0x1ce80\n"
        "s_nop 0\n"
        "global_load_lds_dwordx4 v3, s[54:55] offset:384\n"
        "v_add_u32 v22, 0xa000, v5\n"
        "ds_read_b64_tr_b16 a[224:225], v22\n"
        "ds_read_b64_tr_b16 a[226:227], v22 offset:512\n"
        "v_add_u32 v20, 0xa000, v6\n"
        "ds_read_b64_tr_b16 a[228:229], v20\n"
        "ds_read_b64_tr_b16 a[230:231], v20 offset:512\n"
        "v_add_u32 v21, 0xa000, v7\n"
        "ds_read_b64_tr_b16 a[232:233], v21\n"
        "ds_read_b64_tr_b16 a[234:235], v21 offset:512\n"
        "v_add_u32 v22, 0xa000, v8\n"
        "ds_read_b64_tr_b16 a[236:237], v22\n"
        "ds_read_b64_tr_b16 a[238:239], v22 offset:512\n"
        "s_waitcnt vmcnt(12)\n"
        "s_waitcnt lgkmcnt(0)\n"
        "s_barrier\n"
        "s_add_u32 s54, s92, 0xbe000\n"
        "s_addc_u32 s55, s93, 0\n"
        "s_add_u32 m0, s59, 0x1e000\n"
        "s_nop 0\n"
        "global_load_lds_dwordx4 v3, s[54:55] offset:0\n"
        "s_add_u32 m0, s59, 0x1ef80\n"
        "s_nop 0\n"
        "global_load_lds_dwordx4 v3, s[54:55] offset:128\n"
        "s_add_u32 m0, s59, 0x1ff00\n"
        "s_nop 0\n"
        "global_load_lds_dwordx4 v3, s[54:55] offset:256\n"
        "s_add_u32 m0, s59, 0x20e80\n"
        "s_nop 0\n"
        "global_load_lds_dwordx4 v3, s[54:55] offset:384\n"
        "v_add_u32 v20, 0xe000, v5\n"
        "ds_read_b64_tr_b16 a[240:241], v20\n"
        "ds_read_b64_tr_b16 a[242:243], v20 offset:512\n"
        "v_add_u32 v21, 0xe000, v6\n"
        "ds_read_b64_tr_b16 a[244:245], v21\n"
        "ds_read_b64_tr_b16 a[246:247], v21 offset:512\n"
        "v_add_u32 v22, 0xe000, v7\n"
        "ds_read_b64_tr_b16 a[248:249], v22\n"
        "ds_read_b64_tr_b16 a[250:251], v22 offset:512\n"
        "v_add_u32 v20, 0xe000, v8\n"
        "ds_read_b64_tr_b16 a[252:253], v20\n"
        "ds_read_b64_tr_b16 a[254:255], v20 offset:512\n"
        "s_waitcnt vmcnt(12)\n"
        "s_waitcnt lgkmcnt(0)\n"
        "s_barrier\n"
        "s_add_u32 s54, s92, 0xc8000\n"
        "s_addc_u32 s55, s93, 0\n"
        "s_add_u32 m0, s59, 0x22000\n"
        "s_nop 0\n"
        "global_load_lds_dwordx4 v3, s[54:55] offset:0\n"
        "s_add_u32 m0, s59, 0x22f80\n"
        "s_nop 0\n"
        "global_load_lds_dwordx4 v3, s[54:55] offset:128\n"
        "s_add_u32 m0, s59, 0x23f00\n"
        "s_nop 0\n"
        "global_load_lds_dwordx4 v3, s[54:55] offset:256\n"
        "s_add_u32 m0, s59, 0x24e80\n"
        "s_nop 0\n"
        "global_load_lds_dwordx4 v3, s[54:55] offset:384\n"
        "v_add_u32 v21, 0x12000, v5\n"
        "ds_read_b64_tr_b16 v[128:129], v21\n"
        "ds_read_b64_tr_b16 v[130:131], v21 offset:512\n"
        "v_add_u32 v22, 0x12000, v6\n"
        "ds_read_b64_tr_b16 v[132:133], v22\n"
        "ds_read_b64_tr_b16 v[134:135], v22 offset:512\n"
        "v_add_u32 v20, 0x12000, v7\n"
        "ds_read_b64_tr_b16 v[136:137], v20\n"
        "ds_read_b64_tr_b16 v[138:139], v20 offset:512\n"
        "v_add_u32 v21, 0x12000, v8\n"
        "ds_read_b64_tr_b16 v[140:141], v21\n"
        "ds_read_b64_tr_b16 v[142:143], v21 offset:512\n"
        "s_waitcnt vmcnt(12)\n"
        "s_waitcnt lgkmcnt(0)\n"
        "s_barrier\n"
        "s_add_u32 s54, s92, 0xd2000\n"
        "s_addc_u32 s55, s93, 0\n"
        "s_add_u32 m0, s59, 0xa000\n"
        "s_nop 0\n"
        "global_load_lds_dwordx4 v3, s[54:55] offset:0\n"
        "s_add_u32 m0, s59, 0xaf80\n"
        "s_nop 0\n"
        "global_load_lds_dwordx4 v3, s[54:55] offset:128\n"
        "s_add_u32 m0, s59, 0xbf00\n"
        "s_nop 0\n"
        "global_load_lds_dwordx4 v3, s[54:55] offset:256\n"
        "s_add_u32 m0, s59, 0xce80\n"
        "s_nop 0\n"
        "global_load_lds_dwordx4 v3, s[54:55] offset:384\n"
        "v_add_u32 v22, 0x16000, v5\n"
        "ds_read_b64_tr_b16 v[144:145], v22\n"
        "ds_read_b64_tr_b16 v[146:147], v22 offset:512\n"
        "v_add_u32 v20, 0x16000, v6\n"
        "ds_read_b64_tr_b16 v[148:149], v20\n"
        "ds_read_b64_tr_b16 v[150:151], v20 offset:512\n"
        "v_add_u32 v21, 0x16000, v7\n"
        "ds_read_b64_tr_b16 v[152:153], v21\n"
        "ds_read_b64_tr_b16 v[154:155], v21 offset:512\n"
        "v_add_u32 v22, 0x16000, v8\n"
        "ds_read_b64_tr_b16 v[156:157], v22\n"
        "ds_read_b64_tr_b16 v[158:159], v22 offset:512\n"
        "s_waitcnt vmcnt(12)\n"
        "s_waitcnt lgkmcnt(0)\n"
        "s_barrier\n"
        "s_add_u32 s54, s92, 0xdc000\n"
        "s_addc_u32 s55, s93, 0\n"
        "s_add_u32 m0, s59, 0xe000\n"
        "s_nop 0\n"
        "global_load_lds_dwordx4 v3, s[54:55] offset:0\n"
        "s_add_u32 m0, s59, 0xef80\n"
        "s_nop 0\n"
        "global_load_lds_dwordx4 v3, s[54:55] offset:128\n"
        "s_add_u32 m0, s59, 0xff00\n"
        "s_nop 0\n"
        "global_load_lds_dwordx4 v3, s[54:55] offset:256\n"
        "s_add_u32 m0, s59, 0x10e80\n"
        "s_nop 0\n"
        "global_load_lds_dwordx4 v3, s[54:55] offset:384\n"
        "v_add_u32 v20, 0x1a000, v5\n"
        "ds_read_b64_tr_b16 v[160:161], v20\n"
        "ds_read_b64_tr_b16 v[162:163], v20 offset:512\n"
        "v_add_u32 v21, 0x1a000, v6\n"
        "ds_read_b64_tr_b16 v[164:165], v21\n"
        "ds_read_b64_tr_b16 v[166:167], v21 offset:512\n"
        "v_add_u32 v22, 0x1a000, v7\n"
        "ds_read_b64_tr_b16 v[168:169], v22\n"
        "ds_read_b64_tr_b16 v[170:171], v22 offset:512\n"
        "v_add_u32 v20, 0x1a000, v8\n"
        "ds_read_b64_tr_b16 v[172:173], v20\n"
        "ds_read_b64_tr_b16 v[174:175], v20 offset:512\n"
        "s_waitcnt vmcnt(12)\n"
        "s_waitcnt lgkmcnt(0)\n"
        "s_barrier\n"
        "s_add_u32 s54, s92, 0xe6000\n"
        "s_addc_u32 s55, s93, 0\n"
        "s_add_u32 m0, s59, 0x12000\n"
        "s_nop 0\n"
        "global_load_lds_dwordx4 v3, s[54:55] offset:0\n"
        "s_add_u32 m0, s59, 0x12f80\n"
        "s_nop 0\n"
        "global_load_lds_dwordx4 v3, s[54:55] offset:128\n"
        "s_add_u32 m0, s59, 0x13f00\n"
        "s_nop 0\n"
        "global_load_lds_dwordx4 v3, s[54:55] offset:256\n"
        "s_add_u32 m0, s59, 0x14e80\n"
        "s_nop 0\n"
        "global_load_lds_dwordx4 v3, s[54:55] offset:384\n"
        "v_add_u32 v21, 0x1e000, v5\n"
        "ds_read_b64_tr_b16 v[176:177], v21\n"
        "ds_read_b64_tr_b16 v[178:179], v21 offset:512\n"
        "v_add_u32 v22, 0x1e000, v6\n"
        "ds_read_b64_tr_b16 v[180:181], v22\n"
        "ds_read_b64_tr_b16 v[182:183], v22 offset:512\n"
        "v_add_u32 v20, 0x1e000, v7\n"
        "ds_read_b64_tr_b16 v[184:185], v20\n"
        "ds_read_b64_tr_b16 v[186:187], v20 offset:512\n"
        "v_add_u32 v21, 0x1e000, v8\n"
        "ds_read_b64_tr_b16 v[188:189], v21\n"
        "ds_read_b64_tr_b16 v[190:191], v21 offset:512\n"
        "s_waitcnt vmcnt(12)\n"
        "s_waitcnt lgkmcnt(0)\n"
        "s_barrier\n"
        "v_add_u32 v22, 0x22000, v5\n"
        "ds_read_b64_tr_b16 v[192:193], v22\n"
        "ds_read_b64_tr_b16 v[194:195], v22 offset:512\n"
        "v_add_u32 v20, 0x22000, v6\n"
        "ds_read_b64_tr_b16 v[196:197], v20\n"
        "ds_read_b64_tr_b16 v[198:199], v20 offset:512\n"
        "v_add_u32 v21, 0x22000, v7\n"
        "ds_read_b64_tr_b16 v[200:201], v21\n"
        "ds_read_b64_tr_b16 v[202:203], v21 offset:512\n"
        "v_add_u32 v22, 0x22000, v8\n"
        "ds_read_b64_tr_b16 v[204:205], v22\n"
        "ds_read_b64_tr_b16 v[206:207], v22 offset:512\n"
        "s_waitcnt vmcnt(8)\n"
        "s_waitcnt lgkmcnt(0)\n"
        "s_barrier\n"
        "v_add_u32 v20, 0xa000, v5\n"
        "ds_read_b64_tr_b16 v[208:209], v20\n"
        "ds_read_b64_tr_b16 v[210:211], v20 offset:512\n"
        "v_add_u32 v21, 0xa000, v6\n"
        "ds_read_b64_tr_b16 v[212:213], v21\n"
        "ds_read_b64_tr_b16 v[214:215], v21 offset:512\n"
        "v_add_u32 v22, 0xa000, v7\n"
        "ds_read_b64_tr_b16 v[216:217], v22\n"
        "ds_read_b64_tr_b16 v[218:219], v22 offset:512\n"
        "v_add_u32 v20, 0xa000, v8\n"
        "ds_read_b64_tr_b16 v[220:221], v20\n"
        "ds_read_b64_tr_b16 v[222:223], v20 offset:512\n"
        "s_waitcnt vmcnt(4)\n"
        "s_waitcnt lgkmcnt(0)\n"
        "s_barrier\n"
        "v_add_u32 v21, 0xe000, v5\n"
        "ds_read_b64_tr_b16 v[224:225], v21\n"
        "ds_read_b64_tr_b16 v[226:227], v21 offset:512\n"
        "v_add_u32 v22, 0xe000, v6\n"
        "ds_read_b64_tr_b16 v[228:229], v22\n"
        "ds_read_b64_tr_b16 v[230:231], v22 offset:512\n"
        "v_add_u32 v20, 0xe000, v7\n"
        "ds_read_b64_tr_b16 v[232:233], v20\n"
        "ds_read_b64_tr_b16 v[234:235], v20 offset:512\n"
        "v_add_u32 v21, 0xe000, v8\n"
        "ds_read_b64_tr_b16 v[236:237], v21\n"
        "ds_read_b64_tr_b16 v[238:239], v21 offset:512\n"
        "s_waitcnt vmcnt(0)\n"
        "s_waitcnt lgkmcnt(0)\n"
        "s_barrier\n"
        "v_add_u32 v22, 0x12000, v5\n"
        "ds_read_b64_tr_b16 v[240:241], v22\n"
        "ds_read_b64_tr_b16 v[242:243], v22 offset:512\n"
        "v_add_u32 v20, 0x12000, v6\n"
        "ds_read_b64_tr_b16 v[244:245], v20\n"
        "ds_read_b64_tr_b16 v[246:247], v20 offset:512\n"
        "v_add_u32 v21, 0x12000, v7\n"
        "ds_read_b64_tr_b16 v[248:249], v21\n"
        "ds_read_b64_tr_b16 v[250:251], v21 offset:512\n"
        "v_add_u32 v22, 0x12000, v8\n"
        "ds_read_b64_tr_b16 v[252:253], v22\n"
        "ds_read_b64_tr_b16 v[254:255], v22 offset:512\n"
        "s_waitcnt lgkmcnt(0)\n"
        "s_barrier\n"
        "s_waitcnt lgkmcnt(0)\n"
        "s_lshl_b32 s47, s43, 11\n"
        "v_add_u32 v3, s47, v1\n"
        "s_lshl_b32 s47, s43, 12\n"
        "v_add_u32 v4, s47, v1\n"
        "s_mul_i32 s69, s88, 0x3\n"
        "s_add_u32 s69, s69, s43\n"
        "v_bfe_u32 v10, v0, 2, 2\n"
        "v_and_b32 v11, 3, v0\n"
        "v_lshl_add_u32 v10, v10, 3, v11\n"
        "v_mul_u32_u24 v10, 1280, v10\n"
        "v_lshrrev_b32 v11, 4, v0\n"
        "v_lshl_add_u32 v10, v11, 4, v10\n"
        "s_mul_i32 s47, s58, 0xa000\n"
        "s_lshl_b32 s68, s69, 7\n"
        "s_add_u32 s47, s47, s68\n"
        "v_add_u32 v5, s47, v10\n"
        "v_add_u32 v6, 5120, v5\n"
        "s_mul_i32 s68, s58, 0xa000\n"
        "s_lshl_b32 s69, s69, 12\n"
        "s_add_u32 s47, s68, 0xb00000\n"
        "s_add_u32 s47, s47, s69\n"
        "s_add_u32 s52, s90, s47\n"
        "s_addc_u32 s53, s91, 0\n"
        "s_mul_i32 s68, s58, 0xc000\n"
        "s_add_u32 s47, s68, 0x1400000\n"
        "s_add_u32 s48, s90, s47\n"
        "s_addc_u32 s49, s91, 0\n"
        "s_add_u32 s50, s48, 24576\n"
        "s_addc_u32 s51, s49, 0\n"
        "v_min_u32 v9, 39, v0\n"
        "s_add_u32 s47, s44, 0x1ac800\n"
        "v_lshl_add_u32 v9, v9, 7, s47\n"
        "s_lshr_b32 s34, s58, 1\n"
        "s_branch Lvo_p0_12\n"
        "Lvo_pl_11:\n"
        "s_sleep 8\n"
        "Lvo_p0_12:\n"
        "global_load_dword v8, v9, s[90:91] sc1\n"
        "s_sub_u32 s46, s46, 1\n"
        "s_cbranch_scc1 Lvo_exit\n"
        "s_waitcnt vmcnt(0)\n"
        "v_cmp_ge_u32 vcc, s34, v8\n"
        "s_cbranch_vccnz Lvo_pl_11\n"
        "global_load_dwordx4 v[104:107], v1, s[52:53] offset:0 sc1\n"
        "global_load_dwordx4 v[108:111], v1, s[52:53] offset:1024 sc1\n"
        "global_load_dwordx4 v[112:115], v1, s[52:53] offset:2048 sc1\n"
        "global_load_dwordx4 v[116:119], v1, s[52:53] offset:3072 sc1\n"
        "s_mul_i32 s47, s58, 0x600\n"
        "s_add_u32 s47, s47, 0xad0000\n"
        "v_min_u32 v9, 11, v0\n"
        "v_lshl_add_u32 v7, v9, 7, s47\n"
        "s_waitcnt vmcnt(0) lgkmcnt(0)\n"
        "v_lshlrev_b32 v24, 16, v104\n"
        "v_and_b32 v25, 0xffff0000, v104\n"
        "v_lshlrev_b32 v26, 16, v105\n"
        "v_and_b32 v27, 0xffff0000, v105\n"
        "v_lshlrev_b32 v28, 16, v106\n"
        "v_and_b32 v29, 0xffff0000, v106\n"
        "v_lshlrev_b32 v30, 16, v107\n"
        "v_and_b32 v31, 0xffff0000, v107\n"
        "v_lshlrev_b32 v32, 16, v108\n"
        "v_and_b32 v33, 0xffff0000, v108\n"
        "v_lshlrev_b32 v34, 16, v109\n"
        "v_and_b32 v35, 0xffff0000, v109\n"
        "v_lshlrev_b32 v36, 16, v110\n"
        "v_and_b32 v37, 0xffff0000, v110\n"
        "v_lshlrev_b32 v38, 16, v111\n"
        "v_and_b32 v39, 0xffff0000, v111\n"
        "v_lshlrev_b32 v40, 16, v112\n"
        "v_and_b32 v41, 0xffff0000, v112\n"
        "v_lshlrev_b32 v42, 16, v113\n"
        "v_and_b32 v43, 0xffff0000, v113\n"
        "v_lshlrev_b32 v44, 16, v114\n"
        "v_and_b32 v45, 0xffff0000, v114\n"
        "v_lshlrev_b32 v46, 16, v115\n"
        "v_and_b32 v47, 0xffff0000, v115\n"
        "v_lshlrev_b32 v48, 16, v116\n"
        "v_and_b32 v49, 0xffff0000, v116\n"
        "v_lshlrev_b32 v50, 16, v117\n"
        "v_and_b32 v51, 0xffff0000, v117\n"
        "v_lshlrev_b32 v52, 16, v118\n"
        "v_and_b32 v53, 0xffff0000, v118\n"
        "v_lshlrev_b32 v54, 16, v119\n"
        "v_and_b32 v55, 0xffff0000, v119\n"
        "s_cmp_lg_u32 s43, 0\n"
        "s_cbranch_scc1 Lvo_fs_13\n"
        "s_branch Lvo_p0_15\n"
        "Lvo_pl_14:\n"
        "s_sleep 1\n"
        "Lvo_p0_15:\n"
        "global_load_dword v8, v7, s[90:91] sc1\n"
        "s_sub_u32 s46, s46, 1\n"
        "s_cbranch_scc1 Lvo_exit\n"
        "s_waitcnt vmcnt(0)\n"
        "v_cmp_eq_u32 vcc, 0, v8\n"
        "s_cbranch_vccnz Lvo_pl_14\n"
        "Lvo_fs_13:\n"
        "s_barrier\n"
        "global_load_dwordx4 v[56:59], v3, s[48:49] offset:0 sc1\n"
        "global_load_dwordx4 v[64:67], v3, s[48:49] offset:1024 sc1\n"
        "global_load_dwordx4 v[60:63], v3, s[50:51] offset:0 sc1\n"
        "global_load_dwordx4 v[68:71], v3, s[50:51] offset:1024 sc1\n"
        "s_add_u32 s48, s48, 8192\n"
        "s_addc_u32 s49, s49, 0\n"
        "s_add_u32 s50, s50, 8192\n"
        "s_addc_u32 s51, s51, 0\n"
        "global_load_dwordx4 v[72:75], v3, s[48:49] offset:0 sc1\n"
        "global_load_dwordx4 v[80:83], v3, s[48:49] offset:1024 sc1\n"
        "global_load_dwordx4 v[76:79], v3, s[50:51] offset:0 sc1\n"
        "global_load_dwordx4 v[84:87], v3, s[50:51] offset:1024 sc1\n"
        "s_add_u32 s48, s48, 8192\n"
        "s_addc_u32 s49, s49, 0\n"
        "s_add_u32 s50, s50, 8192\n"
        "s_addc_u32 s51, s51, 0\n"
        "global_load_dwordx4 v[88:91], v3, s[48:49] offset:0 sc1\n"
        "global_load_dwordx4 v[96:99], v3, s[48:49] offset:1024 sc1\n"
        "global_load_dwordx4 v[92:95], v3, s[50:51] offset:0 sc1\n"
        "global_load_dwordx4 v[100:103], v3, s[50:51] offset:1024 sc1\n"
        "s_waitcnt vmcnt(8)\n"
        "ds_write_b128 v4, v[56:59] offset:0\n"
        "ds_write_b128 v4, v[60:63] offset:1024\n"
        "ds_write_b128 v4, v[64:67] offset:2048\n"
        "ds_write_b128 v4, v[68:71] offset:3072\n"
        "s_waitcnt lgkmcnt(0)\n"
        "s_barrier\n"
        "ds_read_b128 v[104:107], v1 offset:0\n"
        "ds_read_b128 v[108:111], v1 offset:1024\n"
        "ds_read_b128 v[112:115], v1 offset:2048\n"
        "ds_read_b128 v[116:119], v1 offset:3072\n"
        "s_waitcnt lgkmcnt(2)\n"
        "v_mfma_f32_16x16x32_bf16 v[24:27], a[0:3], v[104:107], v[24:27]\n"
        "ds_read_b128 v[120:123], v1 offset:4096\n"
        "v_mfma_f32_16x16x32_bf16 v[28:31], a[4:7], v[104:107], v[28:31]\n"
        "ds_read_b128 v[124:127], v1 offset:5120\n"
        "v_mfma_f32_16x16x32_bf16 v[32:35], a[0:3], v[108:111], v[32:35]\n"
        "v_mfma_f32_16x16x32_bf16 v[36:39], a[4:7], v[108:111], v[36:39]\n"
        "v_mfma_f32_16x16x32_bf16 v[40:43], a[8:11], v[104:107], v[40:43]\n"
        "v_mfma_f32_16x16x32_bf16 v[44:47], a[12:15], v[104:107], v[44:47]\n"
        "v_mfma_f32_16x16x32_bf16 v[48:51], a[8:11], v[108:111], v[48:51]\n"
        "v_mfma_f32_16x16x32_bf16 v[52:55], a[12:15], v[108:111], v[52:55]\n"
        "s_waitcnt lgkmcnt(2)\n"
        "v_mfma_f32_16x16x32_bf16 v[24:27], a[16:19], v[112:115], v[24:27]\n"
        "ds_read_b128 v[104:107], v1 offset:6144\n"
        "v_mfma_f32_16x16x32_bf16 v[28:31], a[20:23], v[112:115], v[28:31]\n"
        "ds_read_b128 v[108:111], v1 offset:7168\n"
        "v_mfma_f32_16x16x32_bf16 v[32:35], a[16:19], v[116:119], v[32:35]\n"
        "v_mfma_f32_16x16x32_bf16 v[36:39], a[20:23], v[116:119], v[36:39]\n"
        "v_mfma_f32_16x16x32_bf16 v[40:43], a[24:27], v[112:115], v[40:43]\n"
        "v_mfma_f32_16x16x32_bf16 v[44:47], a[28:31], v[112:115], v[44:47]\n"
        "v_mfma_f32_16x16x32_bf16 v[48:51], a[24:27], v[116:119], v[48:51]\n"
        "v_mfma_f32_16x16x32_bf16 v[52:55], a[28:31], v[116:119], v[52:55]\n"
        "s_waitcnt lgkmcnt(2)\n"
        "v_mfma_f32_16x16x32_bf16 v[24:27], a[32:35], v[120:123], v[24:27]\n"
        "ds_read_b128 v[112:115], v1 offset:8192\n"
        "v_mfma_f32_16x16x32_bf16 v[28:31], a[36:39], v[120:123], v[28:31]\n"
        "ds_read_b128 v[116:119], v1 offset:9216\n"
        "v_mfma_f32_16x16x32_bf16 v[32:35], a[32:35], v[124:127], v[32:35]\n"
        "v_mfma_f32_16x16x32_bf16 v[36:39], a[36:39], v[124:127], v[36:39]\n"
        "v_mfma_f32_16x16x32_bf16 v[40:43], a[40:43], v[120:123], v[40:43]\n"
        "v_mfma_f32_16x16x32_bf16 v[44:47], a[44:47], v[120:123], v[44:47]\n"
        "v_mfma_f32_16x16x32_bf16 v[48:51], a[40:43], v[124:127], v[48:51]\n"
        "v_mfma_f32_16x16x32_bf16 v[52:55], a[44:47], v[124:127], v[52:55]\n"
        "s_waitcnt lgkmcnt(2)\n"
        "v_mfma_f32_16x16x32_bf16 v[24:27], a[48:51], v[104:107], v[24:27]\n"
        "ds_read_b128 v[120:123], v1 offset:10240\n"
        "v_mfma_f32_16x16x32_bf16 v[28:31], a[52:55], v[104:107], v[28:31]\n"
        "ds_read_b128 v[124:127], v1 offset:11264\n"
        "v_mfma_f32_16x16x32_bf16 v[32:35], a[48:51], v[108:111], v[32:35]\n"
        "v_mfma_f32_16x16x32_bf16 v[36:39], a[52:55], v[108:111], v[36:39]\n"
        "v_mfma_f32_16x16x32_bf16 v[40:43], a[56:59], v[104:107], v[40:43]\n"
        "v_mfma_f32_16x16x32_bf16 v[44:47], a[60:63], v[104:107], v[44:47]\n"
        "v_mfma_f32_16x16x32_bf16 v[48:51], a[56:59], v[108:111], v[48:51]\n"
        "v_mfma_f32_16x16x32_bf16 v[52:55], a[60:63], v[108:111], v[52:55]\n"
        "s_waitcnt lgkmcnt(2)\n"
        "v_mfma_f32_16x16x32_bf16 v[24:27], a[64:67], v[112:115], v[24:27]\n"
        "ds_read_b128 v[104:107], v1 offset:12288\n"
        "v_mfma_f32_16x16x32_bf16 v[28:31], a[68:71], v[112:115], v[28:31]\n"
        "ds_read_b128 v[108:111], v1 offset:13312\n"
        "v_mfma_f32_16x16x32_bf16 v[32:35], a[64:67], v[116:119], v[32:35]\n"
        "v_mfma_f32_16x16x32_bf16 v[36:39], a[68:71], v[116:119], v[36:39]\n"
        "v_mfma_f32_16x16x32_bf16 v[40:43], a[72:75], v[112:115], v[40:43]\n"
        "v_mfma_f32_16x16x32_bf16 v[44:47], a[76:79], v[112:115], v[44:47]\n"
        "v_mfma_f32_16x16x32_bf16 v[48:51], a[72:75], v[116:119], v[48:51]\n"
        "v_mfma_f32_16x16x32_bf16 v[52:55], a[76:79], v[116:119], v[52:55]\n"
        "s_waitcnt lgkmcnt(2)\n"
        "v_mfma_f32_16x16x32_bf16 v[24:27], a[80:83], v[120:123], v[24:27]\n"
        "ds_read_b128 v[112:115], v1 offset:14336\n"
        "v_mfma_f32_16x16x32_bf16 v[28:31], a[84:87], v[120:123], v[28:31]\n"
        "ds_read_b128 v[116:119], v1 offset:15360\n"
        "v_mfma_f32_16x16x32_bf16 v[32:35], a[80:83], v[124:127], v[32:35]\n"
        "s_waitcnt vmcnt(4)\n"
        "v_mfma_f32_16x16x32_bf16 v[36:39], a[84:87], v[124:127], v[36:39]\n"
        "ds_write_b128 v4, v[72:75] offset:16384\n"
        "v_mfma_f32_16x16x32_bf16 v[40:43], a[88:91], v[120:123], v[40:43]\n"
        "ds_write_b128 v4, v[76:79] offset:17408\n"
        "v_mfma_f32_16x16x32_bf16 v[44:47], a[92:95], v[120:123], v[44:47]\n"
        "ds_write_b128 v4, v[80:83] offset:18432\n"
        "v_mfma_f32_16x16x32_bf16 v[48:51], a[88:91], v[124:127], v[48:51]\n"
        "ds_write_b128 v4, v[84:87] offset:19456\n"
        "v_mfma_f32_16x16x32_bf16 v[52:55], a[92:95], v[124:127], v[52:55]\n"
        "s_waitcnt lgkmcnt(0)\n"
        "s_barrier\n"
        "s_waitcnt lgkmcnt(6)\n"
        "v_mfma_f32_16x16x32_bf16 v[24:27], a[96:99], v[104:107], v[24:27]\n"
        "ds_read_b128 v[120:123], v1 offset:16384\n"
        "v_mfma_f32_16x16x32_bf16 v[28:31], a[100:103], v[104:107], v[28:31]\n"
        "ds_read_b128 v[124:127], v1 offset:17408\n"
        "v_mfma_f32_16x16x32_bf16 v[32:35], a[96:99], v[108:111], v[32:35]\n"
        "v_mfma_f32_16x16x32_bf16 v[36:39], a[100:103], v[108:111], v[36:39]\n"
        "v_mfma_f32_16x16x32_bf16 v[40:43], a[104:107], v[104:107], v[40:43]\n"
        "v_mfma_f32_16x16x32_bf16 v[44:47], a[108:111], v[104:107], v[44:47]\n"
        "v_mfma_f32_16x16x32_bf16 v[48:51], a[104:107], v[108:111], v[48:51]\n"
        "v_mfma_f32_16x16x32_bf16 v[52:55], a[108:111], v[108:111], v[52:55]\n"
        "s_waitcnt lgkmcnt(6)\n"
        "v_mfma_f32_16x16x32_bf16 v[24:27], a[112:115], v[112:115], v[24:27]\n"
        "ds_read_b128 v[104:107], v1 offset:18432\n"
        "v_mfma_f32_16x16x32_bf16 v[28:31], a[116:119], v[112:115], v[28:31]\n"
        "ds_read_b128 v[108:111], v1 offset:19456\n"
        "v_mfma_f32_16x16x32_bf16 v[32:35], a[112:115], v[116:119], v[32:35]\n"
        "v_mfma_f32_16x16x32_bf16 v[36:39], a[116:119], v[116:119], v[36:39]\n"
        "v_mfma_f32_16x16x32_bf16 v[40:43], a[120:123], v[112:115], v[40:43]\n"
        "v_mfma_f32_16x16x32_bf16 v[44:47], a[124:127], v[112:115], v[44:47]\n"
        "v_mfma_f32_16x16x32_bf16 v[48:51], a[120:123], v[116:119], v[48:51]\n"
        "v_mfma_f32_16x16x32_bf16 v[52:55], a[124:127], v[116:119], v[52:55]\n"
        "s_waitcnt lgkmcnt(2)\n"
        "v_mfma_f32_16x16x32_bf16 v[24:27], a[128:131], v[120:123], v[24:27]\n"
        "ds_read_b128 v[112:115], v1 offset:20480\n"
        "v_mfma_f32_16x16x32_bf16 v[28:31], a[132:135], v[120:123], v[28:31]\n"
        "ds_read_b128 v[116:119], v1 offset:21504\n"
        "v_mfma_f32_16x16x32_bf16 v[32:35], a[128:131], v[124:127], v[32:35]\n"
        "v_mfma_f32_16x16x32_bf16 v[36:39], a[132:135], v[124:127], v[36:39]\n"
        "v_mfma_f32_16x16x32_bf16 v[40:43], a[136:139], v[120:123], v[40:43]\n"
        "v_mfma_f32_16x16x32_bf16 v[44:47], a[140:143], v[120:123], v[44:47]\n"
        "v_mfma_f32_16x16x32_bf16 v[48:51], a[136:139], v[124:127], v[48:51]\n"
        "v_mfma_f32_16x16x32_bf16 v[52:55], a[140:143], v[124:127], v[52:55]\n"
        "s_waitcnt lgkmcnt(2)\n"
        "v_mfma_f32_16x16x32_bf16 v[24:27], a[144:147], v[104:107], v[24:27]\n"
        "ds_read_b128 v[120:123], v1 offset:22528\n"
        "v_mfma_f32_16x16x32_bf16 v[28:31], a[148:151], v[104:107], v[28:31]\n"
        "ds_read_b128 v[124:127], v1 offset:23552\n"
        "v_mfma_f32_16x16x32_bf16 v[32:35], a[144:147], v[108:111], v[32:35]\n"
        "v_mfma_f32_16x16x32_bf16 v[36:39], a[148:151], v[108:111], v[36:39]\n"
        "v_mfma_f32_16x16x32_bf16 v[40:43], a[152:155], v[104:107], v[40:43]\n"
        "v_mfma_f32_16x16x32_bf16 v[44:47], a[156:159], v[104:107], v[44:47]\n"
        "v_mfma_f32_16x16x32_bf16 v[48:51], a[152:155], v[108:111], v[48:51]\n"
        "v_mfma_f32_16x16x32_bf16 v[52:55], a[156:159], v[108:111], v[52:55]\n"
        "s_waitcnt lgkmcnt(2)\n"
        "v_mfma_f32_16x16x32_bf16 v[24:27], a[160:163], v[112:115], v[24:27]\n"
        "ds_read_b128 v[104:107], v1 offset:24576\n"
        "v_mfma_f32_16x16x32_bf16 v[28:31], a[164:167], v[112:115], v[28:31]\n"
        "ds_read_b128 v[108:111], v1 offset:25600\n"
        "v_mfma_f32_16x16x32_bf16 v[32:35], a[160:163], v[116:119], v[32:35]\n"
        "v_mfma_f32_16x16x32_bf16 v[36:39], a[164:167], v[116:119], v[36:39]\n"
        "v_mfma_f32_16x16x32_bf16 v[40:43], a[168:171], v[112:115], v[40:43]\n"
        "v_mfma_f32_16x16x32_bf16 v[44:47], a[172:175], v[112:115], v[44:47]\n"
        "v_mfma_f32_16x16x32_bf16 v[48:51], a[168:171], v[116:119], v[48:51]\n"
        "v_mfma_f32_16x16x32_bf16 v[52:55], a[172:175], v[116:119], v[52:55]\n"
        "s_waitcnt lgkmcnt(2)\n"
        "v_mfma_f32_16x16x32_bf16 v[24:27], a[176:179], v[120:123], v[24:27]\n"
        "ds_read_b128 v[112:115], v1 offset:26624\n"
        "v_mfma_f32_16x16x32_bf16 v[28:31], a[180:183], v[120:123], v[28:31]\n"
        "ds_read_b128 v[116:119], v1 offset:27648\n"
        "v_mfma_f32_16x16x32_bf16 v[32:35], a[176:179], v[124:127], v[32:35]\n"
        "v_mfma_f32_16x16x32_bf16 v[36:39], a[180:183], v[124:127], v[36:39]\n"
        "v_mfma_f32_16x16x32_bf16 v[40:43], a[184:187], v[120:123], v[40:43]\n"
        "v_mfma_f32_16x16x32_bf16 v[44:47], a[188:191], v[120:123], v[44:47]\n"
        "v_mfma_f32_16x16x32_bf16 v[48:51], a[184:187], v[124:127], v[48:51]\n"
        "v_mfma_f32_16x16x32_bf16 v[52:55], a[188:191], v[124:127], v[52:55]\n"
        "s_waitcnt lgkmcnt(2)\n"
        "v_mfma_f32_16x16x32_bf16 v[24:27], a[192:195], v[104:107], v[24:27]\n"
        "ds_read_b128 v[120:123], v1 offset:28672\n"
        "v_mfma_f32_16x16x32_bf16 v[28:31], a[196:199], v[104:107], v[28:31]\n"
        "ds_read_b128 v[124:127], v1 offset:29696\n"
        "v_mfma_f32_16x16x32_bf16 v[32:35], a[192:195], v[108:111], v[32:35]\n"
        "v_mfma_f32_16x16x32_bf16 v[36:39], a[196:199], v[108:111], v[36:39]\n"
        "v_mfma_f32_16x16x32_bf16 v[40:43], a[200:203], v[104:107], v[40:43]\n"
        "v_mfma_f32_16x16x32_bf16 v[44:47], a[204:207], v[104:107], v[44:47]\n"
        "v_mfma_f32_16x16x32_bf16 v[48:51], a[200:203], v[108:111], v[48:51]\n"
        "v_mfma_f32_16x16x32_bf16 v[52:55], a[204:207], v[108:111], v[52:55]\n"
        "s_waitcnt lgkmcnt(2)\n"
        "v_mfma_f32_16x16x32_bf16 v[24:27], a[208:211], v[112:115], v[24:27]\n"
        "ds_read_b128 v[104:107], v1 offset:30720\n"
        "v_mfma_f32_16x16x32_bf16 v[28:31], a[212:215], v[112:115], v[28:31]\n"
        "ds_read_b128 v[108:111], v1 offset:31744\n"
        "v_mfma_f32_16x16x32_bf16 v[32:35], a[208:211], v[116:119], v[32:35]\n"
        "s_waitcnt vmcnt(0)\n"
        "v_mfma_f32_16x16x32_bf16 v[36:39], a[212:215], v[116:119], v[36:39]\n"
        "ds_write_b128 v4, v[88:91] offset:32768\n"
        "v_mfma_f32_16x16x32_bf16 v[40:43], a[216:219], v[112:115], v[40:43]\n"
        "ds_write_b128 v4, v[92:95] offset:33792\n"
        "v_mfma_f32_16x16x32_bf16 v[44:47], a[220:223], v[112:115], v[44:47]\n"
        "ds_write_b128 v4, v[96:99] offset:34816\n"
        "v_mfma_f32_16x16x32_bf16 v[48:51], a[216:219], v[116:119], v[48:51]\n"
        "ds_write_b128 v4, v[100:103] offset:35840\n"
        "v_mfma_f32_16x16x32_bf16 v[52:55], a[220:223], v[116:119], v[52:55]\n"
        "s_waitcnt lgkmcnt(0)\n"
        "s_barrier\n"
        "s_waitcnt lgkmcnt(6)\n"
        "v_mfma_f32_16x16x32_bf16 v[24:27], a[224:227], v[120:123], v[24:27]\n"
        "ds_read_b128 v[112:115], v1 offset:32768\n"
        "v_mfma_f32_16x16x32_bf16 v[28:31], a[228:231], v[120:123], v[28:31]\n"
        "ds_read_b128 v[116:119], v1 offset:33792\n"
        "v_mfma_f32_16x16x32_bf16 v[32:35], a[224:227], v[124:127], v[32:35]\n"
        "v_mfma_f32_16x16x32_bf16 v[36:39], a[228:231], v[124:127], v[36:39]\n"
        "v_mfma_f32_16x16x32_bf16 v[40:43], a[232:235], v[120:123], v[40:43]\n"
        "v_mfma_f32_16x16x32_bf16 v[44:47], a[236:239], v[120:123], v[44:47]\n"
        "v_mfma_f32_16x16x32_bf16 v[48:51], a[232:235], v[124:127], v[48:51]\n"
        "v_mfma_f32_16x16x32_bf16 v[52:55], a[236:239], v[124:127], v[52:55]\n"
        "s_waitcnt lgkmcnt(6)\n"
        "v_mfma_f32_16x16x32_bf16 v[24:27], a[240:243], v[104:107], v[24:27]\n"
        "ds_read_b128 v[120:123], v1 offset:34816\n"
        "v_mfma_f32_16x16x32_bf16 v[28:31], a[244:247], v[104:107], v[28:31]\n"
        "ds_read_b128 v[124:127], v1 offset:35840\n"
        "v_mfma_f32_16x16x32_bf16 v[32:35], a[240:243], v[108:111], v[32:35]\n"
        "v_mfma_f32_16x16x32_bf16 v[36:39], a[244:247], v[108:111], v[36:39]\n"
        "v_mfma_f32_16x16x32_bf16 v[40:43], a[248:251], v[104:107], v[40:43]\n"
        "v_mfma_f32_16x16x32_bf16 v[44:47], a[252:255], v[104:107], v[44:47]\n"
        "v_mfma_f32_16x16x32_bf16 v[48:51], a[248:251], v[108:111], v[48:51]\n"
        "v_mfma_f32_16x16x32_bf16 v[52:55], a[252:255], v[108:111], v[52:55]\n"
        "s_waitcnt lgkmcnt(2)\n"
        "v_mfma_f32_16x16x32_bf16 v[24:27], v[128:131], v[112:115], v[24:27]\n"
        "ds_read_b128 v[104:107], v1 offset:36864\n"
        "v_mfma_f32_16x16x32_bf16 v[28:31], v[132:135], v[112:115], v[28:31]\n"
        "ds_read_b128 v[108:111], v1 offset:37888\n"
        "v_mfma_f32_16x16x32_bf16 v[32:35], v[128:131], v[116:119], v[32:35]\n"
        "v_mfma_f32_16x16x32_bf16 v[36:39], v[132:135], v[116:119], v[36:39]\n"
        "v_mfma_f32_16x16x32_bf16 v[40:43], v[136:139], v[112:115], v[40:43]\n"
        "v_mfma_f32_16x16x32_bf16 v[44:47], v[140:143], v[112:115], v[44:47]\n"
        "v_mfma_f32_16x16x32_bf16 v[48:51], v[136:139], v[116:119], v[48:51]\n"
        "v_mfma_f32_16x16x32_bf16 v[52:55], v[140:143], v[116:119], v[52:55]\n"
        "s_waitcnt lgkmcnt(2)\n"
        "v_mfma_f32_16x16x32_bf16 v[24:27], v[144:147], v[120:123], v[24:27]\n"
        "ds_read_b128 v[112:115], v1 offset:38912\n"
        "v_mfma_f32_16x16x32_bf16 v[28:31], v[148:151], v[120:123], v[28:31]\n"
        "ds_read_b128 v[116:119], v1 offset:39936\n"
        "v_mfma_f32_16x16x32_bf16 v[32:35], v[144:147], v[124:127], v[32:35]\n"
        "v_mfma_f32_16x16x32_bf16 v[36:39], v[148:151], v[124:127], v[36:39]\n"
        "v_mfma_f32_16x16x32_bf16 v[40:43], v[152:155], v[120:123], v[40:43]\n"
        "v_mfma_f32_16x16x32_bf16 v[44:47], v[156:159], v[120:123], v[44:47]\n"
        "v_mfma_f32_16x16x32_bf16 v[48:51], v[152:155], v[124:127], v[48:51]\n"
        "v_mfma_f32_16x16x32_bf16 v[52:55], v[156:159], v[124:127], v[52:55]\n"
        "s_waitcnt lgkmcnt(2)\n"
        "v_mfma_f32_16x16x32_bf16 v[24:27], v[160:163], v[104:107], v[24:27]\n"
        "ds_read_b128 v[120:123], v1 offset:40960\n"
        "v_mfma_f32_16x16x32_bf16 v[28:31], v[164:167], v[104:107], v[28:31]\n"
        "ds_read_b128 v[124:127], v1 offset:41984\n"
        "v_mfma_f32_16x16x32_bf16 v[32:35], v[160:163], v[108:111], v[32:35]\n"
        "v_mfma_f32_16x16x32_bf16 v[36:39], v[164:167], v[108:111], v[36:39]\n"
        "v_mfma_f32_16x16x32_bf16 v[40:43], v[168:171], v[104:107], v[40:43]\n"
        "v_mfma_f32_16x16x32_bf16 v[44:47], v[172:175], v[104:107], v[44:47]\n"
        "v_mfma_f32_16x16x32_bf16 v[48:51], v[168:171], v[108:111], v[48:51]\n"
        "v_mfma_f32_16x16x32_bf16 v[52:55], v[172:175], v[108:111], v[52:55]\n"
        "s_waitcnt lgkmcnt(2)\n"
        "v_mfma_f32_16x16x32_bf16 v[24:27], v[176:179], v[112:115], v[24:27]\n"
        "ds_read_b128 v[104:107], v1 offset:43008\n"
        "v_mfma_f32_16x16x32_bf16 v[28:31], v[180:183], v[112:115], v[28:31]\n"
        "ds_read_b128 v[108:111], v1 offset:44032\n"
        "v_mfma_f32_16x16x32_bf16 v[32:35], v[176:179], v[116:119], v[32:35]\n"
        "v_mfma_f32_16x16x32_bf16 v[36:39], v[180:183], v[116:119], v[36:39]\n"
        "v_mfma_f32_16x16x32_bf16 v[40:43], v[184:187], v[112:115], v[40:43]\n"
        "v_mfma_f32_16x16x32_bf16 v[44:47], v[188:191], v[112:115], v[44:47]\n"
        "v_mfma_f32_16x16x32_bf16 v[48:51], v[184:187], v[116:119], v[48:51]\n"
        "v_mfma_f32_16x16x32_bf16 v[52:55], v[188:191], v[116:119], v[52:55]\n"
        "s_waitcnt lgkmcnt(2)\n"
        "v_mfma_f32_16x16x32_bf16 v[24:27], v[192:195], v[120:123], v[24:27]\n"
        "ds_read_b128 v[112:115], v1 offset:45056\n"
        "v_mfma_f32_16x16x32_bf16 v[28:31], v[196:199], v[120:123], v[28:31]\n"
        "ds_read_b128 v[116:119], v1 offset:46080\n"
        "v_mfma_f32_16x16x32_bf16 v[32:35], v[192:195], v[124:127], v[32:35]\n"
        "v_mfma_f32_16x16x32_bf16 v[36:39], v[196:199], v[124:127], v[36:39]\n"
        "v_mfma_f32_16x16x32_bf16 v[40:43], v[200:203], v[120:123], v[40:43]\n"
        "v_mfma_f32_16x16x32_bf16 v[44:47], v[204:207], v[120:123], v[44:47]\n"
        "v_mfma_f32_16x16x32_bf16 v[48:51], v[200:203], v[124:127], v[48:51]\n"
        "v_mfma_f32_16x16x32_bf16 v[52:55], v[204:207], v[124:127], v[52:55]\n"
        "s_waitcnt lgkmcnt(2)\n"
        "v_mfma_f32_16x16x32_bf16 v[24:27], v[208:211], v[104:107], v[24:27]\n"
        "ds_read_b128 v[120:123], v1 offset:47104\n"
        "v_mfma_f32_16x16x32_bf16 v[28:31], v[212:215], v[104:107], v[28:31]\n"
        "ds_read_b128 v[124:127], v1 offset:48128\n"
        "v_mfma_f32_16x16x32_bf16 v[32:35], v[208:211], v[108:111], v[32:35]\n"
        "v_mfma_f32_16x16x32_bf16 v[36:39], v[212:215], v[108:111], v[36:39]\n"
        "v_mfma_f32_16x16x32_bf16 v[40:43], v[216:219], v[104:107], v[40:43]\n"
        "v_mfma_f32_16x16x32_bf16 v[44:47], v[220:223], v[104:107], v[44:47]\n"
        "v_mfma_f32_16x16x32_bf16 v[48:51], v[216:219], v[108:111], v[48:51]\n"
        "v_mfma_f32_16x16x32_bf16 v[52:55], v[220:223], v[108:111], v[52:55]\n"
        "s_waitcnt lgkmcnt(2)\n"
        "v_mfma_f32_16x16x32_bf16 v[24:27], v[224:227], v[112:115], v[24:27]\n"
        "v_mfma_f32_16x16x32_bf16 v[28:31], v[228:231], v[112:115], v[28:31]\n"
        "v_mfma_f32_16x16x32_bf16 v[32:35], v[224:227], v[116:119], v[32:35]\n"
        "v_mfma_f32_16x16x32_bf16 v[36:39], v[228:231], v[116:119], v[36:39]\n"
        "v_mfma_f32_16x16x32_bf16 v[40:43], v[232:235], v[112:115], v[40:43]\n"
        "v_mfma_f32_16x16x32_bf16 v[44:47], v[236:239], v[112:115], v[44:47]\n"
        "v_mfma_f32_16x16x32_bf16 v[48:51], v[232:235], v[116:119], v[48:51]\n"
        "v_mfma_f32_16x16x32_bf16 v[52:55], v[236:239], v[116:119], v[52:55]\n"
        "s_waitcnt lgkmcnt(0)\n"
        "v_mfma_f32_16x16x32_bf16 v[24:27], v[240:243], v[120:123], v[24:27]\n"
        "v_mfma_f32_16x16x32_bf16 v[28:31], v[244:247], v[120:123], v[28:31]\n"
        "v_mfma_f32_16x16x32_bf16 v[32:35], v[240:243], v[124:127], v[32:35]\n"
        "v_mfma_f32_16x16x32_bf16 v[36:39], v[244:247], v[124:127], v[36:39]\n"
        "v_mfma_f32_16x16x32_bf16 v[40:43], v[248:251], v[120:123], v[40:43]\n"
        "v_mfma_f32_16x16x32_bf16 v[44:47], v[252:255], v[120:123], v[44:47]\n"
        "v_mfma_f32_16x16x32_bf16 v[48:51], v[248:251], v[124:127], v[48:51]\n"
        "v_mfma_f32_16x16x32_bf16 v[52:55], v[252:255], v[124:127], v[52:55]\n"
        "s_nop 0\n"
        "v_cvt_pk_bf16_f32 v56, v24, v25\n"
        "v_cvt_pk_bf16_f32 v57, v26, v27\n"
        "v_cvt_pk_bf16_f32 v58, v28, v29\n"
        "v_cvt_pk_bf16_f32 v59, v30, v31\n"
        "global_store_dwordx4 v5, v[56:59], s[94:95] offset:0\n"
        "v_cvt_pk_bf16_f32 v60, v32, v33\n"
        "v_cvt_pk_bf16_f32 v61, v34, v35\n"
        "v_cvt_pk_bf16_f32 v62, v36, v37\n"
        "v_cvt_pk_bf16_f32 v63, v38, v39\n"
        "global_store_dwordx4 v6, v[60:63], s[94:95] offset:0\n"
        "v_cvt_pk_bf16_f32 v64, v40, v41\n"
        "v_cvt_pk_bf16_f32 v65, v42, v43\n"
        "v_cvt_pk_bf16_f32 v66, v44, v45\n"
        "v_cvt_pk_bf16_f32 v67, v46, v47\n"
        "global_store_dwordx4 v5, v[64:67], s[94:95] offset:64\n"
        "v_cvt_pk_bf16_f32 v68, v48, v49\n"
        "v_cvt_pk_bf16_f32 v69, v50, v51\n"
        "v_cvt_pk_bf16_f32 v70, v52, v53\n"
        "v_cvt_pk_bf16_f32 v71, v54, v55\n"
        "global_store_dwordx4 v6, v[68:71], s[94:95] offset:64\n"
        "Lvo_exit:\n"
        "s_waitcnt vmcnt(0) lgkmcnt(0)\n"
        "s_endpgm\n"
        "Lvo_rs_1:\n"
        "s_sleep 4\n"
        "global_load_dword v4, v15, s[90:91] sc1\n"
        "s_sub_u32 s46, s46, 1\n"
        "s_cbranch_scc1 Lvo_exit\n"
        "s_waitcnt vmcnt(0)\n"
        "v_readfirstlane_b32 s47, v4\n"
        "s_nop 3\n"
        "s_and_b32 s68, s47, -2\n"
        "s_cmp_lg_u32 s68, s88\n"
        "s_cbranch_scc1 Lvo_rs_1\n"
        "s_branch Lvo_rb_2\n"
        "Lvo_first:\n"
        "s_barrier\n"
        "s_lshl_b32 s47, s41, 2\n"
        "s_add_u32 s47, s47, s42\n"
        "s_lshl_b32 s47, s47, 7\n"
        "s_add_u32 s47, s47, 0xaa8400\n"
        "v_mov_b32 v32, s47\n"
        "v_mov_b32 v33, 1\n"
        "s_and_b32 exec_lo, s36, 1\n"
        "s_mov_b32 exec_hi, 0\n"
        "global_atomic_add v32, v33, s[90:91] sc1\n"
        "s_mov_b64 exec, -1\n"
        "s_lshr_b32 s58, s45, 1\n"
        "s_add_u32 s68, s45, 2\n"
        "s_min_u32 s68, s68, 63\n"
        "s_mul_i32 s68, s68, 0xa000\n"
        "s_add_u32 s47, s68, 0x200000\n"
        "s_add_u32 s48, s90, s47\n"
        "s_addc_u32 s49, s91, 0\n"
        "s_add_u32 s47, s96, 0\n"
        "s_add_u32 s54, s48, s47\n"
        "s_addc_u32 s55, s49, 0\n"
        "s_add_u32 m0, s47, 0\n"
        "s_nop 0\n"
        "global_load_lds_dwordx4 v1, s[54:55] offset:0 sc1\n"
        "global_load_lds_dwordx4 v1, s[54:55] offset:1024 sc1\n"
        "global_load_lds_dwordx4 v1, s[54:55] offset:2048 sc1\n"
        "global_load_lds_dwordx4 v1, s[54:55] offset:3072 sc1\n"
        "s_add_u32 s47, s96, 16384\n"
        "s_add_u32 s54, s48, s47\n"
        "s_addc_u32 s55, s49, 0\n"
        "s_add_u32 m0, s47, 0\n"
        "s_nop 0\n"
        "global_load_lds_dwordx4 v1, s[54:55] offset:0 sc1\n"
        "global_load_lds_dwordx4 v1, s[54:55] offset:1024 sc1\n"
        "global_load_lds_dwordx4 v1, s[54:55] offset:2048 sc1\n"
        "global_load_lds_dwordx4 v1, s[54:55] offset:3072 sc1\n"
        "s_and_b32 s88, s58, 1\n"
        "s_lshl_b32 s68, s88, 1\n"
        "s_add_u32 s68, s68, 1\n"
        "s_lshr_b32 s69, s89, s68\n"
        "s_and_b32 s69, s69, 1\n"
        "s_lshl_b32 s69, s69, 13\n"
        "s_and_b32 s88, s58, 1\n"
        "s_lshl_b32 s88, s88, 22\n"
        "s_add_u32 s47, s88, 0x2c00000\n"
        "s_add_u32 s47, s47, s69\n"
        "s_add_u32 s52, s90, s47\n"
        "s_addc_u32 s53, s91, 0\n"
        "s_add_u32 s68, s68, 1\n"
        "s_lshr_b32 s56, s89, s68\n"
        "s_and_b32 s56, s56, 1\n"
        "s_lshl_b32 s56, s56, 13\n"
        "s_sub_u32 s56, s56, s69\n"
        "s_cmp_eq_u32 s43, 3\n"
        "s_cselect_b32 s56, s56, 0\n"
        "s_add_u32 s56, s56, 0x10000\n"
        "ds_read_b128 v[112:115], v2 offset:0\n"
        "ds_read_b128 v[116:119], v2 offset:24576\n"
        "ds_read_b128 v[120:123], v2 offset:1024\n"
        "ds_read_b128 v[124:127], v2 offset:25600\n"
        "ds_read_b128 v[96:99], v2 offset:2048\n"
        "ds_read_b128 v[100:103], v2 offset:26624\n"
        "ds_read_b128 v[104:107], v2 offset:3072\n"
        "ds_read_b128 v[108:111], v2 offset:27648\n"
        "s_branch Lvo_vtail\n"
        "Lvo_zero:\n"
        "s_barrier\n"
        "global_load_dwordx4 v[24:27], v3, s[56:57] offset:-2048 sc1\n"
        "global_load_dwordx4 v[28:31], v3, s[56:57] offset:2048 sc1\n"
        "global_load_dword v32, v4, s[56:57] sc1\n"
        "global_load_dword v33, v4, s[56:57] offset:256 sc1\n"
        "s_mov_b32 s59, s46\n"
        "s_waitcnt vmcnt(0)\n"
        "ds_write_b128 v5, v[24:27] offset:40960\n"
        "ds_write_b128 v5, v[28:31] offset:45056\n"
        "ds_write2st64_b32 v6, v32, v33 offset0:160 offset1:161\n"
        "s_waitcnt lgkmcnt(0)\n"
        "s_waitcnt vmcnt(0)\n"
        "s_barrier\n"
        "s_add_u32 s47, s58, 1\n"
        "v_mov_b32 v22, s47\n"
        "s_lshl_b32 s47, s40, 7\n"
        "v_mov_b32 v20, s47\n"
        "s_and_b32 exec_lo, s36, 1\n"
        "s_mov_b32 exec_hi, 0\n"
        "global_store_dword v20, v22, s[86:87] sc1\n"
        "s_mov_b64 exec, -1\n"
        "s_memrealtime s[38:39]\n"
        "s_branch Lvo_got\n"
        "Lvo_hs_3:\n"
        "s_sleep 4\n"
        "global_load_dword v21, v15, s[94:95] sc1\n"
        "s_sub_u32 s46, s46, 1\n"
        "s_cbranch_scc1 Lvo_exit\n"
        "s_waitcnt vmcnt(0)\n"
        "v_and_b32 v20, 63, v21\n"
        "v_cmp_ne_u32 vcc, s88, v20\n"
        "s_cbranch_vccnz Lvo_hs_3\n"
        "s_branch Lvo_hb_4\n"
        "Lvo_gm_5:\n"
        "global_load_dwordx4 v[24:27], v3, s[56:57] offset:-2048 sc1\n"
        "global_load_dwordx4 v[28:31], v3, s[56:57] offset:2048 sc1\n"
        "global_load_dword v32, v4, s[56:57] sc1\n"
        "global_load_dword v33, v4, s[56:57] offset:256 sc1\n"
        "s_branch Lvo_gb_10\n"
            :: "s"(ws), "s"(wv), "s"(wo), "s"(x), "s"(HEAD_V(r - 32)), "s"(par), "s"(xq), "s"(wave), "s"(karg) : "v0", "v1", "v2", "v3", "v4", "v5", "v6", "v7", "v8", "v9", "v10", "v11", "v12", "v13", "v14", "v15", "v16", "v17", "v18", "v19", "v20", "v21", "v22", "v23", "v24", "v25", "v26", "v27", "v28", "v29", "v30", "v31", "v32", "v33", "v34", "v35", "v36", "v37", "v38", "v39", "v40", "v41", "v42", "v43", "v44", "v45", "v46", "v47", "v48", "v49", "v50", "v51", "v52", "v53", "v54", "v55", "v56", "v57", "v58", "v59", "v60", "v61", "v62", "v63", "v64", "v65", "v66", "v67", "v68", "v69", "v70", "v71", "v72", "v73", "v74", "v75", "v76", "v77", "v78", "v79", "v80", "v81", "v82", "v83", "v84", "v85", "v86", "v87", "v88", "v89", "v90", "v91", "v92", "v93", "v94", "v95", "v96", "v97", "v98", "v99", "v100", "v101", "v102", "v103", "v104", "v105", "v106", "v107", "v108", "v109", "v110", "v111", "v112", "v113", "v114", "v115", "v116", "v117", "v118", "v119", "v120", "v121", "v122", "v123", "v124", "v125", "v126", "v127", "v128", "v129", "v130", "v131", "v132", "v133", "v134", "v135", "v136", "v137", "v138", "v139", "v140", "v141", "v142", "v143", "v144", "v145", "v146", "v147", "v148", "v149", "v150", "v151", "v152", "v153", "v154", "v155", "v156", "v157", "v158", "v159", "v160", "v161", "v162", "v163", "v164", "v165", "v166", "v167", "v168", "v169", "v170", "v171", "v172", "v173", "v174", "v175", "v176", "v177", "v178", "v179", "v180", "v181", "v182", "v183", "v184", "v185", "v186", "v187", "v188", "v189", "v190", "v191", "v192", "v193", "v194", "v195", "v196", "v197", "v198", "v199", "v200", "v201", "v202", "v203", "v204", "v205", "v206", "v207", "v208", "v209", "v210", "v211", "v212", "v213", "v214", "v215", "v216", "v217", "v218", "v219", "v220", "v221", "v222", "v223", "v224", "v225", "v226", "v227", "v228", "v229", "v230", "v231", "v232", "v233", "v234", "v235", "v236", "v237", "v238", "v239", "v240", "v241", "v242", "v243", "v244", "v245", "v246", "v247", "v248", "v249", "v250", "v251", "v252", "v253", "v254", "v255", "a0", "a1", "a2", "a3", "a4", "a5", "a6", "a7", "a8", "a9", "a10", "a11", "a12", "a13", "a14", "a15", "a16", "a17", "a18", "a19", "a20", "a21", "a22", "a23", "a24", "a25", "a26", "a27", "a28", "a29", "a30", "a31", "a32", "a33", "a34", "a35", "a36", "a37", "a38", "a39", "a40", "a41", "a42", "a43", "a44", "a45", "a46", "a47", "a48", "a49", "a50", "a51", "a52", "a53", "a54", "a55", "a56", "a57", "a58", "a59", "a60", "a61", "a62", "a63", "a64", "a65", "a66", "a67", "a68", "a69", "a70", "a71", "a72", "a73", "a74", "a75", "a76", "a77", "a78", "a79", "a80", "a81", "a82", "a83", "a84", "a85", "a86", "a87", "a88", "a89", "a90", "a91", "a92", "a93", "a94", "a95", "a96", "a97", "a98", "a99", "a100", "a101", "a102", "a103", "a104", "a105", "a106", "a107", "a108", "a109", "a110", "a111", "a112", "a113", "a114", "a115", "a116", "a117", "a118", "a119", "a120", "a121", "a122", "a123", "a124", "a125", "a126", "a127", "a128", "a129", "a130", "a131", "a132", "a133", "a134", "a135", "a136", "a137", "a138", "a139", "a140", "a141", "a142", "a143", "a144", "a145", "a146", "a147", "a148", "a149", "a150", "a151", "a152", "a153", "a154", "a155", "a156", "a157", "a158", "a159", "a160", "a161", "a162", "a163", "a164", "a165", "a166", "a167", "a168", "a169", "a170", "a171", "a172", "a173", "a174", "a175", "a176", "a177", "a178", "a179", "a180", "a181", "a182", "a183", "a184", "a185", "a186", "a187", "a188", "a189", "a190", "a191", "a192", "a193", "a194", "a195", "a196", "a197", "a198", "a199", "a200", "a201", "a202", "a203", "a204", "a205", "a206", "a207", "a208", "a209", "a210", "a211", "a212", "a213", "a214", "a215", "a216", "a217", "a218", "a219", "a220", "a221", "a222", "a223", "a224", "a225", "a226", "a227", "a228", "a229", "a230", "a231", "a232", "a233", "a234", "a235", "a236", "a237", "a238", "a239", "a240", "a241", "a242", "a243", "a244", "a245", "a246", "a247", "a248", "a249", "a250", "a251", "a252", "a253", "a254", "a255", "s34", "s35", "s36", "s37", "s38", "s39", "s40", "s41", "s42", "s43", "s44", "s45", "s46", "s47", "s48", "s49", "s50", "s51", "s52", "s53", "s54", "s55", "s56", "s57", "s58", "s59", "s60", "s61", "s62", "s63", "s64", "s65", "s66", "s67", "s68", "s69", "s70", "s71", "s72", "s73", "s74", "s75", "s76", "s77", "s78", "s79", "s80", "s81", "s82", "s83", "s84", "s85", "s86", "s87", "s88", "s89", "s90", "s91", "s92", "s93", "s94", "s95", "s96", "s97", "s98", "s99", "vcc", "scc", "memory");
        __builtin_amdgcn_endpgm();
    } else if (r < 104) {
        asm volatile(
        "s_mov_b64 s[48:49], %8\n"
        "s_mov_b64 s[90:91], %0\n"
        "s_mov_b64 s[92:93], %1\n"
        "s_mov_b64 s[94:95], %2\n"
        "s_mov_b64 s[96:97], %3\n"
        "s_mov_b32 s40, %4\n"
        "s_mov_b32 s41, %5\n"
        "s_mov_b32 s42, %6\n"
        "s_mov_b32 s43, %7\n"
        "s_mov_b64 s[0:1], s[48:49]\n"
        "s_mov_b64 exec, -1\n"
        "s_mov_b32 s45, s41\n"
        "s_mov_b32 s46, 0x200000\n"
        "v_mbcnt_lo_u32_b32 v0, -1, 0\n"
        "v_mbcnt_hi_u32_b32 v0, -1, v0\n"
        "v_lshlrev_b32 v1, 4, v0\n"
        "v_lshlrev_b32 v24, 3, v0\n"
        "v_add_u32 v24, 0xaa0000, v24\n"
        "global_load_dwordx2 v[26:27], v24, s[90:91] sc1\n"
        "s_waitcnt vmcnt(0)\n"
        "v_and_b32 v26, 1, v26\n"
        "v_and_b32 v27, 1, v27\n"
        "v_cmp_ne_u32 s[50:51], 0, v26\n"
        "v_cmp_ne_u32 s[52:53], 0, v27\n"
        "s_nop 3\n"
        "s_bcnt1_i32_b64 s47, s[50:51]\n"
        "s_bcnt1_i32_b64 s68, s[52:53]\n"
        "s_add_u32 s47, s47, s68\n"
        "s_cmp_gt_u32 s47, 64\n"
        "s_cselect_b32 s47, 1, 0\n"
        "s_xor_b32 s47, s47, s41\n"
        "s_mul_i32 s89, s47, 0x3f\n"
        "s_xor_b32 s89, s89, 52\n"
        "s_lshr_b32 s44, s89, 0\n"
        "s_and_b32 s44, s44, 1\n"
        "s_lshl_b32 s44, s44, 13\n"
        "s_add_u32 s47, s44, 0x185000\n"
        "s_add_u32 s86, s90, s47\n"
        "s_addc_u32 s87, s91, 0\n"
        "v_and_b32 v24, 15, v0\n"
        "v_lshrrev_b32 v25, 4, v0\n"
        "v_lshlrev_b32 v1, 4, v0\n"
        "v_and_b32 v26, 1, v0\n"
        "v_mov_b32 v8, 0x4444\n"
        "v_mov_b32 v27, 0xeeee\n"
        "v_cmp_ne_u32 vcc, 0, v26\n"
        "s_nop 1\n"
        "v_cndmask_b32 v8, v8, v27, vcc\n"
        "v_mov_b32 v20, -1\n"
        "v_mov_b32 v21, -1\n"
        "v_mov_b32 v22, -1\n"
        "v_mov_b32 v23, -1\n"
        "v_bfe_u32 v27, v0, 1, 3\n"
        "v_and_b32 v28, 1, v0\n"
        "s_and_b32 s47, s42, 3\n"
        "s_mul_i32 s47, s47, 0x280\n"
        "s_lshr_b32 s68, s89, 5\n"
        "s_and_b32 s68, s68, 1\n"
        "s_lshl_b32 s68, s68, 13\n"
        "s_add_u32 s69, s47, s68\n"
        "s_lshl_b32 s47, s43, 3\n"
        "v_lshlrev_b32 v29, 6, v27\n"
        "v_lshl_add_u32 v29, v28, 4, v29\n"
        "v_and_b32 v30, 1, v25\n"
        "v_lshl_add_u32 v29, v30, 5, v29\n"
        "v_lshrrev_b32 v30, 1, v25\n"
        "v_add_u32 v30, s47, v30\n"
        "v_mul_u32_u24 v31, 86, v30\n"
        "v_lshrrev_b32 v31, 8, v31\n"
        "v_mul_u32_u24 v31, 8704, v31\n"
        "v_mul_u32_u24 v192, 2560, v30\n"
        "v_add3_u32 v192, v192, v31, v29\n"
        "v_add_u32 v192, s69, v192\n"
        "v_add_u32 v30, 2, v30\n"
        "v_mul_u32_u24 v31, 86, v30\n"
        "v_lshrrev_b32 v31, 8, v31\n"
        "v_mul_u32_u24 v31, 8704, v31\n"
        "v_mul_u32_u24 v193, 2560, v30\n"
        "v_add3_u32 v193, v193, v31, v29\n"
        "v_add_u32 v193, s69, v193\n"
        "v_add_u32 v30, 2, v30\n"
        "v_mul_u32_u24 v31, 86, v30\n"
        "v_lshrrev_b32 v31, 8, v31\n"
        "v_mul_u32_u24 v31, 8704, v31\n"
        "v_mul_u32_u24 v194, 2560, v30\n"
        "v_add3_u32 v194, v194, v31, v29\n"
        "v_add_u32 v194, s69, v194\n"
        "v_add_u32 v30, 2, v30\n"
        "v_mul_u32_u24 v31, 86, v30\n"
        "v_lshrrev_b32 v31, 8, v31\n"
        "v_mul_u32_u24 v31, 8704, v31\n"
        "v_mul_u32_u24 v195, 2560, v30\n"
        "v_add3_u32 v195, v195, v31, v29\n"
        "v_add_u32 v195, s69, v195\n"
        "v_add_u32 v30, 2, v30\n"
        "v_lshlrev_b32 v29, 4, v27\n"
        "v_lshl_add_u32 v29, v28, 3, v29\n"
        "v_add_u32 v29, 0x200, v29\n"
        "v_lshl_add_u32 v30, v25, 1, s47\n"
        "v_mul_u32_u24 v31, 86, v30\n"
        "v_lshrrev_b32 v31, 8, v31\n"
        "v_mul_u32_u24 v31, 8704, v31\n"
        "v_mul_u32_u24 v196, 2560, v30\n"
        "v_add3_u32 v196, v196, v31, v29\n"
        "v_add_u32 v196, s69, v196\n"
        "v_add_u32 v30, 1, v30\n"
        "v_mul_u32_u24 v31, 86, v30\n"
        "v_lshrrev_b32 v31, 8, v31\n"
        "v_mul_u32_u24 v31, 8704, v31\n"
        "v_mul_u32_u24 v197, 2560, v30\n"
        "v_add3_u32 v197, v197, v31, v29\n"
        "v_add_u32 v197, s69, v197\n"
        "v_add_u32 v30, 1, v30\n"
        "s_lshl_b32 s47, s40, 14\n"
        "s_lshl_b32 s68, s43, 11\n"
        "s_add_u32 s47, s47, s68\n"
        "v_add_u32 v5, s47, v1\n"
        "s_lshl_b32 s47, s40, 16\n"
        "s_lshl_b32 s68, s43, 14\n"
        "s_add_u32 s47, s47, s68\n"
        "s_add_u32 s47, s47, 4096\n"
        "v_add_u32 v6, s47, v1\n"
        "s_lshl_b32 s47, s43, 16\n"
        "v_add_u32 v16, s47, v1\n"
        "v_min_u32 v26, 9, v0\n"
        "s_mul_i32 s47, s43, 0xa\n"
        "v_add_u32 v26, s47, v26\n"
        "v_lshlrev_b32 v26, 8, v26\n"
        "v_and_b32 v27, 0x2000, v26\n"
        "v_add_u32 v26, v26, v27\n"
        "s_lshl_b32 s47, s42, 15\n"
        "v_add_u32 v19, s47, v26\n"
        "v_and_b32 v26, 7, v0\n"
        "s_lshl_b32 s47, s43, 3\n"
        "v_add_u32 v26, s47, v26\n"
        "v_lshlrev_b32 v7, 7, v26\n"
        "v_add_u32 v3, 0x20000, v7\n"
        "v_lshlrev_b32 v26, 7, v25\n"
        "v_lshl_add_u32 v26, v24, 2, v26\n"
        "s_lshl_b32 s47, s43, 9\n"
        "v_add_u32 v9, s47, v26\n"
        "v_lshrrev_b32 v26, 3, v0\n"
        "v_lshlrev_b32 v26, 6, v26\n"
        "v_and_b32 v27, 3, v0\n"
        "v_lshl_add_u32 v26, v27, 4, v26\n"
        "v_bfe_u32 v27, v0, 2, 1\n"
        "v_lshl_add_u32 v10, v27, 3, v26\n"
        "s_lshr_b32 s47, s40, 2\n"
        "s_lshl_b32 s47, s47, 10\n"
        "s_and_b32 s68, s47, 0x2000\n"
        "s_add_u32 s47, s47, s68\n"
        "s_and_b32 s68, s40, 3\n"
        "s_lshl_b32 s68, s68, 8\n"
        "s_add_u32 s47, s47, s68\n"
        "s_lshl_b32 s68, s43, 15\n"
        "s_add_u32 s47, s47, s68\n"
        "v_lshlrev_b32 v26, 2, v0\n"
        "v_add_u32 v11, s47, v26\n"
        "s_lshl_b32 s47, s43, 11\n"
        "s_add_u32 s47, s47, 4096\n"
        "v_add_u32 v12, s47, v1\n"
        "v_lshrrev_b32 v26, 3, v0\n"
        "s_lshl_b32 s47, s43, 3\n"
        "v_add_u32 v26, s47, v26\n"
        "v_and_b32 v27, 7, v0\n"
        "v_lshrrev_b32 v28, 2, v26\n"
        "v_lshlrev_b32 v28, 8, v28\n"
        "v_lshl_add_u32 v28, v27, 5, v28\n"
        "v_and_b32 v29, 3, v26\n"
        "v_lshl_add_u32 v28, v29, 2, v28\n"
        "v_add_u32 v13, 4096, v28\n"
        "v_add_u32 v14, 16, v13\n"
        "v_mul_u32_u24 v28, 1280, v26\n"
        "v_lshl_add_u32 v28, v27, 2, v28\n"
        "s_lshl_b32 s47, s40, 5\n"
        "v_add_u32 v15, s47, v28\n"
        "v_bfe_u32 v28, v26, 2, 1\n"
        "v_lshlrev_b32 v28, 10, v28\n"
        "v_lshrrev_b32 v29, 3, v26\n"
        "v_lshl_add_u32 v28, v29, 6, v28\n"
        "v_and_b32 v29, 3, v26\n"
        "v_lshl_add_u32 v28, v29, 4, v28\n"
        "v_lshrrev_b32 v29, 2, v27\n"
        "v_lshl_add_u32 v28, v29, 8, v28\n"
        "v_and_b32 v29, 3, v27\n"
        "v_lshl_add_u32 v28, v29, 2, v28\n"
        "s_lshr_b32 s47, s40, 1\n"
        "s_lshl_b32 s47, s47, 11\n"
        "s_and_b32 s68, s40, 1\n"
        "s_lshl_b32 s68, s68, 9\n"
        "s_add_u32 s47, s47, s68\n"
        "v_add_u32 v186, s47, v28\n"
        "s_lshl_b32 s47, s40, 7\n"
        "s_add_u32 s47, s47, s44\n"
        "s_add_u32 s47, s47, 0x1ac800\n"
        "v_mov_b32 v187, s47\n"
        "s_cmp_eq_u32 s43, 3\n"
        "s_cselect_b32 s62, 1, 0\n"
        "s_mov_b32 s63, 0\n"
        "s_add_u32 s47, s44, 0x18d000\n"
        "s_add_u32 s98, s90, s47\n"
        "s_addc_u32 s99, s91, 0\n"
        "s_mul_i32 s47, s40, 0x1a\n"
        "s_lshr_b32 s47, s47, 8\n"
        "s_mul_i32 s47, s47, 0xa\n"
        "s_sub_u32 s47, s40, s47\n"
        "s_lshr_b32 s47, s47, 1\n"
        "s_mul_i32 s47, s47, 0x100\n"
        "s_add_u32 s36, s47, 0xfffffec0\n"
        "v_and_b32 v26, 7, v0\n"
        "s_lshl_b32 s47, s43, 3\n"
        "v_add_u32 v26, s47, v26\n"
        "v_lshlrev_b32 v26, 7, v26\n"
        "v_bfe_u32 v27, v0, 3, 1\n"
        "v_lshl_add_u32 v185, v27, 15, v26\n"
        "s_getpc_b64 s[70:71]\n"
        "Lcb_pc:\n"
        "s_sub_u32 s70, s70, Lcb_pc-k_rec\n"
        "s_subb_u32 s71, s71, 0\n"
        "s_and_b32 s47, s40, 0xfc\n"
        "s_add_u32 s47, s47, s43\n"
        "s_mul_i32 s47, s47, 0x18\n"
        "v_min_u32 v26, 23, v0\n"
        "v_add_u32 v26, s47, v26\n"
        "v_lshlrev_b32 v26, 7, v26\n"
        "v_min_u32 v198, k_end-k_rec-128, v26\n"
        "v_and_b32 v26, 31, v0\n"
        "v_lshlrev_b32 v191, 7, v26\n"
        "s_add_u32 s47, s44, 0x19d000\n"
        "s_add_u32 s94, s90, s47\n"
        "s_addc_u32 s95, s91, 0\n"
        "s_mov_b32 s60, 0\n"
        "Lcb_w0_1:\n"
        "global_load_dword v199, v198, s[70:71]\n"
        "s_sleep 24\n"
        "global_load_dword v24, v7, s[86:87] sc1\n"
        "s_sub_u32 s46, s46, 1\n"
        "s_cbranch_scc1 Lcb_exit\n"
        "s_waitcnt vmcnt(0)\n"
        "v_cmp_ne_u32 vcc, 0, v24\n"
        "s_cbranch_vccz Lcb_w0_1\n"
        "global_load_dword v188, v191, s[94:95] sc1\n"
        "global_load_dword v190, v7, s[86:87] sc1\n"
        "s_waitcnt vmcnt(0)\n"
        ".p2align 6\n"
        "Lcb_loop:\n"
        "s_lshr_b32 s58, s45, 1\n"
        "v_cmp_ge_u32 vcc, s58, v190\n"
        "s_cbranch_vccz Lcb_uo_2\n"
        "s_branch Lcb_w0_4\n"
        "Lcb_wf_3:\n"
        "s_sleep 3\n"
        "Lcb_w0_4:\n"
        "global_load_dword v24, v7, s[86:87] sc1\n"
        "s_sub_u32 s46, s46, 1\n"
        "s_cbranch_scc1 Lcb_exit\n"
        "s_waitcnt vmcnt(0)\n"
        "v_cmp_ge_u32 vcc, s58, v24\n"
        "s_cbranch_vccnz Lcb_wf_3\n"
        "Lcb_uo_2:\n"
        "s_cmp_eq_u32 s45, 0\n"
        "s_cbranch_scc1 Lcb_n0_5\n"
        "s_add_u32 s88, s45, 1\n"
        "s_lshr_b32 s88, s88, 1\n"
        "s_waitcnt vmcnt(63)\n"
        "s_branch Lcb_pc_7\n"
        "Lcb_pf_6:\n"
        "Lcb_p0_8:\n"
        "global_load_dword v188, v191, s[94:95] sc1\n"
        "s_sub_u32 s46, s46, 1\n"
        "s_cbranch_scc1 Lcb_exit\n"
        "s_waitcnt vmcnt(0)\n"
        "Lcb_pc_7:\n"
        "v_and_b32 v189, 63, v188\n"
        "v_cmp_ne_u32 vcc, s88, v189\n"
        "s_cbranch_vccnz Lcb_pf_6\n"
        "v_readfirstlane_b32 s34, v188\n"
        "s_nop 1\n"
        "v_subrev_u32 v188, s34, v188\n"
        "s_nop 1\n"
        "v_max_i32_dpp v188, v188, v188 row_ror:8 row_mask:0xf bank_mask:0xf\n"
        "s_nop 1\n"
        "v_max_i32_dpp v188, v188, v188 row_ror:4 row_mask:0xf bank_mask:0xf\n"
        "s_nop 1\n"
        "v_max_i32_dpp v188, v188, v188 row_ror:2 row_mask:0xf bank_mask:0xf\n"
        "s_nop 1\n"
        "v_max_i32_dpp v188, v188, v188 row_ror:1 row_mask:0xf bank_mask:0xf\n"
        "s_nop 0\n"
        "v_readfirstlane_b32 s47, v188\n"
        "v_readlane_b32 s68, v188, 16\n"
        "s_max_i32 s47, s47, s68\n"
        "s_add_u32 s34, s34, s47\n"
        "s_add_u32 s92, s34, 6848\n"
        "s_mov_b32 s69, 48\n"
        ".p2align 6\n"
        "Lcb_w_9:\n"
        "s_memrealtime s[54:55]\n"
        "s_waitcnt lgkmcnt(0)\n"
        "s_lshl_b32 s47, s54, 6\n"
        "s_sub_u32 s47, s92, s47\n"
        "s_cmp_lt_i32 s47, 768\n"
        "s_cbranch_scc1 Lcb_f_10\n"
        "s_sleep 1\n"
        "s_sub_u32 s69, s69, 1\n"
        "s_cbranch_scc0 Lcb_w_9\n"
        "s_mov_b32 s47, 0\n"
        "Lcb_f_10:\n"
        "s_max_i32 s47, s47, 0\n"
        "Lcb_c_11:\n"
        "s_sub_u32 s47, s47, 80\n"
        "s_cbranch_scc0 Lcb_c_11\n"
        "Lcb_n0_5:\n"
        "s_and_b32 s88, s58, 1\n"
        "s_lshl_b32 s68, s88, 1\n"
        "s_lshr_b32 s69, s40, 5\n"
        "s_add_u32 s68, s68, s69\n"
        "s_add_u32 s68, s68, 1\n"
        "s_lshr_b32 s69, s89, s68\n"
        "s_and_b32 s69, s69, 1\n"
        "s_lshl_b32 s69, s69, 13\n"
        "s_and_b32 s68, s58, 1\n"
        "s_lshl_b32 s68, s68, 22\n"
        "s_add_u32 s47, s68, 0x2c00000\n"
        "s_add_u32 s47, s47, s69\n"
        "s_add_u32 s52, s90, s47\n"
        "s_addc_u32 s53, s91, 0\n"
        "s_and_b32 s68, s58, 3\n"
        "s_lshl_b32 s68, s68, 18\n"
        "s_add_u32 s47, s68, 0x2b00000\n"
        "s_add_u32 s48, s90, s47\n"
        "s_addc_u32 s49, s91, 0\n"
        "s_and_b32 s68, s58, 1\n"
        "s_mul_i32 s68, s68, 0xa0000\n"
        "s_add_u32 s47, s68, 0x0\n"
        "s_add_u32 s47, s47, s44\n"
        "s_add_u32 s50, s90, s47\n"
        "s_addc_u32 s51, s91, 0\n"
        "s_add_u32 s68, s45, 1\n"
        "s_lshr_b32 s68, s68, 1\n"
        "s_and_b32 s68, s68, 3\n"
        "s_lshl_b32 s68, s68, 17\n"
        "s_xor_b32 s69, s44, 0x2000\n"
        "s_add_u32 s47, s68, 0x180000\n"
        "s_add_u32 s47, s47, s69\n"
        "s_add_u32 s56, s90, s47\n"
        "s_addc_u32 s57, s91, 0\n"
        "global_load_dwordx4 v[32:35], v6, s[52:53] offset:-4096 sc1\n"
        "global_load_dwordx4 v[36:39], v6, s[52:53] offset:-3072 sc1\n"
        "global_load_dwordx4 v[40:43], v6, s[52:53] offset:-2048 sc1\n"
        "global_load_dwordx4 v[44:47], v6, s[52:53] offset:-1024 sc1\n"
        "global_load_dwordx4 v[48:51], v6, s[52:53] offset:0 sc1\n"
        "global_load_dwordx4 v[52:55], v6, s[52:53] offset:1024 sc1\n"
        "global_load_dwordx4 v[56:59], v6, s[52:53] offset:2048 sc1\n"
        "global_load_dwordx4 v[60:63], v6, s[52:53] offset:3072 sc1\n"
        "s_cmp_eq_u32 s45, 0\n"
        "s_cbranch_scc1 Lcb_n1_12\n"
        "s_add_u32 s92, s92, 2560\n"
        "s_mov_b32 s69, 48\n"
        ".p2align 6\n"
        "Lcb_w_13:\n"
        "s_memrealtime s[54:55]\n"
        "s_waitcnt lgkmcnt(0)\n"
        "s_lshl_b32 s47, s54, 6\n"
        "s_sub_u32 s47, s92, s47\n"
        "s_cmp_lt_i32 s47, 768\n"
        "s_cbranch_scc1 Lcb_f_14\n"
        "s_sleep 1\n"
        "s_sub_u32 s69, s69, 1\n"
        "s_cbranch_scc0 Lcb_w_13\n"
        "s_mov_b32 s47, 0\n"
        "Lcb_f_14:\n"
        "s_max_i32 s47, s47, 0\n"
        "Lcb_c_15:\n"
        "s_sub_u32 s47, s47, 80\n"
        "s_cbranch_scc0 Lcb_c_15\n"
        "Lcb_n1_12:\n"
        "s_add_u32 s55, s58, 1\n"
        "s_mov_b32 s35, s55\n"
        "Lcb_pf_16:\n"
        "Lcb_p0_18:\n"
        "global_load_dword v24, v185, s[98:99] sc1\n"
        "s_sub_u32 s46, s46, 1\n"
        "s_cbranch_scc1 Lcb_exit\n"
        "s_waitcnt vmcnt(0)\n"
        "Lcb_pc_17:\n"
        "v_and_b32 v25, 63, v24\n"
        "v_cmp_ne_u32 vcc, s35, v25\n"
        "s_cbranch_vccnz Lcb_pf_16\n"
        "v_readfirstlane_b32 s34, v24\n"
        "s_nop 1\n"
        "v_subrev_u32 v24, s34, v24\n"
        "s_nop 1\n"
        "v_max_i32_dpp v24, v24, v24 quad_perm:[1,0,3,2] row_mask:0xf bank_mask:0xf\n"
        "s_nop 1\n"
        "v_max_i32_dpp v24, v24, v24 quad_perm:[2,3,0,1] row_mask:0xf bank_mask:0xf\n"
        "s_nop 1\n"
        "v_max_i32_dpp v24, v24, v24 row_half_mirror row_mask:0xf bank_mask:0xf\n"
        "s_nop 0\n"
        "v_readfirstlane_b32 s47, v24\n"
        "v_readlane_b32 s68, v24, 8\n"
        "s_add_u32 s34, s34, s60\n"
        "s_mov_b32 s59, s46\n"
        "s_add_u32 s38, s34, s68\n"
        "s_add_u32 s38, s38, s36\n"
        "s_add_u32 s34, s34, s47\n"
        "s_add_u32 s34, s34, 448\n"
        "s_mov_b32 s69, 48\n"
        ".p2align 6\n"
        "Lcb_w_19:\n"
        "s_memrealtime s[54:55]\n"
        "s_waitcnt lgkmcnt(0)\n"
        "s_lshl_b32 s47, s54, 6\n"
        "s_sub_u32 s47, s38, s47\n"
        "s_cmp_lt_i32 s47, 768\n"
        "s_cbranch_scc1 Lcb_f_20\n"
        "s_sleep 1\n"
        "s_sub_u32 s69, s69, 1\n"
        "s_cbranch_scc0 Lcb_w_19\n"
        "s_mov_b32 s47, 0\n"
        "Lcb_f_20:\n"
        "s_max_i32 s47, s47, 0\n"
        "Lcb_c_21:\n"
        "s_sub_u32 s47, s47, 80\n"
        "s_cbranch_scc0 Lcb_c_21\n"
        "global_load_dwordx4 v[64:67], v5, s[50:51] sc1\n"
        "global_load_dwordx4 v[68:71], v5, s[50:51] offset:1024 sc1\n"
        "s_cmp_eq_u32 s45, 63\n"
        "s_cbranch_scc1 Lcb_last\n"
        "s_sub_u32 s47, s34, s38\n"
        "s_cmp_gt_i32 s47, 320\n"
        "s_cbranch_scc1 Lcb_pw_24\n"
        "Lcb_pq_23:\n"
        "global_load_dwordx4 v[72:75], v192, s[48:49] offset:0 sc1\n"
        "global_load_dwordx4 v[76:79], v193, s[48:49] offset:0 sc1\n"
        "global_load_dwordx4 v[80:83], v194, s[48:49] offset:0 sc1\n"
        "global_load_dwordx4 v[84:87], v195, s[48:49] offset:0 sc1\n"
        "global_load_dwordx2 v[88:89], v196, s[48:49] sc1\n"
        "global_load_dwordx2 v[90:91], v197, s[48:49] sc1\n"
        "s_sub_u32 s46, s46, 1\n"
        "s_cbranch_scc1 Lcb_exit\n"
        "v_mov_b32 v92, 0\n"
        "v_mov_b32 v93, 0\n"
        "v_mov_b32 v94, 0\n"
        "v_mov_b32 v95, 0\n"
        "s_waitcnt vmcnt(6)\n"
        "v_max3_u32 v24, v64, v65, v66\n"
        "v_max3_u32 v25, v67, v68, v69\n"
        "v_max3_u32 v24, v24, v70, v71\n"
        "s_waitcnt vmcnt(5)\n"
        "v_smfmac_f32_16x16x64_bf16 v[92:95], v[72:75], v[32:39], v8\n"
        "v_max3_u32 v25, v25, v72, v73\n"
        "v_max3_u32 v24, v24, v74, v75\n"
        "s_waitcnt vmcnt(4)\n"
        "v_smfmac_f32_16x16x64_bf16 v[92:95], v[76:79], v[40:47], v8\n"
        "v_max3_u32 v25, v25, v76, v77\n"
        "v_max3_u32 v24, v24, v78, v79\n"
        "s_waitcnt vmcnt(3)\n"
        "v_smfmac_f32_16x16x64_bf16 v[92:95], v[80:83], v[48:55], v8\n"
        "v_max3_u32 v25, v25, v80, v81\n"
        "v_max3_u32 v24, v24, v82, v83\n"
        "s_waitcnt vmcnt(2)\n"
        "v_smfmac_f32_16x16x64_bf16 v[92:95], v[84:87], v[56:63], v8\n"
        "v_max3_u32 v25, v25, v84, v85\n"
        "v_max3_u32 v24, v24, v86, v87\n"
        "s_waitcnt vmcnt(0)\n"
        "v_smfmac_f32_16x16x64_bf16 v[92:95], v[88:91], v[64:71], v8\n"
        "v_max3_u32 v25, v25, v88, v89\n"
        "v_max3_u32 v24, v24, v90, v91\n"
        "v_max_u32 v24, v24, v25\n"
        "v_cmp_eq_u32 vcc, -1, v24\n"
        "s_cbranch_vccnz Lcb_pp_22\n"
        "s_nop 2\n"
        "v_add_f32 v92, v92, v93\n"
        "v_add_f32 v94, v94, v95\n"
        "ds_write2_b32 v9, v92, v94 offset0:0 offset1:16\n"
        "s_waitcnt lgkmcnt(0)\n"
        "s_barrier\n"
        "ds_read2st64_b64 v[24:27], v10 offset0:0 offset1:1\n"
        "ds_read2st64_b64 v[28:31], v10 offset0:2 offset1:3\n"
        "s_waitcnt lgkmcnt(1)\n"
        "v_pk_add_f32 v[24:25], v[24:25], v[26:27]\n"
        "s_waitcnt lgkmcnt(0)\n"
        "v_pk_add_f32 v[28:29], v[28:29], v[30:31]\n"
        "v_pk_add_f32 v[24:25], v[24:25], v[28:29]\n"
        "v_cvt_pk_bf16_f32 v24, v24, v25\n"
        "global_store_dword v11, v24, s[56:57] sc1\n"
        "s_add_u32 s47, s58, 0\n"
        "v_mov_b32 v25, s47\n"
        "s_mov_b64 exec, s[62:63]\n"
        "global_store_dword v187, v25, s[90:91] sc1\n"
        "s_mov_b64 exec, -1\n"
        "s_add_u32 s47, s60, 1\n"
        "s_and_b32 s60, s60, 0xffffffc0\n"
        "s_sub_u32 s68, s59, s46\n"
        "s_cmp_gt_u32 s68, 1\n"
        "s_cselect_b32 s60, s47, s60\n"
        "s_and_b32 s68, s60, 63\n"
        "s_cmp_eq_u32 s68, 2\n"
        "s_cselect_b32 s68, 126, 0\n"
        "s_add_u32 s60, s60, s68\n"
        "s_min_u32 s60, s60, 1920\n"
        "Lcb_window:\n"
        "s_add_u32 s68, s45, 5\n"
        "s_lshr_b32 s68, s68, 1\n"
        "s_and_b32 s68, s68, 3\n"
        "s_lshl_b32 s68, s68, 17\n"
        "s_xor_b32 s69, s44, 0x2000\n"
        "s_add_u32 s47, s68, 0x180000\n"
        "s_add_u32 s47, s47, s69\n"
        "s_add_u32 s56, s90, s47\n"
        "s_addc_u32 s57, s91, 0\n"
        "s_mul_i32 s68, s45, 0xa000\n"
        "s_add_u32 s54, s96, s68\n"
        "s_addc_u32 s55, s97, 0\n"
        "global_load_dword v184, v15, s[54:55]\n"
        "s_add_u32 s47, s68, 0xb00000\n"
        "s_add_u32 s54, s90, s47\n"
        "s_addc_u32 s55, s91, 0\n"
        "s_lshl_b32 s68, s88, 18\n"
        "s_lshr_b32 s69, s89, 5\n"
        "s_and_b32 s69, s69, 1\n"
        "s_lshl_b32 s69, s69, 13\n"
        "s_add_u32 s47, s68, 0x2a00000\n"
        "s_add_u32 s47, s47, s69\n"
        "s_add_u32 s52, s90, s47\n"
        "s_addc_u32 s53, s91, 0\n"
        "s_mov_b64 s[48:49], s[54:55]\n"
        "s_cmp_lt_u32 s45, 62\n"
        "s_cselect_b32 s47, 3840, 0\n"
        "s_add_u32 s47, s47, 4672\n"
        "s_cmp_eq_u32 s45, 63\n"
        "s_cselect_b32 s47, 0x980, s47\n"
        "s_add_u32 s34, s34, s47\n"
        "s_mov_b32 s69, 48\n"
        ".p2align 6\n"
        "Lcb_w_25:\n"
        "s_memrealtime s[54:55]\n"
        "s_waitcnt lgkmcnt(0)\n"
        "s_lshl_b32 s47, s54, 6\n"
        "s_sub_u32 s47, s34, s47\n"
        "s_cmp_lt_i32 s47, 768\n"
        "s_cbranch_scc1 Lcb_f_26\n"
        "s_sleep 1\n"
        "s_sub_u32 s69, s69, 1\n"
        "s_cbranch_scc0 Lcb_w_25\n"
        "s_mov_b32 s47, 0\n"
        "Lcb_f_26:\n"
        "s_max_i32 s47, s47, 0\n"
        "Lcb_c_27:\n"
        "s_sub_u32 s47, s47, 80\n"
        "s_cbranch_scc0 Lcb_c_27\n"
        "s_branch Lcb_w0_29\n"
        "Lcb_wf_28:\n"
        "s_sleep 3\n"
        "Lcb_w0_29:\n"
        "global_load_dword v24, v3, s[86:87] sc1\n"
        "s_sub_u32 s46, s46, 1\n"
        "s_cbranch_scc1 Lcb_exit\n"
        "s_waitcnt vmcnt(0)\n"
        "v_cmp_ge_u32 vcc, s58, v24\n"
        "s_cbranch_vccnz Lcb_wf_28\n"
        "global_load_dwordx4 v[96:99], v16, s[52:53] offset:0 sc1\n"
        "global_load_dwordx4 v[100:103], v16, s[52:53] offset:1024 sc1\n"
        "global_load_dwordx4 v[104:107], v16, s[52:53] offset:2048 sc1\n"
        "global_load_dwordx4 v[108:111], v16, s[52:53] offset:3072 sc1\n"
        "s_add_u32 s52, s52, 4096\n"
        "s_addc_u32 s53, s53, 0\n"
        "global_load_dwordx4 v[112:115], v16, s[52:53] offset:0 sc1\n"
        "global_load_dwordx4 v[116:119], v16, s[52:53] offset:1024 sc1\n"
        "global_load_dwordx4 v[120:123], v16, s[52:53] offset:2048 sc1\n"
        "global_load_dwordx4 v[124:127], v16, s[52:53] offset:3072 sc1\n"
        "s_add_u32 s52, s52, 12288\n"
        "s_addc_u32 s53, s53, 0\n"
        "global_load_dwordx4 v[128:131], v16, s[52:53] offset:0 sc1\n"
        "global_load_dwordx4 v[132:135], v16, s[52:53] offset:1024 sc1\n"
        "global_load_dwordx4 v[136:139], v16, s[52:53] offset:2048 sc1\n"
        "global_load_dwordx4 v[140:143], v16, s[52:53] offset:3072 sc1\n"
        "s_add_u32 s52, s52, 4096\n"
        "s_addc_u32 s53, s53, 0\n"
        "global_load_dwordx4 v[144:147], v16, s[52:53] offset:0 sc1\n"
        "global_load_dwordx4 v[148:151], v16, s[52:53] offset:1024 sc1\n"
        "global_load_dwordx4 v[152:155], v16, s[52:53] offset:2048 sc1\n"
        "global_load_dwordx4 v[156:159], v16, s[52:53] offset:3072 sc1\n"
        "s_add_u32 s52, s52, 12288\n"
        "s_addc_u32 s53, s53, 0\n"
        "global_load_dwordx4 v[160:163], v16, s[52:53] offset:0 sc1\n"
        "global_load_dwordx4 v[164:167], v16, s[52:53] offset:1024 sc1\n"
        "global_load_dwordx4 v[168:171], v16, s[52:53] offset:2048 sc1\n"
        "global_load_dwordx4 v[172:175], v16, s[52:53] offset:3072 sc1\n"
        "global_store_dwordx4 v5, v[20:23], s[50:51] sc1\n"
        "global_store_dwordx4 v5, v[20:23], s[50:51] offset:1024 sc1\n"
        "global_store_dword v11, v20, s[56:57] sc1\n"
        "s_waitcnt vmcnt(22)\n"
        "v_mfma_f32_16x16x32_bf16 v[176:179], v[96:99], v[32:35], 0\n"
        "s_waitcnt vmcnt(21)\n"
        "v_mfma_f32_16x16x32_bf16 v[180:183], v[100:103], v[32:35], 0\n"
        "s_waitcnt vmcnt(20)\n"
        "v_mfma_f32_16x16x32_bf16 v[176:179], v[104:107], v[36:39], v[176:179]\n"
        "s_waitcnt vmcnt(19)\n"
        "v_mfma_f32_16x16x32_bf16 v[180:183], v[108:111], v[36:39], v[180:183]\n"
        "s_waitcnt vmcnt(18)\n"
        "v_mfma_f32_16x16x32_bf16 v[176:179], v[112:115], v[40:43], v[176:179]\n"
        "s_waitcnt vmcnt(17)\n"
        "v_mfma_f32_16x16x32_bf16 v[180:183], v[116:119], v[40:43], v[180:183]\n"
        "s_waitcnt vmcnt(16)\n"
        "v_mfma_f32_16x16x32_bf16 v[176:179], v[120:123], v[44:47], v[176:179]\n"
        "s_waitcnt vmcnt(15)\n"
        "v_mfma_f32_16x16x32_bf16 v[180:183], v[124:127], v[44:47], v[180:183]\n"
        "s_waitcnt vmcnt(14)\n"
        "v_mfma_f32_16x16x32_bf16 v[176:179], v[128:131], v[48:51], v[176:179]\n"
        "s_waitcnt vmcnt(13)\n"
        "v_mfma_f32_16x16x32_bf16 v[180:183], v[132:135], v[48:51], v[180:183]\n"
        "s_waitcnt vmcnt(12)\n"
        "v_mfma_f32_16x16x32_bf16 v[176:179], v[136:139], v[52:55], v[176:179]\n"
        "s_waitcnt vmcnt(11)\n"
        "v_mfma_f32_16x16x32_bf16 v[180:183], v[140:143], v[52:55], v[180:183]\n"
        "s_waitcnt vmcnt(10)\n"
        "v_mfma_f32_16x16x32_bf16 v[176:179], v[144:147], v[56:59], v[176:179]\n"
        "s_waitcnt vmcnt(9)\n"
        "v_mfma_f32_16x16x32_bf16 v[180:183], v[148:151], v[56:59], v[180:183]\n"
        "s_waitcnt vmcnt(8)\n"
        "v_mfma_f32_16x16x32_bf16 v[176:179], v[152:155], v[60:63], v[176:179]\n"
        "s_waitcnt vmcnt(7)\n"
        "v_mfma_f32_16x16x32_bf16 v[180:183], v[156:159], v[60:63], v[180:183]\n"
        "s_waitcnt vmcnt(6)\n"
        "v_mfma_f32_16x16x32_bf16 v[176:179], v[160:163], v[64:67], v[176:179]\n"
        "s_waitcnt vmcnt(5)\n"
        "v_mfma_f32_16x16x32_bf16 v[180:183], v[164:167], v[64:67], v[180:183]\n"
        "s_waitcnt vmcnt(4)\n"
        "v_mfma_f32_16x16x32_bf16 v[176:179], v[168:171], v[68:71], v[176:179]\n"
        "s_waitcnt vmcnt(3)\n"
        "v_mfma_f32_16x16x32_bf16 v[180:183], v[172:175], v[68:71], v[180:183]\n"
        "global_load_dword v199, v198, s[70:71]\n"
        "global_load_dword v188, v191, s[94:95] sc1\n"
        "global_load_dword v190, v7, s[86:87] sc1\n"
        "s_nop 2\n"
        "ds_write_b128 v12, v[176:179]\n"
        "s_nop 0\n"
        "ds_write_b128 v12, v[180:183] offset:1024\n"
        "s_waitcnt lgkmcnt(0)\n"
        "s_barrier\n"
        "ds_read2st64_b32 v[24:25], v13 offset0:0 offset1:8\n"
        "ds_read2st64_b32 v[26:27], v13 offset0:16 offset1:24\n"
        "ds_read2st64_b32 v[28:29], v14 offset0:0 offset1:8\n"
        "ds_read2st64_b32 v[30:31], v14 offset0:16 offset1:24\n"
        "s_waitcnt lgkmcnt(0)\n"
        "v_pk_add_f32 v[24:25], v[24:25], v[26:27]\n"
        "v_pk_add_f32 v[28:29], v[28:29], v[30:31]\n"
        "v_add_f32 v24, v24, v25\n"
        "v_add_f32 v25, v28, v29\n"
        "v_cvt_pk_bf16_f32 v24, v24, v25\n"
        "v_and_b32 v25, 0xffff0000, v24\n"
        "v_lshlrev_b32 v24, 16, v24\n"
        "v_and_b32 v27, 0xffff0000, v184\n"
        "v_lshlrev_b32 v26, 16, v184\n"
        "v_pk_add_f32 v[24:25], v[24:25], v[26:27]\n"
        "v_cvt_pk_bf16_f32 v24, v24, v25\n"
        "global_store_dword v186, v24, s[48:49] sc1\n"
        "s_waitcnt vmcnt(1)\n"
        "s_add_u32 s45, s45, 2\n"
        "s_cmp_lt_u32 s45, 54\n"
        "s_cbranch_scc1 Lcb_loop\n"
        "s_waitcnt vmcnt(0)\n"
        "s_barrier\n"
        "s_add_u32 s47, s58, 1\n"
        "v_mov_b32 v25, s47\n"
        "s_mov_b64 exec, s[62:63]\n"
        "global_store_dword v187, v25, s[90:91] sc1\n"
        "s_mov_b64 exec, -1\n"
        "s_cmp_lt_u32 s45, 64\n"
        "s_cbranch_scc1 Lcb_loop\n"
        "s_mov_b32 s88, 2\n"
        "s_cmp_lt_u32 s40, 32\n"
        "s_cbranch_scc1 Lg3_entry\n"
        "Lcb_exit:\n"
        "s_waitcnt vmcnt(0) lgkmcnt(0)\n"
        "s_endpgm\n"
        "Lcb_last:\n"
        "s_waitcnt vmcnt(0)\n"
        "v_max3_u32 v24, v64, v65, v66\n"
        "v_max3_u32 v25, v67, v68, v69\n"
        "v_max3_u32 v24, v24, v70, v71\n"
        "v_max_u32 v24, v24, v25\n"
        "v_cmp_eq_u32 vcc, -1, v24\n"
        "s_cbranch_vccz Lcb_window\n"
        "global_load_dwordx4 v[64:67], v5, s[50:51] sc1\n"
        "global_load_dwordx4 v[68:71], v5, s[50:51] offset:1024 sc1\n"
        "s_sub_u32 s46, s46, 1\n"
        "s_cbranch_scc0 Lcb_last\n"
        "s_branch Lcb_exit\n"
        "Lcb_pw_24:\n"
        "s_mov_b32 s69, 48\n"
        ".p2align 6\n"
        "Lcb_w_30:\n"
        "s_memrealtime s[54:55]\n"
        "s_waitcnt lgkmcnt(0)\n"
        "s_lshl_b32 s47, s54, 6\n"
        "s_sub_u32 s47, s34, s47\n"
        "s_cmp_lt_i32 s47, 768\n"
        "s_cbranch_scc1 Lcb_f_31\n"
        "s_sleep 1\n"
        "s_sub_u32 s69, s69, 1\n"
        "s_cbranch_scc0 Lcb_w_30\n"
        "s_mov_b32 s47, 0\n"
        "Lcb_f_31:\n"
        "s_max_i32 s47, s47, 0\n"
        "Lcb_c_32:\n"
        "s_sub_u32 s47, s47, 80\n"
        "s_cbranch_scc0 Lcb_c_32\n"
        "s_branch Lcb_pq_23\n"
        "Lcb_pp_22:\n"
        "global_load_dwordx4 v[64:67], v5, s[50:51] sc1\n"
        "global_load_dwordx4 v[68:71], v5, s[50:51] offset:1024 sc1\n"
        "s_branch Lcb_pq_23\n"
            :: "s"(ws), "s"(wv), "s"(wo), "s"(x), "s"(r - 64), "s"(par), "s"(xq), "s"(wave), "s"(karg) : "v0", "v1", "v2", "v3", "v4", "v5", "v6", "v7", "v8", "v9", "v10", "v11", "v12", "v13", "v14", "v15", "v16", "v17", "v18", "v19", "v20", "v21", "v22", "v23", "v24", "v25", "v26", "v27", "v28", "v29", "v30", "v31", "v32", "v33", "v34", "v35", "v36", "v37", "v38", "v39", "v40", "v41", "v42", "v43", "v44", "v45", "v46", "v47", "v48", "v49", "v50", "v51", "v52", "v53", "v54", "v55", "v56", "v57", "v58", "v59", "v60", "v61", "v62", "v63", "v64", "v65", "v66", "v67", "v68", "v69", "v70", "v71", "v72", "v73", "v74", "v75", "v76", "v77", "v78", "v79", "v80", "v81", "v82", "v83", "v84", "v85", "v86", "v87", "v88", "v89", "v90", "v91", "v92", "v93", "v94", "v95", "v96", "v97", "v98", "v99", "v100", "v101", "v102", "v103", "v104", "v105", "v106", "v107", "v108", "v109", "v110", "v111", "v112", "v113", "v114", "v115", "v116", "v117", "v118", "v119", "v120", "v121", "v122", "v123", "v124", "v125", "v126", "v127", "v128", "v129", "v130", "v131", "v132", "v133", "v134", "v135", "v136", "v137", "v138", "v139", "v140", "v141", "v142", "v143", "v144", "v145", "v146", "v147", "v148", "v149", "v150", "v151", "v152", "v153", "v154", "v155", "v156", "v157", "v158", "v159", "v160", "v161", "v162", "v163", "v164", "v165", "v166", "v167", "v168", "v169", "v170", "v171", "v172", "v173", "v174", "v175", "v176", "v177", "v178", "v179", "v180", "v181", "v182", "v183", "v184", "v185", "v186", "v187", "v188", "v189", "v190", "v191", "v192", "v193", "v194", "v195", "v196", "v197", "v198", "v199", "v200", "v201", "v202", "v203", "v204", "v205", "v206", "v207", "v208", "v209", "v210", "v211", "v212", "v213", "v214", "v215", "v216", "v217", "v218", "v219", "v220", "v221", "v222", "v223", "v224", "v225", "v226", "v227", "v228", "v229", "v230", "v231", "v232", "v233", "v234", "v235", "v236", "v237", "v238", "v239", "v240", "v241", "v242", "v243", "v244", "v245", "v246", "v247", "v248", "v249", "v250", "v251", "v252", "v253", "v254", "v255", "a0", "a1", "a2", "a3", "a4", "a5", "a6", "a7", "a8", "a9", "a10", "a11", "a12", "a13", "a14", "a15", "a16", "a17", "a18", "a19", "a20", "a21", "a22", "a23", "a24", "a25", "a26", "a27", "a28", "a29", "a30", "a31", "a32", "a33", "a34", "a35", "a36", "a37", "a38", "a39", "a40", "a41", "a42", "a43", "a44", "a45", "a46", "a47", "a48", "a49", "a50", "a51", "a52", "a53", "a54", "a55", "a56", "a57", "a58", "a59", "a60", "a61", "a62", "a63", "a64", "a65", "a66", "a67", "a68", "a69", "a70", "a71", "a72", "a73", "a74", "a75", "a76", "a77", "a78", "a79", "a80", "a81", "a82", "a83", "a84", "a85", "a86", "a87", "a88", "a89", "a90", "a91", "a92", "a93", "a94", "a95", "a96", "a97", "a98", "a99", "a100", "a101", "a102", "a103", "a104", "a105", "a106", "a107", "a108", "a109", "a110", "a111", "a112", "a113", "a114", "a115", "a116", "a117", "a118", "a119", "a120", "a121", "a122", "a123", "a124", "a125", "a126", "a127", "a128", "a129", "a130", "a131", "a132", "a133", "a134", "a135", "a136", "a137", "a138", "a139", "a140", "a141", "a142", "a143", "a144", "a145", "a146", "a147", "a148", "a149", "a150", "a151", "a152", "a153", "a154", "a155", "a156", "a157", "a158", "a159", "a160", "a161", "a162", "a163", "a164", "a165", "a166", "a167", "a168", "a169", "a170", "a171", "a172", "a173", "a174", "a175", "a176", "a177", "a178", "a179", "a180", "a181", "a182", "a183", "a184", "a185", "a186", "a187", "a188", "a189", "a190", "a191", "a192", "a193", "a194", "a195", "a196", "a197", "a198", "a199", "a200", "a201", "a202", "a203", "a204", "a205", "a206", "a207", "a208", "a209", "a210", "a211", "a212", "a213", "a214", "a215", "a216", "a217", "a218", "a219", "a220", "a221", "a222", "a223", "a224", "a225", "a226", "a227", "a228", "a229", "a230", "a231", "a232", "a233", "a234", "a235", "a236", "a237", "a238", "a239", "a240", "a241", "a242", "a243", "a244", "a245", "a246", "a247", "a248", "a249", "a250", "a251", "a252", "a253", "a254", "a255", "s34", "s35", "s36", "s37", "s38", "s39", "s40", "s41", "s42", "s43", "s44", "s45", "s46", "s47", "s48", "s49", "s50", "s51", "s52", "s53", "s54", "s55", "s56", "s57", "s58", "s59", "s60", "s61", "s62", "s63", "s64", "s65", "s66", "s67", "s68", "s69", "s70", "s71", "s72", "s73", "s74", "s75", "s76", "s77", "s78", "s79", "s80", "s81", "s82", "s83", "s84", "s85", "s86", "s87", "s88", "s89", "s90", "s91", "s92", "s93", "s94", "s95", "s96", "s97", "s98", "s99", "vcc", "scc", "memory");
        __builtin_amdgcn_endpgm();
    } else if (r < 110) {
        asm volatile(
        "s_mov_b64 s[48:49], %8\n"
        "s_mov_b64 s[90:91], %0\n"
        "s_mov_b64 s[92:93], %1\n"
        "s_mov_b64 s[94:95], %2\n"
        "s_mov_b64 s[96:97], %3\n"
        "s_mov_b32 s40, %4\n"
        "s_mov_b32 s41, %5\n"
        "s_mov_b32 s42, %6\n"
        "s_mov_b32 s43, %7\n"
        "s_mov_b64 s[0:1], s[48:49]\n"
        "s_mov_b64 exec, -1\n"
        "s_mov_b32 s45, s41\n"
        "s_mov_b32 s46, 0x200000\n"
        "v_mbcnt_lo_u32_b32 v0, -1, 0\n"
        "v_mbcnt_hi_u32_b32 v0, -1, v0\n"
        "v_lshlrev_b32 v1, 4, v0\n"
        "v_lshlrev_b32 v24, 3, v0\n"
        "v_add_u32 v24, 0xaa0000, v24\n"
        "global_load_dwordx2 v[26:27], v24, s[90:91] sc1\n"
        "s_waitcnt vmcnt(0)\n"
        "v_and_b32 v26, 1, v26\n"
        "v_and_b32 v27, 1, v27\n"
        "v_cmp_ne_u32 s[50:51], 0, v26\n"
        "v_cmp_ne_u32 s[52:53], 0, v27\n"
        "s_nop 3\n"
        "s_bcnt1_i32_b64 s47, s[50:51]\n"
        "s_bcnt1_i32_b64 s68, s[52:53]\n"
        "s_add_u32 s47, s47, s68\n"
        "s_cmp_gt_u32 s47, 64\n"
        "s_cselect_b32 s47, 1, 0\n"
        "s_xor_b32 s47, s47, s41\n"
        "s_mul_i32 s89, s47, 0x3f\n"
        "s_xor_b32 s89, s89, 52\n"
        "s_lshr_b32 s44, s89, 0\n"
        "s_and_b32 s44, s44, 1\n"
        "s_lshl_b32 s44, s44, 13\n"
        "s_sleep 127\n"
        "s_sleep 127\n"
        "s_sleep 127\n"
        "v_lshrrev_b32 v124, 3, v0\n"
        "s_lshl_b32 s47, s43, 3\n"
        "v_add_u32 v124, s47, v124\n"
        "v_bfe_u32 v125, v0, 4, 1\n"
        "s_and_b32 s47, s43, 1\n"
        "s_lshl_b32 s47, s47, 1\n"
        "v_or_b32 v125, s47, v125\n"
        "v_lshlrev_b32 v125, 1, v125\n"
        "v_and_b32 v126, 7, v0\n"
        "v_xor_b32 v125, v125, v126\n"
        "v_lshlrev_b32 v125, 4, v125\n"
        "s_mul_i32 s47, s40, 0x100\n"
        "v_mul_u32_u24 v126, 0xc00, v124\n"
        "v_add3_u32 v13, v126, v125, s47\n"
        "v_lshrrev_b32 v124, 4, v0\n"
        "v_lshlrev_b32 v124, 10, v124\n"
        "v_bfe_u32 v125, v0, 2, 2\n"
        "v_lshl_add_u32 v124, v125, 7, v124\n"
        "v_bfe_u32 v125, v0, 3, 2\n"
        "v_lshl_add_u32 v124, v125, 5, v124\n"
        "v_and_b32 v125, 3, v0\n"
        "v_lshl_add_u32 v124, v125, 3, v124\n"
        "v_lshrrev_b32 v124, 4, v0\n"
        "v_lshlrev_b32 v124, 10, v124\n"
        "v_bfe_u32 v125, v0, 2, 2\n"
        "v_lshl_add_u32 v124, v125, 7, v124\n"
        "v_bfe_u32 v125, v0, 3, 2\n"
        "v_bfe_u32 v126, v0, 1, 1\n"
        "s_and_b32 s47, s43, 1\n"
        "s_lshl_b32 s47, s47, 1\n"
        "v_or_b32 v126, s47, v126\n"
        "v_xor_b32 v125, v125, v126\n"
        "v_lshl_add_u32 v124, v125, 5, v124\n"
        "v_and_b32 v125, 1, v0\n"
        "v_lshl_add_u32 v124, v125, 4, v124\n"
        "s_lshr_b32 s47, s43, 1\n"
        "s_lshl_b32 s47, s47, 12\n"
        "v_add_u32 v14, s47, v124\n"
        "v_add_u32 v15, 8, v14\n"
        "s_lshl_b32 s59, s43, 10\n"
        "s_add_u32 s54, s92, 0x0\n"
        "s_addc_u32 s55, s93, 0\n"
        "s_add_u32 m0, s59, 0xa000\n"
        "s_nop 0\n"
        "global_load_lds_dwordx4 v13, s[54:55] offset:0\n"
        "s_add_u32 m0, s59, 0xaf80\n"
        "s_nop 0\n"
        "global_load_lds_dwordx4 v13, s[54:55] offset:128\n"
        "s_add_u32 s54, s92, 0x18000\n"
        "s_addc_u32 s55, s93, 0\n"
        "s_add_u32 m0, s59, 0xc000\n"
        "s_nop 0\n"
        "global_load_lds_dwordx4 v13, s[54:55] offset:0\n"
        "s_add_u32 m0, s59, 0xcf80\n"
        "s_nop 0\n"
        "global_load_lds_dwordx4 v13, s[54:55] offset:128\n"
        "s_add_u32 s54, s92, 0x30000\n"
        "s_addc_u32 s55, s93, 0\n"
        "s_add_u32 m0, s59, 0xe000\n"
        "s_nop 0\n"
        "global_load_lds_dwordx4 v13, s[54:55] offset:0\n"
        "s_add_u32 m0, s59, 0xef80\n"
        "s_nop 0\n"
        "global_load_lds_dwordx4 v13, s[54:55] offset:128\n"
        "s_add_u32 s54, s92, 0x48000\n"
        "s_addc_u32 s55, s93, 0\n"
        "s_add_u32 m0, s59, 0x10000\n"
        "s_nop 0\n"
        "global_load_lds_dwordx4 v13, s[54:55] offset:0\n"
        "s_add_u32 m0, s59, 0x10f80\n"
        "s_nop 0\n"
        "global_load_lds_dwordx4 v13, s[54:55] offset:128\n"
        "s_add_u32 s54, s92, 0x60000\n"
        "s_addc_u32 s55, s93, 0\n"
        "s_add_u32 m0, s59, 0x12000\n"
        "s_nop 0\n"
        "global_load_lds_dwordx4 v13, s[54:55] offset:0\n"
        "s_add_u32 m0, s59, 0x12f80\n"
        "s_nop 0\n"
        "global_load_lds_dwordx4 v13, s[54:55] offset:128\n"
        "s_add_u32 s54, s92, 0x78000\n"
        "s_addc_u32 s55, s93, 0\n"
        "s_add_u32 m0, s59, 0x14000\n"
        "s_nop 0\n"
        "global_load_lds_dwordx4 v13, s[54:55] offset:0\n"
        "s_add_u32 m0, s59, 0x14f80\n"
        "s_nop 0\n"
        "global_load_lds_dwordx4 v13, s[54:55] offset:128\n"
        "s_add_u32 s54, s92, 0x90000\n"
        "s_addc_u32 s55, s93, 0\n"
        "s_add_u32 m0, s59, 0x16000\n"
        "s_nop 0\n"
        "global_load_lds_dwordx4 v13, s[54:55] offset:0\n"
        "s_add_u32 m0, s59, 0x16f80\n"
        "s_nop 0\n"
        "global_load_lds_dwordx4 v13, s[54:55] offset:128\n"
        "s_add_u32 s54, s92, 0xa8000\n"
        "s_addc_u32 s55, s93, 0\n"
        "s_add_u32 m0, s59, 0x18000\n"
        "s_nop 0\n"
        "global_load_lds_dwordx4 v13, s[54:55] offset:0\n"
        "s_add_u32 m0, s59, 0x18f80\n"
        "s_nop 0\n"
        "global_load_lds_dwordx4 v13, s[54:55] offset:128\n"
        "s_add_u32 s54, s92, 0xc0000\n"
        "s_addc_u32 s55, s93, 0\n"
        "s_add_u32 m0, s59, 0x1a000\n"
        "s_nop 0\n"
        "global_load_lds_dwordx4 v13, s[54:55] offset:0\n"
        "s_add_u32 m0, s59, 0x1af80\n"
        "s_nop 0\n"
        "global_load_lds_dwordx4 v13, s[54:55] offset:128\n"
        "s_add_u32 s54, s92, 0xd8000\n"
        "s_addc_u32 s55, s93, 0\n"
        "s_add_u32 m0, s59, 0x1c000\n"
        "s_nop 0\n"
        "global_load_lds_dwordx4 v13, s[54:55] offset:0\n"
        "s_add_u32 m0, s59, 0x1cf80\n"
        "s_nop 0\n"
        "global_load_lds_dwordx4 v13, s[54:55] offset:128\n"
        "s_add_u32 s54, s92, 0xf0000\n"
        "s_addc_u32 s55, s93, 0\n"
        "s_add_u32 m0, s59, 0x1e000\n"
        "s_nop 0\n"
        "global_load_lds_dwordx4 v13, s[54:55] offset:0\n"
        "s_add_u32 m0, s59, 0x1ef80\n"
        "s_nop 0\n"
        "global_load_lds_dwordx4 v13, s[54:55] offset:128\n"
        "s_add_u32 s54, s92, 0x108000\n"
        "s_addc_u32 s55, s93, 0\n"
        "s_add_u32 m0, s59, 0x20000\n"
        "s_nop 0\n"
        "global_load_lds_dwordx4 v13, s[54:55] offset:0\n"
        "s_add_u32 m0, s59, 0x20f80\n"
        "s_nop 0\n"
        "global_load_lds_dwordx4 v13, s[54:55] offset:128\n"
        "s_add_u32 s54, s92, 0x120000\n"
        "s_addc_u32 s55, s93, 0\n"
        "s_add_u32 m0, s59, 0x22000\n"
        "s_nop 0\n"
        "global_load_lds_dwordx4 v13, s[54:55] offset:0\n"
        "s_add_u32 m0, s59, 0x22f80\n"
        "s_nop 0\n"
        "global_load_lds_dwordx4 v13, s[54:55] offset:128\n"
        "s_add_u32 s54, s92, 0x138000\n"
        "s_addc_u32 s55, s93, 0\n"
        "s_add_u32 m0, s59, 0x24000\n"
        "s_nop 0\n"
        "global_load_lds_dwordx4 v13, s[54:55] offset:0\n"
        "s_add_u32 m0, s59, 0x24f80\n"
        "s_nop 0\n"
        "global_load_lds_dwordx4 v13, s[54:55] offset:128\n"
        "s_add_u32 s54, s92, 0x150000\n"
        "s_addc_u32 s55, s93, 0\n"
        "s_add_u32 m0, s59, 0x26000\n"
        "s_nop 0\n"
        "global_load_lds_dwordx4 v13, s[54:55] offset:0\n"
        "s_add_u32 m0, s59, 0x26f80\n"
        "s_nop 0\n"
        "global_load_lds_dwordx4 v13, s[54:55] offset:128\n"
        "s_waitcnt vmcnt(20)\n"
        "s_waitcnt lgkmcnt(0)\n"
        "s_barrier\n"
        "v_add_u32 v20, 0xa000, v14\n"
        "ds_read_b64_tr_b16 a[0:1], v20\n"
        "ds_read_b64_tr_b16 a[2:3], v20 offset:512\n"
        "v_add_u32 v21, 0xa000, v15\n"
        "ds_read_b64_tr_b16 a[80:81], v21\n"
        "ds_read_b64_tr_b16 a[82:83], v21 offset:512\n"
        "v_add_u32 v22, 0xc000, v14\n"
        "ds_read_b64_tr_b16 a[4:5], v22\n"
        "ds_read_b64_tr_b16 a[6:7], v22 offset:512\n"
        "v_add_u32 v20, 0xc000, v15\n"
        "ds_read_b64_tr_b16 a[84:85], v20\n"
        "ds_read_b64_tr_b16 a[86:87], v20 offset:512\n"
        "v_add_u32 v21, 0xe000, v14\n"
        "ds_read_b64_tr_b16 a[8:9], v21\n"
        "ds_read_b64_tr_b16 a[10:11], v21 offset:512\n"
        "v_add_u32 v22, 0xe000, v15\n"
        "ds_read_b64_tr_b16 a[88:89], v22\n"
        "ds_read_b64_tr_b16 a[90:91], v22 offset:512\n"
        "v_add_u32 v20, 0x10000, v14\n"
        "ds_read_b64_tr_b16 a[12:13], v20\n"
        "ds_read_b64_tr_b16 a[14:15], v20 offset:512\n"
        "v_add_u32 v21, 0x10000, v15\n"
        "ds_read_b64_tr_b16 a[92:93], v21\n"
        "ds_read_b64_tr_b16 a[94:95], v21 offset:512\n"
        "v_add_u32 v22, 0x12000, v14\n"
        "ds_read_b64_tr_b16 a[16:17], v22\n"
        "ds_read_b64_tr_b16 a[18:19], v22 offset:512\n"
        "v_add_u32 v20, 0x12000, v15\n"
        "ds_read_b64_tr_b16 a[96:97], v20\n"
        "ds_read_b64_tr_b16 a[98:99], v20 offset:512\n"
        "s_waitcnt vmcnt(10)\n"
        "s_waitcnt lgkmcnt(0)\n"
        "s_barrier\n"
        "s_add_u32 s54, s92, 0x168000\n"
        "s_addc_u32 s55, s93, 0\n"
        "s_add_u32 m0, s59, 0xa000\n"
        "s_nop 0\n"
        "global_load_lds_dwordx4 v13, s[54:55] offset:0\n"
        "s_add_u32 m0, s59, 0xaf80\n"
        "s_nop 0\n"
        "global_load_lds_dwordx4 v13, s[54:55] offset:128\n"
        "s_add_u32 s54, s92, 0x180000\n"
        "s_addc_u32 s55, s93, 0\n"
        "s_add_u32 m0, s59, 0xc000\n"
        "s_nop 0\n"
        "global_load_lds_dwordx4 v13, s[54:55] offset:0\n"
        "s_add_u32 m0, s59, 0xcf80\n"
        "s_nop 0\n"
        "global_load_lds_dwordx4 v13, s[54:55] offset:128\n"
        "s_add_u32 s54, s92, 0x198000\n"
        "s_addc_u32 s55, s93, 0\n"
        "s_add_u32 m0, s59, 0xe000\n"
        "s_nop 0\n"
        "global_load_lds_dwordx4 v13, s[54:55] offset:0\n"
        "s_add_u32 m0, s59, 0xef80\n"
        "s_nop 0\n"
        "global_load_lds_dwordx4 v13, s[54:55] offset:128\n"
        "s_add_u32 s54, s92, 0x1b0000\n"
        "s_addc_u32 s55, s93, 0\n"
        "s_add_u32 m0, s59, 0x10000\n"
        "s_nop 0\n"
        "global_load_lds_dwordx4 v13, s[54:55] offset:0\n"
        "s_add_u32 m0, s59, 0x10f80\n"
        "s_nop 0\n"
        "global_load_lds_dwordx4 v13, s[54:55] offset:128\n"
        "s_add_u32 s54, s92, 0x1c8000\n"
        "s_addc_u32 s55, s93, 0\n"
        "s_add_u32 m0, s59, 0x12000\n"
        "s_nop 0\n"
        "global_load_lds_dwordx4 v13, s[54:55] offset:0\n"
        "s_add_u32 m0, s59, 0x12f80\n"
        "s_nop 0\n"
        "global_load_lds_dwordx4 v13, s[54:55] offset:128\n"
        "v_add_u32 v21, 0x14000, v14\n"
        "ds_read_b64_tr_b16 a[20:21], v21\n"
        "ds_read_b64_tr_b16 a[22:23], v21 offset:512\n"
        "v_add_u32 v22, 0x14000, v15\n"
        "ds_read_b64_tr_b16 a[100:101], v22\n"
        "ds_read_b64_tr_b16 a[102:103], v22 offset:512\n"
        "v_add_u32 v20, 0x16000, v14\n"
        "ds_read_b64_tr_b16 a[24:25], v20\n"
        "ds_read_b64_tr_b16 a[26:27], v20 offset:512\n"
        "v_add_u32 v21, 0x16000, v15\n"
        "ds_read_b64_tr_b16 a[104:105], v21\n"
        "ds_read_b64_tr_b16 a[106:107], v21 offset:512\n"
        "v_add_u32 v22, 0x18000, v14\n"
        "ds_read_b64_tr_b16 a[28:29], v22\n"
        "ds_read_b64_tr_b16 a[30:31], v22 offset:512\n"
        "v_add_u32 v20, 0x18000, v15\n"
        "ds_read_b64_tr_b16 a[108:109], v20\n"
        "ds_read_b64_tr_b16 a[110:111], v20 offset:512\n"
        "v_add_u32 v21, 0x1a000, v14\n"
        "ds_read_b64_tr_b16 a[32:33], v21\n"
        "ds_read_b64_tr_b16 a[34:35], v21 offset:512\n"
        "v_add_u32 v22, 0x1a000, v15\n"
        "ds_read_b64_tr_b16 a[112:113], v22\n"
        "ds_read_b64_tr_b16 a[114:115], v22 offset:512\n"
        "v_add_u32 v20, 0x1c000, v14\n"
        "ds_read_b64_tr_b16 a[36:37], v20\n"
        "ds_read_b64_tr_b16 a[38:39], v20 offset:512\n"
        "v_add_u32 v21, 0x1c000, v15\n"
        "ds_read_b64_tr_b16 a[116:117], v21\n"
        "ds_read_b64_tr_b16 a[118:119], v21 offset:512\n"
        "s_waitcnt vmcnt(10)\n"
        "s_waitcnt lgkmcnt(0)\n"
        "s_barrier\n"
        "v_add_u32 v22, 0x1e000, v14\n"
        "ds_read_b64_tr_b16 a[40:41], v22\n"
        "ds_read_b64_tr_b16 a[42:43], v22 offset:512\n"
        "v_add_u32 v20, 0x1e000, v15\n"
        "ds_read_b64_tr_b16 a[120:121], v20\n"
        "ds_read_b64_tr_b16 a[122:123], v20 offset:512\n"
        "v_add_u32 v21, 0x20000, v14\n"
        "ds_read_b64_tr_b16 a[44:45], v21\n"
        "ds_read_b64_tr_b16 a[46:47], v21 offset:512\n"
        "v_add_u32 v22, 0x20000, v15\n"
        "ds_read_b64_tr_b16 a[124:125], v22\n"
        "ds_read_b64_tr_b16 a[126:127], v22 offset:512\n"
        "v_add_u32 v20, 0x22000, v14\n"
        "ds_read_b64_tr_b16 a[48:49], v20\n"
        "ds_read_b64_tr_b16 a[50:51], v20 offset:512\n"
        "v_add_u32 v21, 0x22000, v15\n"
        "ds_read_b64_tr_b16 a[128:129], v21\n"
        "ds_read_b64_tr_b16 a[130:131], v21 offset:512\n"
        "v_add_u32 v22, 0x24000, v14\n"
        "ds_read_b64_tr_b16 a[52:53], v22\n"
        "ds_read_b64_tr_b16 a[54:55], v22 offset:512\n"
        "v_add_u32 v20, 0x24000, v15\n"
        "ds_read_b64_tr_b16 a[132:133], v20\n"
        "ds_read_b64_tr_b16 a[134:135], v20 offset:512\n"
        "v_add_u32 v21, 0x26000, v14\n"
        "ds_read_b64_tr_b16 a[56:57], v21\n"
        "ds_read_b64_tr_b16 a[58:59], v21 offset:512\n"
        "v_add_u32 v22, 0x26000, v15\n"
        "ds_read_b64_tr_b16 a[136:137], v22\n"
        "ds_read_b64_tr_b16 a[138:139], v22 offset:512\n"
        "s_waitcnt vmcnt(0)\n"
        "s_waitcnt lgkmcnt(0)\n"
        "s_barrier\n"
        "v_add_u32 v20, 0xa000, v14\n"
        "ds_read_b64_tr_b16 a[60:61], v20\n"
        "ds_read_b64_tr_b16 a[62:63], v20 offset:512\n"
        "v_add_u32 v21, 0xa000, v15\n"
        "ds_read_b64_tr_b16 a[140:141], v21\n"
        "ds_read_b64_tr_b16 a[142:143], v21 offset:512\n"
        "v_add_u32 v22, 0xc000, v14\n"
        "ds_read_b64_tr_b16 a[64:65], v22\n"
        "ds_read_b64_tr_b16 a[66:67], v22 offset:512\n"
        "v_add_u32 v20, 0xc000, v15\n"
        "ds_read_b64_tr_b16 a[144:145], v20\n"
        "ds_read_b64_tr_b16 a[146:147], v20 offset:512\n"
        "v_add_u32 v21, 0xe000, v14\n"
        "ds_read_b64_tr_b16 a[68:69], v21\n"
        "ds_read_b64_tr_b16 a[70:71], v21 offset:512\n"
        "v_add_u32 v22, 0xe000, v15\n"
        "ds_read_b64_tr_b16 a[148:149], v22\n"
        "ds_read_b64_tr_b16 a[150:151], v22 offset:512\n"
        "v_add_u32 v20, 0x10000, v14\n"
        "ds_read_b64_tr_b16 a[72:73], v20\n"
        "ds_read_b64_tr_b16 a[74:75], v20 offset:512\n"
        "v_add_u32 v21, 0x10000, v15\n"
        "ds_read_b64_tr_b16 a[152:153], v21\n"
        "ds_read_b64_tr_b16 a[154:155], v21 offset:512\n"
        "v_add_u32 v22, 0x12000, v14\n"
        "ds_read_b64_tr_b16 a[76:77], v22\n"
        "ds_read_b64_tr_b16 a[78:79], v22 offset:512\n"
        "v_add_u32 v20, 0x12000, v15\n"
        "ds_read_b64_tr_b16 a[156:157], v20\n"
        "ds_read_b64_tr_b16 a[158:159], v20 offset:512\n"
        "s_waitcnt lgkmcnt(0)\n"
        "s_barrier\n"
        "s_waitcnt lgkmcnt(0)\n"
        "s_load_dwordx2 s[48:49], s[0:1], 72\n"
        "s_mul_i32 s47, s41, 0xc\n"
        "s_add_u32 s47, s47, s40\n"
        "s_lshl_b32 s47, s47, 2\n"
        "s_add_u32 s47, s47, s43\n"
        "s_mul_i32 s47, s47, 0x2800\n"
        "v_lshl_add_u32 v16, v0, 7, s47\n"
        "v_min_u32 v17, 15, v0\n"
        "v_lshl_add_u32 v17, v17, 7, s47\n"
        "s_waitcnt lgkmcnt(0)\n"
        "global_load_dword v18, v16, s[48:49]\n"
        "v_add_u32 v17, 0x2000, v17\n"
        "global_load_dword v19, v17, s[48:49]\n"
        "s_waitcnt vmcnt(0)\n"
        "v_mov_b32 v24, 0x3f803f80\n"
        "v_mov_b32 v25, 0x3f803f80\n"
        "v_mov_b32 v26, 0x3f803f80\n"
        "v_mov_b32 v27, 0x3f803f80\n"
        "s_nop 1\n"
        "v_mfma_f32_16x16x32_bf16 v[28:31], a[0:3], v[24:27], 0\n"
        "v_mfma_f32_16x16x32_bf16 v[32:35], a[80:83], v[24:27], 0\n"
        "v_mfma_f32_16x16x32_bf16 v[28:31], a[4:7], v[24:27], v[28:31]\n"
        "v_mfma_f32_16x16x32_bf16 v[32:35], a[84:87], v[24:27], v[32:35]\n"
        "v_mfma_f32_16x16x32_bf16 v[28:31], a[8:11], v[24:27], v[28:31]\n"
        "v_mfma_f32_16x16x32_bf16 v[32:35], a[88:91], v[24:27], v[32:35]\n"
        "v_mfma_f32_16x16x32_bf16 v[28:31], a[12:15], v[24:27], v[28:31]\n"
        "v_mfma_f32_16x16x32_bf16 v[32:35], a[92:95], v[24:27], v[32:35]\n"
        "v_mfma_f32_16x16x32_bf16 v[28:31], a[16:19], v[24:27], v[28:31]\n"
        "v_mfma_f32_16x16x32_bf16 v[32:35], a[96:99], v[24:27], v[32:35]\n"
        "v_mfma_f32_16x16x32_bf16 v[28:31], a[20:23], v[24:27], v[28:31]\n"
        "v_mfma_f32_16x16x32_bf16 v[32:35], a[100:103], v[24:27], v[32:35]\n"
        "v_mfma_f32_16x16x32_bf16 v[28:31], a[24:27], v[24:27], v[28:31]\n"
        "v_mfma_f32_16x16x32_bf16 v[32:35], a[104:107], v[24:27], v[32:35]\n"
        "v_mfma_f32_16x16x32_bf16 v[28:31], a[28:31], v[24:27], v[28:31]\n"
        "v_mfma_f32_16x16x32_bf16 v[32:35], a[108:111], v[24:27], v[32:35]\n"
        "v_mfma_f32_16x16x32_bf16 v[28:31], a[32:35], v[24:27], v[28:31]\n"
        "v_mfma_f32_16x16x32_bf16 v[32:35], a[112:115], v[24:27], v[32:35]\n"
        "v_mfma_f32_16x16x32_bf16 v[28:31], a[36:39], v[24:27], v[28:31]\n"
        "v_mfma_f32_16x16x32_bf16 v[32:35], a[116:119], v[24:27], v[32:35]\n"
        "v_mfma_f32_16x16x32_bf16 v[28:31], a[40:43], v[24:27], v[28:31]\n"
        "v_mfma_f32_16x16x32_bf16 v[32:35], a[120:123], v[24:27], v[32:35]\n"
        "v_mfma_f32_16x16x32_bf16 v[28:31], a[44:47], v[24:27], v[28:31]\n"
        "v_mfma_f32_16x16x32_bf16 v[32:35], a[124:127], v[24:27], v[32:35]\n"
        "v_mfma_f32_16x16x32_bf16 v[28:31], a[48:51], v[24:27], v[28:31]\n"
        "v_mfma_f32_16x16x32_bf16 v[32:35], a[128:131], v[24:27], v[32:35]\n"
        "v_mfma_f32_16x16x32_bf16 v[28:31], a[52:55], v[24:27], v[28:31]\n"
        "v_mfma_f32_16x16x32_bf16 v[32:35], a[132:135], v[24:27], v[32:35]\n"
        "v_mfma_f32_16x16x32_bf16 v[28:31], a[56:59], v[24:27], v[28:31]\n"
        "v_mfma_f32_16x16x32_bf16 v[32:35], a[136:139], v[24:27], v[32:35]\n"
        "v_mfma_f32_16x16x32_bf16 v[28:31], a[60:63], v[24:27], v[28:31]\n"
        "v_mfma_f32_16x16x32_bf16 v[32:35], a[140:143], v[24:27], v[32:35]\n"
        "v_mfma_f32_16x16x32_bf16 v[28:31], a[64:67], v[24:27], v[28:31]\n"
        "v_mfma_f32_16x16x32_bf16 v[32:35], a[144:147], v[24:27], v[32:35]\n"
        "v_mfma_f32_16x16x32_bf16 v[28:31], a[68:71], v[24:27], v[28:31]\n"
        "v_mfma_f32_16x16x32_bf16 v[32:35], a[148:151], v[24:27], v[32:35]\n"
        "v_mfma_f32_16x16x32_bf16 v[28:31], a[72:75], v[24:27], v[28:31]\n"
        "v_mfma_f32_16x16x32_bf16 v[32:35], a[152:155], v[24:27], v[32:35]\n"
        "v_mfma_f32_16x16x32_bf16 v[28:31], a[76:79], v[24:27], v[28:31]\n"
        "v_mfma_f32_16x16x32_bf16 v[32:35], a[156:159], v[24:27], v[32:35]\n"
        "v_and_b32 v16, 15, v0\n"
        "v_lshrrev_b32 v17, 4, v0\n"
        "v_min_u32 v18, 9, v0\n"
        "s_mul_i32 s47, s43, 0xa\n"
        "v_add_u32 v18, s47, v18\n"
        "v_lshlrev_b32 v18, 7, v18\n"
        "s_add_u32 s47, s44, 0x1ac800\n"
        "v_add_u32 v2, s47, v18\n"
        "s_mul_i32 s47, s43, 0x2800\n"
        "v_add_u32 v4, s47, v1\n"
        "v_add_u32 v3, 0x1000, v4\n"
        "v_lshlrev_b32 v18, 7, v16\n"
        "s_lshl_b32 s47, s43, 5\n"
        "s_add_u32 s47, s47, 40960\n"
        "v_add_u32 v5, s47, v18\n"
        "v_and_b32 v19, 31, v0\n"
        "v_lshlrev_b32 v18, 7, v19\n"
        "v_lshrrev_b32 v20, 5, v0\n"
        "v_lshl_add_u32 v18, v20, 6, v18\n"
        "v_and_b32 v20, 3, v0\n"
        "v_lshl_add_u32 v18, v20, 2, v18\n"
        "v_add_u32 v6, 40960, v18\n"
        "s_mov_b32 s70, 0xf0000f\n"
        "s_mov_b32 s71, 0xf0000f00\n"
        "v_lshlrev_b32 v7, 2, v16\n"
        "s_lshl_b32 s47, s40, 13\n"
        "s_lshl_b32 s68, s43, 11\n"
        "s_add_u32 s47, s47, s68\n"
        "s_add_u32 s47, s47, 0x1100000\n"
        "v_add_u32 v8, s47, v1\n"
        "s_lshl_b32 s47, s41, 4\n"
        "s_add_u32 s47, s47, s40\n"
        "s_lshl_b32 s47, s47, 7\n"
        "s_add_u32 s47, s47, 0x1b5000\n"
        "v_lshlrev_b32 v18, 13, v0\n"
        "v_add_u32 v9, s47, v18\n"
        "s_cmp_eq_u32 s43, 0\n"
        "s_cselect_b32 s62, 1, 0\n"
        "s_mov_b32 s63, 0\n"
        "s_mul_i32 s62, s62, 0x3\n"
        "s_memrealtime s[54:55]\n"
        "s_waitcnt lgkmcnt(0)\n"
        "s_add_u32 s34, s54, 0\n"
        "s_lshl_b32 s34, s34, 6\n"
        "s_mov_b32 s61, 0x3dd2d3e8\n"
        "v_mov_b32 v23, 0x40135761\n"
        "s_barrier\n"
        "Lg1a_loop:\n"
        "s_lshr_b32 s58, s45, 1\n"
        "s_add_u32 s88, s58, 1\n"
        "s_mul_i32 s68, s45, 0xa000\n"
        "s_add_u32 s47, s68, 0xb00000\n"
        "s_add_u32 s48, s90, s47\n"
        "s_addc_u32 s49, s91, 0\n"
        "s_add_u32 s50, s48, 0x2000\n"
        "s_addc_u32 s51, s49, 0\n"
        "s_mul_i32 s68, s45, 0xc000\n"
        "s_add_u32 s47, s68, 0x0\n"
        "s_add_u32 s52, s90, s47\n"
        "s_addc_u32 s53, s91, 0\n"
        "s_mul_i32 s68, s45, 0x600\n"
        "s_add_u32 s47, s68, 0x0\n"
        "s_add_u32 s56, s90, s47\n"
        "s_addc_u32 s57, s91, 0\n"
        "v_mov_b32 v22, s88\n"
        "s_cmp_ge_u32 s45, 50\n"
        "s_cbranch_scc1 Lg1a_np_1\n"
        "s_mov_b32 s69, 48\n"
        ".p2align 6\n"
        "Lg1a_w_3:\n"
        "s_memrealtime s[54:55]\n"
        "s_waitcnt lgkmcnt(0)\n"
        "s_lshl_b32 s47, s54, 6\n"
        "s_sub_u32 s47, s34, s47\n"
        "s_cmp_lt_i32 s47, 768\n"
        "s_cbranch_scc1 Lg1a_f_4\n"
        "s_sleep 1\n"
        "s_sub_u32 s69, s69, 1\n"
        "s_cbranch_scc0 Lg1a_w_3\n"
        "s_mov_b32 s47, 0\n"
        "Lg1a_f_4:\n"
        "s_max_i32 s47, s47, 0\n"
        "Lg1a_c_5:\n"
        "s_sub_u32 s47, s47, 80\n"
        "s_cbranch_scc0 Lg1a_c_5\n"
        "s_branch Lg1a_p0_7\n"
        "Lg1a_pl_6:\n"
        "s_sleep 16\n"
        "Lg1a_p0_7:\n"
        "global_load_dword v36, v2, s[90:91] sc1\n"
        "s_sub_u32 s46, s46, 1\n"
        "s_cbranch_scc1 Lg1a_exit\n"
        "s_waitcnt vmcnt(0)\n"
        "v_cmp_lt_u32 vcc, v36, v22\n"
        "s_cbranch_vccnz Lg1a_pl_6\n"
        "s_branch Lg1a_nq_2\n"
        "Lg1a_np_1:\n"
        "s_branch Lg1a_p0_9\n"
        "Lg1a_pl_8:\n"
        "s_sleep 1\n"
        "Lg1a_p0_9:\n"
        "global_load_dword v36, v2, s[90:91] sc1\n"
        "s_sub_u32 s46, s46, 1\n"
        "s_cbranch_scc1 Lg1a_exit\n"
        "s_waitcnt vmcnt(0)\n"
        "v_cmp_lt_u32 vcc, v36, v22\n"
        "s_cbranch_vccnz Lg1a_pl_8\n"
        "Lg1a_nq_2:\n"
        "s_memrealtime s[54:55]\n"
        "s_waitcnt lgkmcnt(0)\n"
        "s_add_u32 s34, s54, 400\n"
        "s_lshl_b32 s34, s34, 6\n"
        "s_cmp_ge_u32 s45, 60\n"
        "s_cbranch_scc1 Lg1a_nd_10\n"
        "s_sleep 12\n"
        "Lg1a_nd_10:\n"
        "global_load_dwordx4 v[40:43], v3, s[48:49] offset:-4096 sc1\n"
        "global_load_dwordx4 v[44:47], v3, s[48:49] offset:-3072 sc1\n"
        "global_load_dwordx4 v[48:51], v3, s[48:49] offset:-2048 sc1\n"
        "global_load_dwordx4 v[52:55], v3, s[48:49] offset:-1024 sc1\n"
        "global_load_dwordx4 v[56:59], v3, s[48:49] offset:0 sc1\n"
        "global_load_dwordx4 v[60:63], v3, s[48:49] offset:1024 sc1\n"
        "global_load_dwordx4 v[64:67], v3, s[48:49] offset:2048 sc1\n"
        "global_load_dwordx4 v[68:71], v3, s[48:49] offset:3072 sc1\n"
        "global_load_dwordx4 v[72:75], v3, s[50:51] offset:-4096 sc1\n"
        "global_load_dwordx4 v[76:79], v3, s[50:51] offset:-3072 sc1\n"
        "s_mov_b64 exec, -1\n"
        "s_waitcnt vmcnt(9)\n"
        "v_mfma_f32_16x16x32_bf16 v[80:83], v[24:27], v[40:43], 0\n"
        "ds_write_b128 v4, v[40:43] offset:0\n"
        "v_mfma_f32_16x16x32_bf16 v[84:87], v[40:43], v[40:43], 0\n"
        "s_waitcnt vmcnt(8)\n"
        "v_mfma_f32_16x16x32_bf16 v[88:91], v[24:27], v[44:47], 0\n"
        "ds_write_b128 v4, v[44:47] offset:1024\n"
        "v_mfma_f32_16x16x32_bf16 v[92:95], v[44:47], v[44:47], 0\n"
        "s_waitcnt vmcnt(7)\n"
        "v_mfma_f32_16x16x32_bf16 v[80:83], v[24:27], v[48:51], v[80:83]\n"
        "ds_write_b128 v4, v[48:51] offset:2048\n"
        "v_mfma_f32_16x16x32_bf16 v[84:87], v[48:51], v[48:51], v[84:87]\n"
        "s_waitcnt vmcnt(6)\n"
        "v_mfma_f32_16x16x32_bf16 v[88:91], v[24:27], v[52:55], v[88:91]\n"
        "ds_write_b128 v4, v[52:55] offset:3072\n"
        "v_mfma_f32_16x16x32_bf16 v[92:95], v[52:55], v[52:55], v[92:95]\n"
        "s_waitcnt vmcnt(5)\n"
        "v_mfma_f32_16x16x32_bf16 v[80:83], v[24:27], v[56:59], v[80:83]\n"
        "ds_write_b128 v4, v[56:59] offset:4096\n"
        "v_mfma_f32_16x16x32_bf16 v[84:87], v[56:59], v[56:59], v[84:87]\n"
        "s_waitcnt vmcnt(4)\n"
        "v_mfma_f32_16x16x32_bf16 v[88:91], v[24:27], v[60:63], v[88:91]\n"
        "ds_write_b128 v4, v[60:63] offset:5120\n"
        "v_mfma_f32_16x16x32_bf16 v[92:95], v[60:63], v[60:63], v[92:95]\n"
        "s_waitcnt vmcnt(3)\n"
        "v_mfma_f32_16x16x32_bf16 v[80:83], v[24:27], v[64:67], v[80:83]\n"
        "ds_write_b128 v4, v[64:67] offset:6144\n"
        "v_mfma_f32_16x16x32_bf16 v[84:87], v[64:67], v[64:67], v[84:87]\n"
        "s_waitcnt vmcnt(2)\n"
        "v_mfma_f32_16x16x32_bf16 v[88:91], v[24:27], v[68:71], v[88:91]\n"
        "ds_write_b128 v4, v[68:71] offset:7168\n"
        "v_mfma_f32_16x16x32_bf16 v[92:95], v[68:71], v[68:71], v[92:95]\n"
        "s_waitcnt vmcnt(1)\n"
        "v_mfma_f32_16x16x32_bf16 v[80:83], v[24:27], v[72:75], v[80:83]\n"
        "ds_write_b128 v4, v[72:75] offset:8192\n"
        "v_mfma_f32_16x16x32_bf16 v[84:87], v[72:75], v[72:75], v[84:87]\n"
        "s_waitcnt vmcnt(0)\n"
        "v_mfma_f32_16x16x32_bf16 v[88:91], v[24:27], v[76:79], v[88:91]\n"
        "ds_write_b128 v4, v[76:79] offset:9216\n"
        "v_mfma_f32_16x16x32_bf16 v[92:95], v[76:79], v[76:79], v[92:95]\n"
        "s_mov_b64 exec, s[70:71]\n"
        "s_nop 0\n"
        "ds_write_b128 v5, v[80:83] offset:0\n"
        "s_nop 0\n"
        "ds_write_b128 v5, v[84:87] offset:16\n"
        "s_nop 0\n"
        "ds_write_b128 v5, v[88:91] offset:2048\n"
        "s_nop 0\n"
        "ds_write_b128 v5, v[92:95] offset:2064\n"
        "s_mov_b64 exec, -1\n"
        "s_waitcnt lgkmcnt(0)\n"
        "s_barrier\n"
        "ds_read_b128 v[96:99], v1 offset:0\n"
        "ds_read_b128 v[100:103], v1 offset:1024\n"
        "ds_read_b128 v[104:107], v1 offset:2048\n"
        "ds_read_b128 v[108:111], v1 offset:3072\n"
        "ds_read_b128 v[112:115], v1 offset:4096\n"
        "ds_read_b128 v[116:119], v1 offset:5120\n"
        "s_waitcnt lgkmcnt(4)\n"
        "v_mfma_f32_16x16x32_bf16 v[80:83], a[0:3], v[96:99], 0\n"
        "ds_read_b128 v[120:123], v1 offset:6144\n"
        "v_mfma_f32_16x16x32_bf16 v[88:91], a[80:83], v[96:99], 0\n"
        "ds_read_b128 v[124:127], v1 offset:7168\n"
        "v_mfma_f32_16x16x32_bf16 v[84:87], a[0:3], v[100:103], 0\n"
        "ds_read_b128 v[128:131], v1 offset:8192\n"
        "v_mfma_f32_16x16x32_bf16 v[92:95], a[80:83], v[100:103], 0\n"
        "ds_read_b128 v[132:135], v1 offset:9216\n"
        "s_waitcnt lgkmcnt(6)\n"
        "v_mfma_f32_16x16x32_bf16 v[80:83], a[4:7], v[104:107], v[80:83]\n"
        "ds_read_b128 v[136:139], v1 offset:10240\n"
        "ds_read2_b32 v[40:41], v6 offset0:0 offset1:4\n"
        "v_mfma_f32_16x16x32_bf16 v[88:91], a[84:87], v[104:107], v[88:91]\n"
        "ds_read_b128 v[140:143], v1 offset:11264\n"
        "v_mfma_f32_16x16x32_bf16 v[84:87], a[4:7], v[108:111], v[84:87]\n"
        "ds_read_b128 v[144:147], v1 offset:12288\n"
        "ds_read2_b32 v[48:49], v6 offset0:8 offset1:12\n"
        "v_mfma_f32_16x16x32_bf16 v[92:95], a[84:87], v[108:111], v[92:95]\n"
        "ds_read_b128 v[148:151], v1 offset:13312\n"
        "s_waitcnt lgkmcnt(10)\n"
        "v_mfma_f32_16x16x32_bf16 v[80:83], a[8:11], v[112:115], v[80:83]\n"
        "ds_read_b128 v[152:155], v1 offset:14336\n"
        "v_mfma_f32_16x16x32_bf16 v[88:91], a[88:91], v[112:115], v[88:91]\n"
        "ds_read_b128 v[156:159], v1 offset:15360\n"
        "v_mfma_f32_16x16x32_bf16 v[84:87], a[8:11], v[116:119], v[84:87]\n"
        "ds_read_b128 v[160:163], v1 offset:16384\n"
        "v_mfma_f32_16x16x32_bf16 v[92:95], a[88:91], v[116:119], v[92:95]\n"
        "ds_read_b128 v[164:167], v1 offset:17408\n"
        "s_waitcnt lgkmcnt(12)\n"
        "v_mfma_f32_16x16x32_bf16 v[80:83], a[12:15], v[120:123], v[80:83]\n"
        "ds_read_b128 v[168:171], v1 offset:18432\n"
        "v_mfma_f32_16x16x32_bf16 v[88:91], a[92:95], v[120:123], v[88:91]\n"
        "ds_read_b128 v[172:175], v1 offset:19456\n"
        "v_mfma_f32_16x16x32_bf16 v[84:87], a[12:15], v[124:127], v[84:87]\n"
        "ds_read_b128 v[176:179], v1 offset:20480\n"
        "v_mfma_f32_16x16x32_bf16 v[92:95], a[92:95], v[124:127], v[92:95]\n"
        "ds_read_b128 v[180:183], v1 offset:21504\n"
        "s_waitcnt lgkmcnt(14)\n"
        "v_mfma_f32_16x16x32_bf16 v[80:83], a[16:19], v[128:131], v[80:83]\n"
        "ds_read_b128 v[184:187], v1 offset:22528\n"
        "s_waitcnt lgkmcnt(10)\n"
        "v_mfma_f32_16x16x32_bf16 v[88:91], a[96:99], v[128:131], v[88:91]\n"
        "ds_read_b128 v[188:191], v1 offset:23552\n"
        "v_add_f32 v40, v40, v48\n"
        "v_add_f32 v41, v41, v49\n"
        "v_mfma_f32_16x16x32_bf16 v[84:87], a[16:19], v[132:135], v[84:87]\n"
        "ds_read_b128 v[192:195], v1 offset:24576\n"
        "v_mov_b32 v42, v40\n"
        "v_mov_b32 v43, v41\n"
        "v_mfma_f32_16x16x32_bf16 v[92:95], a[96:99], v[132:135], v[92:95]\n"
        "ds_read_b128 v[196:199], v1 offset:25600\n"
        "v_permlane32_swap_b32 v40, v42\n"
        "s_waitcnt lgkmcnt(15)\n"
        "v_mfma_f32_16x16x32_bf16 v[80:83], a[20:23], v[136:139], v[80:83]\n"
        "ds_read_b128 v[200:203], v1 offset:26624\n"
        "v_permlane32_swap_b32 v41, v43\n"
        "v_mfma_f32_16x16x32_bf16 v[88:91], a[100:103], v[136:139], v[88:91]\n"
        "ds_read_b128 v[204:207], v1 offset:27648\n"
        "v_add_f32 v40, v40, v42\n"
        "v_add_f32 v41, v41, v43\n"
        "v_mfma_f32_16x16x32_bf16 v[84:87], a[20:23], v[140:143], v[84:87]\n"
        "ds_read_b128 v[208:211], v1 offset:28672\n"
        "v_mul_f32 v40, 0x3acccccd, v40\n"
        "v_mul_f32 v41, 0x3acccccd, v41\n"
        "v_mfma_f32_16x16x32_bf16 v[92:95], a[100:103], v[140:143], v[92:95]\n"
        "ds_read_b128 v[212:215], v1 offset:29696\n"
        "v_fma_f32 v41, -v40, v40, v41\n"
        "v_max_f32 v41, 0, v41\n"
        "s_waitcnt lgkmcnt(15)\n"
        "v_mfma_f32_16x16x32_bf16 v[80:83], a[24:27], v[144:147], v[80:83]\n"
        "ds_read_b128 v[216:219], v1 offset:30720\n"
        "v_add_f32 v41, 0x3727c5ac, v41\n"
        "v_mfma_f32_16x16x32_bf16 v[88:91], a[104:107], v[144:147], v[88:91]\n"
        "ds_read_b128 v[220:223], v1 offset:31744\n"
        "v_rsq_f32 v41, v41\n"
        "v_mfma_f32_16x16x32_bf16 v[84:87], a[24:27], v[148:151], v[84:87]\n"
        "ds_read_b128 v[224:227], v1 offset:32768\n"
        "v_mul_f32 v40, v40, v41\n"
        "ds_bpermute_b32 v56, v7, v41 offset:0\n"
        "v_mfma_f32_16x16x32_bf16 v[92:95], a[104:107], v[148:151], v[92:95]\n"
        "ds_read_b128 v[228:231], v1 offset:33792\n"
        "ds_bpermute_b32 v57, v7, v40 offset:0\n"
        "s_waitcnt lgkmcnt(15)\n"
        "v_mfma_f32_16x16x32_bf16 v[80:83], a[28:31], v[152:155], v[80:83]\n"
        "ds_read_b128 v[232:235], v1 offset:34816\n"
        "ds_bpermute_b32 v58, v7, v41 offset:64\n"
        "v_mfma_f32_16x16x32_bf16 v[88:91], a[108:111], v[152:155], v[88:91]\n"
        "ds_read_b128 v[236:239], v1 offset:35840\n"
        "ds_bpermute_b32 v59, v7, v40 offset:64\n"
        "v_mfma_f32_16x16x32_bf16 v[84:87], a[28:31], v[156:159], v[84:87]\n"
        "ds_read_b128 v[240:243], v1 offset:36864\n"
        "v_mfma_f32_16x16x32_bf16 v[92:95], a[108:111], v[156:159], v[92:95]\n"
        "ds_read_b128 v[244:247], v1 offset:37888\n"
        "s_waitcnt lgkmcnt(15)\n"
        "v_mfma_f32_16x16x32_bf16 v[80:83], a[32:35], v[160:163], v[80:83]\n"
        "ds_read_b128 v[248:251], v1 offset:38912\n"
        "v_mfma_f32_16x16x32_bf16 v[88:91], a[112:115], v[160:163], v[88:91]\n"
        "ds_read_b128 v[252:255], v1 offset:39936\n"
        "v_mfma_f32_16x16x32_bf16 v[84:87], a[32:35], v[164:167], v[84:87]\n"
        "v_mfma_f32_16x16x32_bf16 v[92:95], a[112:115], v[164:167], v[92:95]\n"
        "s_waitcnt lgkmcnt(15)\n"
        "v_mfma_f32_16x16x32_bf16 v[80:83], a[36:39], v[168:171], v[80:83]\n"
        "v_mfma_f32_16x16x32_bf16 v[88:91], a[116:119], v[168:171], v[88:91]\n"
        "v_mfma_f32_16x16x32_bf16 v[84:87], a[36:39], v[172:175], v[84:87]\n"
        "v_mfma_f32_16x16x32_bf16 v[92:95], a[116:119], v[172:175], v[92:95]\n"
        "s_waitcnt lgkmcnt(15)\n"
        "v_mfma_f32_16x16x32_bf16 v[80:83], a[40:43], v[176:179], v[80:83]\n"
        "v_mfma_f32_16x16x32_bf16 v[88:91], a[120:123], v[176:179], v[88:91]\n"
        "v_mfma_f32_16x16x32_bf16 v[84:87], a[40:43], v[180:183], v[84:87]\n"
        "v_mfma_f32_16x16x32_bf16 v[92:95], a[120:123], v[180:183], v[92:95]\n"
        "s_waitcnt lgkmcnt(15)\n"
        "v_mfma_f32_16x16x32_bf16 v[80:83], a[44:47], v[184:187], v[80:83]\n"
        "v_mfma_f32_16x16x32_bf16 v[88:91], a[124:127], v[184:187], v[88:91]\n"
        "v_mfma_f32_16x16x32_bf16 v[84:87], a[44:47], v[188:191], v[84:87]\n"
        "v_mfma_f32_16x16x32_bf16 v[92:95], a[124:127], v[188:191], v[92:95]\n"
        "s_waitcnt lgkmcnt(15)\n"
        "v_mfma_f32_16x16x32_bf16 v[80:83], a[48:51], v[192:195], v[80:83]\n"
        "v_mfma_f32_16x16x32_bf16 v[88:91], a[128:131], v[192:195], v[88:91]\n"
        "v_mfma_f32_16x16x32_bf16 v[84:87], a[48:51], v[196:199], v[84:87]\n"
        "v_mfma_f32_16x16x32_bf16 v[92:95], a[128:131], v[196:199], v[92:95]\n"
        "s_waitcnt lgkmcnt(15)\n"
        "v_mfma_f32_16x16x32_bf16 v[80:83], a[52:55], v[200:203], v[80:83]\n"
        "v_mfma_f32_16x16x32_bf16 v[88:91], a[132:135], v[200:203], v[88:91]\n"
        "v_mfma_f32_16x16x32_bf16 v[84:87], a[52:55], v[204:207], v[84:87]\n"
        "v_mfma_f32_16x16x32_bf16 v[92:95], a[132:135], v[204:207], v[92:95]\n"
        "s_waitcnt lgkmcnt(14)\n"
        "v_mfma_f32_16x16x32_bf16 v[80:83], a[56:59], v[208:211], v[80:83]\n"
        "v_mfma_f32_16x16x32_bf16 v[88:91], a[136:139], v[208:211], v[88:91]\n"
        "v_mfma_f32_16x16x32_bf16 v[84:87], a[56:59], v[212:215], v[84:87]\n"
        "v_mfma_f32_16x16x32_bf16 v[92:95], a[136:139], v[212:215], v[92:95]\n"
        "s_waitcnt lgkmcnt(12)\n"
        "v_mfma_f32_16x16x32_bf16 v[80:83], a[60:63], v[216:219], v[80:83]\n"
        "v_mfma_f32_16x16x32_bf16 v[88:91], a[140:143], v[216:219], v[88:91]\n"
        "v_mfma_f32_16x16x32_bf16 v[84:87], a[60:63], v[220:223], v[84:87]\n"
        "v_mfma_f32_16x16x32_bf16 v[92:95], a[140:143], v[220:223], v[92:95]\n"
        "s_waitcnt lgkmcnt(9)\n"
        "v_mfma_f32_16x16x32_bf16 v[80:83], a[64:67], v[224:227], v[80:83]\n"
        "v_mfma_f32_16x16x32_bf16 v[88:91], a[144:147], v[224:227], v[88:91]\n"
        "v_mfma_f32_16x16x32_bf16 v[84:87], a[64:67], v[228:231], v[84:87]\n"
        "v_mfma_f32_16x16x32_bf16 v[92:95], a[144:147], v[228:231], v[92:95]\n"
        "s_waitcnt lgkmcnt(5)\n"
        "v_mfma_f32_16x16x32_bf16 v[80:83], a[68:71], v[232:235], v[80:83]\n"
        "v_mfma_f32_16x16x32_bf16 v[88:91], a[148:151], v[232:235], v[88:91]\n"
        "v_mfma_f32_16x16x32_bf16 v[84:87], a[68:71], v[236:239], v[84:87]\n"
        "v_mfma_f32_16x16x32_bf16 v[92:95], a[148:151], v[236:239], v[92:95]\n"
        "s_waitcnt lgkmcnt(2)\n"
        "v_mfma_f32_16x16x32_bf16 v[80:83], a[72:75], v[240:243], v[80:83]\n"
        "v_mfma_f32_16x16x32_bf16 v[88:91], a[152:155], v[240:243], v[88:91]\n"
        "v_mfma_f32_16x16x32_bf16 v[84:87], a[72:75], v[244:247], v[84:87]\n"
        "v_mfma_f32_16x16x32_bf16 v[92:95], a[152:155], v[244:247], v[92:95]\n"
        "s_waitcnt lgkmcnt(0)\n"
        "v_mfma_f32_16x16x32_bf16 v[80:83], a[76:79], v[248:251], v[80:83]\n"
        "v_mfma_f32_16x16x32_bf16 v[88:91], a[156:159], v[248:251], v[88:91]\n"
        "v_mfma_f32_16x16x32_bf16 v[84:87], a[76:79], v[252:255], v[84:87]\n"
        "v_mfma_f32_16x16x32_bf16 v[92:95], a[156:159], v[252:255], v[92:95]\n"
        "s_waitcnt lgkmcnt(0)\n"
        "v_mul_f32 v112, v57, v28\n"
        "v_mul_f32 v113, v57, v29\n"
        "v_mul_f32 v114, v57, v30\n"
        "v_mul_f32 v115, v57, v31\n"
        "v_mul_f32 v116, v59, v28\n"
        "v_mul_f32 v117, v59, v29\n"
        "v_mul_f32 v118, v59, v30\n"
        "v_mul_f32 v119, v59, v31\n"
        "v_mul_f32 v120, v57, v32\n"
        "v_mul_f32 v121, v57, v33\n"
        "v_mul_f32 v122, v57, v34\n"
        "v_mul_f32 v123, v57, v35\n"
        "v_mul_f32 v124, v59, v32\n"
        "v_mul_f32 v125, v59, v33\n"
        "v_mul_f32 v126, v59, v34\n"
        "v_mul_f32 v127, v59, v35\n"
        "v_fma_f32 v96, v80, v56, -v112\n"
        "v_fma_f32 v97, v81, v56, -v113\n"
        "v_fma_f32 v98, v82, v56, -v114\n"
        "v_fma_f32 v99, v83, v56, -v115\n"
        "v_fma_f32 v100, v84, v58, -v116\n"
        "v_fma_f32 v101, v85, v58, -v117\n"
        "v_fma_f32 v102, v86, v58, -v118\n"
        "v_fma_f32 v103, v87, v58, -v119\n"
        "v_fma_f32 v104, v88, v56, -v120\n"
        "v_fma_f32 v105, v89, v56, -v121\n"
        "v_fma_f32 v106, v90, v56, -v122\n"
        "v_fma_f32 v107, v91, v56, -v123\n"
        "v_fma_f32 v108, v92, v58, -v124\n"
        "v_fma_f32 v109, v93, v58, -v125\n"
        "v_fma_f32 v110, v94, v58, -v126\n"
        "v_fma_f32 v111, v95, v58, -v127\n"
        "v_mul_f32 v112, v96, v96\n"
        "v_mul_f32 v113, v97, v97\n"
        "v_mul_f32 v114, v98, v98\n"
        "v_mul_f32 v115, v99, v99\n"
        "v_mul_f32 v116, v100, v100\n"
        "v_mul_f32 v117, v101, v101\n"
        "v_mul_f32 v118, v102, v102\n"
        "v_mul_f32 v119, v103, v103\n"
        "v_mul_f32 v120, v104, v104\n"
        "v_mul_f32 v121, v105, v105\n"
        "v_mul_f32 v122, v106, v106\n"
        "v_mul_f32 v123, v107, v107\n"
        "v_mul_f32 v124, v108, v108\n"
        "v_mul_f32 v125, v109, v109\n"
        "v_mul_f32 v126, v110, v110\n"
        "v_mul_f32 v127, v111, v111\n"
        "v_fma_f32 v112, v112, s61, v23\n"
        "v_fma_f32 v113, v113, s61, v23\n"
        "v_fma_f32 v114, v114, s61, v23\n"
        "v_fma_f32 v115, v115, s61, v23\n"
        "v_fma_f32 v116, v116, s61, v23\n"
        "v_fma_f32 v117, v117, s61, v23\n"
        "v_fma_f32 v118, v118, s61, v23\n"
        "v_fma_f32 v119, v119, s61, v23\n"
        "v_fma_f32 v120, v120, s61, v23\n"
        "v_fma_f32 v121, v121, s61, v23\n"
        "v_fma_f32 v122, v122, s61, v23\n"
        "v_fma_f32 v123, v123, s61, v23\n"
        "v_fma_f32 v124, v124, s61, v23\n"
        "v_fma_f32 v125, v125, s61, v23\n"
        "v_fma_f32 v126, v126, s61, v23\n"
        "v_fma_f32 v127, v127, s61, v23\n"
        "v_mul_f32 v112, v112, v96\n"
        "v_mul_f32 v113, v113, v97\n"
        "v_mul_f32 v114, v114, v98\n"
        "v_mul_f32 v115, v115, v99\n"
        "v_mul_f32 v116, v116, v100\n"
        "v_mul_f32 v117, v117, v101\n"
        "v_mul_f32 v118, v118, v102\n"
        "v_mul_f32 v119, v119, v103\n"
        "v_mul_f32 v120, v120, v104\n"
        "v_mul_f32 v121, v121, v105\n"
        "v_mul_f32 v122, v122, v106\n"
        "v_mul_f32 v123, v123, v107\n"
        "v_mul_f32 v124, v124, v108\n"
        "v_mul_f32 v125, v125, v109\n"
        "v_mul_f32 v126, v126, v110\n"
        "v_mul_f32 v127, v127, v111\n"
        "v_exp_f32 v112, v112\n"
        "v_exp_f32 v113, v113\n"
        "v_exp_f32 v114, v114\n"
        "v_exp_f32 v115, v115\n"
        "v_exp_f32 v116, v116\n"
        "v_exp_f32 v117, v117\n"
        "v_exp_f32 v118, v118\n"
        "v_exp_f32 v119, v119\n"
        "v_exp_f32 v120, v120\n"
        "v_exp_f32 v121, v121\n"
        "v_exp_f32 v122, v122\n"
        "v_exp_f32 v123, v123\n"
        "v_exp_f32 v124, v124\n"
        "v_exp_f32 v125, v125\n"
        "v_exp_f32 v126, v126\n"
        "v_exp_f32 v127, v127\n"
        "v_add_f32 v112, 1.0, v112\n"
        "v_add_f32 v113, 1.0, v113\n"
        "v_add_f32 v114, 1.0, v114\n"
        "v_add_f32 v115, 1.0, v115\n"
        "v_add_f32 v116, 1.0, v116\n"
        "v_add_f32 v117, 1.0, v117\n"
        "v_add_f32 v118, 1.0, v118\n"
        "v_add_f32 v119, 1.0, v119\n"
        "v_add_f32 v120, 1.0, v120\n"
        "v_add_f32 v121, 1.0, v121\n"
        "v_add_f32 v122, 1.0, v122\n"
        "v_add_f32 v123, 1.0, v123\n"
        "v_add_f32 v124, 1.0, v124\n"
        "v_add_f32 v125, 1.0, v125\n"
        "v_add_f32 v126, 1.0, v126\n"
        "v_add_f32 v127, 1.0, v127\n"
        "v_rcp_f32 v112, v112\n"
        "v_rcp_f32 v113, v113\n"
        "v_rcp_f32 v114, v114\n"
        "v_rcp_f32 v115, v115\n"
        "v_rcp_f32 v116, v116\n"
        "v_rcp_f32 v117, v117\n"
        "v_rcp_f32 v118, v118\n"
        "v_rcp_f32 v119, v119\n"
        "v_rcp_f32 v120, v120\n"
        "v_rcp_f32 v121, v121\n"
        "v_rcp_f32 v122, v122\n"
        "v_rcp_f32 v123, v123\n"
        "v_rcp_f32 v124, v124\n"
        "v_rcp_f32 v125, v125\n"
        "v_rcp_f32 v126, v126\n"
        "v_rcp_f32 v127, v127\n"
        "v_fma_f32 v96, -v96, v112, v96\n"
        "v_fma_f32 v97, -v97, v113, v97\n"
        "v_fma_f32 v98, -v98, v114, v98\n"
        "v_fma_f32 v99, -v99, v115, v99\n"
        "v_fma_f32 v100, -v100, v116, v100\n"
        "v_fma_f32 v101, -v101, v117, v101\n"
        "v_fma_f32 v102, -v102, v118, v102\n"
        "v_fma_f32 v103, -v103, v119, v103\n"
        "v_fma_f32 v104, -v104, v120, v104\n"
        "v_fma_f32 v105, -v105, v121, v105\n"
        "v_fma_f32 v106, -v106, v122, v106\n"
        "v_fma_f32 v107, -v107, v123, v107\n"
        "v_fma_f32 v108, -v108, v124, v108\n"
        "v_fma_f32 v109, -v109, v125, v109\n"
        "v_fma_f32 v110, -v110, v126, v110\n"
        "v_fma_f32 v111, -v111, v127, v111\n"
        "v_cvt_pk_bf16_f32 v80, v96, v97\n"
        "v_cvt_pk_bf16_f32 v81, v98, v99\n"
        "v_cvt_pk_bf16_f32 v82, v104, v105\n"
        "v_cvt_pk_bf16_f32 v83, v106, v107\n"
        "global_store_dwordx4 v8, v[80:83], s[52:53] offset:0 sc1\n"
        "v_cvt_pk_bf16_f32 v84, v100, v101\n"
        "v_cvt_pk_bf16_f32 v85, v102, v103\n"
        "v_cvt_pk_bf16_f32 v86, v108, v109\n"
        "v_cvt_pk_bf16_f32 v87, v110, v111\n"
        "global_store_dwordx4 v8, v[84:87], s[52:53] offset:1024 sc1\n"
        "s_waitcnt vmcnt(0)\n"
        "s_barrier\n"
        "s_mov_b64 exec, s[62:63]\n"
        "global_store_dword v9, v22, s[90:91] sc1\n"
        "s_mov_b64 exec, -1\n"
        "s_add_u32 s45, s45, 2\n"
        "s_cmp_lt_u32 s45, 64\n"
        "s_cbranch_scc1 Lg1a_loop\n"
        "Lg1a_exit:\n"
        "s_waitcnt vmcnt(0) lgkmcnt(0)\n"
        "s_endpgm\n"
            :: "s"(ws), "s"(w_in), "s"(w_in), "s"(x), "s"(r - 104), "s"(par), "s"(xq), "s"(wave), "s"(karg) : "v0", "v1", "v2", "v3", "v4", "v5", "v6", "v7", "v8", "v9", "v10", "v11", "v12", "v13", "v14", "v15", "v16", "v17", "v18", "v19", "v20", "v21", "v22", "v23", "v24", "v25", "v26", "v27", "v28", "v29", "v30", "v31", "v32", "v33", "v34", "v35", "v36", "v37", "v38", "v39", "v40", "v41", "v42", "v43", "v44", "v45", "v46", "v47", "v48", "v49", "v50", "v51", "v52", "v53", "v54", "v55", "v56", "v57", "v58", "v59", "v60", "v61", "v62", "v63", "v64", "v65", "v66", "v67", "v68", "v69", "v70", "v71", "v72", "v73", "v74", "v75", "v76", "v77", "v78", "v79", "v80", "v81", "v82", "v83", "v84", "v85", "v86", "v87", "v88", "v89", "v90", "v91", "v92", "v93", "v94", "v95", "v96", "v97", "v98", "v99", "v100", "v101", "v102", "v103", "v104", "v105", "v106", "v107", "v108", "v109", "v110", "v111", "v112", "v113", "v114", "v115", "v116", "v117", "v118", "v119", "v120", "v121", "v122", "v123", "v124", "v125", "v126", "v127", "v128", "v129", "v130", "v131", "v132", "v133", "v134", "v135", "v136", "v137", "v138", "v139", "v140", "v141", "v142", "v143", "v144", "v145", "v146", "v147", "v148", "v149", "v150", "v151", "v152", "v153", "v154", "v155", "v156", "v157", "v158", "v159", "v160", "v161", "v162", "v163", "v164", "v165", "v166", "v167", "v168", "v169", "v170", "v171", "v172", "v173", "v174", "v175", "v176", "v177", "v178", "v179", "v180", "v181", "v182", "v183", "v184", "v185", "v186", "v187", "v188", "v189", "v190", "v191", "v192", "v193", "v194", "v195", "v196", "v197", "v198", "v199", "v200", "v201", "v202", "v203", "v204", "v205", "v206", "v207", "v208", "v209", "v210", "v211", "v212", "v213", "v214", "v215", "v216", "v217", "v218", "v219", "v220", "v221", "v222", "v223", "v224", "v225", "v226", "v227", "v228", "v229", "v230", "v231", "v232", "v233", "v234", "v235", "v236", "v237", "v238", "v239", "v240", "v241", "v242", "v243", "v244", "v245", "v246", "v247", "v248", "v249", "v250", "v251", "v252", "v253", "v254", "v255", "a0", "a1", "a2", "a3", "a4", "a5", "a6", "a7", "a8", "a9", "a10", "a11", "a12", "a13", "a14", "a15", "a16", "a17", "a18", "a19", "a20", "a21", "a22", "a23", "a24", "a25", "a26", "a27", "a28", "a29", "a30", "a31", "a32", "a33", "a34", "a35", "a36", "a37", "a38", "a39", "a40", "a41", "a42", "a43", "a44", "a45", "a46", "a47", "a48", "a49", "a50", "a51", "a52", "a53", "a54", "a55", "a56", "a57", "a58", "a59", "a60", "a61", "a62", "a63", "a64", "a65", "a66", "a67", "a68", "a69", "a70", "a71", "a72", "a73", "a74", "a75", "a76", "a77", "a78", "a79", "a80", "a81", "a82", "a83", "a84", "a85", "a86", "a87", "a88", "a89", "a90", "a91", "a92", "a93", "a94", "a95", "a96", "a97", "a98", "a99", "a100", "a101", "a102", "a103", "a104", "a105", "a106", "a107", "a108", "a109", "a110", "a111", "a112", "a113", "a114", "a115", "a116", "a117", "a118", "a119", "a120", "a121", "a122", "a123", "a124", "a125", "a126", "a127", "a128", "a129", "a130", "a131", "a132", "a133", "a134", "a135", "a136", "a137", "a138", "a139", "a140", "a141", "a142", "a143", "a144", "a145", "a146", "a147", "a148", "a149", "a150", "a151", "a152", "a153", "a154", "a155", "a156", "a157", "a158", "a159", "a160", "a161", "a162", "a163", "a164", "a165", "a166", "a167", "a168", "a169", "a170", "a171", "a172", "a173", "a174", "a175", "a176", "a177", "a178", "a179", "a180", "a181", "a182", "a183", "a184", "a185", "a186", "a187", "a188", "a189", "a190", "a191", "a192", "a193", "a194", "a195", "a196", "a197", "a198", "a199", "a200", "a201", "a202", "a203", "a204", "a205", "a206", "a207", "a208", "a209", "a210", "a211", "a212", "a213", "a214", "a215", "a216", "a217", "a218", "a219", "a220", "a221", "a222", "a223", "a224", "a225", "a226", "a227", "a228", "a229", "a230", "a231", "a232", "a233", "a234", "a235", "a236", "a237", "a238", "a239", "a240", "a241", "a242", "a243", "a244", "a245", "a246", "a247", "a248", "a249", "a250", "a251", "a252", "a253", "a254", "a255", "s34", "s35", "s36", "s37", "s38", "s39", "s40", "s41", "s42", "s43", "s44", "s45", "s46", "s47", "s48", "s49", "s50", "s51", "s52", "s53", "s54", "s55", "s56", "s57", "s58", "s59", "s60", "s61", "s62", "s63", "s64", "s65", "s66", "s67", "s68", "s69", "s70", "s71", "s72", "s73", "s74", "s75", "s76", "s77", "s78", "s79", "s80", "s81", "s82", "s83", "s84", "s85", "s86", "s87", "s88", "s89", "s90", "s91", "s92", "s93", "s94", "s95", "s96", "s97", "s98", "s99", "vcc", "scc", "memory");
        __builtin_amdgcn_endpgm();
    } else if (r < 116) {
        asm volatile(
        "s_mov_b64 s[48:49], %8\n"
        "s_mov_b64 s[90:91], %0\n"
        "s_mov_b64 s[92:93], %1\n"
        "s_mov_b64 s[94:95], %2\n"
        "s_mov_b64 s[96:97], %3\n"
        "s_mov_b32 s40, %4\n"
        "s_mov_b32 s41, %5\n"
        "s_mov_b32 s42, %6\n"
        "s_mov_b32 s43, %7\n"
        "s_mov_b64 s[0:1], s[48:49]\n"
        "s_mov_b64 exec, -1\n"
        "s_mov_b32 s45, s41\n"
        "s_mov_b32 s46, 0x200000\n"
        "v_mbcnt_lo_u32_b32 v0, -1, 0\n"
        "v_mbcnt_hi_u32_b32 v0, -1, v0\n"
        "v_lshlrev_b32 v1, 4, v0\n"
        "v_lshlrev_b32 v24, 3, v0\n"
        "v_add_u32 v24, 0xaa0000, v24\n"
        "global_load_dwordx2 v[26:27], v24, s[90:91] sc1\n"
        "s_waitcnt vmcnt(0)\n"
        "v_and_b32 v26, 1, v26\n"
        "v_and_b32 v27, 1, v27\n"
        "v_cmp_ne_u32 s[50:51], 0, v26\n"
        "v_cmp_ne_u32 s[52:53], 0, v27\n"
        "s_nop 3\n"
        "s_bcnt1_i32_b64 s47, s[50:51]\n"
        "s_bcnt1_i32_b64 s68, s[52:53]\n"
        "s_add_u32 s47, s47, s68\n"
        "s_cmp_gt_u32 s47, 64\n"
        "s_cselect_b32 s47, 1, 0\n"
        "s_xor_b32 s47, s47, s41\n"
        "s_mul_i32 s89, s47, 0x3f\n"
        "s_xor_b32 s89, s89, 52\n"
        "s_lshr_b32 s44, s89, 0\n"
        "s_and_b32 s44, s44, 1\n"
        "s_lshl_b32 s44, s44, 13\n"
        "s_sleep 127\n"
        "s_sleep 127\n"
        "s_sleep 127\n"
        "v_lshrrev_b32 v124, 3, v0\n"
        "s_lshl_b32 s47, s43, 3\n"
        "v_add_u32 v124, s47, v124\n"
        "v_bfe_u32 v125, v0, 4, 1\n"
        "s_and_b32 s47, s43, 1\n"
        "s_lshl_b32 s47, s47, 1\n"
        "v_or_b32 v125, s47, v125\n"
        "v_lshlrev_b32 v125, 1, v125\n"
        "v_and_b32 v126, 7, v0\n"
        "v_xor_b32 v125, v125, v126\n"
        "v_lshlrev_b32 v125, 4, v125\n"
        "s_mul_i32 s47, s40, 0x100\n"
        "v_mul_u32_u24 v126, 0xc00, v124\n"
        "v_add3_u32 v13, v126, v125, s47\n"
        "v_lshrrev_b32 v124, 4, v0\n"
        "v_lshlrev_b32 v124, 10, v124\n"
        "v_bfe_u32 v125, v0, 2, 2\n"
        "v_lshl_add_u32 v124, v125, 7, v124\n"
        "v_bfe_u32 v125, v0, 3, 2\n"
        "v_lshl_add_u32 v124, v125, 5, v124\n"
        "v_and_b32 v125, 3, v0\n"
        "v_lshl_add_u32 v124, v125, 3, v124\n"
        "v_lshrrev_b32 v124, 4, v0\n"
        "v_lshlrev_b32 v124, 10, v124\n"
        "v_bfe_u32 v125, v0, 2, 2\n"
        "v_lshl_add_u32 v124, v125, 7, v124\n"
        "v_bfe_u32 v125, v0, 3, 2\n"
        "v_bfe_u32 v126, v0, 1, 1\n"
        "s_and_b32 s47, s43, 1\n"
        "s_lshl_b32 s47, s47, 1\n"
        "v_or_b32 v126, s47, v126\n"
        "v_xor_b32 v125, v125, v126\n"
        "v_lshl_add_u32 v124, v125, 5, v124\n"
        "v_and_b32 v125, 1, v0\n"
        "v_lshl_add_u32 v124, v125, 4, v124\n"
        "s_lshr_b32 s47, s43, 1\n"
        "s_lshl_b32 s47, s47, 12\n"
        "v_add_u32 v14, s47, v124\n"
        "v_add_u32 v15, 8, v14\n"
        "s_lshl_b32 s59, s43, 10\n"
        "s_add_u32 s54, s92, 0x0\n"
        "s_addc_u32 s55, s93, 0\n"
        "s_add_u32 m0, s59, 0xa000\n"
        "s_nop 0\n"
        "global_load_lds_dwordx4 v13, s[54:55] offset:0\n"
        "s_add_u32 m0, s59, 0xaf80\n"
        "s_nop 0\n"
        "global_load_lds_dwordx4 v13, s[54:55] offset:128\n"
        "s_add_u32 s54, s92, 0x18000\n"
        "s_addc_u32 s55, s93, 0\n"
        "s_add_u32 m0, s59, 0xc000\n"
        "s_nop 0\n"
        "global_load_lds_dwordx4 v13, s[54:55] offset:0\n"
        "s_add_u32 m0, s59, 0xcf80\n"
        "s_nop 0\n"
        "global_load_lds_dwordx4 v13, s[54:55] offset:128\n"
        "s_add_u32 s54, s92, 0x30000\n"
        "s_addc_u32 s55, s93, 0\n"
        "s_add_u32 m0, s59, 0xe000\n"
        "s_nop 0\n"
        "global_load_lds_dwordx4 v13, s[54:55] offset:0\n"
        "s_add_u32 m0, s59, 0xef80\n"
        "s_nop 0\n"
        "global_load_lds_dwordx4 v13, s[54:55] offset:128\n"
        "s_add_u32 s54, s92, 0x48000\n"
        "s_addc_u32 s55, s93, 0\n"
        "s_add_u32 m0, s59, 0x10000\n"
        "s_nop 0\n"
        "global_load_lds_dwordx4 v13, s[54:55] offset:0\n"
        "s_add_u32 m0, s59, 0x10f80\n"
        "s_nop 0\n"
        "global_load_lds_dwordx4 v13, s[54:55] offset:128\n"
        "s_add_u32 s54, s92, 0x60000\n"
        "s_addc_u32 s55, s93, 0\n"
        "s_add_u32 m0, s59, 0x12000\n"
        "s_nop 0\n"
        "global_load_lds_dwordx4 v13, s[54:55] offset:0\n"
        "s_add_u32 m0, s59, 0x12f80\n"
        "s_nop 0\n"
        "global_load_lds_dwordx4 v13, s[54:55] offset:128\n"
        "s_add_u32 s54, s92, 0x78000\n"
        "s_addc_u32 s55, s93, 0\n"
        "s_add_u32 m0, s59, 0x14000\n"
        "s_nop 0\n"
        "global_load_lds_dwordx4 v13, s[54:55] offset:0\n"
        "s_add_u32 m0, s59, 0x14f80\n"
        "s_nop 0\n"
        "global_load_lds_dwordx4 v13, s[54:55] offset:128\n"
        "s_add_u32 s54, s92, 0x90000\n"
        "s_addc_u32 s55, s93, 0\n"
        "s_add_u32 m0, s59, 0x16000\n"
        "s_nop 0\n"
        "global_load_lds_dwordx4 v13, s[54:55] offset:0\n"
        "s_add_u32 m0, s59, 0x16f80\n"
        "s_nop 0\n"
        "global_load_lds_dwordx4 v13, s[54:55] offset:128\n"
        "s_add_u32 s54, s92, 0xa8000\n"
        "s_addc_u32 s55, s93, 0\n"
        "s_add_u32 m0, s59, 0x18000\n"
        "s_nop 0\n"
        "global_load_lds_dwordx4 v13, s[54:55] offset:0\n"
        "s_add_u32 m0, s59, 0x18f80\n"
        "s_nop 0\n"
        "global_load_lds_dwordx4 v13, s[54:55] offset:128\n"
        "s_add_u32 s54, s92, 0xc0000\n"
        "s_addc_u32 s55, s93, 0\n"
        "s_add_u32 m0, s59, 0x1a000\n"
        "s_nop 0\n"
        "global_load_lds_dwordx4 v13, s[54:55] offset:0\n"
        "s_add_u32 m0, s59, 0x1af80\n"
        "s_nop 0\n"
        "global_load_lds_dwordx4 v13, s[54:55] offset:128\n"
        "s_add_u32 s54, s92, 0xd8000\n"
        "s_addc_u32 s55, s93, 0\n"
        "s_add_u32 m0, s59, 0x1c000\n"
        "s_nop 0\n"
        "global_load_lds_dwordx4 v13, s[54:55] offset:0\n"
        "s_add_u32 m0, s59, 0x1cf80\n"
        "s_nop 0\n"
        "global_load_lds_dwordx4 v13, s[54:55] offset:128\n"
        "s_add_u32 s54, s92, 0xf0000\n"
        "s_addc_u32 s55, s93, 0\n"
        "s_add_u32 m0, s59, 0x1e000\n"
        "s_nop 0\n"
        "global_load_lds_dwordx4 v13, s[54:55] offset:0\n"
        "s_add_u32 m0, s59, 0x1ef80\n"
        "s_nop 0\n"
        "global_load_lds_dwordx4 v13, s[54:55] offset:128\n"
        "s_add_u32 s54, s92, 0x108000\n"
        "s_addc_u32 s55, s93, 0\n"
        "s_add_u32 m0, s59, 0x20000\n"
        "s_nop 0\n"
        "global_load_lds_dwordx4 v13, s[54:55] offset:0\n"
        "s_add_u32 m0, s59, 0x20f80\n"
        "s_nop 0\n"
        "global_load_lds_dwordx4 v13, s[54:55] offset:128\n"
        "s_add_u32 s54, s92, 0x120000\n"
        "s_addc_u32 s55, s93, 0\n"
        "s_add_u32 m0, s59, 0x22000\n"
        "s_nop 0\n"
        "global_load_lds_dwordx4 v13, s[54:55] offset:0\n"
        "s_add_u32 m0, s59, 0x22f80\n"
        "s_nop 0\n"
        "global_load_lds_dwordx4 v13, s[54:55] offset:128\n"
        "s_add_u32 s54, s92, 0x138000\n"
        "s_addc_u32 s55, s93, 0\n"
        "s_add_u32 m0, s59, 0x24000\n"
        "s_nop 0\n"
        "global_load_lds_dwordx4 v13, s[54:55] offset:0\n"
        "s_add_u32 m0, s59, 0x24f80\n"
        "s_nop 0\n"
        "global_load_lds_dwordx4 v13, s[54:55] offset:128\n"
        "s_add_u32 s54, s92, 0x150000\n"
        "s_addc_u32 s55, s93, 0\n"
        "s_add_u32 m0, s59, 0x26000\n"
        "s_nop 0\n"
        "global_load_lds_dwordx4 v13, s[54:55] offset:0\n"
        "s_add_u32 m0, s59, 0x26f80\n"
        "s_nop 0\n"
        "global_load_lds_dwordx4 v13, s[54:55] offset:128\n"
        "s_waitcnt vmcnt(20)\n"
        "s_waitcnt lgkmcnt(0)\n"
        "s_barrier\n"
        "v_add_u32 v20, 0xa000, v14\n"
        "ds_read_b64_tr_b16 a[0:1], v20\n"
        "ds_read_b64_tr_b16 a[2:3], v20 offset:512\n"
        "v_add_u32 v21, 0xa000, v15\n"
        "ds_read_b64_tr_b16 a[80:81], v21\n"
        "ds_read_b64_tr_b16 a[82:83], v21 offset:512\n"
        "v_add_u32 v22, 0xc000, v14\n"
        "ds_read_b64_tr_b16 a[4:5], v22\n"
        "ds_read_b64_tr_b16 a[6:7], v22 offset:512\n"
        "v_add_u32 v20, 0xc000, v15\n"
        "ds_read_b64_tr_b16 a[84:85], v20\n"
        "ds_read_b64_tr_b16 a[86:87], v20 offset:512\n"
        "v_add_u32 v21, 0xe000, v14\n"
        "ds_read_b64_tr_b16 a[8:9], v21\n"
        "ds_read_b64_tr_b16 a[10:11], v21 offset:512\n"
        "v_add_u32 v22, 0xe000, v15\n"
        "ds_read_b64_tr_b16 a[88:89], v22\n"
        "ds_read_b64_tr_b16 a[90:91], v22 offset:512\n"
        "v_add_u32 v20, 0x10000, v14\n"
        "ds_read_b64_tr_b16 a[12:13], v20\n"
        "ds_read_b64_tr_b16 a[14:15], v20 offset:512\n"
        "v_add_u32 v21, 0x10000, v15\n"
        "ds_read_b64_tr_b16 a[92:93], v21\n"
        "ds_read_b64_tr_b16 a[94:95], v21 offset:512\n"
        "v_add_u32 v22, 0x12000, v14\n"
        "ds_read_b64_tr_b16 a[16:17], v22\n"
        "ds_read_b64_tr_b16 a[18:19], v22 offset:512\n"
        "v_add_u32 v20, 0x12000, v15\n"
        "ds_read_b64_tr_b16 a[96:97], v20\n"
        "ds_read_b64_tr_b16 a[98:99], v20 offset:512\n"
        "s_waitcnt vmcnt(10)\n"
        "s_waitcnt lgkmcnt(0)\n"
        "s_barrier\n"
        "s_add_u32 s54, s92, 0x168000\n"
        "s_addc_u32 s55, s93, 0\n"
        "s_add_u32 m0, s59, 0xa000\n"
        "s_nop 0\n"
        "global_load_lds_dwordx4 v13, s[54:55] offset:0\n"
        "s_add_u32 m0, s59, 0xaf80\n"
        "s_nop 0\n"
        "global_load_lds_dwordx4 v13, s[54:55] offset:128\n"
        "s_add_u32 s54, s92, 0x180000\n"
        "s_addc_u32 s55, s93, 0\n"
        "s_add_u32 m0, s59, 0xc000\n"
        "s_nop 0\n"
        "global_load_lds_dwordx4 v13, s[54:55] offset:0\n"
        "s_add_u32 m0, s59, 0xcf80\n"
        "s_nop 0\n"
        "global_load_lds_dwordx4 v13, s[54:55] offset:128\n"
        "s_add_u32 s54, s92, 0x198000\n"
        "s_addc_u32 s55, s93, 0\n"
        "s_add_u32 m0, s59, 0xe000\n"
        "s_nop 0\n"
        "global_load_lds_dwordx4 v13, s[54:55] offset:0\n"
        "s_add_u32 m0, s59, 0xef80\n"
        "s_nop 0\n"
        "global_load_lds_dwordx4 v13, s[54:55] offset:128\n"
        "s_add_u32 s54, s92, 0x1b0000\n"
        "s_addc_u32 s55, s93, 0\n"
        "s_add_u32 m0, s59, 0x10000\n"
        "s_nop 0\n"
        "global_load_lds_dwordx4 v13, s[54:55] offset:0\n"
        "s_add_u32 m0, s59, 0x10f80\n"
        "s_nop 0\n"
        "global_load_lds_dwordx4 v13, s[54:55] offset:128\n"
        "s_add_u32 s54, s92, 0x1c8000\n"
        "s_addc_u32 s55, s93, 0\n"
        "s_add_u32 m0, s59, 0x12000\n"
        "s_nop 0\n"
        "global_load_lds_dwordx4 v13, s[54:55] offset:0\n"
        "s_add_u32 m0, s59, 0x12f80\n"
        "s_nop 0\n"
        "global_load_lds_dwordx4 v13, s[54:55] offset:128\n"
        "v_add_u32 v21, 0x14000, v14\n"
        "ds_read_b64_tr_b16 a[20:21], v21\n"
        "ds_read_b64_tr_b16 a[22:23], v21 offset:512\n"
        "v_add_u32 v22, 0x14000, v15\n"
        "ds_read_b64_tr_b16 a[100:101], v22\n"
        "ds_read_b64_tr_b16 a[102:103], v22 offset:512\n"
        "v_add_u32 v20, 0x16000, v14\n"
        "ds_read_b64_tr_b16 a[24:25], v20\n"
        "ds_read_b64_tr_b16 a[26:27], v20 offset:512\n"
        "v_add_u32 v21, 0x16000, v15\n"
        "ds_read_b64_tr_b16 a[104:105], v21\n"
        "ds_read_b64_tr_b16 a[106:107], v21 offset:512\n"
        "v_add_u32 v22, 0x18000, v14\n"
        "ds_read_b64_tr_b16 a[28:29], v22\n"
        "ds_read_b64_tr_b16 a[30:31], v22 offset:512\n"
        "v_add_u32 v20, 0x18000, v15\n"
        "ds_read_b64_tr_b16 a[108:109], v20\n"
        "ds_read_b64_tr_b16 a[110:111], v20 offset:512\n"
        "v_add_u32 v21, 0x1a000, v14\n"
        "ds_read_b64_tr_b16 a[32:33], v21\n"
        "ds_read_b64_tr_b16 a[34:35], v21 offset:512\n"
        "v_add_u32 v22, 0x1a000, v15\n"
        "ds_read_b64_tr_b16 a[112:113], v22\n"
        "ds_read_b64_tr_b16 a[114:115], v22 offset:512\n"
        "v_add_u32 v20, 0x1c000, v14\n"
        "ds_read_b64_tr_b16 a[36:37], v20\n"
        "ds_read_b64_tr_b16 a[38:39], v20 offset:512\n"
        "v_add_u32 v21, 0x1c000, v15\n"
        "ds_read_b64_tr_b16 a[116:117], v21\n"
        "ds_read_b64_tr_b16 a[118:119], v21 offset:512\n"
        "s_waitcnt vmcnt(10)\n"
        "s_waitcnt lgkmcnt(0)\n"
        "s_barrier\n"
        "v_add_u32 v22, 0x1e000, v14\n"
        "ds_read_b64_tr_b16 a[40:41], v22\n"
        "ds_read_b64_tr_b16 a[42:43], v22 offset:512\n"
        "v_add_u32 v20, 0x1e000, v15\n"
        "ds_read_b64_tr_b16 a[120:121], v20\n"
        "ds_read_b64_tr_b16 a[122:123], v20 offset:512\n"
        "v_add_u32 v21, 0x20000, v14\n"
        "ds_read_b64_tr_b16 a[44:45], v21\n"
        "ds_read_b64_tr_b16 a[46:47], v21 offset:512\n"
        "v_add_u32 v22, 0x20000, v15\n"
        "ds_read_b64_tr_b16 a[124:125], v22\n"
        "ds_read_b64_tr_b16 a[126:127], v22 offset:512\n"
        "v_add_u32 v20, 0x22000, v14\n"
        "ds_read_b64_tr_b16 a[48:49], v20\n"
        "ds_read_b64_tr_b16 a[50:51], v20 offset:512\n"
        "v_add_u32 v21, 0x22000, v15\n"
        "ds_read_b64_tr_b16 a[128:129], v21\n"
        "ds_read_b64_tr_b16 a[130:131], v21 offset:512\n"
        "v_add_u32 v22, 0x24000, v14\n"
        "ds_read_b64_tr_b16 a[52:53], v22\n"
        "ds_read_b64_tr_b16 a[54:55], v22 offset:512\n"
        "v_add_u32 v20, 0x24000, v15\n"
        "ds_read_b64_tr_b16 a[132:133], v20\n"
        "ds_read_b64_tr_b16 a[134:135], v20 offset:512\n"
        "v_add_u32 v21, 0x26000, v14\n"
        "ds_read_b64_tr_b16 a[56:57], v21\n"
        "ds_read_b64_tr_b16 a[58:59], v21 offset:512\n"
        "v_add_u32 v22, 0x26000, v15\n"
        "ds_read_b64_tr_b16 a[136:137], v22\n"
        "ds_read_b64_tr_b16 a[138:139], v22 offset:512\n"
        "s_waitcnt vmcnt(0)\n"
        "s_waitcnt lgkmcnt(0)\n"
        "s_barrier\n"
        "v_add_u32 v20, 0xa000, v14\n"
        "ds_read_b64_tr_b16 a[60:61], v20\n"
        "ds_read_b64_tr_b16 a[62:63], v20 offset:512\n"
        "v_add_u32 v21, 0xa000, v15\n"
        "ds_read_b64_tr_b16 a[140:141], v21\n"
        "ds_read_b64_tr_b16 a[142:143], v21 offset:512\n"
        "v_add_u32 v22, 0xc000, v14\n"
        "ds_read_b64_tr_b16 a[64:65], v22\n"
        "ds_read_b64_tr_b16 a[66:67], v22 offset:512\n"
        "v_add_u32 v20, 0xc000, v15\n"
        "ds_read_b64_tr_b16 a[144:145], v20\n"
        "ds_read_b64_tr_b16 a[146:147], v20 offset:512\n"
        "v_add_u32 v21, 0xe000, v14\n"
        "ds_read_b64_tr_b16 a[68:69], v21\n"
        "ds_read_b64_tr_b16 a[70:71], v21 offset:512\n"
        "v_add_u32 v22, 0xe000, v15\n"
        "ds_read_b64_tr_b16 a[148:149], v22\n"
        "ds_read_b64_tr_b16 a[150:151], v22 offset:512\n"
        "v_add_u32 v20, 0x10000, v14\n"
        "ds_read_b64_tr_b16 a[72:73], v20\n"
        "ds_read_b64_tr_b16 a[74:75], v20 offset:512\n"
        "v_add_u32 v21, 0x10000, v15\n"
        "ds_read_b64_tr_b16 a[152:153], v21\n"
        "ds_read_b64_tr_b16 a[154:155], v21 offset:512\n"
        "v_add_u32 v22, 0x12000, v14\n"
        "ds_read_b64_tr_b16 a[76:77], v22\n"
        "ds_read_b64_tr_b16 a[78:79], v22 offset:512\n"
        "v_add_u32 v20, 0x12000, v15\n"
        "ds_read_b64_tr_b16 a[156:157], v20\n"
        "ds_read_b64_tr_b16 a[158:159], v20 offset:512\n"
        "s_waitcnt lgkmcnt(0)\n"
        "s_barrier\n"
        "s_waitcnt lgkmcnt(0)\n"
        "s_load_dwordx2 s[48:49], s[0:1], 72\n"
        "s_mul_i32 s47, s41, 0xc\n"
        "s_add_u32 s47, s47, s40\n"
        "s_lshl_b32 s47, s47, 2\n"
        "s_add_u32 s47, s47, s43\n"
        "s_mul_i32 s47, s47, 0x2800\n"
        "v_lshl_add_u32 v16, v0, 7, s47\n"
        "v_min_u32 v17, 15, v0\n"
        "v_lshl_add_u32 v17, v17, 7, s47\n"
        "s_waitcnt lgkmcnt(0)\n"
        "global_load_dword v18, v16, s[48:49]\n"
        "v_add_u32 v17, 0x2000, v17\n"
        "global_load_dword v19, v17, s[48:49]\n"
        "s_waitcnt vmcnt(0)\n"
        "v_mov_b32 v24, 0x3f803f80\n"
        "v_mov_b32 v25, 0x3f803f80\n"
        "v_mov_b32 v26, 0x3f803f80\n"
        "v_mov_b32 v27, 0x3f803f80\n"
        "s_nop 1\n"
        "v_mfma_f32_16x16x32_bf16 v[28:31], v[24:27], a[0:3], 0\n"
        "v_mfma_f32_16x16x32_bf16 v[32:35], v[24:27], a[80:83], 0\n"
        "v_mfma_f32_16x16x32_bf16 v[28:31], v[24:27], a[4:7], v[28:31]\n"
        "v_mfma_f32_16x16x32_bf16 v[32:35], v[24:27], a[84:87], v[32:35]\n"
        "v_mfma_f32_16x16x32_bf16 v[28:31], v[24:27], a[8:11], v[28:31]\n"
        "v_mfma_f32_16x16x32_bf16 v[32:35], v[24:27], a[88:91], v[32:35]\n"
        "v_mfma_f32_16x16x32_bf16 v[28:31], v[24:27], a[12:15], v[28:31]\n"
        "v_mfma_f32_16x16x32_bf16 v[32:35], v[24:27], a[92:95], v[32:35]\n"
        "v_mfma_f32_16x16x32_bf16 v[28:31], v[24:27], a[16:19], v[28:31]\n"
        "v_mfma_f32_16x16x32_bf16 v[32:35], v[24:27], a[96:99], v[32:35]\n"
        "v_mfma_f32_16x16x32_bf16 v[28:31], v[24:27], a[20:23], v[28:31]\n"
        "v_mfma_f32_16x16x32_bf16 v[32:35], v[24:27], a[100:103], v[32:35]\n"
        "v_mfma_f32_16x16x32_bf16 v[28:31], v[24:27], a[24:27], v[28:31]\n"
        "v_mfma_f32_16x16x32_bf16 v[32:35], v[24:27], a[104:107], v[32:35]\n"
        "v_mfma_f32_16x16x32_bf16 v[28:31], v[24:27], a[28:31], v[28:31]\n"
        "v_mfma_f32_16x16x32_bf16 v[32:35], v[24:27], a[108:111], v[32:35]\n"
        "v_mfma_f32_16x16x32_bf16 v[28:31], v[24:27], a[32:35], v[28:31]\n"
        "v_mfma_f32_16x16x32_bf16 v[32:35], v[24:27], a[112:115], v[32:35]\n"
        "v_mfma_f32_16x16x32_bf16 v[28:31], v[24:27], a[36:39], v[28:31]\n"
        "v_mfma_f32_16x16x32_bf16 v[32:35], v[24:27], a[116:119], v[32:35]\n"
        "v_mfma_f32_16x16x32_bf16 v[28:31], v[24:27], a[40:43], v[28:31]\n"
        "v_mfma_f32_16x16x32_bf16 v[32:35], v[24:27], a[120:123], v[32:35]\n"
        "v_mfma_f32_16x16x32_bf16 v[28:31], v[24:27], a[44:47], v[28:31]\n"
        "v_mfma_f32_16x16x32_bf16 v[32:35], v[24:27], a[124:127], v[32:35]\n"
        "v_mfma_f32_16x16x32_bf16 v[28:31], v[24:27], a[48:51], v[28:31]\n"
        "v_mfma_f32_16x16x32_bf16 v[32:35], v[24:27], a[128:131], v[32:35]\n"
        "v_mfma_f32_16x16x32_bf16 v[28:31], v[24:27], a[52:55], v[28:31]\n"
        "v_mfma_f32_16x16x32_bf16 v[32:35], v[24:27], a[132:135], v[32:35]\n"
        "v_mfma_f32_16x16x32_bf16 v[28:31], v[24:27], a[56:59], v[28:31]\n"
        "v_mfma_f32_16x16x32_bf16 v[32:35], v[24:27], a[136:139], v[32:35]\n"
        "v_mfma_f32_16x16x32_bf16 v[28:31], v[24:27], a[60:63], v[28:31]\n"
        "v_mfma_f32_16x16x32_bf16 v[32:35], v[24:27], a[140:143], v[32:35]\n"
        "v_mfma_f32_16x16x32_bf16 v[28:31], v[24:27], a[64:67], v[28:31]\n"
        "v_mfma_f32_16x16x32_bf16 v[32:35], v[24:27], a[144:147], v[32:35]\n"
        "v_mfma_f32_16x16x32_bf16 v[28:31], v[24:27], a[68:71], v[28:31]\n"
        "v_mfma_f32_16x16x32_bf16 v[32:35], v[24:27], a[148:151], v[32:35]\n"
        "v_mfma_f32_16x16x32_bf16 v[28:31], v[24:27], a[72:75], v[28:31]\n"
        "v_mfma_f32_16x16x32_bf16 v[32:35], v[24:27], a[152:155], v[32:35]\n"
        "v_mfma_f32_16x16x32_bf16 v[28:31], v[24:27], a[76:79], v[28:31]\n"
        "v_mfma_f32_16x16x32_bf16 v[32:35], v[24:27], a[156:159], v[32:35]\n"
        "v_and_b32 v16, 15, v0\n"
        "v_lshrrev_b32 v17, 4, v0\n"
        "v_min_u32 v18, 9, v0\n"
        "s_mul_i32 s47, s43, 0xa\n"
        "v_add_u32 v18, s47, v18\n"
        "v_lshlrev_b32 v18, 7, v18\n"
        "s_add_u32 s47, s44, 0x1ac800\n"
        "v_add_u32 v2, s47, v18\n"
        "s_mul_i32 s47, s43, 0x2800\n"
        "v_add_u32 v4, s47, v1\n"
        "v_add_u32 v3, 0x1000, v4\n"
        "v_lshlrev_b32 v18, 7, v16\n"
        "s_lshl_b32 s47, s43, 5\n"
        "s_add_u32 s47, s47, 40960\n"
        "v_add_u32 v5, s47, v18\n"
        "v_and_b32 v19, 31, v0\n"
        "v_lshlrev_b32 v18, 7, v19\n"
        "v_lshrrev_b32 v20, 5, v0\n"
        "v_lshl_add_u32 v18, v20, 6, v18\n"
        "v_and_b32 v20, 3, v0\n"
        "v_lshl_add_u32 v18, v20, 2, v18\n"
        "v_add_u32 v6, 40960, v18\n"
        "s_mov_b32 s70, 0xf0000f\n"
        "s_mov_b32 s71, 0xf0000f00\n"
        "v_lshlrev_b32 v7, 4, v17\n"
        "s_lshl_b32 s47, s40, 13\n"
        "s_lshl_b32 s68, s43, 11\n"
        "s_add_u32 s47, s47, s68\n"
        "s_add_u32 s47, s47, 0xdf4000\n"
        "v_add_u32 v8, s47, v1\n"
        "s_lshl_b32 s47, s41, 4\n"
        "s_add_u32 s47, s47, s40\n"
        "s_lshl_b32 s47, s47, 7\n"
        "s_add_u32 s47, s47, 0x1b5000\n"
        "v_lshlrev_b32 v18, 13, v0\n"
        "v_add_u32 v9, s47, v18\n"
        "s_cmp_eq_u32 s43, 0\n"
        "s_cselect_b32 s62, 1, 0\n"
        "s_mov_b32 s63, 0\n"
        "s_mul_i32 s62, s62, 0x3\n"
        "s_memrealtime s[54:55]\n"
        "s_waitcnt lgkmcnt(0)\n"
        "s_add_u32 s34, s54, 0\n"
        "s_lshl_b32 s34, s34, 6\n"
        "s_lshl_b32 s47, s43, 8\n"
        "s_add_u32 s47, s47, 45056\n"
        "v_lshl_add_u32 v10, v0, 2, s47\n"
        "v_lshlrev_b32 v18, 3, v19\n"
        "v_add_u32 v11, 45056, v18\n"
        "s_lshl_b32 s47, s40, 8\n"
        "s_add_u32 s47, s47, 0x16ffa00\n"
        "v_add_u32 v12, s47, v18\n"
        "s_mov_b32 s64, 0x11111111\n"
        "s_mov_b32 s65, 0x11111111\n"
        "s_cmp_eq_u32 s43, 0\n"
        "s_cselect_b32 s66, -1, 0\n"
        "s_mov_b32 s67, 0\n"
        "s_mov_b32 s61, 0x3dd2d3e8\n"
        "v_mov_b32 v23, 0x40135761\n"
        "s_barrier\n"
        "Lg1b_loop:\n"
        "s_lshr_b32 s58, s45, 1\n"
        "s_add_u32 s88, s58, 1\n"
        "s_mul_i32 s68, s45, 0xa000\n"
        "s_add_u32 s47, s68, 0xb00000\n"
        "s_add_u32 s48, s90, s47\n"
        "s_addc_u32 s49, s91, 0\n"
        "s_add_u32 s50, s48, 0x2000\n"
        "s_addc_u32 s51, s49, 0\n"
        "s_mul_i32 s68, s45, 0xc000\n"
        "s_add_u32 s47, s68, 0x0\n"
        "s_add_u32 s52, s90, s47\n"
        "s_addc_u32 s53, s91, 0\n"
        "s_mul_i32 s68, s45, 0x600\n"
        "s_add_u32 s47, s68, 0x0\n"
        "s_add_u32 s56, s90, s47\n"
        "s_addc_u32 s57, s91, 0\n"
        "v_mov_b32 v22, s88\n"
        "s_cmp_ge_u32 s45, 50\n"
        "s_cbranch_scc1 Lg1b_np_1\n"
        "s_mov_b32 s69, 48\n"
        ".p2align 6\n"
        "Lg1b_w_3:\n"
        "s_memrealtime s[54:55]\n"
        "s_waitcnt lgkmcnt(0)\n"
        "s_lshl_b32 s47, s54, 6\n"
        "s_sub_u32 s47, s34, s47\n"
        "s_cmp_lt_i32 s47, 768\n"
        "s_cbranch_scc1 Lg1b_f_4\n"
        "s_sleep 1\n"
        "s_sub_u32 s69, s69, 1\n"
        "s_cbranch_scc0 Lg1b_w_3\n"
        "s_mov_b32 s47, 0\n"
        "Lg1b_f_4:\n"
        "s_max_i32 s47, s47, 0\n"
        "Lg1b_c_5:\n"
        "s_sub_u32 s47, s47, 80\n"
        "s_cbranch_scc0 Lg1b_c_5\n"
        "s_branch Lg1b_p0_7\n"
        "Lg1b_pl_6:\n"
        "s_sleep 16\n"
        "Lg1b_p0_7:\n"
        "global_load_dword v36, v2, s[90:91] sc1\n"
        "s_sub_u32 s46, s46, 1\n"
        "s_cbranch_scc1 Lg1b_exit\n"
        "s_waitcnt vmcnt(0)\n"
        "v_cmp_lt_u32 vcc, v36, v22\n"
        "s_cbranch_vccnz Lg1b_pl_6\n"
        "s_branch Lg1b_nq_2\n"
        "Lg1b_np_1:\n"
        "s_branch Lg1b_p0_9\n"
        "Lg1b_pl_8:\n"
        "s_sleep 1\n"
        "Lg1b_p0_9:\n"
        "global_load_dword v36, v2, s[90:91] sc1\n"
        "s_sub_u32 s46, s46, 1\n"
        "s_cbranch_scc1 Lg1b_exit\n"
        "s_waitcnt vmcnt(0)\n"
        "v_cmp_lt_u32 vcc, v36, v22\n"
        "s_cbranch_vccnz Lg1b_pl_8\n"
        "Lg1b_nq_2:\n"
        "s_memrealtime s[54:55]\n"
        "s_waitcnt lgkmcnt(0)\n"
        "s_add_u32 s34, s54, 400\n"
        "s_lshl_b32 s34, s34, 6\n"
        "s_cmp_ge_u32 s45, 60\n"
        "s_cbranch_scc1 Lg1b_nd_10\n"
        "s_sleep 12\n"
        "Lg1b_nd_10:\n"
        "global_load_dwordx4 v[40:43], v3, s[48:49] offset:-4096 sc1\n"
        "global_load_dwordx4 v[44:47], v3, s[48:49] offset:-3072 sc1\n"
        "global_load_dwordx4 v[48:51], v3, s[48:49] offset:-2048 sc1\n"
        "global_load_dwordx4 v[52:55], v3, s[48:49] offset:-1024 sc1\n"
        "global_load_dwordx4 v[56:59], v3, s[48:49] offset:0 sc1\n"
        "global_load_dwordx4 v[60:63], v3, s[48:49] offset:1024 sc1\n"
        "global_load_dwordx4 v[64:67], v3, s[48:49] offset:2048 sc1\n"
        "global_load_dwordx4 v[68:71], v3, s[48:49] offset:3072 sc1\n"
        "global_load_dwordx4 v[72:75], v3, s[50:51] offset:-4096 sc1\n"
        "global_load_dwordx4 v[76:79], v3, s[50:51] offset:-3072 sc1\n"
        "s_mov_b64 exec, -1\n"
        "s_waitcnt vmcnt(9)\n"
        "v_mfma_f32_16x16x32_bf16 v[80:83], v[24:27], v[40:43], 0\n"
        "ds_write_b128 v4, v[40:43] offset:0\n"
        "v_mfma_f32_16x16x32_bf16 v[84:87], v[40:43], v[40:43], 0\n"
        "s_waitcnt vmcnt(8)\n"
        "v_mfma_f32_16x16x32_bf16 v[88:91], v[24:27], v[44:47], 0\n"
        "ds_write_b128 v4, v[44:47] offset:1024\n"
        "v_mfma_f32_16x16x32_bf16 v[92:95], v[44:47], v[44:47], 0\n"
        "s_waitcnt vmcnt(7)\n"
        "v_mfma_f32_16x16x32_bf16 v[80:83], v[24:27], v[48:51], v[80:83]\n"
        "ds_write_b128 v4, v[48:51] offset:2048\n"
        "v_mfma_f32_16x16x32_bf16 v[84:87], v[48:51], v[48:51], v[84:87]\n"
        "s_waitcnt vmcnt(6)\n"
        "v_mfma_f32_16x16x32_bf16 v[88:91], v[24:27], v[52:55], v[88:91]\n"
        "ds_write_b128 v4, v[52:55] offset:3072\n"
        "v_mfma_f32_16x16x32_bf16 v[92:95], v[52:55], v[52:55], v[92:95]\n"
        "s_waitcnt vmcnt(5)\n"
        "v_mfma_f32_16x16x32_bf16 v[80:83], v[24:27], v[56:59], v[80:83]\n"
        "ds_write_b128 v4, v[56:59] offset:4096\n"
        "v_mfma_f32_16x16x32_bf16 v[84:87], v[56:59], v[56:59], v[84:87]\n"
        "s_waitcnt vmcnt(4)\n"
        "v_mfma_f32_16x16x32_bf16 v[88:91], v[24:27], v[60:63], v[88:91]\n"
        "ds_write_b128 v4, v[60:63] offset:5120\n"
        "v_mfma_f32_16x16x32_bf16 v[92:95], v[60:63], v[60:63], v[92:95]\n"
        "s_waitcnt vmcnt(3)\n"
        "v_mfma_f32_16x16x32_bf16 v[80:83], v[24:27], v[64:67], v[80:83]\n"
        "ds_write_b128 v4, v[64:67] offset:6144\n"
        "v_mfma_f32_16x16x32_bf16 v[84:87], v[64:67], v[64:67], v[84:87]\n"
        "s_waitcnt vmcnt(2)\n"
        "v_mfma_f32_16x16x32_bf16 v[88:91], v[24:27], v[68:71], v[88:91]\n"
        "ds_write_b128 v4, v[68:71] offset:7168\n"
        "v_mfma_f32_16x16x32_bf16 v[92:95], v[68:71], v[68:71], v[92:95]\n"
        "s_waitcnt vmcnt(1)\n"
        "v_mfma_f32_16x16x32_bf16 v[80:83], v[24:27], v[72:75], v[80:83]\n"
        "ds_write_b128 v4, v[72:75] offset:8192\n"
        "v_mfma_f32_16x16x32_bf16 v[84:87], v[72:75], v[72:75], v[84:87]\n"
        "s_waitcnt vmcnt(0)\n"
        "v_mfma_f32_16x16x32_bf16 v[88:91], v[24:27], v[76:79], v[88:91]\n"
        "ds_write_b128 v4, v[76:79] offset:9216\n"
        "v_mfma_f32_16x16x32_bf16 v[92:95], v[76:79], v[76:79], v[92:95]\n"
        "s_mov_b64 exec, s[70:71]\n"
        "s_nop 0\n"
        "ds_write_b128 v5, v[80:83] offset:0\n"
        "s_nop 0\n"
        "ds_write_b128 v5, v[84:87] offset:16\n"
        "s_nop 0\n"
        "ds_write_b128 v5, v[88:91] offset:2048\n"
        "s_nop 0\n"
        "ds_write_b128 v5, v[92:95] offset:2064\n"
        "s_mov_b64 exec, -1\n"
        "s_waitcnt lgkmcnt(0)\n"
        "s_barrier\n"
        "ds_read_b128 v[96:99], v1 offset:0\n"
        "ds_read_b128 v[100:103], v1 offset:1024\n"
        "ds_read_b128 v[104:107], v1 offset:2048\n"
        "ds_read_b128 v[108:111], v1 offset:3072\n"
        "ds_read_b128 v[112:115], v1 offset:4096\n"
        "ds_read_b128 v[116:119], v1 offset:5120\n"
        "s_waitcnt lgkmcnt(4)\n"
        "v_mfma_f32_16x16x32_bf16 v[80:83], v[96:99], a[0:3], 0\n"
        "ds_read_b128 v[120:123], v1 offset:6144\n"
        "v_mfma_f32_16x16x32_bf16 v[88:91], v[96:99], a[80:83], 0\n"
        "ds_read_b128 v[124:127], v1 offset:7168\n"
        "v_mfma_f32_16x16x32_bf16 v[84:87], v[100:103], a[0:3], 0\n"
        "ds_read_b128 v[128:131], v1 offset:8192\n"
        "v_mfma_f32_16x16x32_bf16 v[92:95], v[100:103], a[80:83], 0\n"
        "ds_read_b128 v[132:135], v1 offset:9216\n"
        "s_waitcnt lgkmcnt(6)\n"
        "v_mfma_f32_16x16x32_bf16 v[80:83], v[104:107], a[4:7], v[80:83]\n"
        "ds_read_b128 v[136:139], v1 offset:10240\n"
        "ds_read2_b32 v[40:41], v6 offset0:0 offset1:4\n"
        "v_mfma_f32_16x16x32_bf16 v[88:91], v[104:107], a[84:87], v[88:91]\n"
        "ds_read_b128 v[140:143], v1 offset:11264\n"
        "v_mfma_f32_16x16x32_bf16 v[84:87], v[108:111], a[4:7], v[84:87]\n"
        "ds_read_b128 v[144:147], v1 offset:12288\n"
        "ds_read2_b32 v[48:49], v6 offset0:8 offset1:12\n"
        "v_mfma_f32_16x16x32_bf16 v[92:95], v[108:111], a[84:87], v[92:95]\n"
        "ds_read_b128 v[148:151], v1 offset:13312\n"
        "s_waitcnt lgkmcnt(10)\n"
        "v_mfma_f32_16x16x32_bf16 v[80:83], v[112:115], a[8:11], v[80:83]\n"
        "ds_read_b128 v[152:155], v1 offset:14336\n"
        "v_mfma_f32_16x16x32_bf16 v[88:91], v[112:115], a[88:91], v[88:91]\n"
        "ds_read_b128 v[156:159], v1 offset:15360\n"
        "v_mfma_f32_16x16x32_bf16 v[84:87], v[116:119], a[8:11], v[84:87]\n"
        "ds_read_b128 v[160:163], v1 offset:16384\n"
        "v_mfma_f32_16x16x32_bf16 v[92:95], v[116:119], a[88:91], v[92:95]\n"
        "ds_read_b128 v[164:167], v1 offset:17408\n"
        "s_waitcnt lgkmcnt(12)\n"
        "v_mfma_f32_16x16x32_bf16 v[80:83], v[120:123], a[12:15], v[80:83]\n"
        "ds_read_b128 v[168:171], v1 offset:18432\n"
        "v_mfma_f32_16x16x32_bf16 v[88:91], v[120:123], a[92:95], v[88:91]\n"
        "ds_read_b128 v[172:175], v1 offset:19456\n"
        "v_mfma_f32_16x16x32_bf16 v[84:87], v[124:127], a[12:15], v[84:87]\n"
        "ds_read_b128 v[176:179], v1 offset:20480\n"
        "v_mfma_f32_16x16x32_bf16 v[92:95], v[124:127], a[92:95], v[92:95]\n"
        "ds_read_b128 v[180:183], v1 offset:21504\n"
        "s_waitcnt lgkmcnt(14)\n"
        "v_mfma_f32_16x16x32_bf16 v[80:83], v[128:131], a[16:19], v[80:83]\n"
        "ds_read_b128 v[184:187], v1 offset:22528\n"
        "s_waitcnt lgkmcnt(10)\n"
        "v_mfma_f32_16x16x32_bf16 v[88:91], v[128:131], a[96:99], v[88:91]\n"
        "ds_read_b128 v[188:191], v1 offset:23552\n"
        "v_add_f32 v40, v40, v48\n"
        "v_add_f32 v41, v41, v49\n"
        "v_mfma_f32_16x16x32_bf16 v[84:87], v[132:135], a[16:19], v[84:87]\n"
        "ds_read_b128 v[192:195], v1 offset:24576\n"
        "v_mov_b32 v42, v40\n"
        "v_mov_b32 v43, v41\n"
        "v_mfma_f32_16x16x32_bf16 v[92:95], v[132:135], a[96:99], v[92:95]\n"
        "ds_read_b128 v[196:199], v1 offset:25600\n"
        "v_permlane32_swap_b32 v40, v42\n"
        "s_waitcnt lgkmcnt(15)\n"
        "v_mfma_f32_16x16x32_bf16 v[80:83], v[136:139], a[20:23], v[80:83]\n"
        "ds_read_b128 v[200:203], v1 offset:26624\n"
        "v_permlane32_swap_b32 v41, v43\n"
        "v_mfma_f32_16x16x32_bf16 v[88:91], v[136:139], a[100:103], v[88:91]\n"
        "ds_read_b128 v[204:207], v1 offset:27648\n"
        "v_add_f32 v40, v40, v42\n"
        "v_add_f32 v41, v41, v43\n"
        "v_mfma_f32_16x16x32_bf16 v[84:87], v[140:143], a[20:23], v[84:87]\n"
        "ds_read_b128 v[208:211], v1 offset:28672\n"
        "v_mul_f32 v40, 0x3acccccd, v40\n"
        "v_mul_f32 v41, 0x3acccccd, v41\n"
        "v_mfma_f32_16x16x32_bf16 v[92:95], v[140:143], a[100:103], v[92:95]\n"
        "ds_read_b128 v[212:215], v1 offset:29696\n"
        "v_fma_f32 v41, -v40, v40, v41\n"
        "v_max_f32 v41, 0, v41\n"
        "s_waitcnt lgkmcnt(15)\n"
        "v_mfma_f32_16x16x32_bf16 v[80:83], v[144:147], a[24:27], v[80:83]\n"
        "ds_read_b128 v[216:219], v1 offset:30720\n"
        "v_add_f32 v41, 0x3727c5ac, v41\n"
        "v_mfma_f32_16x16x32_bf16 v[88:91], v[144:147], a[104:107], v[88:91]\n"
        "ds_read_b128 v[220:223], v1 offset:31744\n"
        "v_rsq_f32 v41, v41\n"
        "v_mfma_f32_16x16x32_bf16 v[84:87], v[148:151], a[24:27], v[84:87]\n"
        "ds_read_b128 v[224:227], v1 offset:32768\n"
        "v_mul_f32 v40, v40, v41\n"
        "ds_bpermute_b32 v56, v7, v41 offset:0\n"
        "v_mfma_f32_16x16x32_bf16 v[92:95], v[148:151], a[104:107], v[92:95]\n"
        "ds_read_b128 v[228:231], v1 offset:33792\n"
        "ds_bpermute_b32 v57, v7, v40 offset:0\n"
        "s_waitcnt lgkmcnt(15)\n"
        "v_mfma_f32_16x16x32_bf16 v[80:83], v[152:155], a[28:31], v[80:83]\n"
        "ds_read_b128 v[232:235], v1 offset:34816\n"
        "ds_bpermute_b32 v58, v7, v41 offset:4\n"
        "v_mfma_f32_16x16x32_bf16 v[88:91], v[152:155], a[108:111], v[88:91]\n"
        "ds_read_b128 v[236:239], v1 offset:35840\n"
        "ds_bpermute_b32 v59, v7, v40 offset:4\n"
        "v_mfma_f32_16x16x32_bf16 v[84:87], v[156:159], a[28:31], v[84:87]\n"
        "ds_read_b128 v[240:243], v1 offset:36864\n"
        "ds_bpermute_b32 v60, v7, v41 offset:8\n"
        "v_mfma_f32_16x16x32_bf16 v[92:95], v[156:159], a[108:111], v[92:95]\n"
        "ds_read_b128 v[244:247], v1 offset:37888\n"
        "ds_bpermute_b32 v61, v7, v40 offset:8\n"
        "s_waitcnt lgkmcnt(15)\n"
        "v_mfma_f32_16x16x32_bf16 v[80:83], v[160:163], a[32:35], v[80:83]\n"
        "ds_read_b128 v[248:251], v1 offset:38912\n"
        "ds_bpermute_b32 v62, v7, v41 offset:12\n"
        "v_mfma_f32_16x16x32_bf16 v[88:91], v[160:163], a[112:115], v[88:91]\n"
        "ds_read_b128 v[252:255], v1 offset:39936\n"
        "ds_bpermute_b32 v63, v7, v40 offset:12\n"
        "v_mfma_f32_16x16x32_bf16 v[84:87], v[164:167], a[32:35], v[84:87]\n"
        "ds_bpermute_b32 v64, v7, v41 offset:64\n"
        "v_mfma_f32_16x16x32_bf16 v[92:95], v[164:167], a[112:115], v[92:95]\n"
        "ds_bpermute_b32 v65, v7, v40 offset:64\n"
        "s_waitcnt lgkmcnt(15)\n"
        "v_mfma_f32_16x16x32_bf16 v[80:83], v[168:171], a[36:39], v[80:83]\n"
        "ds_bpermute_b32 v66, v7, v41 offset:68\n"
        "v_mfma_f32_16x16x32_bf16 v[88:91], v[168:171], a[116:119], v[88:91]\n"
        "ds_bpermute_b32 v67, v7, v40 offset:68\n"
        "v_mfma_f32_16x16x32_bf16 v[84:87], v[172:175], a[36:39], v[84:87]\n"
        "ds_bpermute_b32 v68, v7, v41 offset:72\n"
        "v_mfma_f32_16x16x32_bf16 v[92:95], v[172:175], a[116:119], v[92:95]\n"
        "ds_bpermute_b32 v69, v7, v40 offset:72\n"
        "s_waitcnt lgkmcnt(15)\n"
        "v_mfma_f32_16x16x32_bf16 v[80:83], v[176:179], a[40:43], v[80:83]\n"
        "ds_bpermute_b32 v70, v7, v41 offset:76\n"
        "v_mfma_f32_16x16x32_bf16 v[88:91], v[176:179], a[120:123], v[88:91]\n"
        "ds_bpermute_b32 v71, v7, v40 offset:76\n"
        "v_mfma_f32_16x16x32_bf16 v[84:87], v[180:183], a[40:43], v[84:87]\n"
        "v_mfma_f32_16x16x32_bf16 v[92:95], v[180:183], a[120:123], v[92:95]\n"
        "s_waitcnt lgkmcnt(15)\n"
        "v_mfma_f32_16x16x32_bf16 v[80:83], v[184:187], a[44:47], v[80:83]\n"
        "v_mfma_f32_16x16x32_bf16 v[88:91], v[184:187], a[124:127], v[88:91]\n"
        "v_mfma_f32_16x16x32_bf16 v[84:87], v[188:191], a[44:47], v[84:87]\n"
        "v_mfma_f32_16x16x32_bf16 v[92:95], v[188:191], a[124:127], v[92:95]\n"
        "s_waitcnt lgkmcnt(15)\n"
        "v_mfma_f32_16x16x32_bf16 v[80:83], v[192:195], a[48:51], v[80:83]\n"
        "v_mfma_f32_16x16x32_bf16 v[88:91], v[192:195], a[128:131], v[88:91]\n"
        "v_mfma_f32_16x16x32_bf16 v[84:87], v[196:199], a[48:51], v[84:87]\n"
        "v_mfma_f32_16x16x32_bf16 v[92:95], v[196:199], a[128:131], v[92:95]\n"
        "s_waitcnt lgkmcnt(15)\n"
        "v_mfma_f32_16x16x32_bf16 v[80:83], v[200:203], a[52:55], v[80:83]\n"
        "v_mfma_f32_16x16x32_bf16 v[88:91], v[200:203], a[132:135], v[88:91]\n"
        "v_mfma_f32_16x16x32_bf16 v[84:87], v[204:207], a[52:55], v[84:87]\n"
        "v_mfma_f32_16x16x32_bf16 v[92:95], v[204:207], a[132:135], v[92:95]\n"
        "s_waitcnt lgkmcnt(15)\n"
        "v_mfma_f32_16x16x32_bf16 v[80:83], v[208:211], a[56:59], v[80:83]\n"
        "v_mfma_f32_16x16x32_bf16 v[88:91], v[208:211], a[136:139], v[88:91]\n"
        "v_mfma_f32_16x16x32_bf16 v[84:87], v[212:215], a[56:59], v[84:87]\n"
        "v_mfma_f32_16x16x32_bf16 v[92:95], v[212:215], a[136:139], v[92:95]\n"
        "s_waitcnt lgkmcnt(15)\n"
        "v_mfma_f32_16x16x32_bf16 v[80:83], v[216:219], a[60:63], v[80:83]\n"
        "v_mfma_f32_16x16x32_bf16 v[88:91], v[216:219], a[140:143], v[88:91]\n"
        "v_mfma_f32_16x16x32_bf16 v[84:87], v[220:223], a[60:63], v[84:87]\n"
        "v_mfma_f32_16x16x32_bf16 v[92:95], v[220:223], a[140:143], v[92:95]\n"
        "s_waitcnt lgkmcnt(15)\n"
        "v_mfma_f32_16x16x32_bf16 v[80:83], v[224:227], a[64:67], v[80:83]\n"
        "v_mfma_f32_16x16x32_bf16 v[88:91], v[224:227], a[144:147], v[88:91]\n"
        "v_mfma_f32_16x16x32_bf16 v[84:87], v[228:231], a[64:67], v[84:87]\n"
        "v_mfma_f32_16x16x32_bf16 v[92:95], v[228:231], a[144:147], v[92:95]\n"
        "s_waitcnt lgkmcnt(15)\n"
        "v_mfma_f32_16x16x32_bf16 v[80:83], v[232:235], a[68:71], v[80:83]\n"
        "v_mfma_f32_16x16x32_bf16 v[88:91], v[232:235], a[148:151], v[88:91]\n"
        "v_mfma_f32_16x16x32_bf16 v[84:87], v[236:239], a[68:71], v[84:87]\n"
        "v_mfma_f32_16x16x32_bf16 v[92:95], v[236:239], a[148:151], v[92:95]\n"
        "s_waitcnt lgkmcnt(13)\n"
        "v_mfma_f32_16x16x32_bf16 v[80:83], v[240:243], a[72:75], v[80:83]\n"
        "v_mfma_f32_16x16x32_bf16 v[88:91], v[240:243], a[152:155], v[88:91]\n"
        "v_mfma_f32_16x16x32_bf16 v[84:87], v[244:247], a[72:75], v[84:87]\n"
        "v_mfma_f32_16x16x32_bf16 v[92:95], v[244:247], a[152:155], v[92:95]\n"
        "s_waitcnt lgkmcnt(9)\n"
        "v_mfma_f32_16x16x32_bf16 v[80:83], v[248:251], a[76:79], v[80:83]\n"
        "v_mfma_f32_16x16x32_bf16 v[88:91], v[248:251], a[156:159], v[88:91]\n"
        "v_mfma_f32_16x16x32_bf16 v[84:87], v[252:255], a[76:79], v[84:87]\n"
        "v_mfma_f32_16x16x32_bf16 v[92:95], v[252:255], a[156:159], v[92:95]\n"
        "s_waitcnt lgkmcnt(0)\n"
        "v_mul_f32 v112, v57, v28\n"
        "v_mul_f32 v113, v59, v28\n"
        "v_mul_f32 v114, v61, v28\n"
        "v_mul_f32 v115, v63, v28\n"
        "v_mul_f32 v116, v65, v28\n"
        "v_mul_f32 v117, v67, v28\n"
        "v_mul_f32 v118, v69, v28\n"
        "v_mul_f32 v119, v71, v28\n"
        "v_mul_f32 v120, v57, v32\n"
        "v_mul_f32 v121, v59, v32\n"
        "v_mul_f32 v122, v61, v32\n"
        "v_mul_f32 v123, v63, v32\n"
        "v_mul_f32 v124, v65, v32\n"
        "v_mul_f32 v125, v67, v32\n"
        "v_mul_f32 v126, v69, v32\n"
        "v_mul_f32 v127, v71, v32\n"
        "v_fma_f32 v96, v80, v56, -v112\n"
        "v_fma_f32 v97, v81, v58, -v113\n"
        "v_fma_f32 v98, v82, v60, -v114\n"
        "v_fma_f32 v99, v83, v62, -v115\n"
        "v_fma_f32 v100, v84, v64, -v116\n"
        "v_fma_f32 v101, v85, v66, -v117\n"
        "v_fma_f32 v102, v86, v68, -v118\n"
        "v_fma_f32 v103, v87, v70, -v119\n"
        "v_fma_f32 v104, v88, v56, -v120\n"
        "v_fma_f32 v105, v89, v58, -v121\n"
        "v_fma_f32 v106, v90, v60, -v122\n"
        "v_fma_f32 v107, v91, v62, -v123\n"
        "v_fma_f32 v108, v92, v64, -v124\n"
        "v_fma_f32 v109, v93, v66, -v125\n"
        "v_fma_f32 v110, v94, v68, -v126\n"
        "v_fma_f32 v111, v95, v70, -v127\n"
        "v_mul_f32 v112, v96, v96\n"
        "v_mul_f32 v113, v97, v97\n"
        "v_mul_f32 v114, v98, v98\n"
        "v_mul_f32 v115, v99, v99\n"
        "v_mul_f32 v116, v100, v100\n"
        "v_mul_f32 v117, v101, v101\n"
        "v_mul_f32 v118, v102, v102\n"
        "v_mul_f32 v119, v103, v103\n"
        "v_mul_f32 v120, v104, v104\n"
        "v_mul_f32 v121, v105, v105\n"
        "v_mul_f32 v122, v106, v106\n"
        "v_mul_f32 v123, v107, v107\n"
        "v_mul_f32 v124, v108, v108\n"
        "v_mul_f32 v125, v109, v109\n"
        "v_mul_f32 v126, v110, v110\n"
        "v_mul_f32 v127, v111, v111\n"
        "v_fma_f32 v112, v112, s61, v23\n"
        "v_fma_f32 v113, v113, s61, v23\n"
        "v_fma_f32 v114, v114, s61, v23\n"
        "v_fma_f32 v115, v115, s61, v23\n"
        "v_fma_f32 v116, v116, s61, v23\n"
        "v_fma_f32 v117, v117, s61, v23\n"
        "v_fma_f32 v118, v118, s61, v23\n"
        "v_fma_f32 v119, v119, s61, v23\n"
        "v_fma_f32 v120, v120, s61, v23\n"
        "v_fma_f32 v121, v121, s61, v23\n"
        "v_fma_f32 v122, v122, s61, v23\n"
        "v_fma_f32 v123, v123, s61, v23\n"
        "v_fma_f32 v124, v124, s61, v23\n"
        "v_fma_f32 v125, v125, s61, v23\n"
        "v_fma_f32 v126, v126, s61, v23\n"
        "v_fma_f32 v127, v127, s61, v23\n"
        "v_mul_f32 v112, v112, v96\n"
        "v_mul_f32 v113, v113, v97\n"
        "v_mul_f32 v114, v114, v98\n"
        "v_mul_f32 v115, v115, v99\n"
        "v_mul_f32 v116, v116, v100\n"
        "v_mul_f32 v117, v117, v101\n"
        "v_mul_f32 v118, v118, v102\n"
        "v_mul_f32 v119, v119, v103\n"
        "v_mul_f32 v120, v120, v104\n"
        "v_mul_f32 v121, v121, v105\n"
        "v_mul_f32 v122, v122, v106\n"
        "v_mul_f32 v123, v123, v107\n"
        "v_mul_f32 v124, v124, v108\n"
        "v_mul_f32 v125, v125, v109\n"
        "v_mul_f32 v126, v126, v110\n"
        "v_mul_f32 v127, v127, v111\n"
        "v_exp_f32 v112, v112\n"
        "v_exp_f32 v113, v113\n"
        "v_exp_f32 v114, v114\n"
        "v_exp_f32 v115, v115\n"
        "v_exp_f32 v116, v116\n"
        "v_exp_f32 v117, v117\n"
        "v_exp_f32 v118, v118\n"
        "v_exp_f32 v119, v119\n"
        "v_exp_f32 v120, v120\n"
        "v_exp_f32 v121, v121\n"
        "v_exp_f32 v122, v122\n"
        "v_exp_f32 v123, v123\n"
        "v_exp_f32 v124, v124\n"
        "v_exp_f32 v125, v125\n"
        "v_exp_f32 v126, v126\n"
        "v_exp_f32 v127, v127\n"
        "v_add_f32 v112, 1.0, v112\n"
        "v_add_f32 v113, 1.0, v113\n"
        "v_add_f32 v114, 1.0, v114\n"
        "v_add_f32 v115, 1.0, v115\n"
        "v_add_f32 v116, 1.0, v116\n"
        "v_add_f32 v117, 1.0, v117\n"
        "v_add_f32 v118, 1.0, v118\n"
        "v_add_f32 v119, 1.0, v119\n"
        "v_add_f32 v120, 1.0, v120\n"
        "v_add_f32 v121, 1.0, v121\n"
        "v_add_f32 v122, 1.0, v122\n"
        "v_add_f32 v123, 1.0, v123\n"
        "v_add_f32 v124, 1.0, v124\n"
        "v_add_f32 v125, 1.0, v125\n"
        "v_add_f32 v126, 1.0, v126\n"
        "v_add_f32 v127, 1.0, v127\n"
        "v_rcp_f32 v112, v112\n"
        "v_rcp_f32 v113, v113\n"
        "v_rcp_f32 v114, v114\n"
        "v_rcp_f32 v115, v115\n"
        "v_rcp_f32 v116, v116\n"
        "v_rcp_f32 v117, v117\n"
        "v_rcp_f32 v118, v118\n"
        "v_rcp_f32 v119, v119\n"
        "v_rcp_f32 v120, v120\n"
        "v_rcp_f32 v121, v121\n"
        "v_rcp_f32 v122, v122\n"
        "v_rcp_f32 v123, v123\n"
        "v_rcp_f32 v124, v124\n"
        "v_rcp_f32 v125, v125\n"
        "v_rcp_f32 v126, v126\n"
        "v_rcp_f32 v127, v127\n"
        "v_fma_f32 v96, -v96, v112, v96\n"
        "v_fma_f32 v97, -v97, v113, v97\n"
        "v_fma_f32 v98, -v98, v114, v98\n"
        "v_fma_f32 v99, -v99, v115, v99\n"
        "v_fma_f32 v100, -v100, v116, v100\n"
        "v_fma_f32 v101, -v101, v117, v101\n"
        "v_fma_f32 v102, -v102, v118, v102\n"
        "v_fma_f32 v103, -v103, v119, v103\n"
        "v_fma_f32 v104, -v104, v120, v104\n"
        "v_fma_f32 v105, -v105, v121, v105\n"
        "v_fma_f32 v106, -v106, v122, v106\n"
        "v_fma_f32 v107, -v107, v123, v107\n"
        "v_fma_f32 v108, -v108, v124, v108\n"
        "v_fma_f32 v109, -v109, v125, v109\n"
        "v_fma_f32 v110, -v110, v126, v110\n"
        "v_fma_f32 v111, -v111, v127, v111\n"
        "v_cvt_pk_bf16_f32 v80, v96, v97\n"
        "v_cvt_pk_bf16_f32 v81, v98, v99\n"
        "v_cvt_pk_bf16_f32 v82, v100, v101\n"
        "v_cvt_pk_bf16_f32 v83, v102, v103\n"
        "global_store_dwordx4 v8, v[80:83], s[52:53] offset:0 sc1\n"
        "v_cvt_pk_bf16_f32 v84, v104, v105\n"
        "v_cvt_pk_bf16_f32 v85, v106, v107\n"
        "v_cvt_pk_bf16_f32 v86, v108, v109\n"
        "v_cvt_pk_bf16_f32 v87, v110, v111\n"
        "global_store_dwordx4 v8, v[84:87], s[52:53] offset:1024 sc1\n"
        "v_add_f32 v40, v96, v104\n"
        "v_mul_f32 v41, v96, v96\n"
        "v_add_f32 v42, v97, v105\n"
        "v_mul_f32 v43, v97, v97\n"
        "v_add_f32 v44, v98, v106\n"
        "v_mul_f32 v45, v98, v98\n"
        "v_add_f32 v46, v99, v107\n"
        "v_mul_f32 v47, v99, v99\n"
        "v_add_f32 v48, v100, v108\n"
        "v_mul_f32 v49, v100, v100\n"
        "v_add_f32 v50, v101, v109\n"
        "v_mul_f32 v51, v101, v101\n"
        "v_add_f32 v52, v102, v110\n"
        "v_mul_f32 v53, v102, v102\n"
        "v_add_f32 v54, v103, v111\n"
        "v_mul_f32 v55, v103, v103\n"
        "v_fmac_f32 v41, v104, v104\n"
        "v_fmac_f32 v43, v105, v105\n"
        "v_fmac_f32 v45, v106, v106\n"
        "v_fmac_f32 v47, v107, v107\n"
        "v_fmac_f32 v49, v108, v108\n"
        "v_fmac_f32 v51, v109, v109\n"
        "v_fmac_f32 v53, v110, v110\n"
        "v_fmac_f32 v55, v111, v111\n"
        "v_add_f32_dpp v40, v40, v40 row_ror:8 row_mask:0xf bank_mask:0x3\n"
        "v_add_f32_dpp v41, v41, v41 row_ror:8 row_mask:0xf bank_mask:0x3\n"
        "v_add_f32_dpp v42, v42, v42 row_ror:8 row_mask:0xf bank_mask:0x3\n"
        "v_add_f32_dpp v43, v43, v43 row_ror:8 row_mask:0xf bank_mask:0x3\n"
        "v_add_f32_dpp v44, v44, v44 row_ror:8 row_mask:0xf bank_mask:0x3\n"
        "v_add_f32_dpp v45, v45, v45 row_ror:8 row_mask:0xf bank_mask:0x3\n"
        "v_add_f32_dpp v46, v46, v46 row_ror:8 row_mask:0xf bank_mask:0x3\n"
        "v_add_f32_dpp v47, v47, v47 row_ror:8 row_mask:0xf bank_mask:0x3\n"
        "v_add_f32_dpp v40, v48, v48 row_ror:8 row_mask:0xf bank_mask:0xc\n"
        "v_add_f32_dpp v41, v49, v49 row_ror:8 row_mask:0xf bank_mask:0xc\n"
        "v_add_f32_dpp v42, v50, v50 row_ror:8 row_mask:0xf bank_mask:0xc\n"
        "v_add_f32_dpp v43, v51, v51 row_ror:8 row_mask:0xf bank_mask:0xc\n"
        "v_add_f32_dpp v44, v52, v52 row_ror:8 row_mask:0xf bank_mask:0xc\n"
        "v_add_f32_dpp v45, v53, v53 row_ror:8 row_mask:0xf bank_mask:0xc\n"
        "v_add_f32_dpp v46, v54, v54 row_ror:8 row_mask:0xf bank_mask:0xc\n"
        "v_add_f32_dpp v47, v55, v55 row_ror:8 row_mask:0xf bank_mask:0xc\n"
        "v_add_f32_dpp v40, v40, v40 row_half_mirror row_mask:0xf bank_mask:0x5\n"
        "v_add_f32_dpp v41, v41, v41 row_half_mirror row_mask:0xf bank_mask:0x5\n"
        "v_add_f32_dpp v42, v42, v42 row_half_mirror row_mask:0xf bank_mask:0x5\n"
        "v_add_f32_dpp v43, v43, v43 row_half_mirror row_mask:0xf bank_mask:0x5\n"
        "v_add_f32_dpp v40, v44, v44 row_half_mirror row_mask:0xf bank_mask:0xa\n"
        "v_add_f32_dpp v41, v45, v45 row_half_mirror row_mask:0xf bank_mask:0xa\n"
        "v_add_f32_dpp v42, v46, v46 row_half_mirror row_mask:0xf bank_mask:0xa\n"
        "v_add_f32_dpp v43, v47, v47 row_half_mirror row_mask:0xf bank_mask:0xa\n"
        "v_add_f32_dpp v40, v40, v40 quad_perm:[2,3,0,1] row_mask:0xf bank_mask:0xf\n"
        "v_add_f32_dpp v41, v41, v41 quad_perm:[2,3,0,1] row_mask:0xf bank_mask:0xf\n"
        "v_add_f32_dpp v42, v42, v42 quad_perm:[2,3,0,1] row_mask:0xf bank_mask:0xf\n"
        "v_add_f32_dpp v43, v43, v43 quad_perm:[2,3,0,1] row_mask:0xf bank_mask:0xf\n"
        "v_add_f32_dpp v40, v40, v40 quad_perm:[1,0,3,2] row_mask:0xf bank_mask:0xf\n"
        "v_add_f32_dpp v41, v41, v41 quad_perm:[1,0,3,2] row_mask:0xf bank_mask:0xf\n"
        "v_add_f32_dpp v42, v42, v42 quad_perm:[1,0,3,2] row_mask:0xf bank_mask:0xf\n"
        "v_add_f32_dpp v43, v43, v43 quad_perm:[1,0,3,2] row_mask:0xf bank_mask:0xf\n"
        "s_mov_b64 exec, s[64:65]\n"
        "ds_write_b128 v10, v[40:43]\n"
        "s_mov_b64 exec, -1\n"
        "s_waitcnt lgkmcnt(0)\n"
        "s_barrier\n"
        "v_add_u32 v36, 256, v11\n"
        "s_mov_b64 exec, s[66:67]\n"
        "ds_read2st64_b64 v[40:43], v11 offset0:0 offset1:1\n"
        "ds_read2st64_b64 v[44:47], v36 offset0:0 offset1:1\n"
        "s_waitcnt lgkmcnt(0)\n"
        "v_pk_add_f32 v[40:41], v[40:41], v[42:43]\n"
        "v_pk_add_f32 v[44:45], v[44:45], v[46:47]\n"
        "v_pk_add_f32 v[40:41], v[40:41], v[44:45]\n"
        "global_store_dwordx2 v12, v[40:41], s[56:57] sc1\n"
        "s_mov_b64 exec, -1\n"
        "s_waitcnt vmcnt(0)\n"
        "s_barrier\n"
        "s_mov_b64 exec, s[62:63]\n"
        "global_store_dword v9, v22, s[90:91] sc1\n"
        "s_mov_b64 exec, -1\n"
        "s_add_u32 s45, s45, 2\n"
        "s_cmp_lt_u32 s45, 64\n"
        "s_cbranch_scc1 Lg1b_loop\n"
        "Lg1b_exit:\n"
        "s_waitcnt vmcnt(0) lgkmcnt(0)\n"
        "s_endpgm\n"
            :: "s"(ws), "s"(w_in), "s"(w_in), "s"(x), "s"(r - 104), "s"(par), "s"(xq), "s"(wave), "s"(karg) : "v0", "v1", "v2", "v3", "v4", "v5", "v6", "v7", "v8", "v9", "v10", "v11", "v12", "v13", "v14", "v15", "v16", "v17", "v18", "v19", "v20", "v21", "v22", "v23", "v24", "v25", "v26", "v27", "v28", "v29", "v30", "v31", "v32", "v33", "v34", "v35", "v36", "v37", "v38", "v39", "v40", "v41", "v42", "v43", "v44", "v45", "v46", "v47", "v48", "v49", "v50", "v51", "v52", "v53", "v54", "v55", "v56", "v57", "v58", "v59", "v60", "v61", "v62", "v63", "v64", "v65", "v66", "v67", "v68", "v69", "v70", "v71", "v72", "v73", "v74", "v75", "v76", "v77", "v78", "v79", "v80", "v81", "v82", "v83", "v84", "v85", "v86", "v87", "v88", "v89", "v90", "v91", "v92", "v93", "v94", "v95", "v96", "v97", "v98", "v99", "v100", "v101", "v102", "v103", "v104", "v105", "v106", "v107", "v108", "v109", "v110", "v111", "v112", "v113", "v114", "v115", "v116", "v117", "v118", "v119", "v120", "v121", "v122", "v123", "v124", "v125", "v126", "v127", "v128", "v129", "v130", "v131", "v132", "v133", "v134", "v135", "v136", "v137", "v138", "v139", "v140", "v141", "v142", "v143", "v144", "v145", "v146", "v147", "v148", "v149", "v150", "v151", "v152", "v153", "v154", "v155", "v156", "v157", "v158", "v159", "v160", "v161", "v162", "v163", "v164", "v165", "v166", "v167", "v168", "v169", "v170", "v171", "v172", "v173", "v174", "v175", "v176", "v177", "v178", "v179", "v180", "v181", "v182", "v183", "v184", "v185", "v186", "v187", "v188", "v189", "v190", "v191", "v192", "v193", "v194", "v195", "v196", "v197", "v198", "v199", "v200", "v201", "v202", "v203", "v204", "v205", "v206", "v207", "v208", "v209", "v210", "v211", "v212", "v213", "v214", "v215", "v216", "v217", "v218", "v219", "v220", "v221", "v222", "v223", "v224", "v225", "v226", "v227", "v228", "v229", "v230", "v231", "v232", "v233", "v234", "v235", "v236", "v237", "v238", "v239", "v240", "v241", "v242", "v243", "v244", "v245", "v246", "v247", "v248", "v249", "v250", "v251", "v252", "v253", "v254", "v255", "a0", "a1", "a2", "a3", "a4", "a5", "a6", "a7", "a8", "a9", "a10", "a11", "a12", "a13", "a14", "a15", "a16", "a17", "a18", "a19", "a20", "a21", "a22", "a23", "a24", "a25", "a26", "a27", "a28", "a29", "a30", "a31", "a32", "a33", "a34", "a35", "a36", "a37", "a38", "a39", "a40", "a41", "a42", "a43", "a44", "a45", "a46", "a47", "a48", "a49", "a50", "a51", "a52", "a53", "a54", "a55", "a56", "a57", "a58", "a59", "a60", "a61", "a62", "a63", "a64", "a65", "a66", "a67", "a68", "a69", "a70", "a71", "a72", "a73", "a74", "a75", "a76", "a77", "a78", "a79", "a80", "a81", "a82", "a83", "a84", "a85", "a86", "a87", "a88", "a89", "a90", "a91", "a92", "a93", "a94", "a95", "a96", "a97", "a98", "a99", "a100", "a101", "a102", "a103", "a104", "a105", "a106", "a107", "a108", "a109", "a110", "a111", "a112", "a113", "a114", "a115", "a116", "a117", "a118", "a119", "a120", "a121", "a122", "a123", "a124", "a125", "a126", "a127", "a128", "a129", "a130", "a131", "a132", "a133", "a134", "a135", "a136", "a137", "a138", "a139", "a140", "a141", "a142", "a143", "a144", "a145", "a146", "a147", "a148", "a149", "a150", "a151", "a152", "a153", "a154", "a155", "a156", "a157", "a158", "a159", "a160", "a161", "a162", "a163", "a164", "a165", "a166", "a167", "a168", "a169", "a170", "a171", "a172", "a173", "a174", "a175", "a176", "a177", "a178", "a179", "a180", "a181", "a182", "a183", "a184", "a185", "a186", "a187", "a188", "a189", "a190", "a191", "a192", "a193", "a194", "a195", "a196", "a197", "a198", "a199", "a200", "a201", "a202", "a203", "a204", "a205", "a206", "a207", "a208", "a209", "a210", "a211", "a212", "a213", "a214", "a215", "a216", "a217", "a218", "a219", "a220", "a221", "a222", "a223", "a224", "a225", "a226", "a227", "a228", "a229", "a230", "a231", "a232", "a233", "a234", "a235", "a236", "a237", "a238", "a239", "a240", "a241", "a242", "a243", "a244", "a245", "a246", "a247", "a248", "a249", "a250", "a251", "a252", "a253", "a254", "a255", "s34", "s35", "s36", "s37", "s38", "s39", "s40", "s41", "s42", "s43", "s44", "s45", "s46", "s47", "s48", "s49", "s50", "s51", "s52", "s53", "s54", "s55", "s56", "s57", "s58", "s59", "s60", "s61", "s62", "s63", "s64", "s65", "s66", "s67", "s68", "s69", "s70", "s71", "s72", "s73", "s74", "s75", "s76", "s77", "s78", "s79", "s80", "s81", "s82", "s83", "s84", "s85", "s86", "s87", "s88", "s89", "s90", "s91", "s92", "s93", "s94", "s95", "s96", "s97", "s98", "s99", "vcc", "scc", "memory");
        __builtin_amdgcn_endpgm();
    } else {
        const int i = par * 12 + r - 116;
        asm volatile(
        "s_mov_b64 s[48:49], %8\n"
        "s_mov_b64 s[90:91], %0\n"
        "s_mov_b64 s[92:93], %1\n"
        "s_mov_b64 s[94:95], %2\n"
        "s_mov_b64 s[96:97], %3\n"
        "s_mov_b32 s40, %4\n"
        "s_mov_b32 s41, %5\n"
        "s_mov_b32 s42, %6\n"
        "s_mov_b32 s43, %7\n"
        "s_mov_b64 s[0:1], s[48:49]\n"
        "s_mov_b64 exec, -1\n"
        "s_mov_b32 s45, s41\n"
        "s_mov_b32 s46, 0x200000\n"
        "v_mbcnt_lo_u32_b32 v0, -1, 0\n"
        "v_mbcnt_hi_u32_b32 v0, -1, v0\n"
        "v_lshlrev_b32 v1, 4, v0\n"
        "v_lshlrev_b32 v24, 3, v0\n"
        "v_add_u32 v24, 0xaa0000, v24\n"
        "global_load_dwordx2 v[26:27], v24, s[90:91] sc1\n"
        "s_waitcnt vmcnt(0)\n"
        "v_and_b32 v26, 1, v26\n"
        "v_and_b32 v27, 1, v27\n"
        "v_cmp_ne_u32 s[50:51], 0, v26\n"
        "v_cmp_ne_u32 s[52:53], 0, v27\n"
        "s_nop 3\n"
        "s_bcnt1_i32_b64 s47, s[50:51]\n"
        "s_bcnt1_i32_b64 s68, s[52:53]\n"
        "s_add_u32 s47, s47, s68\n"
        "s_cmp_gt_u32 s47, 64\n"
        "s_cselect_b32 s47, 1, 0\n"
        "s_xor_b32 s47, s47, s41\n"
        "s_mul_i32 s89, s47, 0x3f\n"
        "s_xor_b32 s89, s89, 52\n"
        "s_lshr_b32 s44, s89, 0\n"
        "s_and_b32 s44, s44, 1\n"
        "s_lshl_b32 s44, s44, 13\n"
        "v_and_b32 v24, 15, v0\n"
        "v_lshrrev_b32 v25, 4, v0\n"
        "v_lshrrev_b32 v26, 2, v24\n"
        "v_and_b32 v27, 3, v0\n"
        "v_lshl_add_u32 v26, v26, 3, v27\n"
        "s_lshl_b32 s47, s40, 8\n"
        "v_add_u32 v28, s47, v26\n"
        "v_lshlrev_b32 v28, 1, v28\n"
        "global_load_ushort v128, v28, s[94:95] offset:0\n"
        "global_load_ushort v129, v28, s[94:95] offset:8\n"
        "global_load_ushort v130, v28, s[94:95] offset:64\n"
        "global_load_ushort v131, v28, s[94:95] offset:72\n"
        "global_load_ushort v132, v28, s[94:95] offset:128\n"
        "global_load_ushort v133, v28, s[94:95] offset:136\n"
        "global_load_ushort v134, v28, s[94:95] offset:192\n"
        "global_load_ushort v135, v28, s[94:95] offset:200\n"
        "global_load_ushort v136, v28, s[94:95] offset:256\n"
        "global_load_ushort v137, v28, s[94:95] offset:264\n"
        "global_load_ushort v138, v28, s[94:95] offset:320\n"
        "global_load_ushort v139, v28, s[94:95] offset:328\n"
        "global_load_ushort v140, v28, s[94:95] offset:384\n"
        "global_load_ushort v141, v28, s[94:95] offset:392\n"
        "global_load_ushort v142, v28, s[94:95] offset:448\n"
        "global_load_ushort v143, v28, s[94:95] offset:456\n"
        "v_bfe_u32 v27, v24, 1, 3\n"
        "v_add_u32 v29, 0, v25\n"
        "v_xor_b32 v29, v29, v27\n"
        "v_lshlrev_b32 v29, 4, v29\n"
        "v_lshl_add_u32 v29, v26, 7, v29\n"
        "v_add_u32 v9, 0x20000, v29\n"
        "v_add_u32 v29, 4, v25\n"
        "v_xor_b32 v29, v29, v27\n"
        "v_lshlrev_b32 v29, 4, v29\n"
        "v_lshl_add_u32 v29, v26, 7, v29\n"
        "v_add_u32 v10, 0x20000, v29\n"
        "v_lshrrev_b32 v28, 3, v0\n"
        "s_lshl_b32 s47, s40, 2\n"
        "s_add_u32 s47, s47, s43\n"
        "s_lshl_b32 s47, s47, 6\n"
        "v_add_u32 v28, s47, v28\n"
        "v_lshlrev_b32 v28, 12, v28\n"
        "v_and_b32 v29, 7, v0\n"
        "v_bfe_u32 v30, v0, 4, 1\n"
        "v_xor_b32 v29, v29, v30\n"
        "v_xor_b32 v30, 0, v29\n"
        "v_lshl_add_u32 v8, v30, 4, v28\n"
        "v_add_u32 v8, 0x0, v8\n"
        "v_xor_b32 v30, 2, v29\n"
        "v_lshl_add_u32 v12, v30, 4, v28\n"
        "v_add_u32 v12, 0x8000, v12\n"
        "v_xor_b32 v30, 4, v29\n"
        "v_lshl_add_u32 v13, v30, 4, v28\n"
        "v_add_u32 v13, 0x10000, v13\n"
        "v_xor_b32 v30, 6, v29\n"
        "v_lshl_add_u32 v19, v30, 4, v28\n"
        "v_add_u32 v19, 0x18000, v19\n"
        "s_lshl_b32 s59, s43, 13\n"
        "s_add_u32 s59, s59, 0x20000\n"
        "v_min_u32 v26, 11, v24\n"
        "v_and_b32 v27, 1, v25\n"
        "v_lshl_add_u32 v26, v27, 4, v26\n"
        "v_lshlrev_b32 v26, 7, v26\n"
        "s_add_u32 s47, s44, 0x1b5000\n"
        "v_add_u32 v2, s47, v26\n"
        "s_mov_b32 s64, 0xffff0000\n"
        "s_mov_b32 s65, 0xffff0000\n"
        "v_lshrrev_b32 v26, 5, v0\n"
        "v_mul_u32_u24 v26, 768, v26\n"
        "v_and_b32 v27, 31, v0\n"
        "v_lshl_add_u32 v26, v27, 3, v26\n"
        "v_add_u32 v4, 0x1700000, v26\n"
        "s_lshl_b32 s47, s42, 2\n"
        "s_add_u32 s47, s47, s43\n"
        "s_lshl_b32 s68, s47, 12\n"
        "v_add_u32 v5, s68, v1\n"
        "s_lshl_b32 s47, s47, 11\n"
        "s_mul_i32 s68, s40, 0x60000\n"
        "s_add_u32 s47, s47, s68\n"
        "v_add_u32 v11, s47, v1\n"
        "s_lshl_b32 s47, s43, 15\n"
        "v_add_u32 v7, s47, v1\n"
        "v_lshlrev_b32 v6, 5, v25\n"
        "s_waitcnt vmcnt(0)\n"
        "v_lshlrev_b32 v128, 16, v128\n"
        "v_lshlrev_b32 v129, 16, v129\n"
        "v_lshlrev_b32 v130, 16, v130\n"
        "v_lshlrev_b32 v131, 16, v131\n"
        "v_lshlrev_b32 v132, 16, v132\n"
        "v_lshlrev_b32 v133, 16, v133\n"
        "v_lshlrev_b32 v134, 16, v134\n"
        "v_lshlrev_b32 v135, 16, v135\n"
        "v_lshlrev_b32 v136, 16, v136\n"
        "v_lshlrev_b32 v137, 16, v137\n"
        "v_lshlrev_b32 v138, 16, v138\n"
        "v_lshlrev_b32 v139, 16, v139\n"
        "v_lshlrev_b32 v140, 16, v140\n"
        "v_lshlrev_b32 v141, 16, v141\n"
        "v_lshlrev_b32 v142, 16, v142\n"
        "v_lshlrev_b32 v143, 16, v143\n"
        "v_accvgpr_write_b32 a0, v128\n"
        "v_accvgpr_write_b32 a1, v128\n"
        "v_accvgpr_write_b32 a2, v128\n"
        "v_accvgpr_write_b32 a3, v128\n"
        "v_accvgpr_write_b32 a4, v129\n"
        "v_accvgpr_write_b32 a5, v129\n"
        "v_accvgpr_write_b32 a6, v129\n"
        "v_accvgpr_write_b32 a7, v129\n"
        "v_accvgpr_write_b32 a8, v130\n"
        "v_accvgpr_write_b32 a9, v130\n"
        "v_accvgpr_write_b32 a10, v130\n"
        "v_accvgpr_write_b32 a11, v130\n"
        "v_accvgpr_write_b32 a12, v131\n"
        "v_accvgpr_write_b32 a13, v131\n"
        "v_accvgpr_write_b32 a14, v131\n"
        "v_accvgpr_write_b32 a15, v131\n"
        "v_accvgpr_write_b32 a16, v132\n"
        "v_accvgpr_write_b32 a17, v132\n"
        "v_accvgpr_write_b32 a18, v132\n"
        "v_accvgpr_write_b32 a19, v132\n"
        "v_accvgpr_write_b32 a20, v133\n"
        "v_accvgpr_write_b32 a21, v133\n"
        "v_accvgpr_write_b32 a22, v133\n"
        "v_accvgpr_write_b32 a23, v133\n"
        "v_accvgpr_write_b32 a24, v134\n"
        "v_accvgpr_write_b32 a25, v134\n"
        "v_accvgpr_write_b32 a26, v134\n"
        "v_accvgpr_write_b32 a27, v134\n"
        "v_accvgpr_write_b32 a28, v135\n"
        "v_accvgpr_write_b32 a29, v135\n"
        "v_accvgpr_write_b32 a30, v135\n"
        "v_accvgpr_write_b32 a31, v135\n"
        "v_accvgpr_write_b32 a32, v136\n"
        "v_accvgpr_write_b32 a33, v136\n"
        "v_accvgpr_write_b32 a34, v136\n"
        "v_accvgpr_write_b32 a35, v136\n"
        "v_accvgpr_write_b32 a36, v137\n"
        "v_accvgpr_write_b32 a37, v137\n"
        "v_accvgpr_write_b32 a38, v137\n"
        "v_accvgpr_write_b32 a39, v137\n"
        "v_accvgpr_write_b32 a40, v138\n"
        "v_accvgpr_write_b32 a41, v138\n"
        "v_accvgpr_write_b32 a42, v138\n"
        "v_accvgpr_write_b32 a43, v138\n"
        "v_accvgpr_write_b32 a44, v139\n"
        "v_accvgpr_write_b32 a45, v139\n"
        "v_accvgpr_write_b32 a46, v139\n"
        "v_accvgpr_write_b32 a47, v139\n"
        "v_accvgpr_write_b32 a48, v140\n"
        "v_accvgpr_write_b32 a49, v140\n"
        "v_accvgpr_write_b32 a50, v140\n"
        "v_accvgpr_write_b32 a51, v140\n"
        "v_accvgpr_write_b32 a52, v141\n"
        "v_accvgpr_write_b32 a53, v141\n"
        "v_accvgpr_write_b32 a54, v141\n"
        "v_accvgpr_write_b32 a55, v141\n"
        "v_accvgpr_write_b32 a56, v142\n"
        "v_accvgpr_write_b32 a57, v142\n"
        "v_accvgpr_write_b32 a58, v142\n"
        "v_accvgpr_write_b32 a59, v142\n"
        "v_accvgpr_write_b32 a60, v143\n"
        "v_accvgpr_write_b32 a61, v143\n"
        "v_accvgpr_write_b32 a62, v143\n"
        "v_accvgpr_write_b32 a63, v143\n"
        "v_accvgpr_write_b32 a64, v128\n"
        "v_accvgpr_write_b32 a65, v128\n"
        "v_accvgpr_write_b32 a66, v128\n"
        "v_accvgpr_write_b32 a67, v128\n"
        "v_accvgpr_write_b32 a68, v129\n"
        "v_accvgpr_write_b32 a69, v129\n"
        "v_accvgpr_write_b32 a70, v129\n"
        "v_accvgpr_write_b32 a71, v129\n"
        "v_accvgpr_write_b32 a72, v130\n"
        "v_accvgpr_write_b32 a73, v130\n"
        "v_accvgpr_write_b32 a74, v130\n"
        "v_accvgpr_write_b32 a75, v130\n"
        "v_accvgpr_write_b32 a76, v131\n"
        "v_accvgpr_write_b32 a77, v131\n"
        "v_accvgpr_write_b32 a78, v131\n"
        "v_accvgpr_write_b32 a79, v131\n"
        "v_accvgpr_write_b32 a80, v132\n"
        "v_accvgpr_write_b32 a81, v132\n"
        "v_accvgpr_write_b32 a82, v132\n"
        "v_accvgpr_write_b32 a83, v132\n"
        "v_accvgpr_write_b32 a84, v133\n"
        "v_accvgpr_write_b32 a85, v133\n"
        "v_accvgpr_write_b32 a86, v133\n"
        "v_accvgpr_write_b32 a87, v133\n"
        "v_accvgpr_write_b32 a88, v134\n"
        "v_accvgpr_write_b32 a89, v134\n"
        "v_accvgpr_write_b32 a90, v134\n"
        "v_accvgpr_write_b32 a91, v134\n"
        "v_accvgpr_write_b32 a92, v135\n"
        "v_accvgpr_write_b32 a93, v135\n"
        "v_accvgpr_write_b32 a94, v135\n"
        "v_accvgpr_write_b32 a95, v135\n"
        "v_accvgpr_write_b32 a96, v136\n"
        "v_accvgpr_write_b32 a97, v136\n"
        "v_accvgpr_write_b32 a98, v136\n"
        "v_accvgpr_write_b32 a99, v136\n"
        "v_accvgpr_write_b32 a100, v137\n"
        "v_accvgpr_write_b32 a101, v137\n"
        "v_accvgpr_write_b32 a102, v137\n"
        "v_accvgpr_write_b32 a103, v137\n"
        "v_accvgpr_write_b32 a104, v138\n"
        "v_accvgpr_write_b32 a105, v138\n"
        "v_accvgpr_write_b32 a106, v138\n"
        "v_accvgpr_write_b32 a107, v138\n"
        "v_accvgpr_write_b32 a108, v139\n"
        "v_accvgpr_write_b32 a109, v139\n"
        "v_accvgpr_write_b32 a110, v139\n"
        "v_accvgpr_write_b32 a111, v139\n"
        "v_accvgpr_write_b32 a112, v140\n"
        "v_accvgpr_write_b32 a113, v140\n"
        "v_accvgpr_write_b32 a114, v140\n"
        "v_accvgpr_write_b32 a115, v140\n"
        "v_accvgpr_write_b32 a116, v141\n"
        "v_accvgpr_write_b32 a117, v141\n"
        "v_accvgpr_write_b32 a118, v141\n"
        "v_accvgpr_write_b32 a119, v141\n"
        "v_accvgpr_write_b32 a120, v142\n"
        "v_accvgpr_write_b32 a121, v142\n"
        "v_accvgpr_write_b32 a122, v142\n"
        "v_accvgpr_write_b32 a123, v142\n"
        "v_accvgpr_write_b32 a124, v143\n"
        "v_accvgpr_write_b32 a125, v143\n"
        "v_accvgpr_write_b32 a126, v143\n"
        "v_accvgpr_write_b32 a127, v143\n"
        "v_accvgpr_write_b32 a128, v128\n"
        "v_accvgpr_write_b32 a129, v128\n"
        "v_accvgpr_write_b32 a130, v128\n"
        "v_accvgpr_write_b32 a131, v128\n"
        "v_accvgpr_write_b32 a132, v129\n"
        "v_accvgpr_write_b32 a133, v129\n"
        "v_accvgpr_write_b32 a134, v129\n"
        "v_accvgpr_write_b32 a135, v129\n"
        "v_accvgpr_write_b32 a136, v130\n"
        "v_accvgpr_write_b32 a137, v130\n"
        "v_accvgpr_write_b32 a138, v130\n"
        "v_accvgpr_write_b32 a139, v130\n"
        "v_accvgpr_write_b32 a140, v131\n"
        "v_accvgpr_write_b32 a141, v131\n"
        "v_accvgpr_write_b32 a142, v131\n"
        "v_accvgpr_write_b32 a143, v131\n"
        "v_accvgpr_write_b32 a144, v132\n"
        "v_accvgpr_write_b32 a145, v132\n"
        "v_accvgpr_write_b32 a146, v132\n"
        "v_accvgpr_write_b32 a147, v132\n"
        "v_accvgpr_write_b32 a148, v133\n"
        "v_accvgpr_write_b32 a149, v133\n"
        "v_accvgpr_write_b32 a150, v133\n"
        "v_accvgpr_write_b32 a151, v133\n"
        "v_accvgpr_write_b32 a152, v134\n"
        "v_accvgpr_write_b32 a153, v134\n"
        "v_accvgpr_write_b32 a154, v134\n"
        "v_accvgpr_write_b32 a155, v134\n"
        "v_accvgpr_write_b32 a156, v135\n"
        "v_accvgpr_write_b32 a157, v135\n"
        "v_accvgpr_write_b32 a158, v135\n"
        "v_accvgpr_write_b32 a159, v135\n"
        "v_accvgpr_write_b32 a160, v136\n"
        "v_accvgpr_write_b32 a161, v136\n"
        "v_accvgpr_write_b32 a162, v136\n"
        "v_accvgpr_write_b32 a163, v136\n"
        "v_accvgpr_write_b32 a164, v137\n"
        "v_accvgpr_write_b32 a165, v137\n"
        "v_accvgpr_write_b32 a166, v137\n"
        "v_accvgpr_write_b32 a167, v137\n"
        "v_accvgpr_write_b32 a168, v138\n"
        "v_accvgpr_write_b32 a169, v138\n"
        "v_accvgpr_write_b32 a170, v138\n"
        "v_accvgpr_write_b32 a171, v138\n"
        "v_accvgpr_write_b32 a172, v139\n"
        "v_accvgpr_write_b32 a173, v139\n"
        "v_accvgpr_write_b32 a174, v139\n"
        "v_accvgpr_write_b32 a175, v139\n"
        "v_accvgpr_write_b32 a176, v140\n"
        "v_accvgpr_write_b32 a177, v140\n"
        "v_accvgpr_write_b32 a178, v140\n"
        "v_accvgpr_write_b32 a179, v140\n"
        "v_accvgpr_write_b32 a180, v141\n"
        "v_accvgpr_write_b32 a181, v141\n"
        "v_accvgpr_write_b32 a182, v141\n"
        "v_accvgpr_write_b32 a183, v141\n"
        "v_accvgpr_write_b32 a184, v142\n"
        "v_accvgpr_write_b32 a185, v142\n"
        "v_accvgpr_write_b32 a186, v142\n"
        "v_accvgpr_write_b32 a187, v142\n"
        "v_accvgpr_write_b32 a188, v143\n"
        "v_accvgpr_write_b32 a189, v143\n"
        "v_accvgpr_write_b32 a190, v143\n"
        "v_accvgpr_write_b32 a191, v143\n"
        "v_accvgpr_write_b32 a192, v128\n"
        "v_accvgpr_write_b32 a193, v128\n"
        "v_accvgpr_write_b32 a194, v128\n"
        "v_accvgpr_write_b32 a195, v128\n"
        "v_accvgpr_write_b32 a196, v129\n"
        "v_accvgpr_write_b32 a197, v129\n"
        "v_accvgpr_write_b32 a198, v129\n"
        "v_accvgpr_write_b32 a199, v129\n"
        "v_accvgpr_write_b32 a200, v130\n"
        "v_accvgpr_write_b32 a201, v130\n"
        "v_accvgpr_write_b32 a202, v130\n"
        "v_accvgpr_write_b32 a203, v130\n"
        "v_accvgpr_write_b32 a204, v131\n"
        "v_accvgpr_write_b32 a205, v131\n"
        "v_accvgpr_write_b32 a206, v131\n"
        "v_accvgpr_write_b32 a207, v131\n"
        "v_accvgpr_write_b32 a208, v132\n"
        "v_accvgpr_write_b32 a209, v132\n"
        "v_accvgpr_write_b32 a210, v132\n"
        "v_accvgpr_write_b32 a211, v132\n"
        "v_accvgpr_write_b32 a212, v133\n"
        "v_accvgpr_write_b32 a213, v133\n"
        "v_accvgpr_write_b32 a214, v133\n"
        "v_accvgpr_write_b32 a215, v133\n"
        "v_accvgpr_write_b32 a216, v134\n"
        "v_accvgpr_write_b32 a217, v134\n"
        "v_accvgpr_write_b32 a218, v134\n"
        "v_accvgpr_write_b32 a219, v134\n"
        "v_accvgpr_write_b32 a220, v135\n"
        "v_accvgpr_write_b32 a221, v135\n"
        "v_accvgpr_write_b32 a222, v135\n"
        "v_accvgpr_write_b32 a223, v135\n"
        "v_accvgpr_write_b32 a224, v136\n"
        "v_accvgpr_write_b32 a225, v136\n"
        "v_accvgpr_write_b32 a226, v136\n"
        "v_accvgpr_write_b32 a227, v136\n"
        "v_accvgpr_write_b32 a228, v137\n"
        "v_accvgpr_write_b32 a229, v137\n"
        "v_accvgpr_write_b32 a230, v137\n"
        "v_accvgpr_write_b32 a231, v137\n"
        "v_accvgpr_write_b32 a232, v138\n"
        "v_accvgpr_write_b32 a233, v138\n"
        "v_accvgpr_write_b32 a234, v138\n"
        "v_accvgpr_write_b32 a235, v138\n"
        "v_accvgpr_write_b32 a236, v139\n"
        "v_accvgpr_write_b32 a237, v139\n"
        "v_accvgpr_write_b32 a238, v139\n"
        "v_accvgpr_write_b32 a239, v139\n"
        "v_accvgpr_write_b32 a240, v140\n"
        "v_accvgpr_write_b32 a241, v140\n"
        "v_accvgpr_write_b32 a242, v140\n"
        "v_accvgpr_write_b32 a243, v140\n"
        "v_accvgpr_write_b32 a244, v141\n"
        "v_accvgpr_write_b32 a245, v141\n"
        "v_accvgpr_write_b32 a246, v141\n"
        "v_accvgpr_write_b32 a247, v141\n"
        "v_accvgpr_write_b32 a248, v142\n"
        "v_accvgpr_write_b32 a249, v142\n"
        "v_accvgpr_write_b32 a250, v142\n"
        "v_accvgpr_write_b32 a251, v142\n"
        "v_accvgpr_write_b32 a252, v143\n"
        "v_accvgpr_write_b32 a253, v143\n"
        "v_accvgpr_write_b32 a254, v143\n"
        "v_accvgpr_write_b32 a255, v143\n"
        "s_mov_b32 s58, 0\n"
        "s_lshl_b32 s47, s40, 2\n"
        "s_add_u32 s47, s47, s43\n"
        "s_lshl_b32 s47, s47, 18\n"
        "v_lshl_add_u32 v18, v0, 12, s47\n"
        "s_sleep 127\n"
        "s_sleep 127\n"
        "s_sleep 127\n"
        "s_add_u32 s47, s58, 1\n"
        "s_min_u32 s47, s47, 31\n"
        "s_lshl_b32 s47, s47, 7\n"
        "s_add_u32 s54, s92, s47\n"
        "s_addc_u32 s55, s93, 0\n"
        "global_load_dword v43, v18, s[54:55]\n"
        "s_waitcnt vmcnt(0)\n"
        "s_mul_i32 s47, s42, 0x580\n"
        "v_add_u32 v14, s47, v18\n"
        "global_load_dword v64, v14, s[92:93] offset:0\n"
        "global_load_dword v65, v14, s[92:93] offset:128\n"
        "global_load_dword v66, v14, s[92:93] offset:256\n"
        "global_load_dword v67, v14, s[92:93] offset:384\n"
        "global_load_dword v68, v14, s[92:93] offset:512\n"
        "global_load_dword v69, v14, s[92:93] offset:640\n"
        "global_load_dword v70, v14, s[92:93] offset:768\n"
        "global_load_dword v71, v14, s[92:93] offset:896\n"
        "global_load_dword v72, v14, s[92:93] offset:1024\n"
        "global_load_dword v73, v14, s[92:93] offset:1152\n"
        "s_cmp_eq_u32 s42, 2\n"
        "s_cselect_b32 s47, 128, 0\n"
        "v_subrev_u32 v14, s47, v14\n"
        "global_load_dword v74, v14, s[92:93] offset:1280\n"
        "s_add_u32 s47, s58, 0\n"
        "s_lshl_b32 s47, s47, 7\n"
        "s_add_u32 s54, s92, s47\n"
        "s_addc_u32 s55, s93, 0\n"
        "s_add_u32 m0, s59, 0\n"
        "s_nop 0\n"
        "global_load_lds_dwordx4 v8, s[54:55]\n"
        "s_add_u32 m0, s59, 1024\n"
        "s_nop 0\n"
        "global_load_lds_dwordx4 v12, s[54:55]\n"
        "s_add_u32 m0, s59, 2048\n"
        "s_nop 0\n"
        "global_load_lds_dwordx4 v13, s[54:55]\n"
        "s_add_u32 m0, s59, 3072\n"
        "s_nop 0\n"
        "global_load_lds_dwordx4 v19, s[54:55]\n"
        "s_add_u32 s54, s54, 0x20000\n"
        "s_addc_u32 s55, s55, 0\n"
        "s_add_u32 m0, s59, 4096\n"
        "s_nop 0\n"
        "global_load_lds_dwordx4 v8, s[54:55]\n"
        "s_add_u32 m0, s59, 5120\n"
        "s_nop 0\n"
        "global_load_lds_dwordx4 v12, s[54:55]\n"
        "s_add_u32 m0, s59, 6144\n"
        "s_nop 0\n"
        "global_load_lds_dwordx4 v13, s[54:55]\n"
        "s_add_u32 m0, s59, 7168\n"
        "s_nop 0\n"
        "global_load_lds_dwordx4 v19, s[54:55]\n"
        "s_waitcnt vmcnt(0)\n"
        "s_memrealtime s[54:55]\n"
        "s_waitcnt lgkmcnt(0)\n"
        "s_add_u32 s34, s54, 0\n"
        "s_lshl_b32 s34, s34, 6\n"
        "Lg2_loop:\n"
        "s_add_u32 s88, s58, 1\n"
        "s_cmp_lt_u32 s58, 24\n"
        "s_cselect_b32 s69, s41, 0\n"
        "s_add_u32 s69, s88, s69\n"
        "s_mov_b32 s56, s69\n"
        "s_cmp_lg_u32 s43, 0\n"
        "s_cbranch_scc1 Lg2_fs_1\n"
        "v_mov_b32 v3, s56\n"
        "v_mov_b32 v15, s88\n"
        "v_cndmask_b32 v3, v3, v15, s[64:65]\n"
        "s_cmp_ge_u32 s58, 24\n"
        "s_cbranch_scc1 Lg2_np_2\n"
        "s_mov_b32 s69, 48\n"
        ".p2align 6\n"
        "Lg2_w_3:\n"
        "s_memrealtime s[54:55]\n"
        "s_waitcnt lgkmcnt(0)\n"
        "s_lshl_b32 s47, s54, 6\n"
        "s_sub_u32 s47, s34, s47\n"
        "s_cmp_lt_i32 s47, 768\n"
        "s_cbranch_scc1 Lg2_f_4\n"
        "s_sleep 1\n"
        "s_sub_u32 s69, s69, 1\n"
        "s_cbranch_scc0 Lg2_w_3\n"
        "s_mov_b32 s47, 0\n"
        "Lg2_f_4:\n"
        "s_max_i32 s47, s47, 0\n"
        "Lg2_c_5:\n"
        "s_sub_u32 s47, s47, 80\n"
        "s_cbranch_scc0 Lg2_c_5\n"
        "Lg2_np_2:\n"
        "s_branch Lg2_p0_7\n"
        "Lg2_pl_6:\n"
        "s_sleep 8\n"
        "Lg2_p0_7:\n"
        "global_load_dword v14, v2, s[90:91] sc1\n"
        "s_sub_u32 s46, s46, 1\n"
        "s_cbranch_scc1 Lg2_exit\n"
        "s_waitcnt vmcnt(0)\n"
        "v_cmp_lt_u32 vcc, v14, v3\n"
        "s_cbranch_vccnz Lg2_pl_6\n"
        "s_memrealtime s[54:55]\n"
        "s_waitcnt lgkmcnt(0)\n"
        "s_add_u32 s34, s54, 400\n"
        "s_lshl_b32 s34, s34, 6\n"
        "Lg2_fs_1:\n"
        "s_barrier\n"
        "s_lshl_b32 s45, s58, 1\n"
        "s_add_u32 s45, s45, 0\n"
        "s_mul_i32 s68, s45, 0x600\n"
        "s_add_u32 s47, s68, 0x0\n"
        "s_add_u32 s48, s90, s47\n"
        "s_addc_u32 s49, s91, 0\n"
        "s_mul_i32 s68, s45, 0xc000\n"
        "s_add_u32 s47, s68, 0xe00000\n"
        "s_add_u32 s50, s90, s47\n"
        "s_addc_u32 s51, s91, 0\n"
        "global_load_dwordx2 v[52:53], v4, s[48:49] offset:0 sc1\n"
        "global_load_dwordx2 v[54:55], v4, s[48:49] offset:256 sc1\n"
        "global_load_dwordx2 v[56:57], v4, s[48:49] offset:512 sc1\n"
        "global_load_dwordx4 v[64:67], v5, s[50:51] offset:0 sc1\n"
        "global_load_dwordx4 v[68:71], v5, s[50:51] offset:1024 sc1\n"
        "global_load_dwordx4 v[72:75], v5, s[50:51] offset:2048 sc1\n"
        "global_load_dwordx4 v[76:79], v5, s[50:51] offset:3072 sc1\n"
        "s_mov_b64 exec, -1\n"
        "s_lshl_b32 s45, s58, 1\n"
        "s_add_u32 s45, s45, 1\n"
        "s_mul_i32 s68, s45, 0x600\n"
        "s_add_u32 s47, s68, 0x0\n"
        "s_add_u32 s48, s90, s47\n"
        "s_addc_u32 s49, s91, 0\n"
        "s_mul_i32 s68, s45, 0xc000\n"
        "s_add_u32 s47, s68, 0xe00000\n"
        "s_add_u32 s50, s90, s47\n"
        "s_addc_u32 s51, s91, 0\n"
        "global_load_dwordx2 v[58:59], v4, s[48:49] offset:0 sc1\n"
        "global_load_dwordx2 v[60:61], v4, s[48:49] offset:256 sc1\n"
        "global_load_dwordx2 v[62:63], v4, s[48:49] offset:512 sc1\n"
        "global_load_dwordx4 v[80:83], v5, s[50:51] offset:0 sc1\n"
        "global_load_dwordx4 v[84:87], v5, s[50:51] offset:1024 sc1\n"
        "global_load_dwordx4 v[88:91], v5, s[50:51] offset:2048 sc1\n"
        "global_load_dwordx4 v[92:95], v5, s[50:51] offset:3072 sc1\n"
        "s_mov_b64 exec, -1\n"
        "s_lshr_b32 s47, s58, 2\n"
        "s_cmp_lg_u32 s47, s40\n"
        "s_cbranch_scc1 Lg2_nz_8\n"
        "s_mul_i32 s68, s58, 0x18000\n"
        "s_add_u32 s47, s68, 0x1100000\n"
        "s_add_u32 s52, s90, s47\n"
        "s_addc_u32 s53, s91, 0\n"
        "global_load_dwordx4 v[96:99], v5, s[52:53] offset:0 sc1\n"
        "global_load_dwordx4 v[100:103], v5, s[52:53] offset:1024 sc1\n"
        "global_load_dwordx4 v[104:107], v5, s[52:53] offset:2048 sc1\n"
        "global_load_dwordx4 v[108:111], v5, s[52:53] offset:3072 sc1\n"
        "s_mul_i32 s68, s58, 0x18000\n"
        "s_add_u32 s47, s68, 0x110c000\n"
        "s_add_u32 s52, s90, s47\n"
        "s_addc_u32 s53, s91, 0\n"
        "global_load_dwordx4 v[112:115], v5, s[52:53] offset:0 sc1\n"
        "global_load_dwordx4 v[116:119], v5, s[52:53] offset:1024 sc1\n"
        "global_load_dwordx4 v[120:123], v5, s[52:53] offset:2048 sc1\n"
        "global_load_dwordx4 v[124:127], v5, s[52:53] offset:3072 sc1\n"
        "Lg2_nz_8:\n"
        "ds_read_b128 v[128:131], v9 offset:0\n"
        "ds_read_b128 v[132:135], v9 offset:512\n"
        "ds_read_b128 v[136:139], v9 offset:4096\n"
        "ds_read_b128 v[140:143], v9 offset:4608\n"
        "ds_read_b128 v[144:147], v9 offset:8192\n"
        "ds_read_b128 v[148:151], v9 offset:8704\n"
        "ds_read_b128 v[152:155], v9 offset:12288\n"
        "ds_read_b128 v[156:159], v9 offset:12800\n"
        "ds_read_b128 v[160:163], v9 offset:16384\n"
        "ds_read_b128 v[164:167], v9 offset:16896\n"
        "ds_read_b128 v[168:171], v9 offset:20480\n"
        "ds_read_b128 v[172:175], v9 offset:20992\n"
        "ds_read_b128 v[176:179], v9 offset:24576\n"
        "ds_read_b128 v[180:183], v9 offset:25088\n"
        "ds_read_b128 v[184:187], v9 offset:28672\n"
        "ds_read_b128 v[188:191], v9 offset:29184\n"
        "ds_read_b128 v[192:195], v10 offset:0\n"
        "ds_read_b128 v[196:199], v10 offset:512\n"
        "ds_read_b128 v[200:203], v10 offset:4096\n"
        "ds_read_b128 v[204:207], v10 offset:4608\n"
        "ds_read_b128 v[208:211], v10 offset:8192\n"
        "ds_read_b128 v[212:215], v10 offset:8704\n"
        "ds_read_b128 v[216:219], v10 offset:12288\n"
        "ds_read_b128 v[220:223], v10 offset:12800\n"
        "ds_read_b128 v[224:227], v10 offset:16384\n"
        "ds_read_b128 v[228:231], v10 offset:16896\n"
        "ds_read_b128 v[232:235], v10 offset:20480\n"
        "ds_read_b128 v[236:239], v10 offset:20992\n"
        "ds_read_b128 v[240:243], v10 offset:24576\n"
        "ds_read_b128 v[244:247], v10 offset:25088\n"
        "ds_read_b128 v[248:251], v10 offset:28672\n"
        "ds_read_b128 v[252:255], v10 offset:29184\n"
        "s_waitcnt lgkmcnt(0)\n"
        "s_barrier\n"
        "s_add_u32 s47, s58, 1\n"
        "s_lshl_b32 s47, s47, 7\n"
        "s_add_u32 s54, s92, s47\n"
        "s_addc_u32 s55, s93, 0\n"
        "s_add_u32 m0, s59, 0\n"
        "s_nop 0\n"
        "global_load_lds_dwordx4 v8, s[54:55]\n"
        "s_add_u32 m0, s59, 1024\n"
        "s_nop 0\n"
        "global_load_lds_dwordx4 v12, s[54:55]\n"
        "s_add_u32 m0, s59, 2048\n"
        "s_nop 0\n"
        "global_load_lds_dwordx4 v13, s[54:55]\n"
        "s_add_u32 m0, s59, 3072\n"
        "s_nop 0\n"
        "global_load_lds_dwordx4 v19, s[54:55]\n"
        "s_add_u32 s54, s54, 0x20000\n"
        "s_addc_u32 s55, s55, 0\n"
        "s_add_u32 m0, s59, 4096\n"
        "s_nop 0\n"
        "global_load_lds_dwordx4 v8, s[54:55]\n"
        "s_add_u32 m0, s59, 5120\n"
        "s_nop 0\n"
        "global_load_lds_dwordx4 v12, s[54:55]\n"
        "s_add_u32 m0, s59, 6144\n"
        "s_nop 0\n"
        "global_load_lds_dwordx4 v13, s[54:55]\n"
        "s_add_u32 m0, s59, 7168\n"
        "s_nop 0\n"
        "global_load_lds_dwordx4 v19, s[54:55]\n"
        "s_waitcnt vmcnt(19)\n"
        "v_add_f32 v14, v52, v54\n"
        "v_add_f32 v15, v53, v55\n"
        "v_add_f32 v14, v14, v56\n"
        "v_add_f32 v15, v15, v57\n"
        "v_mov_b32 v16, v14\n"
        "v_mov_b32 v17, v15\n"
        "s_nop 0\n"
        "v_permlane32_swap_b32 v14, v16\n"
        "v_permlane32_swap_b32 v15, v17\n"
        "v_add_f32 v14, v14, v16\n"
        "v_add_f32 v15, v15, v17\n"
        "v_mul_f32 v14, 0x3aaaaaab, v14\n"
        "v_mul_f32 v15, 0x3aaaaaab, v15\n"
        "v_fma_f32 v15, -v14, v14, v15\n"
        "v_max_f32 v15, 0, v15\n"
        "v_add_f32 v15, 0x3727c5ac, v15\n"
        "v_rsq_f32 v15, v15\n"
        "s_nop 0\n"
        "v_mul_f32 v14, v14, v15\n"
        "ds_bpermute_b32 v20, v6, v15 offset:0\n"
        "ds_bpermute_b32 v21, v6, v14 offset:0\n"
        "ds_bpermute_b32 v22, v6, v15 offset:4\n"
        "ds_bpermute_b32 v23, v6, v14 offset:4\n"
        "ds_bpermute_b32 v24, v6, v15 offset:8\n"
        "ds_bpermute_b32 v25, v6, v14 offset:8\n"
        "ds_bpermute_b32 v26, v6, v15 offset:12\n"
        "ds_bpermute_b32 v27, v6, v14 offset:12\n"
        "ds_bpermute_b32 v28, v6, v15 offset:16\n"
        "ds_bpermute_b32 v29, v6, v14 offset:16\n"
        "ds_bpermute_b32 v30, v6, v15 offset:20\n"
        "ds_bpermute_b32 v31, v6, v14 offset:20\n"
        "ds_bpermute_b32 v32, v6, v15 offset:24\n"
        "ds_bpermute_b32 v33, v6, v14 offset:24\n"
        "ds_bpermute_b32 v34, v6, v15 offset:28\n"
        "ds_bpermute_b32 v35, v6, v14 offset:28\n"
        "s_waitcnt lgkmcnt(0)\n"
        "s_waitcnt vmcnt(18)\n"
        "v_lshlrev_b32 v40, 16, v64\n"
        "v_and_b32 v64, 0xffff0000, v64\n"
        "v_fma_f32 v40, v40, v20, -v21\n"
        "v_fma_f32 v64, v64, v22, -v23\n"
        "v_cvt_pk_bf16_f32 v36, v40, v64\n"
        "v_lshlrev_b32 v41, 16, v65\n"
        "v_and_b32 v65, 0xffff0000, v65\n"
        "v_fma_f32 v41, v41, v24, -v25\n"
        "v_fma_f32 v65, v65, v26, -v27\n"
        "v_cvt_pk_bf16_f32 v37, v41, v65\n"
        "v_lshlrev_b32 v40, 16, v66\n"
        "v_and_b32 v66, 0xffff0000, v66\n"
        "v_fma_f32 v40, v40, v28, -v29\n"
        "v_fma_f32 v66, v66, v30, -v31\n"
        "v_cvt_pk_bf16_f32 v38, v40, v66\n"
        "v_lshlrev_b32 v41, 16, v67\n"
        "v_and_b32 v67, 0xffff0000, v67\n"
        "v_fma_f32 v41, v41, v32, -v33\n"
        "v_fma_f32 v67, v67, v34, -v35\n"
        "v_cvt_pk_bf16_f32 v39, v41, v67\n"
        "s_nop 1\n"
        "v_mfma_f32_16x16x32_bf16 a[0:3], v[36:39], v[128:131], a[0:3]\n"
        "s_waitcnt vmcnt(17)\n"
        "v_lshlrev_b32 v48, 16, v68\n"
        "v_and_b32 v68, 0xffff0000, v68\n"
        "v_mfma_f32_16x16x32_bf16 a[4:7], v[36:39], v[132:135], a[4:7]\n"
        "v_fma_f32 v48, v48, v20, -v21\n"
        "v_fma_f32 v68, v68, v22, -v23\n"
        "v_mfma_f32_16x16x32_bf16 a[8:11], v[36:39], v[136:139], a[8:11]\n"
        "v_cvt_pk_bf16_f32 v44, v48, v68\n"
        "v_lshlrev_b32 v49, 16, v69\n"
        "v_mfma_f32_16x16x32_bf16 a[12:15], v[36:39], v[140:143], a[12:15]\n"
        "v_and_b32 v69, 0xffff0000, v69\n"
        "v_fma_f32 v49, v49, v24, -v25\n"
        "v_mfma_f32_16x16x32_bf16 a[16:19], v[36:39], v[144:147], a[16:19]\n"
        "v_fma_f32 v69, v69, v26, -v27\n"
        "v_cvt_pk_bf16_f32 v45, v49, v69\n"
        "v_mfma_f32_16x16x32_bf16 a[20:23], v[36:39], v[148:151], a[20:23]\n"
        "v_lshlrev_b32 v48, 16, v70\n"
        "v_and_b32 v70, 0xffff0000, v70\n"
        "v_mfma_f32_16x16x32_bf16 a[24:27], v[36:39], v[152:155], a[24:27]\n"
        "v_fma_f32 v48, v48, v28, -v29\n"
        "v_fma_f32 v70, v70, v30, -v31\n"
        "v_mfma_f32_16x16x32_bf16 a[28:31], v[36:39], v[156:159], a[28:31]\n"
        "v_cvt_pk_bf16_f32 v46, v48, v70\n"
        "v_lshlrev_b32 v49, 16, v71\n"
        "v_mfma_f32_16x16x32_bf16 a[32:35], v[36:39], v[160:163], a[32:35]\n"
        "v_and_b32 v71, 0xffff0000, v71\n"
        "v_fma_f32 v49, v49, v32, -v33\n"
        "v_mfma_f32_16x16x32_bf16 a[36:39], v[36:39], v[164:167], a[36:39]\n"
        "v_fma_f32 v71, v71, v34, -v35\n"
        "v_cvt_pk_bf16_f32 v47, v49, v71\n"
        "v_mfma_f32_16x16x32_bf16 a[40:43], v[36:39], v[168:171], a[40:43]\n"
        "v_mfma_f32_16x16x32_bf16 a[44:47], v[36:39], v[172:175], a[44:47]\n"
        "v_mfma_f32_16x16x32_bf16 a[48:51], v[36:39], v[176:179], a[48:51]\n"
        "v_mfma_f32_16x16x32_bf16 a[52:55], v[36:39], v[180:183], a[52:55]\n"
        "v_mfma_f32_16x16x32_bf16 a[56:59], v[36:39], v[184:187], a[56:59]\n"
        "v_mfma_f32_16x16x32_bf16 a[60:63], v[36:39], v[188:191], a[60:63]\n"
        "v_mfma_f32_16x16x32_bf16 a[64:67], v[44:47], v[128:131], a[64:67]\n"
        "s_waitcnt vmcnt(16)\n"
        "v_lshlrev_b32 v40, 16, v72\n"
        "v_and_b32 v72, 0xffff0000, v72\n"
        "v_mfma_f32_16x16x32_bf16 a[68:71], v[44:47], v[132:135], a[68:71]\n"
        "v_fma_f32 v40, v40, v20, -v21\n"
        "v_fma_f32 v72, v72, v22, -v23\n"
        "v_mfma_f32_16x16x32_bf16 a[72:75], v[44:47], v[136:139], a[72:75]\n"
        "v_cvt_pk_bf16_f32 v36, v40, v72\n"
        "v_lshlrev_b32 v41, 16, v73\n"
        "v_mfma_f32_16x16x32_bf16 a[76:79], v[44:47], v[140:143], a[76:79]\n"
        "v_and_b32 v73, 0xffff0000, v73\n"
        "v_fma_f32 v41, v41, v24, -v25\n"
        "v_mfma_f32_16x16x32_bf16 a[80:83], v[44:47], v[144:147], a[80:83]\n"
        "v_fma_f32 v73, v73, v26, -v27\n"
        "v_cvt_pk_bf16_f32 v37, v41, v73\n"
        "v_mfma_f32_16x16x32_bf16 a[84:87], v[44:47], v[148:151], a[84:87]\n"
        "v_lshlrev_b32 v40, 16, v74\n"
        "v_and_b32 v74, 0xffff0000, v74\n"
        "v_mfma_f32_16x16x32_bf16 a[88:91], v[44:47], v[152:155], a[88:91]\n"
        "v_fma_f32 v40, v40, v28, -v29\n"
        "v_fma_f32 v74, v74, v30, -v31\n"
        "v_mfma_f32_16x16x32_bf16 a[92:95], v[44:47], v[156:159], a[92:95]\n"
        "v_cvt_pk_bf16_f32 v38, v40, v74\n"
        "v_lshlrev_b32 v41, 16, v75\n"
        "v_mfma_f32_16x16x32_bf16 a[96:99], v[44:47], v[160:163], a[96:99]\n"
        "v_and_b32 v75, 0xffff0000, v75\n"
        "v_fma_f32 v41, v41, v32, -v33\n"
        "v_mfma_f32_16x16x32_bf16 a[100:103], v[44:47], v[164:167], a[100:103]\n"
        "v_fma_f32 v75, v75, v34, -v35\n"
        "v_cvt_pk_bf16_f32 v39, v41, v75\n"
        "v_mfma_f32_16x16x32_bf16 a[104:107], v[44:47], v[168:171], a[104:107]\n"
        "v_mfma_f32_16x16x32_bf16 a[108:111], v[44:47], v[172:175], a[108:111]\n"
        "v_mfma_f32_16x16x32_bf16 a[112:115], v[44:47], v[176:179], a[112:115]\n"
        "v_mfma_f32_16x16x32_bf16 a[116:119], v[44:47], v[180:183], a[116:119]\n"
        "v_mfma_f32_16x16x32_bf16 a[120:123], v[44:47], v[184:187], a[120:123]\n"
        "v_mfma_f32_16x16x32_bf16 a[124:127], v[44:47], v[188:191], a[124:127]\n"
        "v_mfma_f32_16x16x32_bf16 a[128:131], v[36:39], v[128:131], a[128:131]\n"
        "s_waitcnt vmcnt(15)\n"
        "v_lshlrev_b32 v48, 16, v76\n"
        "v_and_b32 v76, 0xffff0000, v76\n"
        "v_mfma_f32_16x16x32_bf16 a[132:135], v[36:39], v[132:135], a[132:135]\n"
        "v_fma_f32 v48, v48, v20, -v21\n"
        "v_fma_f32 v76, v76, v22, -v23\n"
        "v_mfma_f32_16x16x32_bf16 a[136:139], v[36:39], v[136:139], a[136:139]\n"
        "v_cvt_pk_bf16_f32 v44, v48, v76\n"
        "v_lshlrev_b32 v49, 16, v77\n"
        "v_mfma_f32_16x16x32_bf16 a[140:143], v[36:39], v[140:143], a[140:143]\n"
        "v_and_b32 v77, 0xffff0000, v77\n"
        "v_fma_f32 v49, v49, v24, -v25\n"
        "v_mfma_f32_16x16x32_bf16 a[144:147], v[36:39], v[144:147], a[144:147]\n"
        "v_fma_f32 v77, v77, v26, -v27\n"
        "v_cvt_pk_bf16_f32 v45, v49, v77\n"
        "v_mfma_f32_16x16x32_bf16 a[148:151], v[36:39], v[148:151], a[148:151]\n"
        "v_lshlrev_b32 v48, 16, v78\n"
        "v_and_b32 v78, 0xffff0000, v78\n"
        "v_mfma_f32_16x16x32_bf16 a[152:155], v[36:39], v[152:155], a[152:155]\n"
        "v_fma_f32 v48, v48, v28, -v29\n"
        "v_fma_f32 v78, v78, v30, -v31\n"
        "v_mfma_f32_16x16x32_bf16 a[156:159], v[36:39], v[156:159], a[156:159]\n"
        "v_cvt_pk_bf16_f32 v46, v48, v78\n"
        "v_lshlrev_b32 v49, 16, v79\n"
        "v_mfma_f32_16x16x32_bf16 a[160:163], v[36:39], v[160:163], a[160:163]\n"
        "v_and_b32 v79, 0xffff0000, v79\n"
        "v_fma_f32 v49, v49, v32, -v33\n"
        "v_mfma_f32_16x16x32_bf16 a[164:167], v[36:39], v[164:167], a[164:167]\n"
        "v_fma_f32 v79, v79, v34, -v35\n"
        "v_cvt_pk_bf16_f32 v47, v49, v79\n"
        "v_mfma_f32_16x16x32_bf16 a[168:171], v[36:39], v[168:171], a[168:171]\n"
        "v_mfma_f32_16x16x32_bf16 a[172:175], v[36:39], v[172:175], a[172:175]\n"
        "v_mfma_f32_16x16x32_bf16 a[176:179], v[36:39], v[176:179], a[176:179]\n"
        "v_mfma_f32_16x16x32_bf16 a[180:183], v[36:39], v[180:183], a[180:183]\n"
        "v_mfma_f32_16x16x32_bf16 a[184:187], v[36:39], v[184:187], a[184:187]\n"
        "v_mfma_f32_16x16x32_bf16 a[188:191], v[36:39], v[188:191], a[188:191]\n"
        "v_mfma_f32_16x16x32_bf16 a[192:195], v[44:47], v[128:131], a[192:195]\n"
        "v_mfma_f32_16x16x32_bf16 a[196:199], v[44:47], v[132:135], a[196:199]\n"
        "v_mfma_f32_16x16x32_bf16 a[200:203], v[44:47], v[136:139], a[200:203]\n"
        "v_mfma_f32_16x16x32_bf16 a[204:207], v[44:47], v[140:143], a[204:207]\n"
        "v_mfma_f32_16x16x32_bf16 a[208:211], v[44:47], v[144:147], a[208:211]\n"
        "v_mfma_f32_16x16x32_bf16 a[212:215], v[44:47], v[148:151], a[212:215]\n"
        "v_mfma_f32_16x16x32_bf16 a[216:219], v[44:47], v[152:155], a[216:219]\n"
        "v_mfma_f32_16x16x32_bf16 a[220:223], v[44:47], v[156:159], a[220:223]\n"
        "v_mfma_f32_16x16x32_bf16 a[224:227], v[44:47], v[160:163], a[224:227]\n"
        "v_mfma_f32_16x16x32_bf16 a[228:231], v[44:47], v[164:167], a[228:231]\n"
        "v_mfma_f32_16x16x32_bf16 a[232:235], v[44:47], v[168:171], a[232:235]\n"
        "v_mfma_f32_16x16x32_bf16 a[236:239], v[44:47], v[172:175], a[236:239]\n"
        "v_mfma_f32_16x16x32_bf16 a[240:243], v[44:47], v[176:179], a[240:243]\n"
        "v_mfma_f32_16x16x32_bf16 a[244:247], v[44:47], v[180:183], a[244:247]\n"
        "v_mfma_f32_16x16x32_bf16 a[248:251], v[44:47], v[184:187], a[248:251]\n"
        "v_mfma_f32_16x16x32_bf16 a[252:255], v[44:47], v[188:191], a[252:255]\n"
        "s_waitcnt vmcnt(12)\n"
        "v_add_f32 v14, v58, v60\n"
        "v_add_f32 v15, v59, v61\n"
        "v_add_f32 v14, v14, v62\n"
        "v_add_f32 v15, v15, v63\n"
        "v_mov_b32 v16, v14\n"
        "v_mov_b32 v17, v15\n"
        "s_nop 0\n"
        "v_permlane32_swap_b32 v14, v16\n"
        "v_permlane32_swap_b32 v15, v17\n"
        "v_add_f32 v14, v14, v16\n"
        "v_add_f32 v15, v15, v17\n"
        "v_mul_f32 v14, 0x3aaaaaab, v14\n"
        "v_mul_f32 v15, 0x3aaaaaab, v15\n"
        "v_fma_f32 v15, -v14, v14, v15\n"
        "v_max_f32 v15, 0, v15\n"
        "v_add_f32 v15, 0x3727c5ac, v15\n"
        "v_rsq_f32 v15, v15\n"
        "s_nop 0\n"
        "v_mul_f32 v14, v14, v15\n"
        "ds_bpermute_b32 v20, v6, v15 offset:0\n"
        "ds_bpermute_b32 v21, v6, v14 offset:0\n"
        "ds_bpermute_b32 v22, v6, v15 offset:4\n"
        "ds_bpermute_b32 v23, v6, v14 offset:4\n"
        "ds_bpermute_b32 v24, v6, v15 offset:8\n"
        "ds_bpermute_b32 v25, v6, v14 offset:8\n"
        "ds_bpermute_b32 v26, v6, v15 offset:12\n"
        "ds_bpermute_b32 v27, v6, v14 offset:12\n"
        "ds_bpermute_b32 v28, v6, v15 offset:16\n"
        "ds_bpermute_b32 v29, v6, v14 offset:16\n"
        "ds_bpermute_b32 v30, v6, v15 offset:20\n"
        "ds_bpermute_b32 v31, v6, v14 offset:20\n"
        "ds_bpermute_b32 v32, v6, v15 offset:24\n"
        "ds_bpermute_b32 v33, v6, v14 offset:24\n"
        "ds_bpermute_b32 v34, v6, v15 offset:28\n"
        "ds_bpermute_b32 v35, v6, v14 offset:28\n"
        "s_waitcnt lgkmcnt(0)\n"
        "s_waitcnt vmcnt(11)\n"
        "v_lshlrev_b32 v40, 16, v80\n"
        "v_and_b32 v80, 0xffff0000, v80\n"
        "v_fma_f32 v40, v40, v20, -v21\n"
        "v_fma_f32 v80, v80, v22, -v23\n"
        "v_cvt_pk_bf16_f32 v36, v40, v80\n"
        "v_lshlrev_b32 v41, 16, v81\n"
        "v_and_b32 v81, 0xffff0000, v81\n"
        "v_fma_f32 v41, v41, v24, -v25\n"
        "v_fma_f32 v81, v81, v26, -v27\n"
        "v_cvt_pk_bf16_f32 v37, v41, v81\n"
        "v_lshlrev_b32 v40, 16, v82\n"
        "v_and_b32 v82, 0xffff0000, v82\n"
        "v_fma_f32 v40, v40, v28, -v29\n"
        "v_fma_f32 v82, v82, v30, -v31\n"
        "v_cvt_pk_bf16_f32 v38, v40, v82\n"
        "v_lshlrev_b32 v41, 16, v83\n"
        "v_and_b32 v83, 0xffff0000, v83\n"
        "v_fma_f32 v41, v41, v32, -v33\n"
        "v_fma_f32 v83, v83, v34, -v35\n"
        "v_cvt_pk_bf16_f32 v39, v41, v83\n"
        "s_nop 1\n"
        "v_mfma_f32_16x16x32_bf16 a[0:3], v[36:39], v[192:195], a[0:3]\n"
        "s_waitcnt vmcnt(10)\n"
        "v_lshlrev_b32 v48, 16, v84\n"
        "v_and_b32 v84, 0xffff0000, v84\n"
        "v_mfma_f32_16x16x32_bf16 a[4:7], v[36:39], v[196:199], a[4:7]\n"
        "v_fma_f32 v48, v48, v20, -v21\n"
        "v_fma_f32 v84, v84, v22, -v23\n"
        "v_mfma_f32_16x16x32_bf16 a[8:11], v[36:39], v[200:203], a[8:11]\n"
        "v_cvt_pk_bf16_f32 v44, v48, v84\n"
        "v_lshlrev_b32 v49, 16, v85\n"
        "v_mfma_f32_16x16x32_bf16 a[12:15], v[36:39], v[204:207], a[12:15]\n"
        "v_and_b32 v85, 0xffff0000, v85\n"
        "v_fma_f32 v49, v49, v24, -v25\n"
        "v_mfma_f32_16x16x32_bf16 a[16:19], v[36:39], v[208:211], a[16:19]\n"
        "v_fma_f32 v85, v85, v26, -v27\n"
        "v_cvt_pk_bf16_f32 v45, v49, v85\n"
        "v_mfma_f32_16x16x32_bf16 a[20:23], v[36:39], v[212:215], a[20:23]\n"
        "v_lshlrev_b32 v48, 16, v86\n"
        "v_and_b32 v86, 0xffff0000, v86\n"
        "v_mfma_f32_16x16x32_bf16 a[24:27], v[36:39], v[216:219], a[24:27]\n"
        "v_fma_f32 v48, v48, v28, -v29\n"
        "v_fma_f32 v86, v86, v30, -v31\n"
        "v_mfma_f32_16x16x32_bf16 a[28:31], v[36:39], v[220:223], a[28:31]\n"
        "v_cvt_pk_bf16_f32 v46, v48, v86\n"
        "v_lshlrev_b32 v49, 16, v87\n"
        "v_mfma_f32_16x16x32_bf16 a[32:35], v[36:39], v[224:227], a[32:35]\n"
        "v_and_b32 v87, 0xffff0000, v87\n"
        "v_fma_f32 v49, v49, v32, -v33\n"
        "v_mfma_f32_16x16x32_bf16 a[36:39], v[36:39], v[228:231], a[36:39]\n"
        "v_fma_f32 v87, v87, v34, -v35\n"
        "v_cvt_pk_bf16_f32 v47, v49, v87\n"
        "v_mfma_f32_16x16x32_bf16 a[40:43], v[36:39], v[232:235], a[40:43]\n"
        "v_mfma_f32_16x16x32_bf16 a[44:47], v[36:39], v[236:239], a[44:47]\n"
        "v_mfma_f32_16x16x32_bf16 a[48:51], v[36:39], v[240:243], a[48:51]\n"
        "v_mfma_f32_16x16x32_bf16 a[52:55], v[36:39], v[244:247], a[52:55]\n"
        "v_mfma_f32_16x16x32_bf16 a[56:59], v[36:39], v[248:251], a[56:59]\n"
        "v_mfma_f32_16x16x32_bf16 a[60:63], v[36:39], v[252:255], a[60:63]\n"
        "v_mfma_f32_16x16x32_bf16 a[64:67], v[44:47], v[192:195], a[64:67]\n"
        "s_waitcnt vmcnt(9)\n"
        "v_lshlrev_b32 v40, 16, v88\n"
        "v_and_b32 v88, 0xffff0000, v88\n"
        "v_mfma_f32_16x16x32_bf16 a[68:71], v[44:47], v[196:199], a[68:71]\n"
        "v_fma_f32 v40, v40, v20, -v21\n"
        "v_fma_f32 v88, v88, v22, -v23\n"
        "v_mfma_f32_16x16x32_bf16 a[72:75], v[44:47], v[200:203], a[72:75]\n"
        "v_cvt_pk_bf16_f32 v36, v40, v88\n"
        "v_lshlrev_b32 v41, 16, v89\n"
        "v_mfma_f32_16x16x32_bf16 a[76:79], v[44:47], v[204:207], a[76:79]\n"
        "v_and_b32 v89, 0xffff0000, v89\n"
        "v_fma_f32 v41, v41, v24, -v25\n"
        "v_mfma_f32_16x16x32_bf16 a[80:83], v[44:47], v[208:211], a[80:83]\n"
        "v_fma_f32 v89, v89, v26, -v27\n"
        "v_cvt_pk_bf16_f32 v37, v41, v89\n"
        "v_mfma_f32_16x16x32_bf16 a[84:87], v[44:47], v[212:215], a[84:87]\n"
        "v_lshlrev_b32 v40, 16, v90\n"
        "v_and_b32 v90, 0xffff0000, v90\n"
        "v_mfma_f32_16x16x32_bf16 a[88:91], v[44:47], v[216:219], a[88:91]\n"
        "v_fma_f32 v40, v40, v28, -v29\n"
        "v_fma_f32 v90, v90, v30, -v31\n"
        "v_mfma_f32_16x16x32_bf16 a[92:95], v[44:47], v[220:223], a[92:95]\n"
        "v_cvt_pk_bf16_f32 v38, v40, v90\n"
        "v_lshlrev_b32 v41, 16, v91\n"
        "v_mfma_f32_16x16x32_bf16 a[96:99], v[44:47], v[224:227], a[96:99]\n"
        "v_and_b32 v91, 0xffff0000, v91\n"
        "v_fma_f32 v41, v41, v32, -v33\n"
        "v_mfma_f32_16x16x32_bf16 a[100:103], v[44:47], v[228:231], a[100:103]\n"
        "v_fma_f32 v91, v91, v34, -v35\n"
        "v_cvt_pk_bf16_f32 v39, v41, v91\n"
        "v_mfma_f32_16x16x32_bf16 a[104:107], v[44:47], v[232:235], a[104:107]\n"
        "v_mfma_f32_16x16x32_bf16 a[108:111], v[44:47], v[236:239], a[108:111]\n"
        "v_mfma_f32_16x16x32_bf16 a[112:115], v[44:47], v[240:243], a[112:115]\n"
        "v_mfma_f32_16x16x32_bf16 a[116:119], v[44:47], v[244:247], a[116:119]\n"
        "v_mfma_f32_16x16x32_bf16 a[120:123], v[44:47], v[248:251], a[120:123]\n"
        "v_mfma_f32_16x16x32_bf16 a[124:127], v[44:47], v[252:255], a[124:127]\n"
        "v_mfma_f32_16x16x32_bf16 a[128:131], v[36:39], v[192:195], a[128:131]\n"
        "s_waitcnt vmcnt(8)\n"
        "v_lshlrev_b32 v48, 16, v92\n"
        "v_and_b32 v92, 0xffff0000, v92\n"
        "v_mfma_f32_16x16x32_bf16 a[132:135], v[36:39], v[196:199], a[132:135]\n"
        "v_fma_f32 v48, v48, v20, -v21\n"
        "v_fma_f32 v92, v92, v22, -v23\n"
        "v_mfma_f32_16x16x32_bf16 a[136:139], v[36:39], v[200:203], a[136:139]\n"
        "v_cvt_pk_bf16_f32 v44, v48, v92\n"
        "v_lshlrev_b32 v49, 16, v93\n"
        "v_mfma_f32_16x16x32_bf16 a[140:143], v[36:39], v[204:207], a[140:143]\n"
        "v_and_b32 v93, 0xffff0000, v93\n"
        "v_fma_f32 v49, v49, v24, -v25\n"
        "v_mfma_f32_16x16x32_bf16 a[144:147], v[36:39], v[208:211], a[144:147]\n"
        "v_fma_f32 v93, v93, v26, -v27\n"
        "v_cvt_pk_bf16_f32 v45, v49, v93\n"
        "v_mfma_f32_16x16x32_bf16 a[148:151], v[36:39], v[212:215], a[148:151]\n"
        "v_lshlrev_b32 v48, 16, v94\n"
        "v_and_b32 v94, 0xffff0000, v94\n"
        "v_mfma_f32_16x16x32_bf16 a[152:155], v[36:39], v[216:219], a[152:155]\n"
        "v_fma_f32 v48, v48, v28, -v29\n"
        "v_fma_f32 v94, v94, v30, -v31\n"
        "v_mfma_f32_16x16x32_bf16 a[156:159], v[36:39], v[220:223], a[156:159]\n"
        "v_cvt_pk_bf16_f32 v46, v48, v94\n"
        "v_lshlrev_b32 v49, 16, v95\n"
        "v_mfma_f32_16x16x32_bf16 a[160:163], v[36:39], v[224:227], a[160:163]\n"
        "v_and_b32 v95, 0xffff0000, v95\n"
        "v_fma_f32 v49, v49, v32, -v33\n"
        "v_mfma_f32_16x16x32_bf16 a[164:167], v[36:39], v[228:231], a[164:167]\n"
        "v_fma_f32 v95, v95, v34, -v35\n"
        "v_cvt_pk_bf16_f32 v47, v49, v95\n"
        "v_mfma_f32_16x16x32_bf16 a[168:171], v[36:39], v[232:235], a[168:171]\n"
        "v_mfma_f32_16x16x32_bf16 a[172:175], v[36:39], v[236:239], a[172:175]\n"
        "v_mfma_f32_16x16x32_bf16 a[176:179], v[36:39], v[240:243], a[176:179]\n"
        "v_mfma_f32_16x16x32_bf16 a[180:183], v[36:39], v[244:247], a[180:183]\n"
        "v_mfma_f32_16x16x32_bf16 a[184:187], v[36:39], v[248:251], a[184:187]\n"
        "v_mfma_f32_16x16x32_bf16 a[188:191], v[36:39], v[252:255], a[188:191]\n"
        "v_mfma_f32_16x16x32_bf16 a[192:195], v[44:47], v[192:195], a[192:195]\n"
        "v_mfma_f32_16x16x32_bf16 a[196:199], v[44:47], v[196:199], a[196:199]\n"
        "v_mfma_f32_16x16x32_bf16 a[200:203], v[44:47], v[200:203], a[200:203]\n"
        "v_mfma_f32_16x16x32_bf16 a[204:207], v[44:47], v[204:207], a[204:207]\n"
        "v_mfma_f32_16x16x32_bf16 a[208:211], v[44:47], v[208:211], a[208:211]\n"
        "v_mfma_f32_16x16x32_bf16 a[212:215], v[44:47], v[212:215], a[212:215]\n"
        "v_mfma_f32_16x16x32_bf16 a[216:219], v[44:47], v[216:219], a[216:219]\n"
        "v_mfma_f32_16x16x32_bf16 a[220:223], v[44:47], v[220:223], a[220:223]\n"
        "v_mfma_f32_16x16x32_bf16 a[224:227], v[44:47], v[224:227], a[224:227]\n"
        "v_mfma_f32_16x16x32_bf16 a[228:231], v[44:47], v[228:231], a[228:231]\n"
        "v_mfma_f32_16x16x32_bf16 a[232:235], v[44:47], v[232:235], a[232:235]\n"
        "v_mfma_f32_16x16x32_bf16 a[236:239], v[44:47], v[236:239], a[236:239]\n"
        "v_mfma_f32_16x16x32_bf16 a[240:243], v[44:47], v[240:243], a[240:243]\n"
        "v_mfma_f32_16x16x32_bf16 a[244:247], v[44:47], v[244:247], a[244:247]\n"
        "v_mfma_f32_16x16x32_bf16 a[248:251], v[44:47], v[248:251], a[248:251]\n"
        "v_mfma_f32_16x16x32_bf16 a[252:255], v[44:47], v[252:255], a[252:255]\n"
        "s_waitcnt vmcnt(0)\n"
        "s_lshr_b32 s47, s58, 2\n"
        "s_cmp_lg_u32 s47, s40\n"
        "s_cbranch_scc1 Lg2_nw_9\n"
        "s_and_b32 s47, s58, 3\n"
        "s_lshl_b32 s47, s47, 13\n"
        "v_add_u32 v14, s47, v7\n"
        "ds_write_b128 v14, v[96:99] offset:0\n"
        "ds_write_b128 v14, v[100:103] offset:1024\n"
        "ds_write_b128 v14, v[104:107] offset:2048\n"
        "ds_write_b128 v14, v[108:111] offset:3072\n"
        "ds_write_b128 v14, v[112:115] offset:4096\n"
        "ds_write_b128 v14, v[116:119] offset:5120\n"
        "ds_write_b128 v14, v[120:123] offset:6144\n"
        "ds_write_b128 v14, v[124:127] offset:7168\n"
        "s_waitcnt lgkmcnt(0)\n"
        "Lg2_nw_9:\n"
        "s_add_u32 s58, s58, 1\n"
        "s_cmp_lt_u32 s58, 28\n"
        "s_cbranch_scc1 Lg2_loop\n"
        "Lg2_loop2:\n"
        "s_barrier\n"
        "ds_read_b128 v[128:131], v9 offset:0\n"
        "ds_read_b128 v[132:135], v9 offset:512\n"
        "ds_read_b128 v[136:139], v9 offset:4096\n"
        "ds_read_b128 v[140:143], v9 offset:4608\n"
        "ds_read_b128 v[144:147], v9 offset:8192\n"
        "ds_read_b128 v[148:151], v9 offset:8704\n"
        "ds_read_b128 v[152:155], v9 offset:12288\n"
        "ds_read_b128 v[156:159], v9 offset:12800\n"
        "ds_read_b128 v[160:163], v9 offset:16384\n"
        "ds_read_b128 v[164:167], v9 offset:16896\n"
        "ds_read_b128 v[168:171], v9 offset:20480\n"
        "ds_read_b128 v[172:175], v9 offset:20992\n"
        "ds_read_b128 v[176:179], v9 offset:24576\n"
        "ds_read_b128 v[180:183], v9 offset:25088\n"
        "ds_read_b128 v[184:187], v9 offset:28672\n"
        "ds_read_b128 v[188:191], v9 offset:29184\n"
        "ds_read_b128 v[192:195], v10 offset:0\n"
        "ds_read_b128 v[196:199], v10 offset:512\n"
        "ds_read_b128 v[200:203], v10 offset:4096\n"
        "ds_read_b128 v[204:207], v10 offset:4608\n"
        "ds_read_b128 v[208:211], v10 offset:8192\n"
        "ds_read_b128 v[212:215], v10 offset:8704\n"
        "ds_read_b128 v[216:219], v10 offset:12288\n"
        "ds_read_b128 v[220:223], v10 offset:12800\n"
        "ds_read_b128 v[224:227], v10 offset:16384\n"
        "ds_read_b128 v[228:231], v10 offset:16896\n"
        "ds_read_b128 v[232:235], v10 offset:20480\n"
        "ds_read_b128 v[236:239], v10 offset:20992\n"
        "ds_read_b128 v[240:243], v10 offset:24576\n"
        "ds_read_b128 v[244:247], v10 offset:25088\n"
        "ds_read_b128 v[248:251], v10 offset:28672\n"
        "ds_read_b128 v[252:255], v10 offset:29184\n"
        "s_waitcnt lgkmcnt(0)\n"
        "s_cmp_eq_u32 s58, 31\n"
        "s_cbranch_scc1 Lg2_ns_10\n"
        "s_barrier\n"
        "s_add_u32 s47, s58, 1\n"
        "s_lshl_b32 s47, s47, 7\n"
        "s_add_u32 s54, s92, s47\n"
        "s_addc_u32 s55, s93, 0\n"
        "s_add_u32 m0, s59, 0\n"
        "s_nop 0\n"
        "global_load_lds_dwordx4 v8, s[54:55]\n"
        "s_add_u32 m0, s59, 1024\n"
        "s_nop 0\n"
        "global_load_lds_dwordx4 v12, s[54:55]\n"
        "s_add_u32 m0, s59, 2048\n"
        "s_nop 0\n"
        "global_load_lds_dwordx4 v13, s[54:55]\n"
        "s_add_u32 m0, s59, 3072\n"
        "s_nop 0\n"
        "global_load_lds_dwordx4 v19, s[54:55]\n"
        "s_add_u32 s54, s54, 0x20000\n"
        "s_addc_u32 s55, s55, 0\n"
        "s_add_u32 m0, s59, 4096\n"
        "s_nop 0\n"
        "global_load_lds_dwordx4 v8, s[54:55]\n"
        "s_add_u32 m0, s59, 5120\n"
        "s_nop 0\n"
        "global_load_lds_dwordx4 v12, s[54:55]\n"
        "s_add_u32 m0, s59, 6144\n"
        "s_nop 0\n"
        "global_load_lds_dwordx4 v13, s[54:55]\n"
        "s_add_u32 m0, s59, 7168\n"
        "s_nop 0\n"
        "global_load_lds_dwordx4 v19, s[54:55]\n"
        "Lg2_ns_10:\n"
        "s_add_u32 s88, s58, 1\n"
        "s_cmp_lg_u32 s43, 0\n"
        "s_cbranch_scc1 Lg2_fs_11\n"
        "v_mov_b32 v3, s88\n"
        "v_mov_b32 v15, s58\n"
        "v_cndmask_b32 v3, v3, v15, s[64:65]\n"
        "s_branch Lg2_p0_13\n"
        "Lg2_pl_12:\n"
        "s_sleep 2\n"
        "Lg2_p0_13:\n"
        "global_load_dword v14, v2, s[90:91] sc1\n"
        "s_sub_u32 s46, s46, 1\n"
        "s_cbranch_scc1 Lg2_exit\n"
        "s_waitcnt vmcnt(0)\n"
        "v_cmp_lt_u32 vcc, v14, v3\n"
        "s_cbranch_vccnz Lg2_pl_12\n"
        "Lg2_fs_11:\n"
        "s_barrier\n"
        "s_lshl_b32 s45, s58, 1\n"
        "s_add_u32 s45, s45, 0\n"
        "s_mul_i32 s68, s45, 0x600\n"
        "s_add_u32 s47, s68, 0x0\n"
        "s_add_u32 s48, s90, s47\n"
        "s_addc_u32 s49, s91, 0\n"
        "s_mul_i32 s68, s45, 0xc000\n"
        "s_add_u32 s47, s68, 0xe00000\n"
        "s_add_u32 s50, s90, s47\n"
        "s_addc_u32 s51, s91, 0\n"
        "global_load_dwordx2 v[52:53], v4, s[48:49] offset:0 sc1\n"
        "global_load_dwordx2 v[54:55], v4, s[48:49] offset:256 sc1\n"
        "global_load_dwordx2 v[56:57], v4, s[48:49] offset:512 sc1\n"
        "global_load_dwordx4 v[64:67], v5, s[50:51] offset:0 sc1\n"
        "global_load_dwordx4 v[68:71], v5, s[50:51] offset:1024 sc1\n"
        "global_load_dwordx4 v[72:75], v5, s[50:51] offset:2048 sc1\n"
        "global_load_dwordx4 v[76:79], v5, s[50:51] offset:3072 sc1\n"
        "s_mov_b64 exec, -1\n"
        "s_lshr_b32 s47, s58, 2\n"
        "s_cmp_lg_u32 s47, s40\n"
        "s_cbranch_scc1 Lg2_nz_14\n"
        "s_mul_i32 s68, s58, 0x18000\n"
        "s_add_u32 s47, s68, 0x1100000\n"
        "s_add_u32 s52, s90, s47\n"
        "s_addc_u32 s53, s91, 0\n"
        "global_load_dwordx4 v[96:99], v5, s[52:53] offset:0 sc1\n"
        "global_load_dwordx4 v[100:103], v5, s[52:53] offset:1024 sc1\n"
        "global_load_dwordx4 v[104:107], v5, s[52:53] offset:2048 sc1\n"
        "global_load_dwordx4 v[108:111], v5, s[52:53] offset:3072 sc1\n"
        "Lg2_nz_14:\n"
        "s_waitcnt vmcnt(4)\n"
        "v_add_f32 v14, v52, v54\n"
        "v_add_f32 v15, v53, v55\n"
        "v_add_f32 v14, v14, v56\n"
        "v_add_f32 v15, v15, v57\n"
        "v_mov_b32 v16, v14\n"
        "v_mov_b32 v17, v15\n"
        "s_nop 0\n"
        "v_permlane32_swap_b32 v14, v16\n"
        "v_permlane32_swap_b32 v15, v17\n"
        "v_add_f32 v14, v14, v16\n"
        "v_add_f32 v15, v15, v17\n"
        "v_mul_f32 v14, 0x3aaaaaab, v14\n"
        "v_mul_f32 v15, 0x3aaaaaab, v15\n"
        "v_fma_f32 v15, -v14, v14, v15\n"
        "v_max_f32 v15, 0, v15\n"
        "v_add_f32 v15, 0x3727c5ac, v15\n"
        "v_rsq_f32 v15, v15\n"
        "s_nop 0\n"
        "v_mul_f32 v14, v14, v15\n"
        "ds_bpermute_b32 v20, v6, v15 offset:0\n"
        "ds_bpermute_b32 v21, v6, v14 offset:0\n"
        "ds_bpermute_b32 v22, v6, v15 offset:4\n"
        "ds_bpermute_b32 v23, v6, v14 offset:4\n"
        "ds_bpermute_b32 v24, v6, v15 offset:8\n"
        "ds_bpermute_b32 v25, v6, v14 offset:8\n"
        "ds_bpermute_b32 v26, v6, v15 offset:12\n"
        "ds_bpermute_b32 v27, v6, v14 offset:12\n"
        "ds_bpermute_b32 v28, v6, v15 offset:16\n"
        "ds_bpermute_b32 v29, v6, v14 offset:16\n"
        "ds_bpermute_b32 v30, v6, v15 offset:20\n"
        "ds_bpermute_b32 v31, v6, v14 offset:20\n"
        "ds_bpermute_b32 v32, v6, v15 offset:24\n"
        "ds_bpermute_b32 v33, v6, v14 offset:24\n"
        "ds_bpermute_b32 v34, v6, v15 offset:28\n"
        "ds_bpermute_b32 v35, v6, v14 offset:28\n"
        "s_waitcnt lgkmcnt(0)\n"
        "s_waitcnt vmcnt(3)\n"
        "v_lshlrev_b32 v40, 16, v64\n"
        "v_and_b32 v64, 0xffff0000, v64\n"
        "v_fma_f32 v40, v40, v20, -v21\n"
        "v_fma_f32 v64, v64, v22, -v23\n"
        "v_cvt_pk_bf16_f32 v36, v40, v64\n"
        "v_lshlrev_b32 v41, 16, v65\n"
        "v_and_b32 v65, 0xffff0000, v65\n"
        "v_fma_f32 v41, v41, v24, -v25\n"
        "v_fma_f32 v65, v65, v26, -v27\n"
        "v_cvt_pk_bf16_f32 v37, v41, v65\n"
        "v_lshlrev_b32 v40, 16, v66\n"
        "v_and_b32 v66, 0xffff0000, v66\n"
        "v_fma_f32 v40, v40, v28, -v29\n"
        "v_fma_f32 v66, v66, v30, -v31\n"
        "v_cvt_pk_bf16_f32 v38, v40, v66\n"
        "v_lshlrev_b32 v41, 16, v67\n"
        "v_and_b32 v67, 0xffff0000, v67\n"
        "v_fma_f32 v41, v41, v32, -v33\n"
        "v_fma_f32 v67, v67, v34, -v35\n"
        "v_cvt_pk_bf16_f32 v39, v41, v67\n"
        "s_nop 1\n"
        "v_mfma_f32_16x16x32_bf16 a[0:3], v[36:39], v[128:131], a[0:3]\n"
        "s_waitcnt vmcnt(2)\n"
        "v_lshlrev_b32 v48, 16, v68\n"
        "v_and_b32 v68, 0xffff0000, v68\n"
        "v_mfma_f32_16x16x32_bf16 a[4:7], v[36:39], v[132:135], a[4:7]\n"
        "v_fma_f32 v48, v48, v20, -v21\n"
        "v_fma_f32 v68, v68, v22, -v23\n"
        "v_mfma_f32_16x16x32_bf16 a[8:11], v[36:39], v[136:139], a[8:11]\n"
        "v_cvt_pk_bf16_f32 v44, v48, v68\n"
        "v_lshlrev_b32 v49, 16, v69\n"
        "v_mfma_f32_16x16x32_bf16 a[12:15], v[36:39], v[140:143], a[12:15]\n"
        "v_and_b32 v69, 0xffff0000, v69\n"
        "v_fma_f32 v49, v49, v24, -v25\n"
        "v_mfma_f32_16x16x32_bf16 a[16:19], v[36:39], v[144:147], a[16:19]\n"
        "v_fma_f32 v69, v69, v26, -v27\n"
        "v_cvt_pk_bf16_f32 v45, v49, v69\n"
        "v_mfma_f32_16x16x32_bf16 a[20:23], v[36:39], v[148:151], a[20:23]\n"
        "v_lshlrev_b32 v48, 16, v70\n"
        "v_and_b32 v70, 0xffff0000, v70\n"
        "v_mfma_f32_16x16x32_bf16 a[24:27], v[36:39], v[152:155], a[24:27]\n"
        "v_fma_f32 v48, v48, v28, -v29\n"
        "v_fma_f32 v70, v70, v30, -v31\n"
        "v_mfma_f32_16x16x32_bf16 a[28:31], v[36:39], v[156:159], a[28:31]\n"
        "v_cvt_pk_bf16_f32 v46, v48, v70\n"
        "v_lshlrev_b32 v49, 16, v71\n"
        "v_mfma_f32_16x16x32_bf16 a[32:35], v[36:39], v[160:163], a[32:35]\n"
        "v_and_b32 v71, 0xffff0000, v71\n"
        "v_fma_f32 v49, v49, v32, -v33\n"
        "v_mfma_f32_16x16x32_bf16 a[36:39], v[36:39], v[164:167], a[36:39]\n"
        "v_fma_f32 v71, v71, v34, -v35\n"
        "v_cvt_pk_bf16_f32 v47, v49, v71\n"
        "v_mfma_f32_16x16x32_bf16 a[40:43], v[36:39], v[168:171], a[40:43]\n"
        "v_mfma_f32_16x16x32_bf16 a[44:47], v[36:39], v[172:175], a[44:47]\n"
        "v_mfma_f32_16x16x32_bf16 a[48:51], v[36:39], v[176:179], a[48:51]\n"
        "v_mfma_f32_16x16x32_bf16 a[52:55], v[36:39], v[180:183], a[52:55]\n"
        "v_mfma_f32_16x16x32_bf16 a[56:59], v[36:39], v[184:187], a[56:59]\n"
        "v_mfma_f32_16x16x32_bf16 a[60:63], v[36:39], v[188:191], a[60:63]\n"
        "v_mfma_f32_16x16x32_bf16 a[64:67], v[44:47], v[128:131], a[64:67]\n"
        "s_waitcnt vmcnt(1)\n"
        "v_lshlrev_b32 v40, 16, v72\n"
        "v_and_b32 v72, 0xffff0000, v72\n"
        "v_mfma_f32_16x16x32_bf16 a[68:71], v[44:47], v[132:135], a[68:71]\n"
        "v_fma_f32 v40, v40, v20, -v21\n"
        "v_fma_f32 v72, v72, v22, -v23\n"
        "v_mfma_f32_16x16x32_bf16 a[72:75], v[44:47], v[136:139], a[72:75]\n"
        "v_cvt_pk_bf16_f32 v36, v40, v72\n"
        "v_lshlrev_b32 v41, 16, v73\n"
        "v_mfma_f32_16x16x32_bf16 a[76:79], v[44:47], v[140:143], a[76:79]\n"
        "v_and_b32 v73, 0xffff0000, v73\n"
        "v_fma_f32 v41, v41, v24, -v25\n"
        "v_mfma_f32_16x16x32_bf16 a[80:83], v[44:47], v[144:147], a[80:83]\n"
        "v_fma_f32 v73, v73, v26, -v27\n"
        "v_cvt_pk_bf16_f32 v37, v41, v73\n"
        "v_mfma_f32_16x16x32_bf16 a[84:87], v[44:47], v[148:151], a[84:87]\n"
        "v_lshlrev_b32 v40, 16, v74\n"
        "v_and_b32 v74, 0xffff0000, v74\n"
        "v_mfma_f32_16x16x32_bf16 a[88:91], v[44:47], v[152:155], a[88:91]\n"
        "v_fma_f32 v40, v40, v28, -v29\n"
        "v_fma_f32 v74, v74, v30, -v31\n"
        "v_mfma_f32_16x16x32_bf16 a[92:95], v[44:47], v[156:159], a[92:95]\n"
        "v_cvt_pk_bf16_f32 v38, v40, v74\n"
        "v_lshlrev_b32 v41, 16, v75\n"
        "v_mfma_f32_16x16x32_bf16 a[96:99], v[44:47], v[160:163], a[96:99]\n"
        "v_and_b32 v75, 0xffff0000, v75\n"
        "v_fma_f32 v41, v41, v32, -v33\n"
        "v_mfma_f32_16x16x32_bf16 a[100:103], v[44:47], v[164:167], a[100:103]\n"
        "v_fma_f32 v75, v75, v34, -v35\n"
        "v_cvt_pk_bf16_f32 v39, v41, v75\n"
        "v_mfma_f32_16x16x32_bf16 a[104:107], v[44:47], v[168:171], a[104:107]\n"
        "v_mfma_f32_16x16x32_bf16 a[108:111], v[44:47], v[172:175], a[108:111]\n"
        "v_mfma_f32_16x16x32_bf16 a[112:115], v[44:47], v[176:179], a[112:115]\n"
        "v_mfma_f32_16x16x32_bf16 a[116:119], v[44:47], v[180:183], a[116:119]\n"
        "v_mfma_f32_16x16x32_bf16 a[120:123], v[44:47], v[184:187], a[120:123]\n"
        "v_mfma_f32_16x16x32_bf16 a[124:127], v[44:47], v[188:191], a[124:127]\n"
        "v_mfma_f32_16x16x32_bf16 a[128:131], v[36:39], v[128:131], a[128:131]\n"
        "s_waitcnt vmcnt(0)\n"
        "v_lshlrev_b32 v48, 16, v76\n"
        "v_and_b32 v76, 0xffff0000, v76\n"
        "v_mfma_f32_16x16x32_bf16 a[132:135], v[36:39], v[132:135], a[132:135]\n"
        "v_fma_f32 v48, v48, v20, -v21\n"
        "v_fma_f32 v76, v76, v22, -v23\n"
        "v_mfma_f32_16x16x32_bf16 a[136:139], v[36:39], v[136:139], a[136:139]\n"
        "v_cvt_pk_bf16_f32 v44, v48, v76\n"
        "v_lshlrev_b32 v49, 16, v77\n"
        "v_mfma_f32_16x16x32_bf16 a[140:143], v[36:39], v[140:143], a[140:143]\n"
        "v_and_b32 v77, 0xffff0000, v77\n"
        "v_fma_f32 v49, v49, v24, -v25\n"
        "v_mfma_f32_16x16x32_bf16 a[144:147], v[36:39], v[144:147], a[144:147]\n"
        "v_fma_f32 v77, v77, v26, -v27\n"
        "v_cvt_pk_bf16_f32 v45, v49, v77\n"
        "v_mfma_f32_16x16x32_bf16 a[148:151], v[36:39], v[148:151], a[148:151]\n"
        "v_lshlrev_b32 v48, 16, v78\n"
        "v_and_b32 v78, 0xffff0000, v78\n"
        "v_mfma_f32_16x16x32_bf16 a[152:155], v[36:39], v[152:155], a[152:155]\n"
        "v_fma_f32 v48, v48, v28, -v29\n"
        "v_fma_f32 v78, v78, v30, -v31\n"
        "v_mfma_f32_16x16x32_bf16 a[156:159], v[36:39], v[156:159], a[156:159]\n"
        "v_cvt_pk_bf16_f32 v46, v48, v78\n"
        "v_lshlrev_b32 v49, 16, v79\n"
        "v_mfma_f32_16x16x32_bf16 a[160:163], v[36:39], v[160:163], a[160:163]\n"
        "v_and_b32 v79, 0xffff0000, v79\n"
        "v_fma_f32 v49, v49, v32, -v33\n"
        "v_mfma_f32_16x16x32_bf16 a[164:167], v[36:39], v[164:167], a[164:167]\n"
        "v_fma_f32 v79, v79, v34, -v35\n"
        "v_cvt_pk_bf16_f32 v47, v49, v79\n"
        "v_mfma_f32_16x16x32_bf16 a[168:171], v[36:39], v[168:171], a[168:171]\n"
        "v_mfma_f32_16x16x32_bf16 a[172:175], v[36:39], v[172:175], a[172:175]\n"
        "v_mfma_f32_16x16x32_bf16 a[176:179], v[36:39], v[176:179], a[176:179]\n"
        "v_mfma_f32_16x16x32_bf16 a[180:183], v[36:39], v[180:183], a[180:183]\n"
        "v_mfma_f32_16x16x32_bf16 a[184:187], v[36:39], v[184:187], a[184:187]\n"
        "v_mfma_f32_16x16x32_bf16 a[188:191], v[36:39], v[188:191], a[188:191]\n"
        "v_mfma_f32_16x16x32_bf16 a[192:195], v[44:47], v[128:131], a[192:195]\n"
        "v_mfma_f32_16x16x32_bf16 a[196:199], v[44:47], v[132:135], a[196:199]\n"
        "v_mfma_f32_16x16x32_bf16 a[200:203], v[44:47], v[136:139], a[200:203]\n"
        "v_mfma_f32_16x16x32_bf16 a[204:207], v[44:47], v[140:143], a[204:207]\n"
        "v_mfma_f32_16x16x32_bf16 a[208:211], v[44:47], v[144:147], a[208:211]\n"
        "v_mfma_f32_16x16x32_bf16 a[212:215], v[44:47], v[148:151], a[212:215]\n"
        "v_mfma_f32_16x16x32_bf16 a[216:219], v[44:47], v[152:155], a[216:219]\n"
        "v_mfma_f32_16x16x32_bf16 a[220:223], v[44:47], v[156:159], a[220:223]\n"
        "v_mfma_f32_16x16x32_bf16 a[224:227], v[44:47], v[160:163], a[224:227]\n"
        "v_mfma_f32_16x16x32_bf16 a[228:231], v[44:47], v[164:167], a[228:231]\n"
        "v_mfma_f32_16x16x32_bf16 a[232:235], v[44:47], v[168:171], a[232:235]\n"
        "v_mfma_f32_16x16x32_bf16 a[236:239], v[44:47], v[172:175], a[236:239]\n"
        "v_mfma_f32_16x16x32_bf16 a[240:243], v[44:47], v[176:179], a[240:243]\n"
        "v_mfma_f32_16x16x32_bf16 a[244:247], v[44:47], v[180:183], a[244:247]\n"
        "v_mfma_f32_16x16x32_bf16 a[248:251], v[44:47], v[184:187], a[248:251]\n"
        "v_mfma_f32_16x16x32_bf16 a[252:255], v[44:47], v[188:191], a[252:255]\n"
        "s_waitcnt vmcnt(0)\n"
        "s_lshr_b32 s47, s58, 2\n"
        "s_cmp_lg_u32 s47, s40\n"
        "s_cbranch_scc1 Lg2_nw_15\n"
        "s_and_b32 s47, s58, 3\n"
        "s_lshl_b32 s47, s47, 13\n"
        "v_add_u32 v14, s47, v7\n"
        "ds_write_b128 v14, v[96:99] offset:0\n"
        "ds_write_b128 v14, v[100:103] offset:1024\n"
        "ds_write_b128 v14, v[104:107] offset:2048\n"
        "ds_write_b128 v14, v[108:111] offset:3072\n"
        "s_waitcnt lgkmcnt(0)\n"
        "Lg2_nw_15:\n"
        "s_cmp_lg_u32 s43, 0\n"
        "s_cbranch_scc1 Lg2_fs_16\n"
        "v_mov_b32 v3, s88\n"
        "v_mov_b32 v15, s88\n"
        "v_cndmask_b32 v3, v3, v15, s[64:65]\n"
        "s_branch Lg2_p0_18\n"
        "Lg2_pl_17:\n"
        "s_sleep 1\n"
        "Lg2_p0_18:\n"
        "global_load_dword v14, v2, s[90:91] sc1\n"
        "s_sub_u32 s46, s46, 1\n"
        "s_cbranch_scc1 Lg2_exit\n"
        "s_waitcnt vmcnt(0)\n"
        "v_cmp_lt_u32 vcc, v14, v3\n"
        "s_cbranch_vccnz Lg2_pl_17\n"
        "Lg2_fs_16:\n"
        "s_barrier\n"
        "s_lshl_b32 s45, s58, 1\n"
        "s_add_u32 s45, s45, 1\n"
        "s_mul_i32 s68, s45, 0x600\n"
        "s_add_u32 s47, s68, 0x0\n"
        "s_add_u32 s48, s90, s47\n"
        "s_addc_u32 s49, s91, 0\n"
        "s_mul_i32 s68, s45, 0xc000\n"
        "s_add_u32 s47, s68, 0xe00000\n"
        "s_add_u32 s50, s90, s47\n"
        "s_addc_u32 s51, s91, 0\n"
        "global_load_dwordx2 v[58:59], v4, s[48:49] offset:0 sc1\n"
        "global_load_dwordx2 v[60:61], v4, s[48:49] offset:256 sc1\n"
        "global_load_dwordx2 v[62:63], v4, s[48:49] offset:512 sc1\n"
        "global_load_dwordx4 v[80:83], v5, s[50:51] offset:0 sc1\n"
        "global_load_dwordx4 v[84:87], v5, s[50:51] offset:1024 sc1\n"
        "global_load_dwordx4 v[88:91], v5, s[50:51] offset:2048 sc1\n"
        "global_load_dwordx4 v[92:95], v5, s[50:51] offset:3072 sc1\n"
        "s_mov_b64 exec, -1\n"
        "s_lshr_b32 s47, s58, 2\n"
        "s_cmp_lg_u32 s47, s40\n"
        "s_cbranch_scc1 Lg2_nz_19\n"
        "s_mul_i32 s68, s58, 0x18000\n"
        "s_add_u32 s47, s68, 0x110c000\n"
        "s_add_u32 s52, s90, s47\n"
        "s_addc_u32 s53, s91, 0\n"
        "global_load_dwordx4 v[112:115], v5, s[52:53] offset:0 sc1\n"
        "global_load_dwordx4 v[116:119], v5, s[52:53] offset:1024 sc1\n"
        "global_load_dwordx4 v[120:123], v5, s[52:53] offset:2048 sc1\n"
        "global_load_dwordx4 v[124:127], v5, s[52:53] offset:3072 sc1\n"
        "Lg2_nz_19:\n"
        "s_waitcnt vmcnt(4)\n"
        "v_add_f32 v14, v58, v60\n"
        "v_add_f32 v15, v59, v61\n"
        "v_add_f32 v14, v14, v62\n"
        "v_add_f32 v15, v15, v63\n"
        "v_mov_b32 v16, v14\n"
        "v_mov_b32 v17, v15\n"
        "s_nop 0\n"
        "v_permlane32_swap_b32 v14, v16\n"
        "v_permlane32_swap_b32 v15, v17\n"
        "v_add_f32 v14, v14, v16\n"
        "v_add_f32 v15, v15, v17\n"
        "v_mul_f32 v14, 0x3aaaaaab, v14\n"
        "v_mul_f32 v15, 0x3aaaaaab, v15\n"
        "v_fma_f32 v15, -v14, v14, v15\n"
        "v_max_f32 v15, 0, v15\n"
        "v_add_f32 v15, 0x3727c5ac, v15\n"
        "v_rsq_f32 v15, v15\n"
        "s_nop 0\n"
        "v_mul_f32 v14, v14, v15\n"
        "ds_bpermute_b32 v20, v6, v15 offset:0\n"
        "ds_bpermute_b32 v21, v6, v14 offset:0\n"
        "ds_bpermute_b32 v22, v6, v15 offset:4\n"
        "ds_bpermute_b32 v23, v6, v14 offset:4\n"
        "ds_bpermute_b32 v24, v6, v15 offset:8\n"
        "ds_bpermute_b32 v25, v6, v14 offset:8\n"
        "ds_bpermute_b32 v26, v6, v15 offset:12\n"
        "ds_bpermute_b32 v27, v6, v14 offset:12\n"
        "ds_bpermute_b32 v28, v6, v15 offset:16\n"
        "ds_bpermute_b32 v29, v6, v14 offset:16\n"
        "ds_bpermute_b32 v30, v6, v15 offset:20\n"
        "ds_bpermute_b32 v31, v6, v14 offset:20\n"
        "ds_bpermute_b32 v32, v6, v15 offset:24\n"
        "ds_bpermute_b32 v33, v6, v14 offset:24\n"
        "ds_bpermute_b32 v34, v6, v15 offset:28\n"
        "ds_bpermute_b32 v35, v6, v14 offset:28\n"
        "s_waitcnt lgkmcnt(0)\n"
        "s_waitcnt vmcnt(3)\n"
        "v_lshlrev_b32 v40, 16, v80\n"
        "v_and_b32 v80, 0xffff0000, v80\n"
        "v_fma_f32 v40, v40, v20, -v21\n"
        "v_fma_f32 v80, v80, v22, -v23\n"
        "v_cvt_pk_bf16_f32 v36, v40, v80\n"
        "v_lshlrev_b32 v41, 16, v81\n"
        "v_and_b32 v81, 0xffff0000, v81\n"
        "v_fma_f32 v41, v41, v24, -v25\n"
        "v_fma_f32 v81, v81, v26, -v27\n"
        "v_cvt_pk_bf16_f32 v37, v41, v81\n"
        "v_lshlrev_b32 v40, 16, v82\n"
        "v_and_b32 v82, 0xffff0000, v82\n"
        "v_fma_f32 v40, v40, v28, -v29\n"
        "v_fma_f32 v82, v82, v30, -v31\n"
        "v_cvt_pk_bf16_f32 v38, v40, v82\n"
        "v_lshlrev_b32 v41, 16, v83\n"
        "v_and_b32 v83, 0xffff0000, v83\n"
        "v_fma_f32 v41, v41, v32, -v33\n"
        "v_fma_f32 v83, v83, v34, -v35\n"
        "v_cvt_pk_bf16_f32 v39, v41, v83\n"
        "s_nop 1\n"
        "v_mfma_f32_16x16x32_bf16 a[0:3], v[36:39], v[192:195], a[0:3]\n"
        "s_waitcnt vmcnt(2)\n"
        "v_lshlrev_b32 v48, 16, v84\n"
        "v_and_b32 v84, 0xffff0000, v84\n"
        "v_mfma_f32_16x16x32_bf16 a[4:7], v[36:39], v[196:199], a[4:7]\n"
        "v_fma_f32 v48, v48, v20, -v21\n"
        "v_fma_f32 v84, v84, v22, -v23\n"
        "v_mfma_f32_16x16x32_bf16 a[8:11], v[36:39], v[200:203], a[8:11]\n"
        "v_cvt_pk_bf16_f32 v44, v48, v84\n"
        "v_lshlrev_b32 v49, 16, v85\n"
        "v_mfma_f32_16x16x32_bf16 a[12:15], v[36:39], v[204:207], a[12:15]\n"
        "v_and_b32 v85, 0xffff0000, v85\n"
        "v_fma_f32 v49, v49, v24, -v25\n"
        "v_mfma_f32_16x16x32_bf16 a[16:19], v[36:39], v[208:211], a[16:19]\n"
        "v_fma_f32 v85, v85, v26, -v27\n"
        "v_cvt_pk_bf16_f32 v45, v49, v85\n"
        "v_mfma_f32_16x16x32_bf16 a[20:23], v[36:39], v[212:215], a[20:23]\n"
        "v_lshlrev_b32 v48, 16, v86\n"
        "v_and_b32 v86, 0xffff0000, v86\n"
        "v_mfma_f32_16x16x32_bf16 a[24:27], v[36:39], v[216:219], a[24:27]\n"
        "v_fma_f32 v48, v48, v28, -v29\n"
        "v_fma_f32 v86, v86, v30, -v31\n"
        "v_mfma_f32_16x16x32_bf16 a[28:31], v[36:39], v[220:223], a[28:31]\n"
        "v_cvt_pk_bf16_f32 v46, v48, v86\n"
        "v_lshlrev_b32 v49, 16, v87\n"
        "v_mfma_f32_16x16x32_bf16 a[32:35], v[36:39], v[224:227], a[32:35]\n"
        "v_and_b32 v87, 0xffff0000, v87\n"
        "v_fma_f32 v49, v49, v32, -v33\n"
        "v_mfma_f32_16x16x32_bf16 a[36:39], v[36:39], v[228:231], a[36:39]\n"
        "v_fma_f32 v87, v87, v34, -v35\n"
        "v_cvt_pk_bf16_f32 v47, v49, v87\n"
        "v_mfma_f32_16x16x32_bf16 a[40:43], v[36:39], v[232:235], a[40:43]\n"
        "v_mfma_f32_16x16x32_bf16 a[44:47], v[36:39], v[236:239], a[44:47]\n"
        "v_mfma_f32_16x16x32_bf16 a[48:51], v[36:39], v[240:243], a[48:51]\n"
        "v_mfma_f32_16x16x32_bf16 a[52:55], v[36:39], v[244:247], a[52:55]\n"
        "v_mfma_f32_16x16x32_bf16 a[56:59], v[36:39], v[248:251], a[56:59]\n"
        "v_mfma_f32_16x16x32_bf16 a[60:63], v[36:39], v[252:255], a[60:63]\n"
        "v_mfma_f32_16x16x32_bf16 a[64:67], v[44:47], v[192:195], a[64:67]\n"
        "s_waitcnt vmcnt(1)\n"
        "v_lshlrev_b32 v40, 16, v88\n"
        "v_and_b32 v88, 0xffff0000, v88\n"
        "v_mfma_f32_16x16x32_bf16 a[68:71], v[44:47], v[196:199], a[68:71]\n"
        "v_fma_f32 v40, v40, v20, -v21\n"
        "v_fma_f32 v88, v88, v22, -v23\n"
        "v_mfma_f32_16x16x32_bf16 a[72:75], v[44:47], v[200:203], a[72:75]\n"
        "v_cvt_pk_bf16_f32 v36, v40, v88\n"
        "v_lshlrev_b32 v41, 16, v89\n"
        "v_mfma_f32_16x16x32_bf16 a[76:79], v[44:47], v[204:207], a[76:79]\n"
        "v_and_b32 v89, 0xffff0000, v89\n"
        "v_fma_f32 v41, v41, v24, -v25\n"
        "v_mfma_f32_16x16x32_bf16 a[80:83], v[44:47], v[208:211], a[80:83]\n"
        "v_fma_f32 v89, v89, v26, -v27\n"
        "v_cvt_pk_bf16_f32 v37, v41, v89\n"
        "v_mfma_f32_16x16x32_bf16 a[84:87], v[44:47], v[212:215], a[84:87]\n"
        "v_lshlrev_b32 v40, 16, v90\n"
        "v_and_b32 v90, 0xffff0000, v90\n"
        "v_mfma_f32_16x16x32_bf16 a[88:91], v[44:47], v[216:219], a[88:91]\n"
        "v_fma_f32 v40, v40, v28, -v29\n"
        "v_fma_f32 v90, v90, v30, -v31\n"
        "v_mfma_f32_16x16x32_bf16 a[92:95], v[44:47], v[220:223], a[92:95]\n"
        "v_cvt_pk_bf16_f32 v38, v40, v90\n"
        "v_lshlrev_b32 v41, 16, v91\n"
        "v_mfma_f32_16x16x32_bf16 a[96:99], v[44:47], v[224:227], a[96:99]\n"
        "v_and_b32 v91, 0xffff0000, v91\n"
        "v_fma_f32 v41, v41, v32, -v33\n"
        "v_mfma_f32_16x16x32_bf16 a[100:103], v[44:47], v[228:231], a[100:103]\n"
        "v_fma_f32 v91, v91, v34, -v35\n"
        "v_cvt_pk_bf16_f32 v39, v41, v91\n"
        "v_mfma_f32_16x16x32_bf16 a[104:107], v[44:47], v[232:235], a[104:107]\n"
        "v_mfma_f32_16x16x32_bf16 a[108:111], v[44:47], v[236:239], a[108:111]\n"
        "v_mfma_f32_16x16x32_bf16 a[112:115], v[44:47], v[240:243], a[112:115]\n"
        "v_mfma_f32_16x16x32_bf16 a[116:119], v[44:47], v[244:247], a[116:119]\n"
        "v_mfma_f32_16x16x32_bf16 a[120:123], v[44:47], v[248:251], a[120:123]\n"
        "v_mfma_f32_16x16x32_bf16 a[124:127], v[44:47], v[252:255], a[124:127]\n"
        "v_mfma_f32_16x16x32_bf16 a[128:131], v[36:39], v[192:195], a[128:131]\n"
        "s_waitcnt vmcnt(0)\n"
        "v_lshlrev_b32 v48, 16, v92\n"
        "v_and_b32 v92, 0xffff0000, v92\n"
        "v_mfma_f32_16x16x32_bf16 a[132:135], v[36:39], v[196:199], a[132:135]\n"
        "v_fma_f32 v48, v48, v20, -v21\n"
        "v_fma_f32 v92, v92, v22, -v23\n"
        "v_mfma_f32_16x16x32_bf16 a[136:139], v[36:39], v[200:203], a[136:139]\n"
        "v_cvt_pk_bf16_f32 v44, v48, v92\n"
        "v_lshlrev_b32 v49, 16, v93\n"
        "v_mfma_f32_16x16x32_bf16 a[140:143], v[36:39], v[204:207], a[140:143]\n"
        "v_and_b32 v93, 0xffff0000, v93\n"
        "v_fma_f32 v49, v49, v24, -v25\n"
        "v_mfma_f32_16x16x32_bf16 a[144:147], v[36:39], v[208:211], a[144:147]\n"
        "v_fma_f32 v93, v93, v26, -v27\n"
        "v_cvt_pk_bf16_f32 v45, v49, v93\n"
        "v_mfma_f32_16x16x32_bf16 a[148:151], v[36:39], v[212:215], a[148:151]\n"
        "v_lshlrev_b32 v48, 16, v94\n"
        "v_and_b32 v94, 0xffff0000, v94\n"
        "v_mfma_f32_16x16x32_bf16 a[152:155], v[36:39], v[216:219], a[152:155]\n"
        "v_fma_f32 v48, v48, v28, -v29\n"
        "v_fma_f32 v94, v94, v30, -v31\n"
        "v_mfma_f32_16x16x32_bf16 a[156:159], v[36:39], v[220:223], a[156:159]\n"
        "v_cvt_pk_bf16_f32 v46, v48, v94\n"
        "v_lshlrev_b32 v49, 16, v95\n"
        "v_mfma_f32_16x16x32_bf16 a[160:163], v[36:39], v[224:227], a[160:163]\n"
        "v_and_b32 v95, 0xffff0000, v95\n"
        "v_fma_f32 v49, v49, v32, -v33\n"
        "v_mfma_f32_16x16x32_bf16 a[164:167], v[36:39], v[228:231], a[164:167]\n"
        "v_fma_f32 v95, v95, v34, -v35\n"
        "v_cvt_pk_bf16_f32 v47, v49, v95\n"
        "v_mfma_f32_16x16x32_bf16 a[168:171], v[36:39], v[232:235], a[168:171]\n"
        "v_mfma_f32_16x16x32_bf16 a[172:175], v[36:39], v[236:239], a[172:175]\n"
        "v_mfma_f32_16x16x32_bf16 a[176:179], v[36:39], v[240:243], a[176:179]\n"
        "v_mfma_f32_16x16x32_bf16 a[180:183], v[36:39], v[244:247], a[180:183]\n"
        "v_mfma_f32_16x16x32_bf16 a[184:187], v[36:39], v[248:251], a[184:187]\n"
        "v_mfma_f32_16x16x32_bf16 a[188:191], v[36:39], v[252:255], a[188:191]\n"
        "v_mfma_f32_16x16x32_bf16 a[192:195], v[44:47], v[192:195], a[192:195]\n"
        "v_mfma_f32_16x16x32_bf16 a[196:199], v[44:47], v[196:199], a[196:199]\n"
        "v_mfma_f32_16x16x32_bf16 a[200:203], v[44:47], v[200:203], a[200:203]\n"
        "v_mfma_f32_16x16x32_bf16 a[204:207], v[44:47], v[204:207], a[204:207]\n"
        "v_mfma_f32_16x16x32_bf16 a[208:211], v[44:47], v[208:211], a[208:211]\n"
        "v_mfma_f32_16x16x32_bf16 a[212:215], v[44:47], v[212:215], a[212:215]\n"
        "v_mfma_f32_16x16x32_bf16 a[216:219], v[44:47], v[216:219], a[216:219]\n"
        "v_mfma_f32_16x16x32_bf16 a[220:223], v[44:47], v[220:223], a[220:223]\n"
        "v_mfma_f32_16x16x32_bf16 a[224:227], v[44:47], v[224:227], a[224:227]\n"
        "v_mfma_f32_16x16x32_bf16 a[228:231], v[44:47], v[228:231], a[228:231]\n"
        "v_mfma_f32_16x16x32_bf16 a[232:235], v[44:47], v[232:235], a[232:235]\n"
        "v_mfma_f32_16x16x32_bf16 a[236:239], v[44:47], v[236:239], a[236:239]\n"
        "v_mfma_f32_16x16x32_bf16 a[240:243], v[44:47], v[240:243], a[240:243]\n"
        "v_mfma_f32_16x16x32_bf16 a[244:247], v[44:47], v[244:247], a[244:247]\n"
        "v_mfma_f32_16x16x32_bf16 a[248:251], v[44:47], v[248:251], a[248:251]\n"
        "v_mfma_f32_16x16x32_bf16 a[252:255], v[44:47], v[252:255], a[252:255]\n"
        "s_waitcnt vmcnt(0)\n"
        "s_lshr_b32 s47, s58, 2\n"
        "s_cmp_lg_u32 s47, s40\n"
        "s_cbranch_scc1 Lg2_nw_20\n"
        "s_and_b32 s47, s58, 3\n"
        "s_lshl_b32 s47, s47, 13\n"
        "v_add_u32 v14, s47, v7\n"
        "ds_write_b128 v14, v[112:115] offset:4096\n"
        "ds_write_b128 v14, v[116:119] offset:5120\n"
        "ds_write_b128 v14, v[120:123] offset:6144\n"
        "ds_write_b128 v14, v[124:127] offset:7168\n"
        "s_waitcnt lgkmcnt(0)\n"
        "Lg2_nw_20:\n"
        "s_add_u32 s58, s58, 1\n"
        "s_cmp_lt_u32 s58, 32\n"
        "s_cbranch_scc1 Lg2_loop2\n"
        "s_mov_b32 s58, 31\n"
        "s_add_u32 s48, s90, 0x1400000\n"
        "s_addc_u32 s49, s91, 0\n"
        "s_mul_i32 s47, s40, 0x3000\n"
        "s_lshl_b32 s68, s42, 2\n"
        "s_add_u32 s68, s68, s43\n"
        "s_lshl_b32 s68, s68, 7\n"
        "s_add_u32 s47, s47, s68\n"
        "v_mov_b32 v2, s47\n"
        "v_mul_u32_u24 v3, 1536, v0\n"
        "v_add_u32 v2, v2, v3\n"
        "v_mov_b32 v3, 1\n"
        "s_add_u32 s50, s90, 0xad0000\n"
        "s_addc_u32 s51, s91, 0\n"
        "ds_read_b128 v[64:67], v7 offset:0\n"
        "ds_read_b128 v[68:71], v7 offset:2048\n"
        "ds_read_b128 v[72:75], v7 offset:1024\n"
        "ds_read_b128 v[76:79], v7 offset:3072\n"
        "v_accvgpr_read_b32 v128, a0\n"
        "v_accvgpr_read_b32 v129, a1\n"
        "v_accvgpr_read_b32 v130, a2\n"
        "v_accvgpr_read_b32 v131, a3\n"
        "v_accvgpr_read_b32 v132, a64\n"
        "v_accvgpr_read_b32 v133, a65\n"
        "v_accvgpr_read_b32 v134, a66\n"
        "v_accvgpr_read_b32 v135, a67\n"
        "s_waitcnt lgkmcnt(3)\n"
        "v_lshlrev_b32 v14, 16, v64\n"
        "v_and_b32 v15, 0xffff0000, v64\n"
        "v_pk_mul_f32 v[128:129], v[128:129], v[14:15]\n"
        "v_cvt_pk_bf16_f32 v144, v128, v129\n"
        "v_lshlrev_b32 v16, 16, v65\n"
        "v_and_b32 v17, 0xffff0000, v65\n"
        "v_pk_mul_f32 v[130:131], v[130:131], v[16:17]\n"
        "v_cvt_pk_bf16_f32 v145, v130, v131\n"
        "v_lshlrev_b32 v14, 16, v66\n"
        "v_and_b32 v15, 0xffff0000, v66\n"
        "v_pk_mul_f32 v[132:133], v[132:133], v[14:15]\n"
        "v_cvt_pk_bf16_f32 v146, v132, v133\n"
        "v_lshlrev_b32 v16, 16, v67\n"
        "v_and_b32 v17, 0xffff0000, v67\n"
        "v_pk_mul_f32 v[134:135], v[134:135], v[16:17]\n"
        "v_cvt_pk_bf16_f32 v147, v134, v135\n"
        "global_store_dwordx4 v11, v[144:147], s[48:49] offset:0 sc1\n"
        "ds_read_b128 v[64:67], v7 offset:4096\n"
        "v_accvgpr_read_b32 v136, a128\n"
        "v_accvgpr_read_b32 v137, a129\n"
        "v_accvgpr_read_b32 v138, a130\n"
        "v_accvgpr_read_b32 v139, a131\n"
        "v_accvgpr_read_b32 v140, a192\n"
        "v_accvgpr_read_b32 v141, a193\n"
        "v_accvgpr_read_b32 v142, a194\n"
        "v_accvgpr_read_b32 v143, a195\n"
        "s_waitcnt lgkmcnt(3)\n"
        "v_lshlrev_b32 v14, 16, v68\n"
        "v_and_b32 v15, 0xffff0000, v68\n"
        "v_pk_mul_f32 v[136:137], v[136:137], v[14:15]\n"
        "v_cvt_pk_bf16_f32 v148, v136, v137\n"
        "v_lshlrev_b32 v16, 16, v69\n"
        "v_and_b32 v17, 0xffff0000, v69\n"
        "v_pk_mul_f32 v[138:139], v[138:139], v[16:17]\n"
        "v_cvt_pk_bf16_f32 v149, v138, v139\n"
        "v_lshlrev_b32 v14, 16, v70\n"
        "v_and_b32 v15, 0xffff0000, v70\n"
        "v_pk_mul_f32 v[140:141], v[140:141], v[14:15]\n"
        "v_cvt_pk_bf16_f32 v150, v140, v141\n"
        "v_lshlrev_b32 v16, 16, v71\n"
        "v_and_b32 v17, 0xffff0000, v71\n"
        "v_pk_mul_f32 v[142:143], v[142:143], v[16:17]\n"
        "v_cvt_pk_bf16_f32 v151, v142, v143\n"
        "global_store_dwordx4 v11, v[148:151], s[48:49] offset:1024 sc1\n"
        "s_add_u32 s48, s48, 24576\n"
        "s_addc_u32 s49, s49, 0\n"
        "ds_read_b128 v[68:71], v7 offset:6144\n"
        "v_accvgpr_read_b32 v128, a4\n"
        "v_accvgpr_read_b32 v129, a5\n"
        "v_accvgpr_read_b32 v130, a6\n"
        "v_accvgpr_read_b32 v131, a7\n"
        "v_accvgpr_read_b32 v132, a68\n"
        "v_accvgpr_read_b32 v133, a69\n"
        "v_accvgpr_read_b32 v134, a70\n"
        "v_accvgpr_read_b32 v135, a71\n"
        "s_waitcnt lgkmcnt(3)\n"
        "v_lshlrev_b32 v14, 16, v72\n"
        "v_and_b32 v15, 0xffff0000, v72\n"
        "v_pk_mul_f32 v[128:129], v[128:129], v[14:15]\n"
        "v_cvt_pk_bf16_f32 v144, v128, v129\n"
        "v_lshlrev_b32 v16, 16, v73\n"
        "v_and_b32 v17, 0xffff0000, v73\n"
        "v_pk_mul_f32 v[130:131], v[130:131], v[16:17]\n"
        "v_cvt_pk_bf16_f32 v145, v130, v131\n"
        "v_lshlrev_b32 v14, 16, v74\n"
        "v_and_b32 v15, 0xffff0000, v74\n"
        "v_pk_mul_f32 v[132:133], v[132:133], v[14:15]\n"
        "v_cvt_pk_bf16_f32 v146, v132, v133\n"
        "v_lshlrev_b32 v16, 16, v75\n"
        "v_and_b32 v17, 0xffff0000, v75\n"
        "v_pk_mul_f32 v[134:135], v[134:135], v[16:17]\n"
        "v_cvt_pk_bf16_f32 v147, v134, v135\n"
        "global_store_dwordx4 v11, v[144:147], s[48:49] offset:0 sc1\n"
        "ds_read_b128 v[72:75], v7 offset:5120\n"
        "v_accvgpr_read_b32 v136, a132\n"
        "v_accvgpr_read_b32 v137, a133\n"
        "v_accvgpr_read_b32 v138, a134\n"
        "v_accvgpr_read_b32 v139, a135\n"
        "v_accvgpr_read_b32 v140, a196\n"
        "v_accvgpr_read_b32 v141, a197\n"
        "v_accvgpr_read_b32 v142, a198\n"
        "v_accvgpr_read_b32 v143, a199\n"
        "s_waitcnt lgkmcnt(3)\n"
        "v_lshlrev_b32 v14, 16, v76\n"
        "v_and_b32 v15, 0xffff0000, v76\n"
        "v_pk_mul_f32 v[136:137], v[136:137], v[14:15]\n"
        "v_cvt_pk_bf16_f32 v148, v136, v137\n"
        "v_lshlrev_b32 v16, 16, v77\n"
        "v_and_b32 v17, 0xffff0000, v77\n"
        "v_pk_mul_f32 v[138:139], v[138:139], v[16:17]\n"
        "v_cvt_pk_bf16_f32 v149, v138, v139\n"
        "v_lshlrev_b32 v14, 16, v78\n"
        "v_and_b32 v15, 0xffff0000, v78\n"
        "v_pk_mul_f32 v[140:141], v[140:141], v[14:15]\n"
        "v_cvt_pk_bf16_f32 v150, v140, v141\n"
        "v_lshlrev_b32 v16, 16, v79\n"
        "v_and_b32 v17, 0xffff0000, v79\n"
        "v_pk_mul_f32 v[142:143], v[142:143], v[16:17]\n"
        "v_cvt_pk_bf16_f32 v151, v142, v143\n"
        "global_store_dwordx4 v11, v[148:151], s[48:49] offset:1024 sc1\n"
        "s_add_u32 s48, s48, 24576\n"
        "s_addc_u32 s49, s49, 0\n"
        "ds_read_b128 v[76:79], v7 offset:7168\n"
        "v_accvgpr_read_b32 v128, a8\n"
        "v_accvgpr_read_b32 v129, a9\n"
        "v_accvgpr_read_b32 v130, a10\n"
        "v_accvgpr_read_b32 v131, a11\n"
        "v_accvgpr_read_b32 v132, a72\n"
        "v_accvgpr_read_b32 v133, a73\n"
        "v_accvgpr_read_b32 v134, a74\n"
        "v_accvgpr_read_b32 v135, a75\n"
        "s_waitcnt lgkmcnt(3)\n"
        "v_lshlrev_b32 v14, 16, v64\n"
        "v_and_b32 v15, 0xffff0000, v64\n"
        "v_pk_mul_f32 v[128:129], v[128:129], v[14:15]\n"
        "v_cvt_pk_bf16_f32 v144, v128, v129\n"
        "v_lshlrev_b32 v16, 16, v65\n"
        "v_and_b32 v17, 0xffff0000, v65\n"
        "v_pk_mul_f32 v[130:131], v[130:131], v[16:17]\n"
        "v_cvt_pk_bf16_f32 v145, v130, v131\n"
        "v_lshlrev_b32 v14, 16, v66\n"
        "v_and_b32 v15, 0xffff0000, v66\n"
        "v_pk_mul_f32 v[132:133], v[132:133], v[14:15]\n"
        "v_cvt_pk_bf16_f32 v146, v132, v133\n"
        "v_lshlrev_b32 v16, 16, v67\n"
        "v_and_b32 v17, 0xffff0000, v67\n"
        "v_pk_mul_f32 v[134:135], v[134:135], v[16:17]\n"
        "v_cvt_pk_bf16_f32 v147, v134, v135\n"
        "global_store_dwordx4 v11, v[144:147], s[48:49] offset:0 sc1\n"
        "ds_read_b128 v[64:67], v7 offset:8192\n"
        "v_accvgpr_read_b32 v136, a136\n"
        "v_accvgpr_read_b32 v137, a137\n"
        "v_accvgpr_read_b32 v138, a138\n"
        "v_accvgpr_read_b32 v139, a139\n"
        "v_accvgpr_read_b32 v140, a200\n"
        "v_accvgpr_read_b32 v141, a201\n"
        "v_accvgpr_read_b32 v142, a202\n"
        "v_accvgpr_read_b32 v143, a203\n"
        "s_waitcnt lgkmcnt(3)\n"
        "v_lshlrev_b32 v14, 16, v68\n"
        "v_and_b32 v15, 0xffff0000, v68\n"
        "v_pk_mul_f32 v[136:137], v[136:137], v[14:15]\n"
        "v_cvt_pk_bf16_f32 v148, v136, v137\n"
        "v_lshlrev_b32 v16, 16, v69\n"
        "v_and_b32 v17, 0xffff0000, v69\n"
        "v_pk_mul_f32 v[138:139], v[138:139], v[16:17]\n"
        "v_cvt_pk_bf16_f32 v149, v138, v139\n"
        "v_lshlrev_b32 v14, 16, v70\n"
        "v_and_b32 v15, 0xffff0000, v70\n"
        "v_pk_mul_f32 v[140:141], v[140:141], v[14:15]\n"
        "v_cvt_pk_bf16_f32 v150, v140, v141\n"
        "v_lshlrev_b32 v16, 16, v71\n"
        "v_and_b32 v17, 0xffff0000, v71\n"
        "v_pk_mul_f32 v[142:143], v[142:143], v[16:17]\n"
        "v_cvt_pk_bf16_f32 v151, v142, v143\n"
        "global_store_dwordx4 v11, v[148:151], s[48:49] offset:1024 sc1\n"
        "s_add_u32 s48, s48, 24576\n"
        "s_addc_u32 s49, s49, 0\n"
        "ds_read_b128 v[68:71], v7 offset:10240\n"
        "v_accvgpr_read_b32 v128, a12\n"
        "v_accvgpr_read_b32 v129, a13\n"
        "v_accvgpr_read_b32 v130, a14\n"
        "v_accvgpr_read_b32 v131, a15\n"
        "v_accvgpr_read_b32 v132, a76\n"
        "v_accvgpr_read_b32 v133, a77\n"
        "v_accvgpr_read_b32 v134, a78\n"
        "v_accvgpr_read_b32 v135, a79\n"
        "s_waitcnt lgkmcnt(3)\n"
        "v_lshlrev_b32 v14, 16, v72\n"
        "v_and_b32 v15, 0xffff0000, v72\n"
        "v_pk_mul_f32 v[128:129], v[128:129], v[14:15]\n"
        "v_cvt_pk_bf16_f32 v144, v128, v129\n"
        "v_lshlrev_b32 v16, 16, v73\n"
        "v_and_b32 v17, 0xffff0000, v73\n"
        "v_pk_mul_f32 v[130:131], v[130:131], v[16:17]\n"
        "v_cvt_pk_bf16_f32 v145, v130, v131\n"
        "v_lshlrev_b32 v14, 16, v74\n"
        "v_and_b32 v15, 0xffff0000, v74\n"
        "v_pk_mul_f32 v[132:133], v[132:133], v[14:15]\n"
        "v_cvt_pk_bf16_f32 v146, v132, v133\n"
        "v_lshlrev_b32 v16, 16, v75\n"
        "v_and_b32 v17, 0xffff0000, v75\n"
        "v_pk_mul_f32 v[134:135], v[134:135], v[16:17]\n"
        "v_cvt_pk_bf16_f32 v147, v134, v135\n"
        "global_store_dwordx4 v11, v[144:147], s[48:49] offset:0 sc1\n"
        "ds_read_b128 v[72:75], v7 offset:9216\n"
        "v_accvgpr_read_b32 v136, a140\n"
        "v_accvgpr_read_b32 v137, a141\n"
        "v_accvgpr_read_b32 v138, a142\n"
        "v_accvgpr_read_b32 v139, a143\n"
        "v_accvgpr_read_b32 v140, a204\n"
        "v_accvgpr_read_b32 v141, a205\n"
        "v_accvgpr_read_b32 v142, a206\n"
        "v_accvgpr_read_b32 v143, a207\n"
        "s_waitcnt lgkmcnt(3)\n"
        "v_lshlrev_b32 v14, 16, v76\n"
        "v_and_b32 v15, 0xffff0000, v76\n"
        "v_pk_mul_f32 v[136:137], v[136:137], v[14:15]\n"
        "v_cvt_pk_bf16_f32 v148, v136, v137\n"
        "v_lshlrev_b32 v16, 16, v77\n"
        "v_and_b32 v17, 0xffff0000, v77\n"
        "v_pk_mul_f32 v[138:139], v[138:139], v[16:17]\n"
        "v_cvt_pk_bf16_f32 v149, v138, v139\n"
        "v_lshlrev_b32 v14, 16, v78\n"
        "v_and_b32 v15, 0xffff0000, v78\n"
        "v_pk_mul_f32 v[140:141], v[140:141], v[14:15]\n"
        "v_cvt_pk_bf16_f32 v150, v140, v141\n"
        "v_lshlrev_b32 v16, 16, v79\n"
        "v_and_b32 v17, 0xffff0000, v79\n"
        "v_pk_mul_f32 v[142:143], v[142:143], v[16:17]\n"
        "v_cvt_pk_bf16_f32 v151, v142, v143\n"
        "global_store_dwordx4 v11, v[148:151], s[48:49] offset:1024 sc1\n"
        "s_add_u32 s48, s48, 24576\n"
        "s_addc_u32 s49, s49, 0\n"
        "ds_read_b128 v[76:79], v7 offset:11264\n"
        "v_accvgpr_read_b32 v128, a16\n"
        "v_accvgpr_read_b32 v129, a17\n"
        "v_accvgpr_read_b32 v130, a18\n"
        "v_accvgpr_read_b32 v131, a19\n"
        "v_accvgpr_read_b32 v132, a80\n"
        "v_accvgpr_read_b32 v133, a81\n"
        "v_accvgpr_read_b32 v134, a82\n"
        "v_accvgpr_read_b32 v135, a83\n"
        "s_waitcnt lgkmcnt(3)\n"
        "v_lshlrev_b32 v14, 16, v64\n"
        "v_and_b32 v15, 0xffff0000, v64\n"
        "v_pk_mul_f32 v[128:129], v[128:129], v[14:15]\n"
        "v_cvt_pk_bf16_f32 v144, v128, v129\n"
        "v_lshlrev_b32 v16, 16, v65\n"
        "v_and_b32 v17, 0xffff0000, v65\n"
        "v_pk_mul_f32 v[130:131], v[130:131], v[16:17]\n"
        "v_cvt_pk_bf16_f32 v145, v130, v131\n"
        "v_lshlrev_b32 v14, 16, v66\n"
        "v_and_b32 v15, 0xffff0000, v66\n"
        "v_pk_mul_f32 v[132:133], v[132:133], v[14:15]\n"
        "v_cvt_pk_bf16_f32 v146, v132, v133\n"
        "v_lshlrev_b32 v16, 16, v67\n"
        "v_and_b32 v17, 0xffff0000, v67\n"
        "v_pk_mul_f32 v[134:135], v[134:135], v[16:17]\n"
        "v_cvt_pk_bf16_f32 v147, v134, v135\n"
        "global_store_dwordx4 v11, v[144:147], s[48:49] offset:0 sc1\n"
        "ds_read_b128 v[64:67], v7 offset:12288\n"
        "v_accvgpr_read_b32 v136, a144\n"
        "v_accvgpr_read_b32 v137, a145\n"
        "v_accvgpr_read_b32 v138, a146\n"
        "v_accvgpr_read_b32 v139, a147\n"
        "v_accvgpr_read_b32 v140, a208\n"
        "v_accvgpr_read_b32 v141, a209\n"
        "v_accvgpr_read_b32 v142, a210\n"
        "v_accvgpr_read_b32 v143, a211\n"
        "s_waitcnt lgkmcnt(3)\n"
        "v_lshlrev_b32 v14, 16, v68\n"
        "v_and_b32 v15, 0xffff0000, v68\n"
        "v_pk_mul_f32 v[136:137], v[136:137], v[14:15]\n"
        "v_cvt_pk_bf16_f32 v148, v136, v137\n"
        "v_lshlrev_b32 v16, 16, v69\n"
        "v_and_b32 v17, 0xffff0000, v69\n"
        "v_pk_mul_f32 v[138:139], v[138:139], v[16:17]\n"
        "v_cvt_pk_bf16_f32 v149, v138, v139\n"
        "v_lshlrev_b32 v14, 16, v70\n"
        "v_and_b32 v15, 0xffff0000, v70\n"
        "v_pk_mul_f32 v[140:141], v[140:141], v[14:15]\n"
        "v_cvt_pk_bf16_f32 v150, v140, v141\n"
        "v_lshlrev_b32 v16, 16, v71\n"
        "v_and_b32 v17, 0xffff0000, v71\n"
        "v_pk_mul_f32 v[142:143], v[142:143], v[16:17]\n"
        "v_cvt_pk_bf16_f32 v151, v142, v143\n"
        "global_store_dwordx4 v11, v[148:151], s[48:49] offset:1024 sc1\n"
        "s_add_u32 s48, s48, 24576\n"
        "s_addc_u32 s49, s49, 0\n"
        "ds_read_b128 v[68:71], v7 offset:14336\n"
        "v_accvgpr_read_b32 v128, a20\n"
        "v_accvgpr_read_b32 v129, a21\n"
        "v_accvgpr_read_b32 v130, a22\n"
        "v_accvgpr_read_b32 v131, a23\n"
        "v_accvgpr_read_b32 v132, a84\n"
        "v_accvgpr_read_b32 v133, a85\n"
        "v_accvgpr_read_b32 v134, a86\n"
        "v_accvgpr_read_b32 v135, a87\n"
        "s_waitcnt lgkmcnt(3)\n"
        "v_lshlrev_b32 v14, 16, v72\n"
        "v_and_b32 v15, 0xffff0000, v72\n"
        "v_pk_mul_f32 v[128:129], v[128:129], v[14:15]\n"
        "v_cvt_pk_bf16_f32 v144, v128, v129\n"
        "v_lshlrev_b32 v16, 16, v73\n"
        "v_and_b32 v17, 0xffff0000, v73\n"
        "v_pk_mul_f32 v[130:131], v[130:131], v[16:17]\n"
        "v_cvt_pk_bf16_f32 v145, v130, v131\n"
        "v_lshlrev_b32 v14, 16, v74\n"
        "v_and_b32 v15, 0xffff0000, v74\n"
        "v_pk_mul_f32 v[132:133], v[132:133], v[14:15]\n"
        "v_cvt_pk_bf16_f32 v146, v132, v133\n"
        "v_lshlrev_b32 v16, 16, v75\n"
        "v_and_b32 v17, 0xffff0000, v75\n"
        "v_pk_mul_f32 v[134:135], v[134:135], v[16:17]\n"
        "v_cvt_pk_bf16_f32 v147, v134, v135\n"
        "global_store_dwordx4 v11, v[144:147], s[48:49] offset:0 sc1\n"
        "ds_read_b128 v[72:75], v7 offset:13312\n"
        "v_accvgpr_read_b32 v136, a148\n"
        "v_accvgpr_read_b32 v137, a149\n"
        "v_accvgpr_read_b32 v138, a150\n"
        "v_accvgpr_read_b32 v139, a151\n"
        "v_accvgpr_read_b32 v140, a212\n"
        "v_accvgpr_read_b32 v141, a213\n"
        "v_accvgpr_read_b32 v142, a214\n"
        "v_accvgpr_read_b32 v143, a215\n"
        "s_waitcnt lgkmcnt(3)\n"
        "v_lshlrev_b32 v14, 16, v76\n"
        "v_and_b32 v15, 0xffff0000, v76\n"
        "v_pk_mul_f32 v[136:137], v[136:137], v[14:15]\n"
        "v_cvt_pk_bf16_f32 v148, v136, v137\n"
        "v_lshlrev_b32 v16, 16, v77\n"
        "v_and_b32 v17, 0xffff0000, v77\n"
        "v_pk_mul_f32 v[138:139], v[138:139], v[16:17]\n"
        "v_cvt_pk_bf16_f32 v149, v138, v139\n"
        "v_lshlrev_b32 v14, 16, v78\n"
        "v_and_b32 v15, 0xffff0000, v78\n"
        "v_pk_mul_f32 v[140:141], v[140:141], v[14:15]\n"
        "v_cvt_pk_bf16_f32 v150, v140, v141\n"
        "v_lshlrev_b32 v16, 16, v79\n"
        "v_and_b32 v17, 0xffff0000, v79\n"
        "v_pk_mul_f32 v[142:143], v[142:143], v[16:17]\n"
        "v_cvt_pk_bf16_f32 v151, v142, v143\n"
        "global_store_dwordx4 v11, v[148:151], s[48:49] offset:1024 sc1\n"
        "s_add_u32 s48, s48, 24576\n"
        "s_addc_u32 s49, s49, 0\n"
        "ds_read_b128 v[76:79], v7 offset:15360\n"
        "v_accvgpr_read_b32 v128, a24\n"
        "v_accvgpr_read_b32 v129, a25\n"
        "v_accvgpr_read_b32 v130, a26\n"
        "v_accvgpr_read_b32 v131, a27\n"
        "v_accvgpr_read_b32 v132, a88\n"
        "v_accvgpr_read_b32 v133, a89\n"
        "v_accvgpr_read_b32 v134, a90\n"
        "v_accvgpr_read_b32 v135, a91\n"
        "s_waitcnt lgkmcnt(3)\n"
        "v_lshlrev_b32 v14, 16, v64\n"
        "v_and_b32 v15, 0xffff0000, v64\n"
        "v_pk_mul_f32 v[128:129], v[128:129], v[14:15]\n"
        "v_cvt_pk_bf16_f32 v144, v128, v129\n"
        "v_lshlrev_b32 v16, 16, v65\n"
        "v_and_b32 v17, 0xffff0000, v65\n"
        "v_pk_mul_f32 v[130:131], v[130:131], v[16:17]\n"
        "v_cvt_pk_bf16_f32 v145, v130, v131\n"
        "v_lshlrev_b32 v14, 16, v66\n"
        "v_and_b32 v15, 0xffff0000, v66\n"
        "v_pk_mul_f32 v[132:133], v[132:133], v[14:15]\n"
        "v_cvt_pk_bf16_f32 v146, v132, v133\n"
        "v_lshlrev_b32 v16, 16, v67\n"
        "v_and_b32 v17, 0xffff0000, v67\n"
        "v_pk_mul_f32 v[134:135], v[134:135], v[16:17]\n"
        "v_cvt_pk_bf16_f32 v147, v134, v135\n"
        "global_store_dwordx4 v11, v[144:147], s[48:49] offset:0 sc1\n"
        "ds_read_b128 v[64:67], v7 offset:16384\n"
        "v_accvgpr_read_b32 v136, a152\n"
        "v_accvgpr_read_b32 v137, a153\n"
        "v_accvgpr_read_b32 v138, a154\n"
        "v_accvgpr_read_b32 v139, a155\n"
        "v_accvgpr_read_b32 v140, a216\n"
        "v_accvgpr_read_b32 v141, a217\n"
        "v_accvgpr_read_b32 v142, a218\n"
        "v_accvgpr_read_b32 v143, a219\n"
        "s_waitcnt lgkmcnt(3)\n"
        "v_lshlrev_b32 v14, 16, v68\n"
        "v_and_b32 v15, 0xffff0000, v68\n"
        "v_pk_mul_f32 v[136:137], v[136:137], v[14:15]\n"
        "v_cvt_pk_bf16_f32 v148, v136, v137\n"
        "v_lshlrev_b32 v16, 16, v69\n"
        "v_and_b32 v17, 0xffff0000, v69\n"
        "v_pk_mul_f32 v[138:139], v[138:139], v[16:17]\n"
        "v_cvt_pk_bf16_f32 v149, v138, v139\n"
        "v_lshlrev_b32 v14, 16, v70\n"
        "v_and_b32 v15, 0xffff0000, v70\n"
        "v_pk_mul_f32 v[140:141], v[140:141], v[14:15]\n"
        "v_cvt_pk_bf16_f32 v150, v140, v141\n"
        "v_lshlrev_b32 v16, 16, v71\n"
        "v_and_b32 v17, 0xffff0000, v71\n"
        "v_pk_mul_f32 v[142:143], v[142:143], v[16:17]\n"
        "v_cvt_pk_bf16_f32 v151, v142, v143\n"
        "global_store_dwordx4 v11, v[148:151], s[48:49] offset:1024 sc1\n"
        "s_add_u32 s48, s48, 24576\n"
        "s_addc_u32 s49, s49, 0\n"
        "ds_read_b128 v[68:71], v7 offset:18432\n"
        "v_accvgpr_read_b32 v128, a28\n"
        "v_accvgpr_read_b32 v129, a29\n"
        "v_accvgpr_read_b32 v130, a30\n"
        "v_accvgpr_read_b32 v131, a31\n"
        "v_accvgpr_read_b32 v132, a92\n"
        "v_accvgpr_read_b32 v133, a93\n"
        "v_accvgpr_read_b32 v134, a94\n"
        "v_accvgpr_read_b32 v135, a95\n"
        "s_waitcnt lgkmcnt(3)\n"
        "v_lshlrev_b32 v14, 16, v72\n"
        "v_and_b32 v15, 0xffff0000, v72\n"
        "v_pk_mul_f32 v[128:129], v[128:129], v[14:15]\n"
        "v_cvt_pk_bf16_f32 v144, v128, v129\n"
        "v_lshlrev_b32 v16, 16, v73\n"
        "v_and_b32 v17, 0xffff0000, v73\n"
        "v_pk_mul_f32 v[130:131], v[130:131], v[16:17]\n"
        "v_cvt_pk_bf16_f32 v145, v130, v131\n"
        "v_lshlrev_b32 v14, 16, v74\n"
        "v_and_b32 v15, 0xffff0000, v74\n"
        "v_pk_mul_f32 v[132:133], v[132:133], v[14:15]\n"
        "v_cvt_pk_bf16_f32 v146, v132, v133\n"
        "v_lshlrev_b32 v16, 16, v75\n"
        "v_and_b32 v17, 0xffff0000, v75\n"
        "v_pk_mul_f32 v[134:135], v[134:135], v[16:17]\n"
        "v_cvt_pk_bf16_f32 v147, v134, v135\n"
        "global_store_dwordx4 v11, v[144:147], s[48:49] offset:0 sc1\n"
        "ds_read_b128 v[72:75], v7 offset:17408\n"
        "v_accvgpr_read_b32 v136, a156\n"
        "v_accvgpr_read_b32 v137, a157\n"
        "v_accvgpr_read_b32 v138, a158\n"
        "v_accvgpr_read_b32 v139, a159\n"
        "v_accvgpr_read_b32 v140, a220\n"
        "v_accvgpr_read_b32 v141, a221\n"
        "v_accvgpr_read_b32 v142, a222\n"
        "v_accvgpr_read_b32 v143, a223\n"
        "s_waitcnt lgkmcnt(3)\n"
        "v_lshlrev_b32 v14, 16, v76\n"
        "v_and_b32 v15, 0xffff0000, v76\n"
        "v_pk_mul_f32 v[136:137], v[136:137], v[14:15]\n"
        "v_cvt_pk_bf16_f32 v148, v136, v137\n"
        "v_lshlrev_b32 v16, 16, v77\n"
        "v_and_b32 v17, 0xffff0000, v77\n"
        "v_pk_mul_f32 v[138:139], v[138:139], v[16:17]\n"
        "v_cvt_pk_bf16_f32 v149, v138, v139\n"
        "v_lshlrev_b32 v14, 16, v78\n"
        "v_and_b32 v15, 0xffff0000, v78\n"
        "v_pk_mul_f32 v[140:141], v[140:141], v[14:15]\n"
        "v_cvt_pk_bf16_f32 v150, v140, v141\n"
        "v_lshlrev_b32 v16, 16, v79\n"
        "v_and_b32 v17, 0xffff0000, v79\n"
        "v_pk_mul_f32 v[142:143], v[142:143], v[16:17]\n"
        "v_cvt_pk_bf16_f32 v151, v142, v143\n"
        "global_store_dwordx4 v11, v[148:151], s[48:49] offset:1024 sc1\n"
        "s_add_u32 s48, s48, 24576\n"
        "s_addc_u32 s49, s49, 0\n"
        "ds_read_b128 v[76:79], v7 offset:19456\n"
        "v_accvgpr_read_b32 v128, a32\n"
        "v_accvgpr_read_b32 v129, a33\n"
        "v_accvgpr_read_b32 v130, a34\n"
        "v_accvgpr_read_b32 v131, a35\n"
        "v_accvgpr_read_b32 v132, a96\n"
        "v_accvgpr_read_b32 v133, a97\n"
        "v_accvgpr_read_b32 v134, a98\n"
        "v_accvgpr_read_b32 v135, a99\n"
        "s_waitcnt lgkmcnt(3)\n"
        "v_lshlrev_b32 v14, 16, v64\n"
        "v_and_b32 v15, 0xffff0000, v64\n"
        "v_pk_mul_f32 v[128:129], v[128:129], v[14:15]\n"
        "v_cvt_pk_bf16_f32 v144, v128, v129\n"
        "v_lshlrev_b32 v16, 16, v65\n"
        "v_and_b32 v17, 0xffff0000, v65\n"
        "v_pk_mul_f32 v[130:131], v[130:131], v[16:17]\n"
        "v_cvt_pk_bf16_f32 v145, v130, v131\n"
        "v_lshlrev_b32 v14, 16, v66\n"
        "v_and_b32 v15, 0xffff0000, v66\n"
        "v_pk_mul_f32 v[132:133], v[132:133], v[14:15]\n"
        "v_cvt_pk_bf16_f32 v146, v132, v133\n"
        "v_lshlrev_b32 v16, 16, v67\n"
        "v_and_b32 v17, 0xffff0000, v67\n"
        "v_pk_mul_f32 v[134:135], v[134:135], v[16:17]\n"
        "v_cvt_pk_bf16_f32 v147, v134, v135\n"
        "global_store_dwordx4 v11, v[144:147], s[48:49] offset:0 sc1\n"
        "ds_read_b128 v[64:67], v7 offset:20480\n"
        "v_accvgpr_read_b32 v136, a160\n"
        "v_accvgpr_read_b32 v137, a161\n"
        "v_accvgpr_read_b32 v138, a162\n"
        "v_accvgpr_read_b32 v139, a163\n"
        "v_accvgpr_read_b32 v140, a224\n"
        "v_accvgpr_read_b32 v141, a225\n"
        "v_accvgpr_read_b32 v142, a226\n"
        "v_accvgpr_read_b32 v143, a227\n"
        "s_waitcnt lgkmcnt(3)\n"
        "v_lshlrev_b32 v14, 16, v68\n"
        "v_and_b32 v15, 0xffff0000, v68\n"
        "v_pk_mul_f32 v[136:137], v[136:137], v[14:15]\n"
        "v_cvt_pk_bf16_f32 v148, v136, v137\n"
        "v_lshlrev_b32 v16, 16, v69\n"
        "v_and_b32 v17, 0xffff0000, v69\n"
        "v_pk_mul_f32 v[138:139], v[138:139], v[16:17]\n"
        "v_cvt_pk_bf16_f32 v149, v138, v139\n"
        "v_lshlrev_b32 v14, 16, v70\n"
        "v_and_b32 v15, 0xffff0000, v70\n"
        "v_pk_mul_f32 v[140:141], v[140:141], v[14:15]\n"
        "v_cvt_pk_bf16_f32 v150, v140, v141\n"
        "v_lshlrev_b32 v16, 16, v71\n"
        "v_and_b32 v17, 0xffff0000, v71\n"
        "v_pk_mul_f32 v[142:143], v[142:143], v[16:17]\n"
        "v_cvt_pk_bf16_f32 v151, v142, v143\n"
        "global_store_dwordx4 v11, v[148:151], s[48:49] offset:1024 sc1\n"
        "s_add_u32 s48, s48, 24576\n"
        "s_addc_u32 s49, s49, 0\n"
        "ds_read_b128 v[68:71], v7 offset:22528\n"
        "v_accvgpr_read_b32 v128, a36\n"
        "v_accvgpr_read_b32 v129, a37\n"
        "v_accvgpr_read_b32 v130, a38\n"
        "v_accvgpr_read_b32 v131, a39\n"
        "v_accvgpr_read_b32 v132, a100\n"
        "v_accvgpr_read_b32 v133, a101\n"
        "v_accvgpr_read_b32 v134, a102\n"
        "v_accvgpr_read_b32 v135, a103\n"
        "s_waitcnt lgkmcnt(3)\n"
        "v_lshlrev_b32 v14, 16, v72\n"
        "v_and_b32 v15, 0xffff0000, v72\n"
        "v_pk_mul_f32 v[128:129], v[128:129], v[14:15]\n"
        "v_cvt_pk_bf16_f32 v144, v128, v129\n"
        "v_lshlrev_b32 v16, 16, v73\n"
        "v_and_b32 v17, 0xffff0000, v73\n"
        "v_pk_mul_f32 v[130:131], v[130:131], v[16:17]\n"
        "v_cvt_pk_bf16_f32 v145, v130, v131\n"
        "v_lshlrev_b32 v14, 16, v74\n"
        "v_and_b32 v15, 0xffff0000, v74\n"
        "v_pk_mul_f32 v[132:133], v[132:133], v[14:15]\n"
        "v_cvt_pk_bf16_f32 v146, v132, v133\n"
        "v_lshlrev_b32 v16, 16, v75\n"
        "v_and_b32 v17, 0xffff0000, v75\n"
        "v_pk_mul_f32 v[134:135], v[134:135], v[16:17]\n"
        "v_cvt_pk_bf16_f32 v147, v134, v135\n"
        "global_store_dwordx4 v11, v[144:147], s[48:49] offset:0 sc1\n"
        "ds_read_b128 v[72:75], v7 offset:21504\n"
        "v_accvgpr_read_b32 v136, a164\n"
        "v_accvgpr_read_b32 v137, a165\n"
        "v_accvgpr_read_b32 v138, a166\n"
        "v_accvgpr_read_b32 v139, a167\n"
        "v_accvgpr_read_b32 v140, a228\n"
        "v_accvgpr_read_b32 v141, a229\n"
        "v_accvgpr_read_b32 v142, a230\n"
        "v_accvgpr_read_b32 v143, a231\n"
        "s_waitcnt lgkmcnt(3)\n"
        "v_lshlrev_b32 v14, 16, v76\n"
        "v_and_b32 v15, 0xffff0000, v76\n"
        "v_pk_mul_f32 v[136:137], v[136:137], v[14:15]\n"
        "v_cvt_pk_bf16_f32 v148, v136, v137\n"
        "v_lshlrev_b32 v16, 16, v77\n"
        "v_and_b32 v17, 0xffff0000, v77\n"
        "v_pk_mul_f32 v[138:139], v[138:139], v[16:17]\n"
        "v_cvt_pk_bf16_f32 v149, v138, v139\n"
        "v_lshlrev_b32 v14, 16, v78\n"
        "v_and_b32 v15, 0xffff0000, v78\n"
        "v_pk_mul_f32 v[140:141], v[140:141], v[14:15]\n"
        "v_cvt_pk_bf16_f32 v150, v140, v141\n"
        "v_lshlrev_b32 v16, 16, v79\n"
        "v_and_b32 v17, 0xffff0000, v79\n"
        "v_pk_mul_f32 v[142:143], v[142:143], v[16:17]\n"
        "v_cvt_pk_bf16_f32 v151, v142, v143\n"
        "global_store_dwordx4 v11, v[148:151], s[48:49] offset:1024 sc1\n"
        "s_add_u32 s48, s48, 24576\n"
        "s_addc_u32 s49, s49, 0\n"
        "ds_read_b128 v[76:79], v7 offset:23552\n"
        "v_accvgpr_read_b32 v128, a40\n"
        "v_accvgpr_read_b32 v129, a41\n"
        "v_accvgpr_read_b32 v130, a42\n"
        "v_accvgpr_read_b32 v131, a43\n"
        "v_accvgpr_read_b32 v132, a104\n"
        "v_accvgpr_read_b32 v133, a105\n"
        "v_accvgpr_read_b32 v134, a106\n"
        "v_accvgpr_read_b32 v135, a107\n"
        "s_waitcnt lgkmcnt(3)\n"
        "v_lshlrev_b32 v14, 16, v64\n"
        "v_and_b32 v15, 0xffff0000, v64\n"
        "v_pk_mul_f32 v[128:129], v[128:129], v[14:15]\n"
        "v_cvt_pk_bf16_f32 v144, v128, v129\n"
        "v_lshlrev_b32 v16, 16, v65\n"
        "v_and_b32 v17, 0xffff0000, v65\n"
        "v_pk_mul_f32 v[130:131], v[130:131], v[16:17]\n"
        "v_cvt_pk_bf16_f32 v145, v130, v131\n"
        "v_lshlrev_b32 v14, 16, v66\n"
        "v_and_b32 v15, 0xffff0000, v66\n"
        "v_pk_mul_f32 v[132:133], v[132:133], v[14:15]\n"
        "v_cvt_pk_bf16_f32 v146, v132, v133\n"
        "v_lshlrev_b32 v16, 16, v67\n"
        "v_and_b32 v17, 0xffff0000, v67\n"
        "v_pk_mul_f32 v[134:135], v[134:135], v[16:17]\n"
        "v_cvt_pk_bf16_f32 v147, v134, v135\n"
        "global_store_dwordx4 v11, v[144:147], s[48:49] offset:0 sc1\n"
        "ds_read_b128 v[64:67], v7 offset:24576\n"
        "v_accvgpr_read_b32 v136, a168\n"
        "v_accvgpr_read_b32 v137, a169\n"
        "v_accvgpr_read_b32 v138, a170\n"
        "v_accvgpr_read_b32 v139, a171\n"
        "v_accvgpr_read_b32 v140, a232\n"
        "v_accvgpr_read_b32 v141, a233\n"
        "v_accvgpr_read_b32 v142, a234\n"
        "v_accvgpr_read_b32 v143, a235\n"
        "s_waitcnt lgkmcnt(3)\n"
        "v_lshlrev_b32 v14, 16, v68\n"
        "v_and_b32 v15, 0xffff0000, v68\n"
        "v_pk_mul_f32 v[136:137], v[136:137], v[14:15]\n"
        "v_cvt_pk_bf16_f32 v148, v136, v137\n"
        "v_lshlrev_b32 v16, 16, v69\n"
        "v_and_b32 v17, 0xffff0000, v69\n"
        "v_pk_mul_f32 v[138:139], v[138:139], v[16:17]\n"
        "v_cvt_pk_bf16_f32 v149, v138, v139\n"
        "v_lshlrev_b32 v14, 16, v70\n"
        "v_and_b32 v15, 0xffff0000, v70\n"
        "v_pk_mul_f32 v[140:141], v[140:141], v[14:15]\n"
        "v_cvt_pk_bf16_f32 v150, v140, v141\n"
        "v_lshlrev_b32 v16, 16, v71\n"
        "v_and_b32 v17, 0xffff0000, v71\n"
        "v_pk_mul_f32 v[142:143], v[142:143], v[16:17]\n"
        "v_cvt_pk_bf16_f32 v151, v142, v143\n"
        "global_store_dwordx4 v11, v[148:151], s[48:49] offset:1024 sc1\n"
        "s_add_u32 s48, s48, 24576\n"
        "s_addc_u32 s49, s49, 0\n"
        "ds_read_b128 v[68:71], v7 offset:26624\n"
        "v_accvgpr_read_b32 v128, a44\n"
        "v_accvgpr_read_b32 v129, a45\n"
        "v_accvgpr_read_b32 v130, a46\n"
        "v_accvgpr_read_b32 v131, a47\n"
        "v_accvgpr_read_b32 v132, a108\n"
        "v_accvgpr_read_b32 v133, a109\n"
        "v_accvgpr_read_b32 v134, a110\n"
        "v_accvgpr_read_b32 v135, a111\n"
        "s_waitcnt lgkmcnt(3)\n"
        "v_lshlrev_b32 v14, 16, v72\n"
        "v_and_b32 v15, 0xffff0000, v72\n"
        "v_pk_mul_f32 v[128:129], v[128:129], v[14:15]\n"
        "v_cvt_pk_bf16_f32 v144, v128, v129\n"
        "v_lshlrev_b32 v16, 16, v73\n"
        "v_and_b32 v17, 0xffff0000, v73\n"
        "v_pk_mul_f32 v[130:131], v[130:131], v[16:17]\n"
        "v_cvt_pk_bf16_f32 v145, v130, v131\n"
        "v_lshlrev_b32 v14, 16, v74\n"
        "v_and_b32 v15, 0xffff0000, v74\n"
        "v_pk_mul_f32 v[132:133], v[132:133], v[14:15]\n"
        "v_cvt_pk_bf16_f32 v146, v132, v133\n"
        "v_lshlrev_b32 v16, 16, v75\n"
        "v_and_b32 v17, 0xffff0000, v75\n"
        "v_pk_mul_f32 v[134:135], v[134:135], v[16:17]\n"
        "v_cvt_pk_bf16_f32 v147, v134, v135\n"
        "global_store_dwordx4 v11, v[144:147], s[48:49] offset:0 sc1\n"
        "ds_read_b128 v[72:75], v7 offset:25600\n"
        "v_accvgpr_read_b32 v136, a172\n"
        "v_accvgpr_read_b32 v137, a173\n"
        "v_accvgpr_read_b32 v138, a174\n"
        "v_accvgpr_read_b32 v139, a175\n"
        "v_accvgpr_read_b32 v140, a236\n"
        "v_accvgpr_read_b32 v141, a237\n"
        "v_accvgpr_read_b32 v142, a238\n"
        "v_accvgpr_read_b32 v143, a239\n"
        "s_waitcnt lgkmcnt(3)\n"
        "v_lshlrev_b32 v14, 16, v76\n"
        "v_and_b32 v15, 0xffff0000, v76\n"
        "v_pk_mul_f32 v[136:137], v[136:137], v[14:15]\n"
        "v_cvt_pk_bf16_f32 v148, v136, v137\n"
        "v_lshlrev_b32 v16, 16, v77\n"
        "v_and_b32 v17, 0xffff0000, v77\n"
        "v_pk_mul_f32 v[138:139], v[138:139], v[16:17]\n"
        "v_cvt_pk_bf16_f32 v149, v138, v139\n"
        "v_lshlrev_b32 v14, 16, v78\n"
        "v_and_b32 v15, 0xffff0000, v78\n"
        "v_pk_mul_f32 v[140:141], v[140:141], v[14:15]\n"
        "v_cvt_pk_bf16_f32 v150, v140, v141\n"
        "v_lshlrev_b32 v16, 16, v79\n"
        "v_and_b32 v17, 0xffff0000, v79\n"
        "v_pk_mul_f32 v[142:143], v[142:143], v[16:17]\n"
        "v_cvt_pk_bf16_f32 v151, v142, v143\n"
        "global_store_dwordx4 v11, v[148:151], s[48:49] offset:1024 sc1\n"
        "s_add_u32 s48, s48, 24576\n"
        "s_addc_u32 s49, s49, 0\n"
        "ds_read_b128 v[76:79], v7 offset:27648\n"
        "v_accvgpr_read_b32 v128, a48\n"
        "v_accvgpr_read_b32 v129, a49\n"
        "v_accvgpr_read_b32 v130, a50\n"
        "v_accvgpr_read_b32 v131, a51\n"
        "v_accvgpr_read_b32 v132, a112\n"
        "v_accvgpr_read_b32 v133, a113\n"
        "v_accvgpr_read_b32 v134, a114\n"
        "v_accvgpr_read_b32 v135, a115\n"
        "s_waitcnt lgkmcnt(3)\n"
        "v_lshlrev_b32 v14, 16, v64\n"
        "v_and_b32 v15, 0xffff0000, v64\n"
        "v_pk_mul_f32 v[128:129], v[128:129], v[14:15]\n"
        "v_cvt_pk_bf16_f32 v144, v128, v129\n"
        "v_lshlrev_b32 v16, 16, v65\n"
        "v_and_b32 v17, 0xffff0000, v65\n"
        "v_pk_mul_f32 v[130:131], v[130:131], v[16:17]\n"
        "v_cvt_pk_bf16_f32 v145, v130, v131\n"
        "v_lshlrev_b32 v14, 16, v66\n"
        "v_and_b32 v15, 0xffff0000, v66\n"
        "v_pk_mul_f32 v[132:133], v[132:133], v[14:15]\n"
        "v_cvt_pk_bf16_f32 v146, v132, v133\n"
        "v_lshlrev_b32 v16, 16, v67\n"
        "v_and_b32 v17, 0xffff0000, v67\n"
        "v_pk_mul_f32 v[134:135], v[134:135], v[16:17]\n"
        "v_cvt_pk_bf16_f32 v147, v134, v135\n"
        "global_store_dwordx4 v11, v[144:147], s[48:49] offset:0 sc1\n"
        "ds_read_b128 v[64:67], v7 offset:28672\n"
        "v_accvgpr_read_b32 v136, a176\n"
        "v_accvgpr_read_b32 v137, a177\n"
        "v_accvgpr_read_b32 v138, a178\n"
        "v_accvgpr_read_b32 v139, a179\n"
        "v_accvgpr_read_b32 v140, a240\n"
        "v_accvgpr_read_b32 v141, a241\n"
        "v_accvgpr_read_b32 v142, a242\n"
        "v_accvgpr_read_b32 v143, a243\n"
        "s_waitcnt lgkmcnt(3)\n"
        "v_lshlrev_b32 v14, 16, v68\n"
        "v_and_b32 v15, 0xffff0000, v68\n"
        "v_pk_mul_f32 v[136:137], v[136:137], v[14:15]\n"
        "v_cvt_pk_bf16_f32 v148, v136, v137\n"
        "v_lshlrev_b32 v16, 16, v69\n"
        "v_and_b32 v17, 0xffff0000, v69\n"
        "v_pk_mul_f32 v[138:139], v[138:139], v[16:17]\n"
        "v_cvt_pk_bf16_f32 v149, v138, v139\n"
        "v_lshlrev_b32 v14, 16, v70\n"
        "v_and_b32 v15, 0xffff0000, v70\n"
        "v_pk_mul_f32 v[140:141], v[140:141], v[14:15]\n"
        "v_cvt_pk_bf16_f32 v150, v140, v141\n"
        "v_lshlrev_b32 v16, 16, v71\n"
        "v_and_b32 v17, 0xffff0000, v71\n"
        "v_pk_mul_f32 v[142:143], v[142:143], v[16:17]\n"
        "v_cvt_pk_bf16_f32 v151, v142, v143\n"
        "global_store_dwordx4 v11, v[148:151], s[48:49] offset:1024 sc1\n"
        "s_add_u32 s48, s48, 24576\n"
        "s_addc_u32 s49, s49, 0\n"
        "ds_read_b128 v[68:71], v7 offset:30720\n"
        "v_accvgpr_read_b32 v128, a52\n"
        "v_accvgpr_read_b32 v129, a53\n"
        "v_accvgpr_read_b32 v130, a54\n"
        "v_accvgpr_read_b32 v131, a55\n"
        "v_accvgpr_read_b32 v132, a116\n"
        "v_accvgpr_read_b32 v133, a117\n"
        "v_accvgpr_read_b32 v134, a118\n"
        "v_accvgpr_read_b32 v135, a119\n"
        "s_waitcnt lgkmcnt(3)\n"
        "v_lshlrev_b32 v14, 16, v72\n"
        "v_and_b32 v15, 0xffff0000, v72\n"
        "v_pk_mul_f32 v[128:129], v[128:129], v[14:15]\n"
        "v_cvt_pk_bf16_f32 v144, v128, v129\n"
        "v_lshlrev_b32 v16, 16, v73\n"
        "v_and_b32 v17, 0xffff0000, v73\n"
        "v_pk_mul_f32 v[130:131], v[130:131], v[16:17]\n"
        "v_cvt_pk_bf16_f32 v145, v130, v131\n"
        "v_lshlrev_b32 v14, 16, v74\n"
        "v_and_b32 v15, 0xffff0000, v74\n"
        "v_pk_mul_f32 v[132:133], v[132:133], v[14:15]\n"
        "v_cvt_pk_bf16_f32 v146, v132, v133\n"
        "v_lshlrev_b32 v16, 16, v75\n"
        "v_and_b32 v17, 0xffff0000, v75\n"
        "v_pk_mul_f32 v[134:135], v[134:135], v[16:17]\n"
        "v_cvt_pk_bf16_f32 v147, v134, v135\n"
        "global_store_dwordx4 v11, v[144:147], s[48:49] offset:0 sc1\n"
        "ds_read_b128 v[72:75], v7 offset:29696\n"
        "v_accvgpr_read_b32 v136, a180\n"
        "v_accvgpr_read_b32 v137, a181\n"
        "v_accvgpr_read_b32 v138, a182\n"
        "v_accvgpr_read_b32 v139, a183\n"
        "v_accvgpr_read_b32 v140, a244\n"
        "v_accvgpr_read_b32 v141, a245\n"
        "v_accvgpr_read_b32 v142, a246\n"
        "v_accvgpr_read_b32 v143, a247\n"
        "s_waitcnt lgkmcnt(3)\n"
        "v_lshlrev_b32 v14, 16, v76\n"
        "v_and_b32 v15, 0xffff0000, v76\n"
        "v_pk_mul_f32 v[136:137], v[136:137], v[14:15]\n"
        "v_cvt_pk_bf16_f32 v148, v136, v137\n"
        "v_lshlrev_b32 v16, 16, v77\n"
        "v_and_b32 v17, 0xffff0000, v77\n"
        "v_pk_mul_f32 v[138:139], v[138:139], v[16:17]\n"
        "v_cvt_pk_bf16_f32 v149, v138, v139\n"
        "v_lshlrev_b32 v14, 16, v78\n"
        "v_and_b32 v15, 0xffff0000, v78\n"
        "v_pk_mul_f32 v[140:141], v[140:141], v[14:15]\n"
        "v_cvt_pk_bf16_f32 v150, v140, v141\n"
        "v_lshlrev_b32 v16, 16, v79\n"
        "v_and_b32 v17, 0xffff0000, v79\n"
        "v_pk_mul_f32 v[142:143], v[142:143], v[16:17]\n"
        "v_cvt_pk_bf16_f32 v151, v142, v143\n"
        "global_store_dwordx4 v11, v[148:151], s[48:49] offset:1024 sc1\n"
        "s_add_u32 s48, s48, 24576\n"
        "s_addc_u32 s49, s49, 0\n"
        "ds_read_b128 v[76:79], v7 offset:31744\n"
        "v_accvgpr_read_b32 v128, a56\n"
        "v_accvgpr_read_b32 v129, a57\n"
        "v_accvgpr_read_b32 v130, a58\n"
        "v_accvgpr_read_b32 v131, a59\n"
        "v_accvgpr_read_b32 v132, a120\n"
        "v_accvgpr_read_b32 v133, a121\n"
        "v_accvgpr_read_b32 v134, a122\n"
        "v_accvgpr_read_b32 v135, a123\n"
        "s_waitcnt lgkmcnt(3)\n"
        "v_lshlrev_b32 v14, 16, v64\n"
        "v_and_b32 v15, 0xffff0000, v64\n"
        "v_pk_mul_f32 v[128:129], v[128:129], v[14:15]\n"
        "v_cvt_pk_bf16_f32 v144, v128, v129\n"
        "v_lshlrev_b32 v16, 16, v65\n"
        "v_and_b32 v17, 0xffff0000, v65\n"
        "v_pk_mul_f32 v[130:131], v[130:131], v[16:17]\n"
        "v_cvt_pk_bf16_f32 v145, v130, v131\n"
        "v_lshlrev_b32 v14, 16, v66\n"
        "v_and_b32 v15, 0xffff0000, v66\n"
        "v_pk_mul_f32 v[132:133], v[132:133], v[14:15]\n"
        "v_cvt_pk_bf16_f32 v146, v132, v133\n"
        "v_lshlrev_b32 v16, 16, v67\n"
        "v_and_b32 v17, 0xffff0000, v67\n"
        "v_pk_mul_f32 v[134:135], v[134:135], v[16:17]\n"
        "v_cvt_pk_bf16_f32 v147, v134, v135\n"
        "global_store_dwordx4 v11, v[144:147], s[48:49] offset:0 sc1\n"
        "v_accvgpr_read_b32 v136, a184\n"
        "v_accvgpr_read_b32 v137, a185\n"
        "v_accvgpr_read_b32 v138, a186\n"
        "v_accvgpr_read_b32 v139, a187\n"
        "v_accvgpr_read_b32 v140, a248\n"
        "v_accvgpr_read_b32 v141, a249\n"
        "v_accvgpr_read_b32 v142, a250\n"
        "v_accvgpr_read_b32 v143, a251\n"
        "s_waitcnt lgkmcnt(2)\n"
        "v_lshlrev_b32 v14, 16, v68\n"
        "v_and_b32 v15, 0xffff0000, v68\n"
        "v_pk_mul_f32 v[136:137], v[136:137], v[14:15]\n"
        "v_cvt_pk_bf16_f32 v148, v136, v137\n"
        "v_lshlrev_b32 v16, 16, v69\n"
        "v_and_b32 v17, 0xffff0000, v69\n"
        "v_pk_mul_f32 v[138:139], v[138:139], v[16:17]\n"
        "v_cvt_pk_bf16_f32 v149, v138, v139\n"
        "v_lshlrev_b32 v14, 16, v70\n"
        "v_and_b32 v15, 0xffff0000, v70\n"
        "v_pk_mul_f32 v[140:141], v[140:141], v[14:15]\n"
        "v_cvt_pk_bf16_f32 v150, v140, v141\n"
        "v_lshlrev_b32 v16, 16, v71\n"
        "v_and_b32 v17, 0xffff0000, v71\n"
        "v_pk_mul_f32 v[142:143], v[142:143], v[16:17]\n"
        "v_cvt_pk_bf16_f32 v151, v142, v143\n"
        "global_store_dwordx4 v11, v[148:151], s[48:49] offset:1024 sc1\n"
        "s_add_u32 s48, s48, 24576\n"
        "s_addc_u32 s49, s49, 0\n"
        "v_accvgpr_read_b32 v128, a60\n"
        "v_accvgpr_read_b32 v129, a61\n"
        "v_accvgpr_read_b32 v130, a62\n"
        "v_accvgpr_read_b32 v131, a63\n"
        "v_accvgpr_read_b32 v132, a124\n"
        "v_accvgpr_read_b32 v133, a125\n"
        "v_accvgpr_read_b32 v134, a126\n"
        "v_accvgpr_read_b32 v135, a127\n"
        "s_waitcnt lgkmcnt(1)\n"
        "v_lshlrev_b32 v14, 16, v72\n"
        "v_and_b32 v15, 0xffff0000, v72\n"
        "v_pk_mul_f32 v[128:129], v[128:129], v[14:15]\n"
        "v_cvt_pk_bf16_f32 v144, v128, v129\n"
        "v_lshlrev_b32 v16, 16, v73\n"
        "v_and_b32 v17, 0xffff0000, v73\n"
        "v_pk_mul_f32 v[130:131], v[130:131], v[16:17]\n"
        "v_cvt_pk_bf16_f32 v145, v130, v131\n"
        "v_lshlrev_b32 v14, 16, v74\n"
        "v_and_b32 v15, 0xffff0000, v74\n"
        "v_pk_mul_f32 v[132:133], v[132:133], v[14:15]\n"
        "v_cvt_pk_bf16_f32 v146, v132, v133\n"
        "v_lshlrev_b32 v16, 16, v75\n"
        "v_and_b32 v17, 0xffff0000, v75\n"
        "v_pk_mul_f32 v[134:135], v[134:135], v[16:17]\n"
        "v_cvt_pk_bf16_f32 v147, v134, v135\n"
        "global_store_dwordx4 v11, v[144:147], s[48:49] offset:0 sc1\n"
        "v_accvgpr_read_b32 v136, a188\n"
        "v_accvgpr_read_b32 v137, a189\n"
        "v_accvgpr_read_b32 v138, a190\n"
        "v_accvgpr_read_b32 v139, a191\n"
        "v_accvgpr_read_b32 v140, a252\n"
        "v_accvgpr_read_b32 v141, a253\n"
        "v_accvgpr_read_b32 v142, a254\n"
        "v_accvgpr_read_b32 v143, a255\n"
        "s_waitcnt lgkmcnt(0)\n"
        "v_lshlrev_b32 v14, 16, v76\n"
        "v_and_b32 v15, 0xffff0000, v76\n"
        "v_pk_mul_f32 v[136:137], v[136:137], v[14:15]\n"
        "v_cvt_pk_bf16_f32 v148, v136, v137\n"
        "v_lshlrev_b32 v16, 16, v77\n"
        "v_and_b32 v17, 0xffff0000, v77\n"
        "v_pk_mul_f32 v[138:139], v[138:139], v[16:17]\n"
        "v_cvt_pk_bf16_f32 v149, v138, v139\n"
        "v_lshlrev_b32 v14, 16, v78\n"
        "v_and_b32 v15, 0xffff0000, v78\n"
        "v_pk_mul_f32 v[140:141], v[140:141], v[14:15]\n"
        "v_cvt_pk_bf16_f32 v150, v140, v141\n"
        "v_lshlrev_b32 v16, 16, v79\n"
        "v_and_b32 v17, 0xffff0000, v79\n"
        "v_pk_mul_f32 v[142:143], v[142:143], v[16:17]\n"
        "v_cvt_pk_bf16_f32 v151, v142, v143\n"
        "global_store_dwordx4 v11, v[148:151], s[48:49] offset:1024 sc1\n"
        "s_add_u32 s48, s48, 24576\n"
        "s_addc_u32 s49, s49, 0\n"
        "s_waitcnt vmcnt(0)\n"
        "s_mov_b64 exec, 0xff\n"
        "global_store_dword v2, v3, s[50:51] sc1\n"
        "s_mov_b64 exec, -1\n"
        "Lg2_exit:\n"
        "s_waitcnt vmcnt(0) lgkmcnt(0)\n"
        "s_endpgm\n"
            :: "s"(ws), "s"(w_sp), "s"(b_sp), "s"(x), "s"(par * 4 + xq), "s"(par), "s"((r - 116) >> 2), "s"(wave), "s"(karg) : "v0", "v1", "v2", "v3", "v4", "v5", "v6", "v7", "v8", "v9", "v10", "v11", "v12", "v13", "v14", "v15", "v16", "v17", "v18", "v19", "v20", "v21", "v22", "v23", "v24", "v25", "v26", "v27", "v28", "v29", "v30", "v31", "v32", "v33", "v34", "v35", "v36", "v37", "v38", "v39", "v40", "v41", "v42", "v43", "v44", "v45", "v46", "v47", "v48", "v49", "v50", "v51", "v52", "v53", "v54", "v55", "v56", "v57", "v58", "v59", "v60", "v61", "v62", "v63", "v64", "v65", "v66", "v67", "v68", "v69", "v70", "v71", "v72", "v73", "v74", "v75", "v76", "v77", "v78", "v79", "v80", "v81", "v82", "v83", "v84", "v85", "v86", "v87", "v88", "v89", "v90", "v91", "v92", "v93", "v94", "v95", "v96", "v97", "v98", "v99", "v100", "v101", "v102", "v103", "v104", "v105", "v106", "v107", "v108", "v109", "v110", "v111", "v112", "v113", "v114", "v115", "v116", "v117", "v118", "v119", "v120", "v121", "v122", "v123", "v124", "v125", "v126", "v127", "v128", "v129", "v130", "v131", "v132", "v133", "v134", "v135", "v136", "v137", "v138", "v139", "v140", "v141", "v142", "v143", "v144", "v145", "v146", "v147", "v148", "v149", "v150", "v151", "v152", "v153", "v154", "v155", "v156", "v157", "v158", "v159", "v160", "v161", "v162", "v163", "v164", "v165", "v166", "v167", "v168", "v169", "v170", "v171", "v172", "v173", "v174", "v175", "v176", "v177", "v178", "v179", "v180", "v181", "v182", "v183", "v184", "v185", "v186", "v187", "v188", "v189", "v190", "v191", "v192", "v193", "v194", "v195", "v196", "v197", "v198", "v199", "v200", "v201", "v202", "v203", "v204", "v205", "v206", "v207", "v208", "v209", "v210", "v211", "v212", "v213", "v214", "v215", "v216", "v217", "v218", "v219", "v220", "v221", "v222", "v223", "v224", "v225", "v226", "v227", "v228", "v229", "v230", "v231", "v232", "v233", "v234", "v235", "v236", "v237", "v238", "v239", "v240", "v241", "v242", "v243", "v244", "v245", "v246", "v247", "v248", "v249", "v250", "v251", "v252", "v253", "v254", "v255", "a0", "a1", "a2", "a3", "a4", "a5", "a6", "a7", "a8", "a9", "a10", "a11", "a12", "a13", "a14", "a15", "a16", "a17", "a18", "a19", "a20", "a21", "a22", "a23", "a24", "a25", "a26", "a27", "a28", "a29", "a30", "a31", "a32", "a33", "a34", "a35", "a36", "a37", "a38", "a39", "a40", "a41", "a42", "a43", "a44", "a45", "a46", "a47", "a48", "a49", "a50", "a51", "a52", "a53", "a54", "a55", "a56", "a57", "a58", "a59", "a60", "a61", "a62", "a63", "a64", "a65", "a66", "a67", "a68", "a69", "a70", "a71", "a72", "a73", "a74", "a75", "a76", "a77", "a78", "a79", "a80", "a81", "a82", "a83", "a84", "a85", "a86", "a87", "a88", "a89", "a90", "a91", "a92", "a93", "a94", "a95", "a96", "a97", "a98", "a99", "a100", "a101", "a102", "a103", "a104", "a105", "a106", "a107", "a108", "a109", "a110", "a111", "a112", "a113", "a114", "a115", "a116", "a117", "a118", "a119", "a120", "a121", "a122", "a123", "a124", "a125", "a126", "a127", "a128", "a129", "a130", "a131", "a132", "a133", "a134", "a135", "a136", "a137", "a138", "a139", "a140", "a141", "a142", "a143", "a144", "a145", "a146", "a147", "a148", "a149", "a150", "a151", "a152", "a153", "a154", "a155", "a156", "a157", "a158", "a159", "a160", "a161", "a162", "a163", "a164", "a165", "a166", "a167", "a168", "a169", "a170", "a171", "a172", "a173", "a174", "a175", "a176", "a177", "a178", "a179", "a180", "a181", "a182", "a183", "a184", "a185", "a186", "a187", "a188", "a189", "a190", "a191", "a192", "a193", "a194", "a195", "a196", "a197", "a198", "a199", "a200", "a201", "a202", "a203", "a204", "a205", "a206", "a207", "a208", "a209", "a210", "a211", "a212", "a213", "a214", "a215", "a216", "a217", "a218", "a219", "a220", "a221", "a222", "a223", "a224", "a225", "a226", "a227", "a228", "a229", "a230", "a231", "a232", "a233", "a234", "a235", "a236", "a237", "a238", "a239", "a240", "a241", "a242", "a243", "a244", "a245", "a246", "a247", "a248", "a249", "a250", "a251", "a252", "a253", "a254", "a255", "s34", "s35", "s36", "s37", "s38", "s39", "s40", "s41", "s42", "s43", "s44", "s45", "s46", "s47", "s48", "s49", "s50", "s51", "s52", "s53", "s54", "s55", "s56", "s57", "s58", "s59", "s60", "s61", "s62", "s63", "s64", "s65", "s66", "s67", "s68", "s69", "s70", "s71", "s72", "s73", "s74", "s75", "s76", "s77", "s78", "s79", "s80", "s81", "s82", "s83", "s84", "s85", "s86", "s87", "s88", "s89", "s90", "s91", "s92", "s93", "s94", "s95", "s96", "s97", "s98", "s99", "vcc", "scc", "memory");
        __builtin_amdgcn_endpgm();
    }
}

extern "C" __global__ void k_end() {}

extern "C" void kernel_launch(void* const* d_in, const int* in_sizes, int n_in, void* d_out, int out_size, void* d_ws, size_t ws_size,
                              hipStream_t stream) {
    const u16* x = (const u16*)d_in[0];
    char* ws = (char*)(((uintptr_t)d_ws + (2u << 20) - 1) & ~(uintptr_t)((2u << 20) - 1));
    hipFuncSetAttribute((const void*)k_rec, hipFuncAttributeMaxDynamicSharedMemorySize, 160 * 1024);
    k_rec<<<256, 256, 160 * 1024, stream>>>((const u16*)d_in[2], (const u16*)d_in[3], (const u16*)d_in[4], (const u16*)d_in[5], x, (const u16*)d_in[8],
                                            (const u16*)d_in[9], (const u16*)d_in[10], ws, (const u16*)d_in[11], (u16*)d_out, (const u16*)d_in[1]);
}
```
